```python
import torch

def _softmax(x, axis=-1):
    return torch.softmax(x, dim=axis)
def _gelu(x):
    return torch.nn.functional.gelu(x, approximate="tanh")
def _silu(x):
    return torch.nn.functional.silu(x)
def _sigmoid(x):
    return torch.sigmoid(x)
def _layernorm(x, axis=-1, eps=1e-5):
    m = x.mean(dim=axis, keepdim=True)
    v = x.var(dim=axis, keepdim=True, unbiased=False)
    return (x - m) / torch.sqrt(v + eps)
def _scatter_add(tgt, idx, src, axis=0):
    return tgt.clone().index_add_(axis, idx, src)
def _segment_sum(src, seg, n):
    out = torch.zeros((n,) + src.shape[1:], dtype=src.dtype)
    return out.index_add_(0, seg, src)
def _pad(x, pw):
    flat = tuple(v for pair in reversed(pw) for v in pair)
    return torch.nn.functional.pad(x, flat)

kernel_name = 'interleave_layers__sliding_local_dilated__ssm_conv_s4__prompt'

def setup_inputs():

    _w = (lambda a: (lambda t: t.to(torch.bfloat16) if t.is_floating_point() else t)(torch.as_tensor(a)))
    def _A_setup():
        B, S, D = 1, 2048, 512
        return {
            "x": _w((lambda *s: torch.randn(*s, dtype=torch.bfloat16) * 0.05)(B, S, D)),
            "wqkv": _w((lambda *s: torch.randn(*s, dtype=torch.bfloat16) * 0.05)(D, 3 * D)),
            "wo": _w((lambda *s: torch.randn(*s, dtype=torch.bfloat16) * 0.05)(D, D)),
        }
    _A_in = _A_setup()

    def _B_setup():
        B, L, D, N = 1, 2048, 512, 128
        A_re = -(lambda _x: torch.exp(torch.as_tensor(_x)))((lambda *s: torch.randn(*s, dtype=torch.bfloat16) * 3.2)(D, N))
        A_im = torch.pi * (lambda *s: torch.randn(*s, dtype=torch.bfloat16) * 0.1)(D, N)
        B_re = (lambda *s: torch.randn(*s, dtype=torch.bfloat16) * 0.1)(D, N)
        B_im = (lambda *s: torch.randn(*s, dtype=torch.bfloat16) * 0.1)(D, N)
        C_re = (lambda *s: torch.randn(*s, dtype=torch.bfloat16) * 0.1)(D, N)
        C_im = (lambda *s: torch.randn(*s, dtype=torch.bfloat16) * 0.1)(D, N)
        log_dt = (lambda *s: torch.randn(*s, dtype=torch.bfloat16) * 0.1)(D) * 0.5 - 4.0
        Dskip = (lambda *s: torch.randn(*s, dtype=torch.bfloat16) * 0.1)(D)
        return {
            "u": _w((lambda *s: torch.randn(*s, dtype=torch.bfloat16) * 0.05)(B, L, D)),
            "A_re": _w(A_re),
            "A_im": _w(A_im),
            "B_re": _w(B_re),
            "B_im": _w(B_im),
            "C_re": _w(C_re),
            "C_im": _w(C_im),
            "log_dt": _w(log_dt),
            "Dskip": _w(Dskip),
        }
    _B_in = _B_setup()

    return {
        "A_x": _A_in["x"],
        "A_wqkv": _A_in["wqkv"],
        "A_wo": _A_in["wo"],
        "B_u": _B_in["u"],
        "B_A_re": _B_in["A_re"],
        "B_A_im": _B_in["A_im"],
        "B_B_re": _B_in["B_re"],
        "B_B_im": _B_in["B_im"],
        "B_C_re": _B_in["C_re"],
        "B_C_im": _B_in["C_im"],
        "B_log_dt": _B_in["log_dt"],
        "B_Dskip": _B_in["Dskip"],
    }

def reference(A_x, A_wqkv, A_wo, B_u, B_A_re, B_A_im, B_B_re, B_B_im, B_C_re, B_C_im, B_log_dt, B_Dskip):
    def _A_ref(x, wqkv, wo):
        B, S, D = x.shape
        H = 32; Dh = D // H
        W, dil = 8, 2
        qkv = (lambda x, *d: x.permute(*d))((x @ wqkv).reshape(B, S, 3, H, Dh), 2, 0, 3, 1, 4)
        q, k, v = qkv[0], qkv[1], qkv[2]
        scale = Dh ** -0.5
        pos = torch.arange(S)
        off = torch.arange(W) * dil
        idx = pos[:, None] - off[None, :]
        mask = idx >= 0
        idx_c = torch.where(mask, idx, 0).reshape(-1)
        kg = k[:, :, idx_c, :].reshape(B, H, S, W, Dh)
        vg = v[:, :, idx_c, :].reshape(B, H, S, W, Dh)
        logits = torch.einsum("bhsd,bhswd->bhsw", q, kg) * scale
        logits = torch.where(mask[None, None, :, :], logits, -1e9)
        attn = _softmax(logits, axis=-1)
        o = torch.einsum("bhsw,bhswd->bhsd", attn, vg)
        return (lambda x, *d: x.permute(*d))(o, 0, 2, 1, 3).reshape(B, S, D) @ wo

    def _B_ref(u, A_re, A_im, B_re, B_im, C_re, C_im, log_dt, Dskip):
        Bsz, L, D = u.shape
        dt = torch.exp(log_dt)[:, None]
        A = A_re + 1j * A_im
        Bc = B_re + 1j * B_im
        Cc = C_re + 1j * C_im
        logdA = dt * A
        dB = dt * Bc
        l = (lambda x, dt: x.to(dt))(torch.arange(L), A_re.dtype)
        P = torch.exp(logdA[:, :, None] * l[None, None, :])
        K = torch.einsum("dn,dnl,dn->dl", Cc, P, dB).real
        L2 = 2 * L
        Kp = _pad(K, ((0, 0), (0, L)))
        up = (lambda x, *d: x.permute(*d))(u, 0, 2, 1)
        up = _pad(up, ((0, 0), (0, 0), (0, L)))
        Kf = (lambda x, axes=(-2,-1): torch.fft.rfft2(x.float(), dim=axes))(Kp[:, None, :], axes=(-2, -1))
        Uf = (lambda x, axes=(-2,-1): torch.fft.rfft2(x.float(), dim=axes))(up[:, :, None, :], axes=(-2, -1))
        Yf = Uf * Kf[None, :, :, :]
        y = (lambda x, s, axes=(-2,-1): torch.fft.irfft2(x, s=s, dim=axes).to(torch.bfloat16))(Yf, (1, L2), axes=(-2, -1))[:, :, 0, :L]
        y = (lambda x, *d: x.permute(*d))(y, 0, 2, 1) + u * Dskip[None, None, :]
        return y

    x = A_x
    x = x + _A_ref(_layernorm(x, axis=-1), A_wqkv, A_wo)
    x = x + _B_ref(_layernorm(x, axis=-1), B_A_re, B_A_im, B_B_re, B_B_im, B_C_re, B_C_im, B_log_dt, B_Dskip)
    x = x + _A_ref(_layernorm(x, axis=-1), A_wqkv, A_wo)
    x = x + _B_ref(_layernorm(x, axis=-1), B_A_re, B_A_im, B_B_re, B_B_im, B_C_re, B_C_im, B_log_dt, B_Dskip)
    return x
```

```cpp
#include <hip/hip_runtime.h>
#include <stdint.h>
typedef unsigned short u16;
constexpr int rnd_l(int v) { int e = 0; while ((v >> e) >= 256) ++e; if (!e) return v; const int h = 1 << (e - 1), q = v >> e, r = v & ((1 << e) - 1); return (q + ((r > h || (r == h && (q & 1))) ? 1 : 0)) << e; }
constexpr int slot_l(int v) { v = rnd_l(v); int e = 0; while ((v >> e) >= 256) ++e; return e * 128 + (v >> e); }
struct Tabs { u16 img[1024]; u16 prd[1024]; };
constexpr Tabs mk_tabs() {
  Tabs t{};
  for (int i = 0; i < 1024; ++i) {
    const int f = 1023 - i, o = f >> 3, a = (o - 74) / 12, m = f - 8 * (74 + 12 * a);
    int v = -1;
    if (f >= 32 && f - 32 <= 525) v = f - 32;
    if (o >= 74 && o < 122 && m < 64) v = a >= 2 ? 16 * (65 + m) - 8 * (a - 2) : (m < 62 ? 8 * (66 + m) + 4 * a : (m == 62 ? 1024 : -1));
    t.img[i] = (u16)(2 * (v < 0 ? 700 : slot_l(v)));
  }
  for (int w = 0; w < 1024; ++w) {
    const int lane = (w >> 2) & 63, r = (lane >> 4) * 4 + (w & 3), C = (w >> 9) * 16 + (lane & 15);
    const int lr = 8 * C + (((w >> 8) & 1) ? (r < 8 ? 256 * (r + 1) : 516 + 256 * (r - 8)) : (r < 8 ? r : 258 + 2 * (r - 8)));
    const bool ok = r < 11 && lr <= 2048 && rnd_l(lr) == lr;
    t.prd[w] = (u16)(2 * (ok ? slot_l(lr) : 704 + lane));
  }
  return t;
}
__device__ __attribute__((aligned(16))) const Tabs g_tabs = mk_tabs();

struct Args { const u16 *x, *w, *wo, *A_re, *A_im, *B_re, *B_im, *C_re, *C_im, *log_dt, *Dskip; u16* out; char* ws; };

__global__ __launch_bounds__(512, 2) void k_all(const Args ar) {
  __shared__ __attribute__((aligned(128))) char smem[163840];
  asm volatile(
    "s_load_dwordx16 s[8:23], s[0:1], 0\ns_load_dwordx8 s[24:31], s[0:1], 64\ns_load_dwordx2 s[32:33], s[0:1], 96\nv_readfirstlane_b32 s3, v0\ns_nop 0\ns_cmpk_gt_u32 s3, 255\n"
    "s_cbranch_scc1 .LH2\ns_lshr_b32 s3, s3, 6\ns_cmp_lt_u32 s3, 2\ns_cbranch_scc0 .LG1\ns_lshr_b32 s77, s2, 3\ns_cmp_eq_u32 s3, 0\ns_cbranch_scc0 .LL4\n"
    "v_lshl_add_u32 v254, v0, 5, s77\nv_lshlrev_b32 v254, 7, v254\ns_getpc_b64 vcc\n.Lpc:\ns_sub_u32 vcc_lo, vcc_lo, s4\ns_add_u32 vcc_lo, vcc_lo, .Lend-.Lpc-4\n"
    "v_min_u32 v254, vcc_lo, v254\ns_mov_b64 exec, 0xffff\nglobal_load_dword v254, v254, s[4:5]\ns_mov_b64 exec, -1\n.LL4:\ns_and_b32 s79, s2, 7\ns_mul_i32 s78, s3, 12288\n"
    "s_add_u32 m0, s78, 106496\nv_lshrrev_b32 v252, 4, v0\nv_xor_b32 v252, v252, v0\nv_and_b32 v252, 2, v252\nv_and_or_b32 v252, v0, 1, v252\nv_lshlrev_b32 v252, 4, v252\n"
    "v_lshrrev_b32 v253, 2, v0\nv_lshl_or_b32 v252, v253, 10, v252\ns_bfe_u32 s78, s2, 0x40003\nv_lshl_or_b32 v252, s78, 15, v252\nv_lshl_or_b32 v252, s79, 7, v252\n"
    "s_lshr_b32 s78, s2, 7\nv_lshl_or_b32 v252, s78, 6, v252\ns_waitcnt lgkmcnt(0)\nglobal_load_lds_dwordx4 v252, s[12:13]\ns_branch .LG1\n.LH2:\ns_bfe_u32 s3, s3, 0x20006\n"
    "v_and_b32 v77, 63, v0\ns_bfe_u32 s86, s2, 0x20003\ns_bfe_u32 s63, s2, 0x20001\ns_lshl2_add_u32 s63, s63, s86\ns_lshr_b32 s87, s2, 5\ns_lshl1_add_u32 s58, s86, s87\n"
    "s_and_b32 s58, s58, 7\ns_mov_b32 s60, s58\ns_and_b32 s86, s2, 1\ns_lshl3_add_u32 s87, s86, s87\ns_lshl_b32 s87, s87, 6\ns_mul_i32 s86, s3, 49152\ns_add_u32 s67, s87, s86\n"
    "s_lshl2_add_u32 s86, s63, s3\ns_lshl_b32 s68, s86, 15\nv_lshrrev_b32 v80, 4, v77\nv_xor_b32 v81, v80, v77\nv_and_b32 v83, 2, v81\nv_and_or_b32 v83, v77, 1, v83\n"
    "v_lshlrev_b32 v83, 4, v83\nv_lshrrev_b32 v82, 2, v77\ns_movk_i32 s86, 0xc00\nv_mad_u32_u24 v76, v82, s86, v83\nv_and_b32 v81, 7, v81\nv_lshlrev_b32 v81, 4, v81\n"
    "v_lshrrev_b32 v83, 3, v77\nv_lshl_or_b32 v72, v83, 10, v81\nv_xor_b32 v73, 0x2040, v72\nv_add_u32 v74, 0x4000, v72\nv_add_u32 v75, 0x4000, v73\ns_mul_i32 s64, s3, 12288\n"
    "s_add_u32 s64, s64, 98304\ns_lshl_b32 s69, s3, 10\ns_waitcnt lgkmcnt(0)\ns_add_u32 s10, s10, s67\ns_addc_u32 s11, s11, 0\ns_add_u32 s70, s8, s68\ns_addc_u32 s71, s9, 0\n"
    "s_mul_i32 s86, s60, 196608\ns_add_u32 s54, s10, s86\ns_addc_u32 s55, s11, 0\ns_lshl_b32 s86, s60, 12\ns_add_u32 m0, s69, s86\ns_add_u32 s60, s60, 1\n"
    "global_load_lds_dwordx4 v76, s[54:55]\ns_add_u32 m0, m0, 31744\ns_and_b32 s60, s60, 7\nglobal_load_lds_dwordx4 v76, s[54:55] offset:1024\ns_add_u32 m0, m0, 31744\ns_nop 0\n"
    "global_load_lds_dwordx4 v76, s[54:55] offset:2048\ns_add_u32 m0, s64, 0\ns_lshl_b32 s86, s58, 7\ns_add_u32 s52, s70, s86\ns_addc_u32 s53, s71, 0\n"
    "global_load_lds_dwordx4 v72, s[52:53]\ns_add_u32 m0, s64, 1024\ns_add_u32 s58, s58, 1\nglobal_load_lds_dwordx4 v73, s[52:53]\ns_add_u32 m0, s64, 2048\n"
    "s_and_b32 s58, s58, 7\nglobal_load_lds_dwordx4 v74, s[52:53]\ns_add_u32 m0, s64, 3072\ns_nop 0\nglobal_load_lds_dwordx4 v75, s[52:53]\ns_setprio 3\n"
    "s_mov_b32 s86, 16777216\ns_load_dword s34, s[32:33], s86\ns_mov_b32 s6, 0\ns_mov_b32 s7, 0\ns_and_b32 s35, s2, 7\ns_lshr_b32 s36, s2, 3\ns_bfe_u32 s44, s2, 0x20001\n"
    "s_bfe_u32 s45, s2, 0x20003\ns_lshl2_add_u32 s37, s44, s45\ns_and_b32 s46, s2, 1\ns_lshr_b32 s47, s2, 5\ns_lshl3_add_u32 s38, s46, s47\ns_bfe_u32 s48, s2, 0x30003\n"
    "s_lshl3_add_u32 s39, s35, s48\ns_lshr_b32 s40, s2, 6\ns_lshl_b32 s49, s35, 5\ns_add_u32 s41, s49, s36\ns_lshl1_add_u32 s50, s45, s47\ns_and_b32 s42, s50, 7\n"
    "s_lshl_b32 s86, s41, 2\ns_load_dword s96, s[26:27], s86\ns_load_dword s95, s[28:29], s86\nv_mov_b32 v96, 0\nv_mov_b32 v97, 0xf149f2ca\nv_bfe_u32 v100, v0, 4, 2\n"
    "v_lshlrev_b32 v99, 2, v100\nv_and_b32 v102, 7, v0\nv_add_u32 v101, 1, v102\nv_sub_u32 v98, v99, v101\nv_add_u32 v104, 8, v102\nv_sub_u32 v103, v104, v101\n"
    "v_cmp_le_u32 vcc, v98, v103\ns_nop 1\nv_cndmask_b32 v128, v97, v96, vcc\nv_add_u32 v106, 1, v99\nv_sub_u32 v105, v106, v101\nv_cmp_le_u32 vcc, v105, v103\ns_nop 1\n"
    "v_cndmask_b32 v129, v97, v96, vcc\nv_add_u32 v108, 2, v99\nv_sub_u32 v107, v108, v101\nv_cmp_le_u32 vcc, v107, v103\ns_nop 1\nv_cndmask_b32 v130, v97, v96, vcc\n"
    "v_add_u32 v110, 3, v99\nv_sub_u32 v109, v110, v101\nv_cmp_le_u32 vcc, v109, v103\ns_nop 1\nv_cndmask_b32 v131, v97, v96, vcc\ns_add_u32 s46, s3, 4\n"
    "s_mul_i32 s45, s46, 0xd00\ns_add_u32 s44, s45, 0x18040\nv_lshl_add_u32 v252, v100, 4, s44\nv_and_b32 v112, 15, v0\nv_max_u32 v111, 1, v112\ns_mov_b32 s47, 0x1a0\n"
    "v_mad_u32_u24 v252, v111, s47, v252\ns_add_u32 s48, s45, 0x18d00\nv_lshl_add_u32 v253, v100, 4, s48\nv_mad_u32_u24 v253, v102, s47, v253\nv_and_b32 v113, 3, v0\n"
    "s_add_u32 s49, s45, 0x18080\nv_lshl_add_u32 v254, v113, 3, s49\nv_bfe_u32 v116, v0, 2, 2\nv_add_u32 v115, v99, v116\nv_max_u32 v114, 1, v115\n"
    "v_mad_u32_u24 v254, v114, s47, v254\nv_bfe_u32 v117, v0, 3, 1\ns_lshr_b32 s59, s46, 1\ns_lshl2_add_u32 s57, s37, s59\ns_lshl_b32 s56, s57, 15\ns_lshl_b32 s65, s38, 11\n"
    "s_add_u32 s51, s56, s65\ns_and_b32 s72, s46, 1\ns_lshl_b32 s66, s72, 8\ns_add_u32 s50, s51, s66\nv_lshl_add_u32 v1, v117, 10, s50\nv_bfe_u32 v118, v0, 5, 1\n"
    "v_lshl_add_u32 v1, v118, 9, v1\nv_bfe_u32 v119, v0, 4, 1\nv_lshl_add_u32 v1, v119, 4, v1\nv_lshl_add_u32 v1, v102, 5, v1\nv_mov_b64 v[10:11], 0\nv_mov_b64 v[12:13], 0\n"
    "v_mov_b64 v[14:15], 0\nv_mov_b64 v[16:17], 0\nv_mov_b64 v[18:19], 0\nv_mov_b64 v[20:21], 0\nv_mov_b64 v[22:23], 0\nv_mov_b64 v[24:25], 0\nv_mov_b32 v96, 0x27f70\n"
    "s_lshr_b32 s45, s3, 1\ns_lshl_b32 s44, s45, 6\nv_and_b32 v99, 15, v0\ns_mov_b32 s46, 0x23c00\nv_lshl_add_u32 v98, v99, 10, s46\nv_bfe_u32 v101, v0, 4, 2\n"
    "v_xor_b32 v100, v101, v99\nv_lshl_add_u32 v98, v100, 4, v98\nv_xor_b32 v97, s44, v98\nv_cmp_gt_u32 vcc, 2, v99\ns_nop 1\nv_cndmask_b32 v26, v97, v96, vcc\n"
    "v_mov_b32 v102, 0\nv_cmp_gt_u32 vcc, 2, v99\ns_nop 1\nv_cndmask_b32 v27, -1, v102, vcc\nv_bfe_u32 v104, v0, 2, 2\nv_lshl_add_u32 v103, v101, 3, v104\n"
    "s_lshl_b32 s47, s45, 11\nv_lshl_add_u32 v29, v103, 6, s47\nv_and_b32 v105, 3, v0\nv_lshl_add_u32 v29, v105, 3, v29\nv_xor_b32 v107, s3, v101\nv_and_b32 v106, 1, v107\n"
    "v_lshl_add_u32 v29, v106, 5, v29\ns_add_u32 s49, s45, 1\ns_lshl_b32 s48, s49, 15\nv_add_u32 v28, s48, v29\ns_lshl_b32 s51, s3, 6\ns_add_u32 s50, s51, 0x27c80\n"
    "v_lshl_add_u32 v30, v101, 4, s50\ns_and_b32 s59, s3, 1\ns_lshl_b32 s57, s59, 6\ns_add_u32 s56, s57, 0x27c00\nv_lshl_add_u32 v31, v101, 4, s56\ns_lshl_b32 s66, s3, 7\n"
    "s_add_u32 s65, s66, 0x24000\nv_lshl_add_u32 v32, v99, 3, s65\ns_mov_b32 s72, 0x24000\nv_lshl_add_u32 v33, v99, 3, s72\ns_lshl_b32 s74, s3, 5\ns_add_u32 s73, s74, 0x18040\n"
    "v_lshl_add_u32 v34, v101, 3, s73\ns_mov_b32 s75, 0xd0\nv_mad_u32_u24 v34, v99, s75, v34\nv_and_b32 v108, 0xff, v0\nv_lshlrev_b32 v35, 4, v108\n"
    "s_lshl2_add_u32 s78, s40, s3\ns_lshl_b32 s77, s78, 9\ns_add_u32 s76, s77, 0x1002000\nv_lshl_add_u32 v36, v105, 7, s76\nv_and_b32 v109, 4, v0\n"
    "v_lshl_add_u32 v36, v109, 2, v36\nv_and_b32 v110, 63, v0\ns_lshl2_add_u32 s81, s37, s3\ns_lshl_b32 s80, s81, 15\ns_lshl_b32 s82, s38, 11\ns_add_u32 s79, s80, s82\n"
    "v_lshl_add_u32 v37, v110, 4, s79\ns_lshl_b32 s84, s78, 15\ns_add_u32 s83, s84, 0xe01000\nv_lshl_add_u32 v58, v110, 4, s83\ns_and_b32 s45, s38, 3\n"
    "s_lshl2_add_u32 s49, s37, s3\ns_lshl_b32 s48, s49, 9\ns_add_u32 s47, s48, 0x1008000\ns_lshr_b32 s51, s38, 2\ns_lshl_b32 s50, s51, 7\ns_add_u32 s46, s47, s50\n"
    "s_lshl2_add_u32 s44, s45, s46\nv_mov_b32 v39, s44\nv_mov_b32 v96, 0x27f70\ns_lshl_b32 s56, s3, 8\nv_and_b32 v99, 15, v0\ns_mov_b32 s57, 0x23c00\n"
    "v_lshl_add_u32 v98, v99, 10, s57\nv_bfe_u32 v101, v0, 4, 2\nv_xor_b32 v100, v101, v99\nv_lshl_add_u32 v98, v100, 4, v98\nv_xor_b32 v97, s56, v98\nv_cmp_gt_u32 vcc, 2, v99\n"
    "s_nop 1\nv_cndmask_b32 v40, v97, v96, vcc\nv_and_b32 v102, 63, v0\ns_lshl_b32 s59, s41, 9\nv_lshl_add_u32 v41, v102, 4, s59\ns_lshl_b32 s72, s41, 13\n"
    "s_lshl_b32 s73, s3, 11\ns_add_u32 s66, s72, s73\ns_add_u32 s65, s66, 0x200000\nv_lshl_add_u32 v60, v102, 4, s65\nv_and_b32 v103, 3, v0\ns_lshl_b32 s76, s2, 8\n"
    "s_lshl_b32 s77, s3, 6\ns_add_u32 s75, s76, s77\ns_add_u32 s74, s75, 0x400000\nv_lshl_add_u32 v61, v103, 4, s74\ns_and_b32 s80, s2, 3\ns_lshr_b32 s83, s2, 2\n"
    "s_lshl_b32 s82, s83, 7\ns_add_u32 s81, s82, 0x1004000\ns_lshl4_add_u32 s79, s80, s81\ns_lshl2_add_u32 s78, s3, s79\nv_mov_b32 v2, s78\ns_mul_i32 s85, s3, 0x1760\n"
    "s_add_u32 s84, s85, 0x18000\nv_lshl_add_u32 v3, v102, 3, s84\nv_and_b32 v96, 63, v0\ns_lshl2_add_u32 s46, s40, s3\ns_lshl_b32 s45, s46, 15\ns_add_u32 s44, s45, 0xe05000\n"
    "v_lshl_add_u32 v4, v96, 4, s44\ns_add_u32 s47, s45, 0xe07000\nv_lshl_add_u32 v5, v96, 4, s47\nv_and_b32 v97, 31, v0\ns_lshl_b32 s50, s39, 15\ns_lshl_b32 s51, s40, 8\n"
    "s_add_u32 s49, s50, s51\ns_lshl_b32 s56, s3, 6\ns_add_u32 s48, s49, s56\nv_lshl_add_u32 v6, v97, 10, s48\nv_bfe_u32 v98, v0, 5, 1\nv_lshl_add_u32 v6, v98, 4, v6\n"
    "s_add_u32 s57, s45, 0xe03000\nv_lshl_add_u32 v59, v96, 4, s57\ns_lshr_b32 s66, s3, 1\ns_lshl_b32 s65, s66, 8\ns_add_u32 s59, s65, 0x20000\nv_lshl_add_u32 v62, v96, 2, s59\n"
    "v_and_b32 v99, 0x7f, v0\ns_mov_b32 s72, 0x23c00\nv_lshl_add_u32 v63, v99, 3, s72\ns_lshl2_add_u32 s73, s66, 0x27d90\nv_mov_b32 v7, s73\ns_mul_i32 s75, s66, 0x600\n"
    "s_add_u32 s74, s75, 0x1ed78\nv_mov_b32 v8, s74\nv_bfe_u32 v100, v0, 4, 2\ns_mul_i32 s77, s3, 0x1760\ns_add_u32 s76, s77, 0x18000\nv_lshl_add_u32 v250, v100, 4, s76\n"
    "v_and_b32 v101, 15, v0\ns_mov_b32 s78, 0x220\nv_mad_u32_u24 v250, v101, s78, v250\ns_mul_i32 s80, s66, 0x4400\ns_add_u32 s79, s80, 0x21600\n"
    "v_lshl_add_u32 v251, v100, 4, s79\nv_mad_u32_u24 v251, v101, s78, v251\ns_lshl_b32 s86, s63, 17\ns_sub_u32 s86, s86, 0x4000\ns_max_i32 s86, s86, 0\ns_lshl_b32 s73, s3, 2\n"
    "s_max_u32 s87, s73, 2\ns_add_u32 s73, s73, 4\ns_lshl_b32 s72, s87, 10\ns_add_u32 s86, s86, s72\ns_add_u32 s52, s8, s86\ns_addc_u32 s53, s9, 0\ns_add_u32 s72, s72, 146432\n"
    ".LL5:\ns_mov_b32 m0, s72\nv_xor_b32 v82, s87, v77\nv_lshlrev_b32 v82, 4, v82\nglobal_load_lds_dwordx4 v82, s[52:53]\ns_add_u32 s52, s52, 1024\ns_addc_u32 s53, s53, 0\n"
    "s_add_u32 s72, s72, 1024\ns_add_u32 s87, s87, 1\ns_cmp_lg_u32 s87, s73\ns_cbranch_scc1 .LL5\ns_mul_i32 s86, s60, 196608\ns_add_u32 s54, s10, s86\ns_addc_u32 s55, s11, 0\n"
    "s_lshl_b32 s86, s60, 12\ns_add_u32 m0, s69, s86\ns_add_u32 s60, s60, 1\nglobal_load_lds_dwordx4 v76, s[54:55]\ns_add_u32 m0, m0, 31744\ns_and_b32 s60, s60, 7\n"
    "global_load_lds_dwordx4 v76, s[54:55] offset:1024\ns_add_u32 m0, m0, 31744\ns_nop 0\nglobal_load_lds_dwordx4 v76, s[54:55] offset:2048\ns_add_u32 m0, s64, 4096\n"
    "s_lshl_b32 s86, s58, 7\ns_add_u32 s52, s70, s86\ns_addc_u32 s53, s71, 0\nglobal_load_lds_dwordx4 v72, s[52:53]\ns_add_u32 m0, s64, 5120\ns_add_u32 s58, s58, 1\n"
    "global_load_lds_dwordx4 v73, s[52:53]\ns_add_u32 m0, s64, 6144\ns_and_b32 s58, s58, 7\nglobal_load_lds_dwordx4 v74, s[52:53]\ns_add_u32 m0, s64, 7168\ns_nop 0\n"
    "global_load_lds_dwordx4 v75, s[52:53]\nv_mov_b32 v232, -1\nv_mov_b32 v233, -1\nv_mov_b32 v234, -1\nv_mov_b32 v235, -1\nv_bitop3_b32 v84, 64, v27, v26 bitop3:0x6a\n"
    "s_mov_b32 s61, s42\ns_mov_b32 s62, s42\ns_movk_i32 s86, 128\nv_bitop3_b32 v85, 64, v27, v40 bitop3:0x6a\nv_bitop3_b32 v86, s86, v27, v40 bitop3:0x6a\n"
    "v_bitop3_b32 v87, s86, v27, v85 bitop3:0x6a\ns_lshl_b32 s87, s61, 12\ns_lshl_b32 s86, s61, 7\nv_add_u32 v120, s87, v28\nv_add_u32 v122, s87, v29\ns_add_u32 s61, s61, 1\n"
    "v_xor_b32 v121, 0x800, v120\ns_and_b32 s61, s61, 7\nv_bitop3_b32 v88, s86, v27, v26 bitop3:0x6a\nv_bitop3_b32 v89, s86, v27, v84 bitop3:0x6a\n"
    "s_waitcnt vmcnt(7) lgkmcnt(0)\ns_barrier\ns_setprio 0\nds_read_b128 v[132:135], v88\nds_read_b128 v[136:139], v89\nds_read_b64_tr_b16 v[96:97], v120\n"
    "ds_read_b64_tr_b16 v[98:99], v120 offset:256\nds_read_b64_tr_b16 v[100:101], v121\nds_read_b64_tr_b16 v[102:103], v121 offset:256\nds_read_b64_tr_b16 v[104:105], v122\n"
    "ds_read_b64_tr_b16 v[106:107], v122 offset:256\ns_setprio 0\ns_mul_i32 s86, s60, 196608\ns_add_u32 s54, s10, s86\ns_addc_u32 s55, s11, 0\ns_lshl_b32 s86, s60, 12\n"
    "s_add_u32 m0, s69, s86\ns_add_u32 s60, s60, 1\nglobal_load_lds_dwordx4 v76, s[54:55]\ns_add_u32 m0, m0, 31744\ns_and_b32 s60, s60, 7\n"
    "global_load_lds_dwordx4 v76, s[54:55] offset:1024\ns_add_u32 m0, m0, 31744\ns_nop 0\nglobal_load_lds_dwordx4 v76, s[54:55] offset:2048\ns_add_u32 m0, s64, 8192\n"
    "s_lshl_b32 s86, s58, 7\ns_add_u32 s52, s70, s86\ns_addc_u32 s53, s71, 0\nglobal_load_lds_dwordx4 v72, s[52:53]\ns_add_u32 m0, s64, 9216\ns_add_u32 s58, s58, 1\n"
    "global_load_lds_dwordx4 v73, s[52:53]\ns_add_u32 m0, s64, 10240\ns_and_b32 s58, s58, 7\nglobal_load_lds_dwordx4 v74, s[52:53]\ns_add_u32 m0, s64, 11264\ns_nop 0\n"
    "global_load_lds_dwordx4 v75, s[52:53]\ns_setprio 0\ns_lshl_b32 s97, s96, 16\ns_and_b32 s96, s96, 0xffff0000\ns_cmp_lt_u32 s3, 2\ns_cselect_b32 s96, s97, s96\n"
    "s_mov_b32 s97, 0x3fb8aa3b\nv_mov_b32 v108, s96\nv_mul_f32 v109, s97, v108\nv_fma_f32 v110, s97, v108, -v109\nv_exp_f32 v109, v109\nv_fmac_f32 v110, 0x32a57060, v108\n"
    "v_mul_f32 v110, 0x3f317218, v110\nv_fmac_f32 v109, v109, v110\nv_cvt_pk_bf16_f32 v109, 0, v109\nv_mul_f32 v108, s97, v109\nv_mul_f32 v110, 0xbe22f983, v109\n"
    "v_readfirstlane_b32 s92, v109\nv_readfirstlane_b32 s93, v108\nv_readfirstlane_b32 s94, v110\ns_and_b32 s97, s95, 0xffff0000\ns_lshl_b32 s96, s95, 16\n"
    "s_lshl_b32 s87, s61, 12\ns_lshl_b32 s86, s61, 7\nv_add_u32 v120, s87, v28\nv_add_u32 v122, s87, v29\ns_add_u32 s61, s61, 1\nv_xor_b32 v121, 0x800, v120\n"
    "s_and_b32 s61, s61, 7\nv_bitop3_b32 v88, s86, v27, v26 bitop3:0x6a\nv_bitop3_b32 v89, s86, v27, v84 bitop3:0x6a\ns_waitcnt vmcnt(7) lgkmcnt(0)\ns_barrier\ns_setprio 2\n"
    "v_mfma_f32_16x16x32_bf16 v[64:67], v[96:99], v[132:135], 0\nv_mfma_f32_16x16x32_bf16 v[68:71], v[104:107], v[132:135], 0\n"
    "v_mfma_f32_16x16x32_bf16 v[64:67], v[100:103], v[136:139], v[64:67]\ns_setprio 0\nds_read_b128 v[132:135], v88\nds_read_b128 v[136:139], v89\n"
    "ds_read_b64_tr_b16 v[96:97], v120\nds_read_b64_tr_b16 v[98:99], v120 offset:256\nds_read_b64_tr_b16 v[100:101], v121\nds_read_b64_tr_b16 v[102:103], v121 offset:256\n"
    "ds_read_b64_tr_b16 v[104:105], v122\nds_read_b64_tr_b16 v[106:107], v122 offset:256\ns_setprio 0\ns_mul_i32 s86, s60, 196608\ns_add_u32 s54, s10, s86\n"
    "s_addc_u32 s55, s11, 0\ns_lshl_b32 s86, s60, 12\ns_add_u32 m0, s69, s86\ns_add_u32 s60, s60, 1\nglobal_load_lds_dwordx4 v76, s[54:55]\ns_add_u32 m0, m0, 31744\n"
    "s_and_b32 s60, s60, 7\nglobal_load_lds_dwordx4 v76, s[54:55] offset:1024\ns_add_u32 m0, m0, 31744\ns_nop 0\nglobal_load_lds_dwordx4 v76, s[54:55] offset:2048\n"
    "s_add_u32 m0, s64, 0\ns_lshl_b32 s86, s58, 7\ns_add_u32 s52, s70, s86\ns_addc_u32 s53, s71, 0\nglobal_load_lds_dwordx4 v72, s[52:53]\ns_add_u32 m0, s64, 1024\n"
    "s_add_u32 s58, s58, 1\nglobal_load_lds_dwordx4 v73, s[52:53]\ns_add_u32 m0, s64, 2048\ns_and_b32 s58, s58, 7\nglobal_load_lds_dwordx4 v74, s[52:53]\n"
    "s_add_u32 m0, s64, 3072\ns_nop 0\nglobal_load_lds_dwordx4 v75, s[52:53]\ns_setprio 0\nds_read_b128 v[196:199], v40\nds_read_b128 v[200:203], v85\n"
    "ds_read_b128 v[204:207], v86\nds_read_b128 v[208:211], v87\ns_lshl_b32 s87, s61, 12\ns_lshl_b32 s86, s61, 7\nv_add_u32 v120, s87, v28\nv_add_u32 v122, s87, v29\n"
    "s_add_u32 s61, s61, 1\nv_xor_b32 v121, 0x800, v120\ns_and_b32 s61, s61, 7\nv_bitop3_b32 v88, s86, v27, v26 bitop3:0x6a\nv_bitop3_b32 v89, s86, v27, v84 bitop3:0x6a\n"
    "s_waitcnt vmcnt(7) lgkmcnt(0)\ns_barrier\ns_setprio 2\nv_mfma_f32_16x16x32_bf16 v[64:67], v[96:99], v[132:135], v[64:67]\n"
    "v_mfma_f32_16x16x32_bf16 v[68:71], v[104:107], v[132:135], v[68:71]\nv_mfma_f32_16x16x32_bf16 v[64:67], v[100:103], v[136:139], v[64:67]\n"
    "v_mfma_f32_16x16x32_bf16 v[92:95], v[196:199], v[196:199], 0\nv_mfma_f32_16x16x32_bf16 v[92:95], v[200:203], v[200:203], v[92:95]\n"
    "v_mfma_f32_16x16x32_bf16 v[92:95], v[204:207], v[204:207], v[92:95]\nv_mfma_f32_16x16x32_bf16 v[92:95], v[208:211], v[208:211], v[92:95]\ns_setprio 0\n"
    "ds_read_b128 v[132:135], v88\nds_read_b128 v[136:139], v89\nds_read_b64_tr_b16 v[96:97], v120\nds_read_b64_tr_b16 v[98:99], v120 offset:256\n"
    "ds_read_b64_tr_b16 v[100:101], v121\nds_read_b64_tr_b16 v[102:103], v121 offset:256\nds_read_b64_tr_b16 v[104:105], v122\nds_read_b64_tr_b16 v[106:107], v122 offset:256\n"
    "s_setprio 0\ns_mul_i32 s86, s60, 196608\ns_add_u32 s54, s10, s86\ns_addc_u32 s55, s11, 0\ns_lshl_b32 s86, s60, 12\ns_add_u32 m0, s69, s86\ns_add_u32 s60, s60, 1\n"
    "global_load_lds_dwordx4 v76, s[54:55]\ns_add_u32 m0, m0, 31744\ns_and_b32 s60, s60, 7\nglobal_load_lds_dwordx4 v76, s[54:55] offset:1024\ns_add_u32 m0, m0, 31744\ns_nop 0\n"
    "global_load_lds_dwordx4 v76, s[54:55] offset:2048\ns_add_u32 m0, s64, 4096\ns_lshl_b32 s86, s58, 7\ns_add_u32 s52, s70, s86\ns_addc_u32 s53, s71, 0\n"
    "global_load_lds_dwordx4 v72, s[52:53]\ns_add_u32 m0, s64, 5120\ns_add_u32 s58, s58, 1\nglobal_load_lds_dwordx4 v73, s[52:53]\ns_add_u32 m0, s64, 6144\n"
    "s_and_b32 s58, s58, 7\nglobal_load_lds_dwordx4 v74, s[52:53]\ns_add_u32 m0, s64, 7168\ns_nop 0\nglobal_load_lds_dwordx4 v75, s[52:53]\ns_setprio 0\n"
    "s_lshl_b32 s87, s61, 12\ns_lshl_b32 s86, s61, 7\nv_add_u32 v120, s87, v28\nv_add_u32 v122, s87, v29\ns_add_u32 s61, s61, 1\nv_xor_b32 v121, 0x800, v120\n"
    "s_and_b32 s61, s61, 7\nv_bitop3_b32 v88, s86, v27, v26 bitop3:0x6a\nv_bitop3_b32 v89, s86, v27, v84 bitop3:0x6a\ns_waitcnt vmcnt(7) lgkmcnt(0)\ns_barrier\ns_setprio 2\n"
    "v_mfma_f32_16x16x32_bf16 v[64:67], v[96:99], v[132:135], v[64:67]\nv_mfma_f32_16x16x32_bf16 v[68:71], v[104:107], v[132:135], v[68:71]\n"
    "v_mfma_f32_16x16x32_bf16 v[64:67], v[100:103], v[136:139], v[64:67]\ns_setprio 0\nds_read_b128 v[132:135], v88\nds_read_b128 v[136:139], v89\n"
    "ds_read_b64_tr_b16 v[96:97], v120\nds_read_b64_tr_b16 v[98:99], v120 offset:256\nds_read_b64_tr_b16 v[100:101], v121\nds_read_b64_tr_b16 v[102:103], v121 offset:256\n"
    "ds_read_b64_tr_b16 v[104:105], v122\nds_read_b64_tr_b16 v[106:107], v122 offset:256\ns_setprio 0\ns_mul_i32 s86, s60, 196608\ns_add_u32 s54, s10, s86\n"
    "s_addc_u32 s55, s11, 0\ns_lshl_b32 s86, s60, 12\ns_add_u32 m0, s69, s86\ns_add_u32 s60, s60, 1\nglobal_load_lds_dwordx4 v76, s[54:55]\ns_add_u32 m0, m0, 31744\n"
    "s_and_b32 s60, s60, 7\nglobal_load_lds_dwordx4 v76, s[54:55] offset:1024\ns_add_u32 m0, m0, 31744\ns_nop 0\nglobal_load_lds_dwordx4 v76, s[54:55] offset:2048\n"
    "s_add_u32 m0, s64, 8192\ns_lshl_b32 s86, s58, 7\ns_add_u32 s52, s70, s86\ns_addc_u32 s53, s71, 0\nglobal_load_lds_dwordx4 v72, s[52:53]\ns_add_u32 m0, s64, 9216\n"
    "s_add_u32 s58, s58, 1\nglobal_load_lds_dwordx4 v73, s[52:53]\ns_add_u32 m0, s64, 10240\ns_and_b32 s58, s58, 7\nglobal_load_lds_dwordx4 v74, s[52:53]\n"
    "s_add_u32 m0, s64, 11264\ns_nop 0\nglobal_load_lds_dwordx4 v75, s[52:53]\ns_setprio 0\ns_mov_b64 exec, 0xffff\nds_write_b32 v32, v93\ns_mov_b32 exec_lo, 0x100001\n"
    "s_mov_b32 exec_hi, 0x10000100\nds_write_b32 v32, v92 offset:4\ns_mov_b32 exec_lo, 0x200002\ns_mov_b32 exec_hi, 0x20000200\nds_write_b32 v32, v93 offset:4\n"
    "s_mov_b32 exec_lo, 0x400004\ns_mov_b32 exec_hi, 0x40000400\nds_write_b32 v32, v94 offset:4\ns_mov_b32 exec_lo, 0x800008\ns_mov_b32 exec_hi, 0x80000800\n"
    "ds_write_b32 v32, v95 offset:4\ns_mov_b64 exec, -1\ns_lshl_b32 s87, s61, 12\ns_lshl_b32 s86, s61, 7\nv_add_u32 v120, s87, v28\nv_add_u32 v122, s87, v29\n"
    "s_add_u32 s61, s61, 1\nv_xor_b32 v121, 0x800, v120\ns_and_b32 s61, s61, 7\nv_bitop3_b32 v88, s86, v27, v26 bitop3:0x6a\nv_bitop3_b32 v89, s86, v27, v84 bitop3:0x6a\n"
    "s_waitcnt vmcnt(7) lgkmcnt(0)\ns_barrier\ns_setprio 2\nv_mfma_f32_16x16x32_bf16 v[64:67], v[96:99], v[132:135], v[64:67]\n"
    "v_mfma_f32_16x16x32_bf16 v[68:71], v[104:107], v[132:135], v[68:71]\nv_mfma_f32_16x16x32_bf16 v[64:67], v[100:103], v[136:139], v[64:67]\ns_setprio 0\n"
    "ds_read_b128 v[132:135], v88\nds_read_b128 v[136:139], v89\nds_read_b64_tr_b16 v[96:97], v120\nds_read_b64_tr_b16 v[98:99], v120 offset:256\n"
    "ds_read_b64_tr_b16 v[100:101], v121\nds_read_b64_tr_b16 v[102:103], v121 offset:256\nds_read_b64_tr_b16 v[104:105], v122\nds_read_b64_tr_b16 v[106:107], v122 offset:256\n"
    "s_setprio 0\ns_mul_i32 s86, s60, 196608\ns_add_u32 s54, s10, s86\ns_addc_u32 s55, s11, 0\ns_lshl_b32 s86, s60, 12\ns_add_u32 m0, s69, s86\ns_add_u32 s60, s60, 1\n"
    "global_load_lds_dwordx4 v76, s[54:55]\ns_add_u32 m0, m0, 31744\ns_and_b32 s60, s60, 7\nglobal_load_lds_dwordx4 v76, s[54:55] offset:1024\ns_add_u32 m0, m0, 31744\ns_nop 0\n"
    "global_load_lds_dwordx4 v76, s[54:55] offset:2048\ns_add_u32 m0, s64, 0\ns_lshl_b32 s86, s58, 7\ns_add_u32 s52, s70, s86\ns_addc_u32 s53, s71, 0\n"
    "global_load_lds_dwordx4 v72, s[52:53]\ns_add_u32 m0, s64, 1024\ns_add_u32 s58, s58, 1\nglobal_load_lds_dwordx4 v73, s[52:53]\ns_add_u32 m0, s64, 2048\n"
    "s_and_b32 s58, s58, 7\nglobal_load_lds_dwordx4 v74, s[52:53]\ns_add_u32 m0, s64, 3072\ns_nop 0\nglobal_load_lds_dwordx4 v75, s[52:53]\ns_setprio 0\n"
    "ds_read_b64 v[108:109], v33 offset:0\nds_read_b64 v[110:111], v33 offset:128\nds_read_b64 v[112:113], v33 offset:256\nds_read_b64 v[114:115], v33 offset:384\n"
    "s_lshl_b32 s87, s61, 12\ns_lshl_b32 s86, s61, 7\nv_add_u32 v120, s87, v28\nv_add_u32 v122, s87, v29\ns_add_u32 s61, s61, 1\nv_xor_b32 v121, 0x800, v120\n"
    "s_and_b32 s61, s61, 7\nv_bitop3_b32 v88, s86, v27, v26 bitop3:0x6a\nv_bitop3_b32 v89, s86, v27, v84 bitop3:0x6a\ns_waitcnt vmcnt(7) lgkmcnt(0)\ns_barrier\ns_setprio 2\n"
    "v_mfma_f32_16x16x32_bf16 v[64:67], v[96:99], v[132:135], v[64:67]\nv_mfma_f32_16x16x32_bf16 v[68:71], v[104:107], v[132:135], v[68:71]\n"
    "v_mfma_f32_16x16x32_bf16 v[64:67], v[100:103], v[136:139], v[64:67]\ns_setprio 0\nds_read_b128 v[132:135], v88\nds_read_b128 v[136:139], v89\n"
    "ds_read_b64_tr_b16 v[96:97], v120\nds_read_b64_tr_b16 v[98:99], v120 offset:256\nds_read_b64_tr_b16 v[100:101], v121\nds_read_b64_tr_b16 v[102:103], v121 offset:256\n"
    "ds_read_b64_tr_b16 v[104:105], v122\nds_read_b64_tr_b16 v[106:107], v122 offset:256\ns_setprio 0\ns_mul_i32 s86, s60, 196608\ns_add_u32 s54, s10, s86\n"
    "s_addc_u32 s55, s11, 0\ns_lshl_b32 s86, s60, 12\ns_add_u32 m0, s69, s86\ns_add_u32 s60, s60, 1\nglobal_load_lds_dwordx4 v76, s[54:55]\ns_add_u32 m0, m0, 31744\n"
    "s_and_b32 s60, s60, 7\nglobal_load_lds_dwordx4 v76, s[54:55] offset:1024\ns_add_u32 m0, m0, 31744\ns_nop 0\nglobal_load_lds_dwordx4 v76, s[54:55] offset:2048\n"
    "s_add_u32 m0, s64, 4096\ns_lshl_b32 s86, s58, 7\ns_add_u32 s52, s70, s86\ns_addc_u32 s53, s71, 0\nglobal_load_lds_dwordx4 v72, s[52:53]\ns_add_u32 m0, s64, 5120\n"
    "s_add_u32 s58, s58, 1\nglobal_load_lds_dwordx4 v73, s[52:53]\ns_add_u32 m0, s64, 6144\ns_and_b32 s58, s58, 7\nglobal_load_lds_dwordx4 v74, s[52:53]\n"
    "s_add_u32 m0, s64, 7168\ns_nop 0\nglobal_load_lds_dwordx4 v75, s[52:53]\ns_setprio 0\ns_lshl_b32 s74, s3, 9\ns_add_u32 s74, s74, 131072\ns_bitcmp1_b32 s3, 0\n"
    "s_cselect_b64 s[76:77], s[16:17], s[14:15]\ns_cselect_b64 s[72:73], s[20:21], s[18:19]\ns_cselect_b64 s[78:79], s[24:25], s[22:23]\ns_bitcmp1_b32 s3, 1\n"
    "s_cselect_b64 s[76:77], s[72:73], s[76:77]\ns_lshl_b32 s87, s61, 12\ns_lshl_b32 s86, s61, 7\nv_add_u32 v120, s87, v28\nv_add_u32 v122, s87, v29\ns_add_u32 s61, s61, 1\n"
    "v_xor_b32 v121, 0x800, v120\ns_and_b32 s61, s61, 7\nv_bitop3_b32 v88, s86, v27, v26 bitop3:0x6a\nv_bitop3_b32 v89, s86, v27, v84 bitop3:0x6a\n"
    "s_waitcnt vmcnt(7) lgkmcnt(0)\ns_barrier\ns_setprio 2\nv_mfma_f32_16x16x32_bf16 v[64:67], v[96:99], v[132:135], v[64:67]\n"
    "v_mfma_f32_16x16x32_bf16 v[68:71], v[104:107], v[132:135], v[68:71]\nv_mfma_f32_16x16x32_bf16 v[64:67], v[100:103], v[136:139], v[64:67]\ns_setprio 0\n"
    "ds_read_b128 v[132:135], v88\nds_read_b128 v[136:139], v89\nds_read_b64_tr_b16 v[96:97], v120\nds_read_b64_tr_b16 v[98:99], v120 offset:256\n"
    "ds_read_b64_tr_b16 v[100:101], v121\nds_read_b64_tr_b16 v[102:103], v121 offset:256\nds_read_b64_tr_b16 v[104:105], v122\nds_read_b64_tr_b16 v[106:107], v122 offset:256\n"
    "s_setprio 0\nglobal_store_dwordx4 v37, v[232:235], s[32:33] offset:0 sc1\nglobal_store_dwordx4 v37, v[232:235], s[32:33] offset:1024 sc1\ns_mov_b32 exec_hi, 0\n"
    "s_cmp_lt_u32 s3, 2\ns_cbranch_scc0 .LL6\ns_add_u32 m0, s74, 2048\ns_nop 0\nglobal_load_lds_dwordx4 v41, s[78:79]\n.LL6:\ns_mov_b32 m0, s74\ns_nop 0\n"
    "global_load_lds_dwordx4 v41, s[76:77]\ns_mov_b64 exec, -1\ns_add_u32 m0, s69, 143360\ns_nop 0\nglobal_load_lds_dwordx4 v35, s[4:5]\n"
    "global_load_dwordx4 v[140:143], v36, s[32:33] sc1\nglobal_store_dwordx4 v60, v[232:235], s[32:33] offset:0 sc1\n"
    "global_store_dwordx4 v60, v[232:235], s[32:33] offset:1024 sc1\ns_mov_b64 exec, 15\nglobal_store_dwordx4 v61, v[232:235], s[32:33] sc1\ns_mov_b64 exec, -1\ns_setprio 0\n"
    "v_add_f32 v108, v108, v110\nv_add_f32 v109, v109, v111\nv_add_f32 v112, v112, v114\nv_add_f32 v113, v113, v115\nv_add_f32 v108, v108, v112\nv_add_f32 v109, v109, v113\n"
    "s_mov_b32 s86, 0x3b000000\nv_mul_f32 v108, s86, v108\nv_mul_f32 v110, v108, v108\nv_fma_f32 v109, v109, s86, -v110\nv_max_f32 v109, 0, v109\n"
    "v_cvt_pk_bf16_f32 v109, 0, v109\nv_add_f32 v109, 0x3727c5ac, v109\nv_cvt_pk_bf16_f32 v109, 0, v109\nv_sqrt_f32 v109, v109\ns_nop 0\nv_cvt_pk_bf16_f32 v109, 0, v109\n"
    "v_rcp_f32 v109, v109\nv_cvt_pk_bf16_f32 v90, 0, -v108\ns_cmp_lt_u32 s3, 2\ns_cbranch_scc1 .LL7\ns_waitcnt vmcnt(8)\n.LL7:\ns_lshl_b32 s87, s61, 12\ns_lshl_b32 s86, s61, 7\n"
    "v_add_u32 v120, s87, v28\nv_add_u32 v122, s87, v29\ns_add_u32 s61, s61, 1\nv_xor_b32 v121, 0x800, v120\ns_and_b32 s61, s61, 7\nv_bitop3_b32 v88, s86, v27, v26 bitop3:0x6a\n"
    "v_bitop3_b32 v89, s86, v27, v84 bitop3:0x6a\ns_waitcnt vmcnt(9) lgkmcnt(0)\ns_barrier\ns_setprio 2\nv_mfma_f32_16x16x32_bf16 v[64:67], v[96:99], v[132:135], v[64:67]\n"
    "v_mfma_f32_16x16x32_bf16 v[68:71], v[104:107], v[132:135], v[68:71]\nv_mfma_f32_16x16x32_bf16 v[64:67], v[100:103], v[136:139], v[64:67]\ns_setprio 0\n"
    "ds_read_b128 v[132:135], v88\nds_read_b128 v[136:139], v89\nds_read_b64_tr_b16 v[96:97], v120\nds_read_b64_tr_b16 v[98:99], v120 offset:256\n"
    "ds_read_b64_tr_b16 v[100:101], v121\nds_read_b64_tr_b16 v[102:103], v121 offset:256\nds_read_b64_tr_b16 v[104:105], v122\nds_read_b64_tr_b16 v[106:107], v122 offset:256\n"
    "s_setprio 0\ns_setprio 0\ns_waitcnt lgkmcnt(0)\ns_setprio 2\nv_mfma_f32_16x16x32_bf16 v[64:67], v[96:99], v[132:135], v[64:67]\n"
    "v_mfma_f32_16x16x32_bf16 v[68:71], v[104:107], v[132:135], v[68:71]\nv_mfma_f32_16x16x32_bf16 v[64:67], v[100:103], v[136:139], v[64:67]\ns_add_u32 s34, s34, 1\n"
    "s_mov_b32 exec_lo, 0x20002\ns_mov_b32 exec_hi, 0x20002\ns_nop 3\nds_add_f32 v31, v68 offset:0\nds_add_f32 v31, v69 offset:4\nds_add_f32 v31, v70 offset:8\n"
    "ds_add_f32 v31, v71 offset:12\nds_write_b128 v30, v[64:67]\ns_mov_b64 exec, -1\ns_waitcnt lgkmcnt(0)\ns_barrier\ns_setprio 0\n"
    "v_fmac_f32_dpp v64, v64, v90 row_newbcast:1 row_mask:0xf bank_mask:0xf\nv_fmac_f32_dpp v65, v65, v90 row_newbcast:1 row_mask:0xf bank_mask:0xf\n"
    "v_fmac_f32_dpp v66, v66, v90 row_newbcast:1 row_mask:0xf bank_mask:0xf\nv_fmac_f32_dpp v67, v67, v90 row_newbcast:1 row_mask:0xf bank_mask:0xf\nv_mul_f32 v64, v64, v109\n"
    "v_mul_f32 v65, v65, v109\nv_mul_f32 v66, v66, v109\nv_mul_f32 v67, v67, v109\nv_cvt_pk_bf16_f32 v64, v64, v65\nv_cvt_pk_bf16_f32 v65, v66, v67\nds_write_b64 v34, v[64:65]\n"
    "v_mov_b32 v9, s34\ns_waitcnt vmcnt(3)\nv_xor_b32 v140, s34, v140\nv_xor_b32 v141, s34, v141\nv_xor_b32 v142, s34, v142\nv_xor_b32 v143, s34, v143\n"
    "v_or3_b32 v140, v140, v141, v142\nv_or_b32 v140, v140, v143\nv_cmp_ne_u32 vcc, 0, v140\ns_mov_b64 exec, 1\nglobal_store_dword v39, v9, s[32:33] sc1\ns_mov_b64 exec, -1\n"
    "s_cmp_eq_u64 vcc, 0\ns_cselect_b32 s43, 1, 0\ns_cbranch_scc1 .LL8\ns_waitcnt vmcnt(0)\ns_branch .LL9\n.LL8:\n"
    "global_load_dwordx4 v[132:135], v58, s[32:33] offset:-4096 sc1\nglobal_load_dwordx4 v[136:139], v58, s[32:33] offset:-3072 sc1\n"
    "global_load_dwordx4 v[140:143], v58, s[32:33] offset:-2048 sc1\nglobal_load_dwordx4 v[144:147], v58, s[32:33] offset:-1024 sc1\n"
    "global_load_dwordx4 v[148:151], v58, s[32:33] offset:0 sc1\nglobal_load_dwordx4 v[152:155], v58, s[32:33] offset:1024 sc1\n"
    "global_load_dwordx4 v[156:159], v58, s[32:33] offset:2048 sc1\nglobal_load_dwordx4 v[160:163], v58, s[32:33] offset:3072 sc1\n.LL9:\n.LHL12:\ns_setprio 1\n"
    "s_waitcnt lgkmcnt(0)\ns_barrier\nds_read_b128 v[28:31], v252\nds_read_b128 v[32:35], v252 offset:208\ns_mov_b32 exec_lo, 0x00ff00ff\ns_mov_b32 exec_hi, 0xff00ff00\n"
    "ds_read_b128 v[10:13], v253\nds_read_b128 v[14:17], v253 offset:208\ns_mov_b64 exec, -1\nds_read_b64_tr_b16 v[36:37], v254 offset:0\n"
    "ds_read_b64_tr_b16 v[38:39], v254 offset:32\nds_read_b64_tr_b16 v[40:41], v254 offset:208\nds_read_b64_tr_b16 v[42:43], v254 offset:240\nds_read_b32 v240, v62 offset:0\n"
    "ds_read_b32 v241, v62 offset:512\nds_read_b32 v242, v62 offset:1024\nds_read_b32 v243, v62 offset:1536\nds_read_b32 v244, v62 offset:2048\n"
    "ds_read_b32 v245, v62 offset:2560\nds_read_b64 v[248:249], v63\ns_cmp_eq_u32 s43, 0\ns_cbranch_scc1 .LL14\nglobal_load_dwordx4 v[96:99], v5, s[32:33] offset:-4096 sc1\n"
    "global_load_dwordx4 v[100:103], v5, s[32:33] offset:-3072 sc1\nglobal_load_dwordx4 v[104:107], v5, s[32:33] offset:-2048 sc1\n"
    "global_load_dwordx4 v[108:111], v5, s[32:33] offset:-1024 sc1\nglobal_load_dwordx4 v[112:115], v5, s[32:33] offset:0 sc1\n"
    "global_load_dwordx4 v[116:119], v5, s[32:33] offset:1024 sc1\nglobal_load_dwordx4 v[120:123], v5, s[32:33] offset:2048 sc1\n"
    "global_load_dwordx4 v[124:127], v5, s[32:33] offset:3072 sc1\n.LL14:\ns_mov_b32 s81, 0x3eb8aa3b\ns_waitcnt lgkmcnt(12)\n"
    "v_mfma_f32_16x16x32_bf16 v[44:47], v[28:31], v[10:13], v[128:131]\ns_waitcnt lgkmcnt(11)\nv_mfma_f32_16x16x32_bf16 v[48:51], v[32:35], v[14:17], v[128:131]\ns_nop 5\n"
    "v_cvt_pk_bf16_f32 v44, 0, v44\nv_cvt_pk_bf16_f32 v45, 0, v45\nv_cvt_pk_bf16_f32 v46, 0, v46\nv_cvt_pk_bf16_f32 v47, 0, v47\nv_cvt_pk_bf16_f32 v48, 0, v48\n"
    "v_cvt_pk_bf16_f32 v49, 0, v49\nv_cvt_pk_bf16_f32 v50, 0, v50\nv_cvt_pk_bf16_f32 v51, 0, v51\nv_max_f32 v52, v44, v45\nv_max_f32 v53, v48, v49\n"
    "v_max3_f32 v52, v52, v46, v47\nv_max3_f32 v53, v53, v50, v51\ns_nop 1\nv_permlane16_swap_b32 v52, v53\nv_max_f32 v54, v52, v53\nv_max_f32 v53, v52, v53\ns_nop 1\n"
    "v_permlane32_swap_b32 v54, v53\nv_max_f32 v54, v54, v53\nv_mul_f32 v52, 0xbeb8aa3b, v54\nv_mul_f32 v53, 0xbeb8aa3b, v54\ns_nop 1\nv_permlane16_swap_b32 v52, v53\n"
    "v_fma_f32 v44, v44, s81, v52\nv_fma_f32 v45, v45, s81, v52\nv_fma_f32 v46, v46, s81, v52\nv_fma_f32 v47, v47, s81, v52\nv_fma_f32 v48, v48, s81, v53\n"
    "v_fma_f32 v49, v49, s81, v53\nv_fma_f32 v50, v50, s81, v53\nv_fma_f32 v51, v51, s81, v53\nv_exp_f32 v44, v44\nv_exp_f32 v45, v45\nv_exp_f32 v46, v46\nv_exp_f32 v47, v47\n"
    "v_exp_f32 v48, v48\nv_exp_f32 v49, v49\nv_exp_f32 v50, v50\nv_exp_f32 v51, v51\nv_add_f32 v52, v44, v45\nv_add_f32 v55, v46, v47\nv_add_f32 v53, v48, v49\n"
    "v_add_f32 v56, v50, v51\nv_add_f32 v52, v52, v55\nv_add_f32 v53, v53, v56\ns_nop 1\nv_permlane16_swap_b32 v52, v53\nv_add_f32 v54, v52, v53\nv_add_f32 v53, v52, v53\n"
    "s_nop 1\nv_permlane32_swap_b32 v54, v53\nv_add_f32 v54, v54, v53\nv_rcp_f32 v52, v54\nv_rcp_f32 v53, v54\ns_nop 1\nv_permlane16_swap_b32 v52, v53\nv_mul_f32 v44, v44, v52\n"
    "v_mul_f32 v45, v45, v52\nv_mul_f32 v46, v46, v52\nv_mul_f32 v47, v47, v52\nv_mul_f32 v48, v48, v53\nv_mul_f32 v49, v49, v53\nv_mul_f32 v50, v50, v53\n"
    "v_mul_f32 v51, v51, v53\ns_mov_b32 exec_lo, 0x00ff00ff\ns_mov_b32 exec_hi, 0x00ff00ff\nv_cvt_pk_bf16_f32 v18, v44, v45\nv_cvt_pk_bf16_f32 v19, v46, v47\n"
    "v_cvt_pk_bf16_f32 v22, v48, v49\nv_cvt_pk_bf16_f32 v23, v50, v51\ns_not_b64 exec, exec\nv_cvt_pk_bf16_f32 v20, v44, v45\nv_cvt_pk_bf16_f32 v21, v46, v47\n"
    "v_cvt_pk_bf16_f32 v24, v48, v49\nv_cvt_pk_bf16_f32 v25, v50, v51\ns_mov_b64 exec, -1\ns_waitcnt lgkmcnt(0)\nv_mfma_f32_16x16x32_bf16 v[28:31], v[36:39], v[18:21], 0\n"
    "v_mfma_f32_16x16x32_bf16 v[32:35], v[40:43], v[22:25], 0\ns_nop 6\nv_cvt_pk_bf16_f32 v44, v28, v29\nv_cvt_pk_bf16_f32 v45, v30, v31\nv_cvt_pk_bf16_f32 v46, v32, v33\n"
    "v_cvt_pk_bf16_f32 v47, v34, v35\nv_min_u32 v44, -2, v44\nv_min_u32 v46, -2, v46\nv_min_u32 v45, -2, v45\nv_min_u32 v47, -2, v47\nv_permlane16_swap_b32 v44, v46\ns_nop 0\n"
    "v_permlane16_swap_b32 v45, v47\nglobal_store_dwordx4 v1, v[44:47], s[32:33] sc1\ns_setprio 0\ns_cmp_eq_u32 s6, 0\ns_cbranch_scc0 .LL17\ns_cmp_eq_u32 s43, 1\n"
    "s_cselect_b64 vcc, -1, 0\ns_waitcnt vmcnt(18)\ns_mov_b64 exec, 1\nglobal_store_dword v2, v9, s[32:33] sc1\ns_mov_b64 exec, -1\nv_lshlrev_b32 v220, 16, v244\n"
    "s_cbranch_vccz .Lcn18\nglobal_load_dwordx4 v[164:167], v59, s[32:33] offset:-4096 sc1\nglobal_load_dwordx4 v[168:171], v59, s[32:33] offset:-3072 sc1\n"
    "global_load_dwordx4 v[172:175], v59, s[32:33] offset:-2048 sc1\nglobal_load_dwordx4 v[176:179], v59, s[32:33] offset:-1024 sc1\n.Lcn18:\nv_and_b32 v221, 0xffff0000, v244\n"
    "v_lshlrev_b32 v222, 16, v245\nv_and_b32 v223, 0xffff0000, v245\nv_lshlrev_b32 v224, 16, v242\nv_and_b32 v225, 0xffff0000, v242\nv_lshlrev_b32 v226, 16, v243\n"
    "v_and_b32 v227, 0xffff0000, v243\nv_mul_f32 v212, v220, v224\nv_mul_f32 v214, v221, v225\nv_mul_f32 v213, v220, v226\nv_mul_f32 v215, v221, v227\n"
    "v_fma_f32 v212, -v222, v226, v212\nv_fma_f32 v214, -v223, v227, v214\nv_fma_f32 v213, -v222, v224, -v213\nv_fma_f32 v215, -v223, v225, -v215\ns_bitcmp1_b32 s3, 0\n"
    "s_cselect_b32 s86, 0x43800000, 1.0\ns_mov_b32 s87, 0x43810000\ns_cselect_b32 s87, 0x44010000, s87\nv_max3_f32 v236, |v212|, |v213|, |v214|\n"
    "v_max_f32_e64 v236, v236, |v215|\nv_lshlrev_b32 v196, 16, v240\nv_and_b32 v197, 0xffff0000, v240\nv_lshlrev_b32 v198, 16, v241\nv_and_b32 v199, 0xffff0000, v241\n"
    "v_mul_f32 v196, s93, v196\nv_mul_f32 v197, s93, v197\nv_mul_f32 v198, s94, v198\nv_mul_f32 v199, s94, v199\nv_mul_f32 v208, s86, v196\ns_cbranch_vccz .Lcn19\n"
    "global_load_dwordx4 v[180:183], v59, s[32:33] offset:0 sc1\nglobal_load_dwordx4 v[184:187], v59, s[32:33] offset:1024 sc1\n"
    "global_load_dwordx4 v[188:191], v59, s[32:33] offset:2048 sc1\nglobal_load_dwordx4 v[192:195], v59, s[32:33] offset:3072 sc1\n.Lcn19:\nv_mul_f32 v209, s86, v197\n"
    "v_mul_f32 v210, s87, v196\nv_mul_f32 v211, s87, v197\nv_max_f32_dpp v236, v236, v236 row_shr:1 row_mask:0xf bank_mask:0xf bound_ctrl:0\nv_mul_f32 v229, s86, v198\n"
    "v_mul_f32 v231, s86, v199\nv_mul_f32 v217, s87, v198\nv_mul_f32 v219, s87, v199\nv_max_f32_dpp v236, v236, v236 row_shr:2 row_mask:0xf bank_mask:0xf bound_ctrl:0\n"
    "s_barrier\ns_setprio 0\nv_exp_f32 v208, v208\nv_exp_f32 v209, v209\nv_exp_f32 v210, v210\nv_exp_f32 v211, v211\n"
    "v_max_f32_dpp v236, v236, v236 row_shr:4 row_mask:0xf bank_mask:0xf bound_ctrl:0\nv_fract_f32 v229, v229\nv_fract_f32 v231, v231\nv_fract_f32 v217, v217\n"
    "v_fract_f32 v219, v219\nv_max_f32_dpp v236, v236, v236 row_shr:8 row_mask:0xf bank_mask:0xf bound_ctrl:0\nv_cos_f32 v228, v229\nv_cos_f32 v230, v231\nv_cos_f32 v216, v217\n"
    "v_cos_f32 v218, v219\nv_max_f32_dpp v236, v236, v236 row_bcast:15 row_mask:0xf bank_mask:0xf bound_ctrl:0\nv_sin_f32 v229, v229\nv_sin_f32 v231, v231\n"
    "v_sin_f32 v217, v217\nv_sin_f32 v219, v219\ns_cbranch_vccz .Lcn20\nglobal_load_dwordx4 v[64:67], v4, s[32:33] offset:-4096 sc1\n"
    "global_load_dwordx4 v[68:71], v4, s[32:33] offset:-3072 sc1\nglobal_load_dwordx4 v[72:75], v4, s[32:33] offset:-2048 sc1\n"
    "global_load_dwordx4 v[76:79], v4, s[32:33] offset:-1024 sc1\n.Lcn20:\nv_max_f32_dpp v236, v236, v236 row_bcast:31 row_mask:0xf bank_mask:0xf bound_ctrl:0\n"
    "v_mul_f32 v228, v228, v208\nv_mul_f32 v230, v230, v209\nv_mul_f32 v216, v216, v210\nv_mul_f32 v218, v218, v211\nv_mul_f32 v229, v229, v208\nv_mul_f32 v231, v231, v209\n"
    "v_mul_f32 v217, v217, v210\nv_mul_f32 v219, v219, v211\nv_readlane_b32 s86, v236, 63\ns_lshr_b32 s83, s3, 1\ns_mul_i32 s83, s83, 0x6000600\ns_lshr_b32 s86, s86, 23\n"
    "s_max_u32 s86, s86, 9\ns_min_u32 s86, s86, 254\ns_sub_u32 s87, 261, s86\ns_lshl_b32 s87, s87, 23\ns_sub_u32 s86, s86, 7\ns_lshl_b32 s86, s86, 23\n"
    "v_mul_f32 v212, s87, v212\nv_mul_f32 v213, s87, v213\nv_mul_f32 v214, s87, v214\nv_mul_f32 v215, s87, v215\nv_mov_b32 v236, s86\nv_mul_f32 v236, s92, v236\n"
    "v_add_u32 v248, s83, v248\nv_add_u32 v249, s83, v249\nv_mov_b32 v26, 0x10000\nv_mov_b32 v27, 0\nv_pk_mul_f32 v[200:201], v[216:217], v[212:213] op_sel_hi:[0,1]\n"
    "s_cbranch_vccz .Lcn21\nglobal_load_dwordx4 v[80:83], v4, s[32:33] offset:0 sc1\nglobal_load_dwordx4 v[84:87], v4, s[32:33] offset:1024 sc1\n"
    "global_load_dwordx4 v[88:91], v4, s[32:33] offset:2048 sc1\nglobal_load_dwordx4 v[92:95], v4, s[32:33] offset:3072 sc1\n.Lcn21:\n"
    "v_pk_mul_f32 v[202:203], v[218:219], v[214:215] op_sel_hi:[0,1]\n"
    "v_pk_fma_f32 v[216:217], v[216:217], v[212:213], v[200:201] op_sel:[1,1,0] op_sel_hi:[1,0,1] neg_lo:[0,1,0]\n"
    "v_pk_fma_f32 v[218:219], v[218:219], v[214:215], v[202:203] op_sel:[1,1,0] op_sel_hi:[1,0,1] neg_lo:[0,1,0]\ns_bitcmp1_b32 s3, 0\ns_cbranch_scc0 .LL10\n"
    "v_pk_mul_f32 v[200:201], v[212:213], v[228:229] op_sel_hi:[0,1]\nv_pk_mul_f32 v[202:203], v[214:215], v[230:231] op_sel_hi:[0,1]\n"
    "v_pk_fma_f32 v[212:213], v[212:213], v[228:229], v[200:201] op_sel:[1,1,0] op_sel_hi:[1,0,1] neg_lo:[0,1,0]\n"
    "v_pk_fma_f32 v[214:215], v[214:215], v[230:231], v[202:203] op_sel:[1,1,0] op_sel_hi:[1,0,1] neg_lo:[0,1,0]\n.LL10:\ns_mov_b64 exec, 1\nds_write_b32 v7, v236\n"
    "ds_write_b16 v8, v27\ns_mov_b64 exec, -1\nv_cvt_pk_f16_f32 v204, v212, v213\nv_cvt_pk_f16_f32 v205, v214, v215\n"
    "v_pk_mul_f32 v[200:201], v[212:213], v[228:229] op_sel_hi:[0,1]\nv_pk_mul_f32 v[202:203], v[214:215], v[230:231] op_sel_hi:[0,1]\nds_write_b64 v3, v[204:205] offset:0\n"
    "v_pk_fma_f32 v[212:213], v[212:213], v[228:229], v[200:201] op_sel:[1,1,0] op_sel_hi:[1,0,1] neg_lo:[0,1,0]\n"
    "v_pk_fma_f32 v[214:215], v[214:215], v[230:231], v[202:203] op_sel:[1,1,0] op_sel_hi:[1,0,1] neg_lo:[0,1,0]\nv_cvt_pk_f16_f32 v206, v212, v213\n"
    "v_cvt_pk_f16_f32 v207, v214, v215\nv_pk_mul_f32 v[200:201], v[212:213], v[228:229] op_sel_hi:[0,1]\nv_pk_mul_f32 v[202:203], v[214:215], v[230:231] op_sel_hi:[0,1]\n"
    "ds_write_b64 v3, v[206:207] offset:544\nv_pk_fma_f32 v[212:213], v[212:213], v[228:229], v[200:201] op_sel:[1,1,0] op_sel_hi:[1,0,1] neg_lo:[0,1,0]\n"
    "v_pk_fma_f32 v[214:215], v[214:215], v[230:231], v[202:203] op_sel:[1,1,0] op_sel_hi:[1,0,1] neg_lo:[0,1,0]\nv_cvt_pk_f16_f32 v204, v212, v213\n"
    "v_cvt_pk_f16_f32 v205, v214, v215\nv_pk_mul_f32 v[200:201], v[212:213], v[228:229] op_sel_hi:[0,1]\nv_pk_mul_f32 v[202:203], v[214:215], v[230:231] op_sel_hi:[0,1]\n"
    "ds_write_b64 v3, v[204:205] offset:1088\nv_pk_fma_f32 v[212:213], v[212:213], v[228:229], v[200:201] op_sel:[1,1,0] op_sel_hi:[1,0,1] neg_lo:[0,1,0]\n"
    "v_pk_fma_f32 v[214:215], v[214:215], v[230:231], v[202:203] op_sel:[1,1,0] op_sel_hi:[1,0,1] neg_lo:[0,1,0]\nv_cvt_pk_f16_f32 v206, v212, v213\n"
    "v_cvt_pk_f16_f32 v207, v214, v215\nv_pk_mul_f32 v[200:201], v[212:213], v[228:229] op_sel_hi:[0,1]\nv_pk_mul_f32 v[202:203], v[214:215], v[230:231] op_sel_hi:[0,1]\n"
    "ds_write_b64 v3, v[206:207] offset:1632\nv_pk_fma_f32 v[212:213], v[212:213], v[228:229], v[200:201] op_sel:[1,1,0] op_sel_hi:[1,0,1] neg_lo:[0,1,0]\n"
    "v_pk_fma_f32 v[214:215], v[214:215], v[230:231], v[202:203] op_sel:[1,1,0] op_sel_hi:[1,0,1] neg_lo:[0,1,0]\nv_cvt_pk_f16_f32 v204, v212, v213\n"
    "v_cvt_pk_f16_f32 v205, v214, v215\nv_pk_mul_f32 v[200:201], v[212:213], v[228:229] op_sel_hi:[0,1]\nv_pk_mul_f32 v[202:203], v[214:215], v[230:231] op_sel_hi:[0,1]\n"
    "ds_write_b64 v3, v[204:205] offset:2176\nv_pk_fma_f32 v[212:213], v[212:213], v[228:229], v[200:201] op_sel:[1,1,0] op_sel_hi:[1,0,1] neg_lo:[0,1,0]\n"
    "v_pk_fma_f32 v[214:215], v[214:215], v[230:231], v[202:203] op_sel:[1,1,0] op_sel_hi:[1,0,1] neg_lo:[0,1,0]\nv_cvt_pk_f16_f32 v206, v212, v213\n"
    "v_cvt_pk_f16_f32 v207, v214, v215\nv_pk_mul_f32 v[200:201], v[212:213], v[228:229] op_sel_hi:[0,1]\nv_pk_mul_f32 v[202:203], v[214:215], v[230:231] op_sel_hi:[0,1]\n"
    "ds_write_b64 v3, v[206:207] offset:2720\nv_pk_fma_f32 v[212:213], v[212:213], v[228:229], v[200:201] op_sel:[1,1,0] op_sel_hi:[1,0,1] neg_lo:[0,1,0]\n"
    "v_pk_fma_f32 v[214:215], v[214:215], v[230:231], v[202:203] op_sel:[1,1,0] op_sel_hi:[1,0,1] neg_lo:[0,1,0]\nv_cvt_pk_f16_f32 v204, v212, v213\n"
    "v_cvt_pk_f16_f32 v205, v214, v215\nv_pk_mul_f32 v[200:201], v[212:213], v[228:229] op_sel_hi:[0,1]\nv_pk_mul_f32 v[202:203], v[214:215], v[230:231] op_sel_hi:[0,1]\n"
    "ds_write_b64 v3, v[204:205] offset:3264\nv_pk_fma_f32 v[212:213], v[212:213], v[228:229], v[200:201] op_sel:[1,1,0] op_sel_hi:[1,0,1] neg_lo:[0,1,0]\n"
    "v_pk_fma_f32 v[214:215], v[214:215], v[230:231], v[202:203] op_sel:[1,1,0] op_sel_hi:[1,0,1] neg_lo:[0,1,0]\nv_cvt_pk_f16_f32 v206, v212, v213\n"
    "v_cvt_pk_f16_f32 v207, v214, v215\nds_write_b64 v3, v[206:207] offset:3808\ns_bitcmp1_b32 s3, 0\ns_cbranch_scc1 .LL11\n"
    "v_pk_mul_f32 v[200:201], v[228:229], v[228:229] op_sel_hi:[0,1]\nv_pk_mul_f32 v[202:203], v[230:231], v[230:231] op_sel_hi:[0,1]\n"
    "v_pk_fma_f32 v[228:229], v[228:229], v[228:229], v[200:201] op_sel:[1,1,0] op_sel_hi:[1,0,1] neg_lo:[0,1,0]\n"
    "v_pk_fma_f32 v[230:231], v[230:231], v[230:231], v[202:203] op_sel:[1,1,0] op_sel_hi:[1,0,1] neg_lo:[0,1,0]\n.LL11:\nv_cvt_pk_f16_f32 v204, v216, v217\n"
    "v_cvt_pk_f16_f32 v205, v218, v219\nv_pk_mul_f32 v[200:201], v[216:217], v[228:229] op_sel_hi:[0,1]\nv_pk_mul_f32 v[202:203], v[218:219], v[230:231] op_sel_hi:[0,1]\n"
    "ds_write_b64 v3, v[204:205] offset:4352\nv_pk_fma_f32 v[216:217], v[216:217], v[228:229], v[200:201] op_sel:[1,1,0] op_sel_hi:[1,0,1] neg_lo:[0,1,0]\n"
    "v_pk_fma_f32 v[218:219], v[218:219], v[230:231], v[202:203] op_sel:[1,1,0] op_sel_hi:[1,0,1] neg_lo:[0,1,0]\nv_cvt_pk_f16_f32 v206, v216, v217\n"
    "v_cvt_pk_f16_f32 v207, v218, v219\nv_pk_mul_f32 v[200:201], v[216:217], v[228:229] op_sel_hi:[0,1]\nv_pk_mul_f32 v[202:203], v[218:219], v[230:231] op_sel_hi:[0,1]\n"
    "ds_write_b64 v3, v[206:207] offset:4896\nv_pk_fma_f32 v[216:217], v[216:217], v[228:229], v[200:201] op_sel:[1,1,0] op_sel_hi:[1,0,1] neg_lo:[0,1,0]\n"
    "v_pk_fma_f32 v[218:219], v[218:219], v[230:231], v[202:203] op_sel:[1,1,0] op_sel_hi:[1,0,1] neg_lo:[0,1,0]\nv_cvt_pk_f16_f32 v204, v216, v217\n"
    "v_cvt_pk_f16_f32 v205, v218, v219\nds_write_b64 v3, v[204:205] offset:5440\ns_mov_b32 s84, 0xffff\ns_setprio 0\ns_waitcnt lgkmcnt(0)\ns_barrier\n"
    "ds_read_b128 v[196:199], v250 offset:0\nds_read_b128 v[200:203], v251 offset:0\nds_read_b128 v[204:207], v250 offset:64\nds_read_b128 v[208:211], v251 offset:64\n"
    "ds_read_b128 v[212:215], v250 offset:128\nds_read_b128 v[216:219], v251 offset:128\nds_read_b128 v[220:223], v250 offset:192\nds_read_b128 v[224:227], v251 offset:192\n"
    "ds_read_b128 v[228:231], v250 offset:256\nds_read_b128 v[232:235], v251 offset:256\ns_waitcnt lgkmcnt(8)\nv_mfma_f32_16x16x32_f16 v[236:239], v[196:199], v[200:203], 0\n"
    "ds_read_b128 v[196:199], v250 offset:320\nds_read_b128 v[200:203], v251 offset:320\ns_waitcnt lgkmcnt(8)\n"
    "v_mfma_f32_16x16x32_f16 v[236:239], v[204:207], v[208:211], v[236:239]\nds_read_b128 v[204:207], v250 offset:384\nds_read_b128 v[208:211], v251 offset:384\n"
    "s_waitcnt lgkmcnt(8)\nv_mfma_f32_16x16x32_f16 v[236:239], v[212:215], v[216:219], v[236:239]\nds_read_b128 v[212:215], v250 offset:448\n"
    "ds_read_b128 v[216:219], v251 offset:448\ns_waitcnt lgkmcnt(8)\nv_mfma_f32_16x16x32_f16 v[236:239], v[220:223], v[224:227], v[236:239]\ns_waitcnt lgkmcnt(6)\n"
    "v_mfma_f32_16x16x32_f16 v[236:239], v[228:231], v[232:235], v[236:239]\ns_waitcnt lgkmcnt(4)\nv_mfma_f32_16x16x32_f16 v[236:239], v[196:199], v[200:203], v[236:239]\n"
    "s_waitcnt lgkmcnt(2)\nv_mfma_f32_16x16x32_f16 v[236:239], v[204:207], v[208:211], v[236:239]\ns_waitcnt lgkmcnt(0)\n"
    "v_mfma_f32_16x16x32_f16 v[236:239], v[212:215], v[216:219], v[236:239]\nv_and_or_b32 v220, v248, s84, v26\nv_alignbit_b32 v221, 1, v248, 16\n"
    "v_and_or_b32 v222, v249, s84, v26\nv_alignbit_b32 v223, 1, v249, 16\ns_nop 3\nv_cvt_pk_f16_f32 v236, v236, v237\nv_cvt_pk_f16_f32 v237, v238, v239\n"
    "ds_write_b16 v220, v236 offset:59392\nds_write_b16_d16_hi v221, v236 offset:59392\nds_write_b16 v222, v237 offset:59392\nds_write_b16_d16_hi v223, v237 offset:59392\n"
    "v_mov_b32 v26, 125952\ns_waitcnt lgkmcnt(0)\ns_barrier\nglobal_load_dwordx4 v[240:243], v6, s[8:9]\nglobal_load_dwordx4 v[244:247], v6, s[8:9] offset:32\n"
    "ds_read_u16 v196, v26\nds_read_u16 v197, v26 offset:1536\ns_waitcnt lgkmcnt(0)\nv_cvt_f32_f16 v196, v196\nv_cvt_f32_f16 v197, v197\nv_readfirstlane_b32 s92, v196\n"
    "v_readfirstlane_b32 s94, v197\ns_cmp_eq_u32 s43, 1\ns_cbranch_scc1 .LL16\nv_and_b32 v10, 3, v0\ns_lshl2_add_u32 s16, s40, s3\ns_lshl_b32 s15, s16, 9\n"
    "s_add_u32 s14, s15, 0x1002000\nv_lshl_add_u32 v3, v10, 7, s14\nv_and_b32 v11, 4, v0\nv_lshl_add_u32 v3, v11, 2, v3\ns_mov_b32 s80, 0x10000\n"
    "global_load_dwordx4 v[196:199], v3, s[32:33] sc1\ns_waitcnt vmcnt(0)\n.Ltp22:\nv_xor_b32 v196, s34, v196\nv_xor_b32 v197, s34, v197\nv_xor_b32 v198, s34, v198\n"
    "v_xor_b32 v199, s34, v199\nv_or3_b32 v196, v196, v197, v198\nv_or_b32 v196, v196, v199\nv_cmp_ne_u32 vcc, 0, v196\ns_cbranch_vccz .Ltq23\ns_sub_u32 s80, s80, 1\n"
    "s_cbranch_scc1 .Ltq23\ns_sleep 2\nglobal_load_dwordx4 v[196:199], v3, s[32:33] sc1\ns_waitcnt vmcnt(0)\ns_branch .Ltp22\n.Ltq23:\n"
    "global_load_dwordx4 v[132:135], v58, s[32:33] offset:-4096 sc1\nglobal_load_dwordx4 v[136:139], v58, s[32:33] offset:-3072 sc1\n"
    "global_load_dwordx4 v[140:143], v58, s[32:33] offset:-2048 sc1\nglobal_load_dwordx4 v[144:147], v58, s[32:33] offset:-1024 sc1\n"
    "global_load_dwordx4 v[148:151], v58, s[32:33] offset:0 sc1\nglobal_load_dwordx4 v[152:155], v58, s[32:33] offset:1024 sc1\n"
    "global_load_dwordx4 v[156:159], v58, s[32:33] offset:2048 sc1\nglobal_load_dwordx4 v[160:163], v58, s[32:33] offset:3072 sc1\n"
    "global_load_dwordx4 v[164:167], v59, s[32:33] offset:-4096 sc1\nglobal_load_dwordx4 v[168:171], v59, s[32:33] offset:-3072 sc1\n"
    "global_load_dwordx4 v[172:175], v59, s[32:33] offset:-2048 sc1\nglobal_load_dwordx4 v[176:179], v59, s[32:33] offset:-1024 sc1\n"
    "global_load_dwordx4 v[180:183], v59, s[32:33] offset:0 sc1\nglobal_load_dwordx4 v[184:187], v59, s[32:33] offset:1024 sc1\n"
    "global_load_dwordx4 v[188:191], v59, s[32:33] offset:2048 sc1\nglobal_load_dwordx4 v[192:195], v59, s[32:33] offset:3072 sc1\n"
    "global_load_dwordx4 v[64:67], v4, s[32:33] offset:-4096 sc1\nglobal_load_dwordx4 v[68:71], v4, s[32:33] offset:-3072 sc1\n"
    "global_load_dwordx4 v[72:75], v4, s[32:33] offset:-2048 sc1\nglobal_load_dwordx4 v[76:79], v4, s[32:33] offset:-1024 sc1\n"
    "global_load_dwordx4 v[80:83], v4, s[32:33] offset:0 sc1\nglobal_load_dwordx4 v[84:87], v4, s[32:33] offset:1024 sc1\n"
    "global_load_dwordx4 v[88:91], v4, s[32:33] offset:2048 sc1\nglobal_load_dwordx4 v[92:95], v4, s[32:33] offset:3072 sc1\n"
    "global_load_dwordx4 v[96:99], v5, s[32:33] offset:-4096 sc1\nglobal_load_dwordx4 v[100:103], v5, s[32:33] offset:-3072 sc1\n"
    "global_load_dwordx4 v[104:107], v5, s[32:33] offset:-2048 sc1\nglobal_load_dwordx4 v[108:111], v5, s[32:33] offset:-1024 sc1\n"
    "global_load_dwordx4 v[112:115], v5, s[32:33] offset:0 sc1\nglobal_load_dwordx4 v[116:119], v5, s[32:33] offset:1024 sc1\n"
    "global_load_dwordx4 v[120:123], v5, s[32:33] offset:2048 sc1\nglobal_load_dwordx4 v[124:127], v5, s[32:33] offset:3072 sc1\ns_waitcnt vmcnt(0)\ns_mov_b32 s43, 1\n.LL16:\n"
    "s_branch .LL15\n.LL17:\ns_barrier\nv_and_b32 v10, 63, v0\ns_lshl_b32 s16, s39, 15\ns_lshl2_add_u32 s18, s40, s3\ns_lshl_b32 s17, s18, 11\ns_add_u32 s15, s16, s17\n"
    "s_add_u32 s14, s15, 0x600000\nv_lshl_add_u32 v2, v10, 4, s14\ns_lshl_b32 s20, s3, 11\ns_add_u32 s19, s20, 0x1c200\nv_lshl_add_u32 v3, v10, 4, s19\nv_bfe_u32 v12, v0, 5, 1\n"
    "v_bfe_u32 v13, v0, 2, 2\nv_lshl_add_u32 v11, v12, 2, v13\nv_lshl_add_u32 v4, v11, 6, s19\nv_bfe_u32 v14, v0, 4, 1\nv_lshl_add_u32 v4, v14, 5, v4\nv_and_b32 v15, 3, v0\n"
    "v_lshl_add_u32 v4, v15, 3, v4\nglobal_load_dwordx4 v[240:243], v2, s[32:33] offset:0 sc1\nglobal_load_dwordx4 v[244:247], v2, s[32:33] offset:1024 sc1\n"
    "s_mov_b32 s80, 0x10000\n.LL24:\ns_waitcnt vmcnt(0)\ns_mov_b64 s[82:83], 0\nv_max3_u32 v5, v240, v241, v242\nv_max_u32 v5, v5, v243\nv_cmp_eq_u32 vcc, -1, v5\ns_nop 0\n"
    "s_or_b64 s[82:83], s[82:83], vcc\ns_cbranch_vccz .Lag26\ns_mov_b64 exec, vcc\nglobal_load_dwordx4 v[240:243], v2, s[32:33] offset:0 sc1\ns_mov_b64 exec, -1\n.Lag26:\n"
    "v_max3_u32 v5, v244, v245, v246\nv_max_u32 v5, v5, v247\nv_cmp_eq_u32 vcc, -1, v5\ns_nop 0\ns_or_b64 s[82:83], s[82:83], vcc\ns_cbranch_vccz .Lag27\ns_mov_b64 exec, vcc\n"
    "global_load_dwordx4 v[244:247], v2, s[32:33] offset:1024 sc1\ns_mov_b64 exec, -1\n.Lag27:\ns_cmp_eq_u64 s[82:83], 0\ns_cbranch_scc1 .LL25\ns_sub_u32 s80, s80, 1\n"
    "s_cbranch_scc0 .LL24\ns_waitcnt vmcnt(0)\n.LL25:\nds_write_b128 v3, v[240:243]\nds_write_b128 v3, v[244:247] offset:1024\nds_read_b64_tr_b16 v[240:241], v4 offset:0\n"
    "ds_read_b64_tr_b16 v[242:243], v4 offset:512\nds_read_b64_tr_b16 v[244:245], v4 offset:1024\nds_read_b64_tr_b16 v[246:247], v4 offset:1536\ns_waitcnt lgkmcnt(0)\n.LL15:\n"
    "v_bfe_u32 v40, v0, 5, 1\ns_lshl_b32 s15, s3, 11\ns_add_u32 s14, s15, 0x1c200\nv_lshl_add_u32 v18, v40, 8, s14\nv_and_b32 v41, 31, v0\nv_lshl_add_u32 v18, v41, 2, v18\n"
    "s_add_u32 s16, s15, 0x1c600\nv_lshl_add_u32 v19, v40, 8, s16\nv_lshl_add_u32 v19, v41, 2, v19\nv_and_b32 v42, 63, v0\nv_lshl_add_u32 v20, v42, 4, s14\n"
    "s_mov_b32 s17, 0x1f600\nv_lshl_add_u32 v21, v42, 4, s17\nv_bfe_u32 v43, v0, 3, 3\ns_lshl_b32 s23, s40, 6\ns_lshl4_add_u32 s22, s3, s23\ns_lshl_b32 s21, s22, 13\n"
    "s_add_u32 s20, s7, s21\ns_lshl_b32 s24, s39, 7\ns_add_u32 s19, s20, s24\ns_add_u32 s18, s19, 0x200000\nv_lshl_add_u32 v22, v43, 13, s18\nv_and_b32 v44, 7, v0\n"
    "v_lshl_add_u32 v22, v44, 4, v22\ns_add_u32 s25, s19, 0x210000\nv_lshl_add_u32 v23, v43, 13, s25\nv_lshl_add_u32 v23, v44, 4, v23\ns_lshl_b32 s45, s3, 8\n"
    "s_add_u32 s44, s45, 0x27600\nv_lshl_add_u32 v24, v42, 2, s44\nv_and_b32 v45, 15, v0\ns_mov_b32 s46, 0x27600\nv_lshl_add_u32 v25, v45, 4, s46\nv_bfe_u32 v46, v0, 3, 1\n"
    "s_lshl_b32 s50, s40, 14\ns_add_u32 s49, s7, s50\ns_add_u32 s48, s49, s24\ns_add_u32 s47, s48, 0x400000\nv_lshl_add_u32 v26, v46, 13, s47\nv_lshl_add_u32 v26, v44, 4, v26\n"
    "s_lshl2_add_u32 s51, s3, 0x27ff0\nv_mov_b32 v27, s51\nv_and_b32 v47, 3, v0\ns_mov_b32 s52, 0x27ff0\nv_lshl_add_u32 v28, v47, 2, s52\ns_add_u32 s53, s6, 2\n"
    "v_mov_b32 v29, s53\ns_mov_b32 s54, 0x27fb0\nv_lshl_add_u32 v30, v47, 2, s54\nv_mov_b32 v36, 0x3f803f80\ns_waitcnt vmcnt(2)\ns_setprio 2\ns_barrier\n"
    "ds_read_b128 v[196:199], v21 offset:0\nds_read_b128 v[200:203], v21 offset:1024\nds_read_b128 v[204:207], v21 offset:2048\nds_read_b128 v[208:211], v21 offset:3072\n"
    "ds_read_b128 v[212:215], v21 offset:4096\nds_read_b128 v[216:219], v21 offset:5120\ns_waitcnt lgkmcnt(5)\nv_mfma_f32_32x32x16_bf16 v[2:17], v[132:135], v[196:199], 0\n"
    "ds_read_b128 v[196:199], v21 offset:6144\ns_waitcnt lgkmcnt(5)\nv_mfma_f32_32x32x16_bf16 v[2:17], v[136:139], v[200:203], v[2:17]\n"
    "ds_read_b128 v[200:203], v21 offset:7168\ns_waitcnt lgkmcnt(5)\nv_mfma_f32_32x32x16_bf16 v[2:17], v[140:143], v[204:207], v[2:17]\n"
    "ds_read_b128 v[204:207], v21 offset:8192\ns_waitcnt lgkmcnt(5)\nv_mfma_f32_32x32x16_bf16 v[2:17], v[144:147], v[208:211], v[2:17]\n"
    "ds_read_b128 v[208:211], v21 offset:9216\ns_waitcnt lgkmcnt(5)\nv_mfma_f32_32x32x16_bf16 v[2:17], v[148:151], v[212:215], v[2:17]\n"
    "ds_read_b128 v[212:215], v21 offset:10240\ns_waitcnt lgkmcnt(5)\nv_mfma_f32_32x32x16_bf16 v[2:17], v[152:155], v[216:219], v[2:17]\n"
    "ds_read_b128 v[216:219], v21 offset:11264\ns_waitcnt lgkmcnt(5)\nv_mfma_f32_32x32x16_bf16 v[2:17], v[156:159], v[196:199], v[2:17]\n"
    "ds_read_b128 v[196:199], v21 offset:12288\ns_waitcnt lgkmcnt(5)\nv_mfma_f32_32x32x16_bf16 v[2:17], v[160:163], v[200:203], v[2:17]\n"
    "ds_read_b128 v[200:203], v21 offset:13312\ns_waitcnt lgkmcnt(5)\nv_mfma_f32_32x32x16_bf16 v[2:17], v[164:167], v[204:207], v[2:17]\n"
    "ds_read_b128 v[204:207], v21 offset:14336\ns_waitcnt lgkmcnt(5)\nv_mfma_f32_32x32x16_bf16 v[2:17], v[168:171], v[208:211], v[2:17]\n"
    "ds_read_b128 v[208:211], v21 offset:15360\ns_waitcnt lgkmcnt(5)\nv_mfma_f32_32x32x16_bf16 v[2:17], v[172:175], v[212:215], v[2:17]\n"
    "ds_read_b128 v[212:215], v21 offset:16384\ns_waitcnt lgkmcnt(5)\nv_mfma_f32_32x32x16_bf16 v[2:17], v[176:179], v[216:219], v[2:17]\n"
    "ds_read_b128 v[216:219], v21 offset:17408\ns_waitcnt lgkmcnt(5)\nv_mfma_f32_32x32x16_bf16 v[2:17], v[180:183], v[196:199], v[2:17]\n"
    "ds_read_b128 v[196:199], v21 offset:18432\ns_waitcnt lgkmcnt(5)\nv_mfma_f32_32x32x16_bf16 v[2:17], v[184:187], v[200:203], v[2:17]\n"
    "ds_read_b128 v[200:203], v21 offset:19456\ns_waitcnt lgkmcnt(5)\nv_mfma_f32_32x32x16_bf16 v[2:17], v[188:191], v[204:207], v[2:17]\n"
    "ds_read_b128 v[204:207], v21 offset:20480\ns_waitcnt lgkmcnt(5)\nv_mfma_f32_32x32x16_bf16 v[2:17], v[192:195], v[208:211], v[2:17]\n"
    "ds_read_b128 v[208:211], v21 offset:21504\ns_waitcnt lgkmcnt(5)\nv_mfma_f32_32x32x16_bf16 v[2:17], v[64:67], v[212:215], v[2:17]\n"
    "ds_read_b128 v[212:215], v21 offset:22528\ns_waitcnt lgkmcnt(5)\nv_mfma_f32_32x32x16_bf16 v[2:17], v[68:71], v[216:219], v[2:17]\n"
    "ds_read_b128 v[216:219], v21 offset:23552\ns_waitcnt lgkmcnt(5)\nv_mfma_f32_32x32x16_bf16 v[2:17], v[72:75], v[196:199], v[2:17]\n"
    "ds_read_b128 v[196:199], v21 offset:24576\ns_waitcnt lgkmcnt(5)\nv_mfma_f32_32x32x16_bf16 v[2:17], v[76:79], v[200:203], v[2:17]\n"
    "ds_read_b128 v[200:203], v21 offset:25600\ns_waitcnt lgkmcnt(5)\nv_mfma_f32_32x32x16_bf16 v[2:17], v[80:83], v[204:207], v[2:17]\n"
    "ds_read_b128 v[204:207], v21 offset:26624\ns_waitcnt lgkmcnt(5)\nv_mfma_f32_32x32x16_bf16 v[2:17], v[84:87], v[208:211], v[2:17]\n"
    "ds_read_b128 v[208:211], v21 offset:27648\ns_waitcnt lgkmcnt(5)\nv_mfma_f32_32x32x16_bf16 v[2:17], v[88:91], v[212:215], v[2:17]\n"
    "ds_read_b128 v[212:215], v21 offset:28672\ns_waitcnt lgkmcnt(5)\nv_mfma_f32_32x32x16_bf16 v[2:17], v[92:95], v[216:219], v[2:17]\n"
    "ds_read_b128 v[216:219], v21 offset:29696\ns_waitcnt lgkmcnt(5)\nv_mfma_f32_32x32x16_bf16 v[2:17], v[96:99], v[196:199], v[2:17]\n"
    "ds_read_b128 v[196:199], v21 offset:30720\ns_waitcnt lgkmcnt(5)\nv_mfma_f32_32x32x16_bf16 v[2:17], v[100:103], v[200:203], v[2:17]\n"
    "ds_read_b128 v[200:203], v21 offset:31744\ns_cmp_eq_u32 s6, 0\ns_cbranch_scc0 .LL28\ns_waitcnt vmcnt(0)\nv_permlane32_swap_b32 v240, v242\n"
    "v_permlane32_swap_b32 v241, v243\nv_permlane32_swap_b32 v244, v246\nv_permlane32_swap_b32 v245, v247\n.LL28:\ns_waitcnt lgkmcnt(5)\n"
    "v_mfma_f32_32x32x16_bf16 v[2:17], v[104:107], v[204:207], v[2:17]\ns_waitcnt lgkmcnt(4)\nv_mfma_f32_32x32x16_bf16 v[2:17], v[108:111], v[208:211], v[2:17]\n"
    "v_lshlrev_b32 v220, 16, v240\nv_and_b32 v221, 0xffff0000, v240\nv_lshlrev_b32 v222, 16, v241\nv_and_b32 v223, 0xffff0000, v241\ns_waitcnt lgkmcnt(3)\n"
    "v_mfma_f32_32x32x16_bf16 v[2:17], v[112:115], v[212:215], v[2:17]\nv_lshlrev_b32 v224, 16, v242\nv_and_b32 v225, 0xffff0000, v242\nv_lshlrev_b32 v226, 16, v243\n"
    "v_and_b32 v227, 0xffff0000, v243\nds_read_b32 v31, v30\ns_waitcnt lgkmcnt(3)\nv_mfma_f32_32x32x16_bf16 v[2:17], v[116:119], v[216:219], v[2:17]\n"
    "v_lshlrev_b32 v228, 16, v244\nv_and_b32 v229, 0xffff0000, v244\nv_lshlrev_b32 v230, 16, v245\nv_and_b32 v231, 0xffff0000, v245\ns_waitcnt lgkmcnt(2)\n"
    "v_mfma_f32_32x32x16_bf16 v[2:17], v[120:123], v[196:199], v[2:17]\nv_lshlrev_b32 v232, 16, v246\nv_and_b32 v233, 0xffff0000, v246\nv_lshlrev_b32 v234, 16, v247\n"
    "v_and_b32 v235, 0xffff0000, v247\ns_waitcnt lgkmcnt(1)\nv_mfma_f32_32x32x16_bf16 v[2:17], v[124:127], v[200:203], v[2:17]\ns_nop 11\nv_cvt_pk_bf16_f32 v2, 0, v2\n"
    "v_cvt_pk_bf16_f32 v3, 0, v3\nv_cvt_pk_bf16_f32 v4, 0, v4\nv_cvt_pk_bf16_f32 v5, 0, v5\nv_cvt_pk_bf16_f32 v6, 0, v6\nv_cvt_pk_bf16_f32 v7, 0, v7\n"
    "v_cvt_pk_bf16_f32 v8, 0, v8\nv_cvt_pk_bf16_f32 v9, 0, v9\nv_cvt_pk_bf16_f32 v10, 0, v10\nv_cvt_pk_bf16_f32 v11, 0, v11\nv_cvt_pk_bf16_f32 v12, 0, v12\n"
    "v_cvt_pk_bf16_f32 v13, 0, v13\nv_cvt_pk_bf16_f32 v14, 0, v14\nv_cvt_pk_bf16_f32 v15, 0, v15\nv_cvt_pk_bf16_f32 v16, 0, v16\nv_cvt_pk_bf16_f32 v17, 0, v17\n"
    "v_pk_add_f32 v[2:3], v[2:3], v[220:221]\nv_pk_add_f32 v[4:5], v[4:5], v[222:223]\nv_pk_add_f32 v[6:7], v[6:7], v[224:225]\nv_pk_add_f32 v[8:9], v[8:9], v[226:227]\n"
    "v_pk_add_f32 v[10:11], v[10:11], v[228:229]\nv_pk_add_f32 v[12:13], v[12:13], v[230:231]\nv_pk_add_f32 v[14:15], v[14:15], v[232:233]\n"
    "v_pk_add_f32 v[16:17], v[16:17], v[234:235]\nv_cvt_pk_bf16_f32 v40, v2, v3\nv_cvt_pk_bf16_f32 v41, v4, v5\nv_cvt_pk_bf16_f32 v42, v6, v7\nv_cvt_pk_bf16_f32 v43, v8, v9\n"
    "v_cvt_pk_bf16_f32 v44, v10, v11\nv_cvt_pk_bf16_f32 v45, v12, v13\nv_cvt_pk_bf16_f32 v46, v14, v15\nv_cvt_pk_bf16_f32 v47, v16, v17\nv_min_u32 v48, -2, v40\n"
    "v_min_u32 v49, -2, v41\nds_write2_b32 v18, v48, v49 offset0:0 offset1:32\nv_min_u32 v50, -2, v42\nv_min_u32 v51, -2, v43\n"
    "ds_write2_b32 v18, v50, v51 offset0:128 offset1:160\nv_min_u32 v52, -2, v44\nv_min_u32 v53, -2, v45\nds_write2_b32 v19, v52, v53 offset0:0 offset1:32\n"
    "v_min_u32 v54, -2, v46\nv_min_u32 v55, -2, v47\nds_write2_b32 v19, v54, v55 offset0:128 offset1:160\nds_read_b128 v[2:5], v20\nds_read_b128 v[6:9], v20 offset:1024\n"
    "v_dot2_f32_bf16 v32, v40, v36, 0\nv_dot2_f32_bf16 v33, v40, v40, 0\nv_dot2_f32_bf16 v34, v41, v36, 0\nv_dot2_f32_bf16 v35, v41, v41, 0\nv_dot2c_f32_bf16 v32, v36, v42\n"
    "v_dot2c_f32_bf16 v33, v42, v42\nv_dot2c_f32_bf16 v34, v36, v43\nv_dot2c_f32_bf16 v35, v43, v43\nv_dot2c_f32_bf16 v32, v36, v44\nv_dot2c_f32_bf16 v33, v44, v44\n"
    "v_dot2c_f32_bf16 v34, v36, v45\nv_dot2c_f32_bf16 v35, v45, v45\nv_dot2c_f32_bf16 v32, v36, v46\nv_dot2c_f32_bf16 v33, v46, v46\nv_dot2c_f32_bf16 v34, v36, v47\n"
    "v_dot2c_f32_bf16 v35, v47, v47\ns_nop 1\nv_add_f32 v32, v32, v34\nv_add_f32 v33, v33, v35\ns_nop 1\nv_permlane32_swap_b32 v32, v33\nv_add_f32 v32, v32, v33\n"
    "ds_write_b32 v24, v32\ns_mov_b64 exec, 1\nds_write_b32 v27, v29\ns_mov_b64 exec, -1\ns_waitcnt lgkmcnt(2)\nv_cmp_gt_i32 vcc, 2, v31\ns_cbranch_vccnz .Lfs32\n.Lfr33:\n"
    "global_store_dwordx4 v22, v[2:5], s[32:33] sc1\nglobal_store_dwordx4 v23, v[6:9], s[32:33] sc1\ns_cmp_eq_u32 s3, 0\ns_cbranch_scc0 .LL29\ns_mov_b32 s80, 0x100000\n.LL30:\n"
    "ds_read_b32 v31, v28\nds_read_b128 v[40:43], v25 offset:0\nds_read_b128 v[44:47], v25 offset:256\nds_read_b128 v[48:51], v25 offset:512\n"
    "ds_read_b128 v[52:55], v25 offset:768\ns_waitcnt lgkmcnt(0)\nv_cmp_gt_i32 vcc, v29, v31\nv_pk_add_f32 v[40:41], v[40:41], v[44:45]\n"
    "v_pk_add_f32 v[42:43], v[42:43], v[46:47]\ns_cbranch_vccz .LL31\ns_sub_u32 s80, s80, 1\ns_cbranch_scc0 .LL30\n.LL31:\nv_pk_add_f32 v[48:49], v[48:49], v[52:53]\n"
    "v_pk_add_f32 v[50:51], v[50:51], v[54:55]\nv_pk_add_f32 v[40:41], v[40:41], v[48:49]\nv_pk_add_f32 v[42:43], v[42:43], v[50:51]\nv_min_u32 v40, -2, v40\n"
    "v_min_u32 v41, -2, v41\nv_min_u32 v42, -2, v42\nv_min_u32 v43, -2, v43\ns_mov_b64 exec, 0xffff\nglobal_store_dwordx4 v26, v[40:43], s[32:33] sc1\ns_mov_b64 exec, -1\n"
    ".LL29:\ns_setprio 0\ns_add_u32 s14, s7, 0x401000\nv_lshl_add_u32 v20, v0, 4, s14\ns_lshl_b32 s17, s41, 13\ns_add_u32 s16, s7, s17\ns_add_u32 s15, s16, 0x200000\n"
    "v_lshl_add_u32 v21, v0, 4, s15\nv_and_b32 v22, 7, v0\ns_mov_b32 s18, 0x1d550\nv_lshl_add_u32 v14, v22, 3, s18\nv_lshrrev_b32 v23, 3, v0\ns_mov_b32 s19, 0x50\n"
    "v_mad_u32_u24 v14, v23, s19, v14\ns_mov_b32 s20, 0x20300\nv_lshl_add_u32 v15, v23, 7, s20\nv_xor_b32 v25, v0, v23\nv_and_b32 v24, 7, v25\nv_lshl_add_u32 v15, v24, 4, v15\n"
    "s_mov_b32 s21, 0x17800\nv_lshl_add_u32 v16, v0, 4, s21\ns_lshl_b32 s23, s41, 7\ns_add_u32 s22, s23, 0x600000\nv_lshl_add_u32 v26, v23, 15, s22\nv_and_b32 v27, 1, v0\n"
    "v_lshl_add_u32 v26, v27, 6, v26\nv_bfe_u32 v28, v0, 1, 2\nv_lshl_add_u32 v26, v28, 4, v26\nv_lshrrev_b32 v30, 4, v0\ns_lshl_b32 s44, s35, 18\ns_lshl_b32 s45, s36, 8\n"
    "s_add_u32 s25, s44, s45\ns_add_u32 s24, s25, 0x1200000\nv_lshl_add_u32 v29, v30, 13, s24\nv_and_b32 v31, 15, v0\nv_lshl_add_u32 v29, v31, 4, v29\ns_cmp_eq_u32 s6, 0\n"
    "s_cselect_b64 vcc, -1, 0\nv_cndmask_b32 v17, v29, v26, vcc\nv_and_b32 v46, 0xff, v0\nv_mov_b32 v48, 0x1e950\nv_mov_b32 v50, 0x22f30\nv_mov_b32 v52, 0x23c90\n"
    "v_mov_b32 v53, 0x249f0\nv_cmp_gt_u32 vcc, 0xa1, v46\ns_nop 1\nv_cndmask_b32 v51, v53, v52, vcc\nv_cmp_gt_u32 vcc, 0x7b, v46\ns_nop 1\nv_cndmask_b32 v49, v51, v50, vcc\n"
    "v_cmp_gt_u32 vcc, 0x55, v46\ns_nop 1\nv_cndmask_b32 v47, v49, v48, vcc\nv_lshl_add_u32 v18, v46, 4, v47\nv_bfe_u32 v56, v0, 5, 1\nv_and_b32 v58, 31, v0\n"
    "v_sub_u32 v57, 0x3ff, v58\nv_lshl_add_u32 v55, v56, 3, v57\nv_lshrrev_b32 v54, 2, v55\ns_lshr_b32 s48, s3, 1\ns_mul_i32 s47, s48, 0x2100\ns_add_u32 s46, s47, 0x17800\n"
    "v_lshl_add_u32 v19, v54, 3, s46\nv_and_b32 v59, 3, v55\ns_mov_b32 s49, 0x840\nv_mad_u32_u24 v19, v59, s49, v19\ns_lshl2_add_u32 s50, s3, 0x27f80\nv_mov_b32 v12, s50\n"
    "s_add_u32 s51, s6, 12\nv_mov_b32 v13, s51\nv_add_u32 v61, 0xffffff86, v0\nv_and_b32 v60, 7, v61\ns_mov_b32 s52, 0x1d004\nv_lshl_add_u32 v5, v60, 3, s52\n"
    "v_lshrrev_b32 v62, 3, v61\nv_mad_u32_u24 v5, v62, s19, v5\nv_add_u32 v196, 0xffffff87, v0\nv_and_b32 v63, 7, v196\ns_mov_b32 s53, 0x1d000\nv_lshl_add_u32 v6, v63, 3, s53\n"
    "v_lshrrev_b32 v197, 3, v196\nv_mad_u32_u24 v6, v197, s19, v6\nv_bfe_u32 v199, v0, 2, 2\nv_lshl_add_u32 v198, v56, 3, v199\ns_mul_i32 s55, s48, 0x3180\n"
    "s_add_u32 s54, s55, 0x20300\nv_lshl_add_u32 v2, v198, 4, s54\nv_lshl_add_u32 v2, v27, 3, v2\ns_and_b32 s57, s3, 1\ns_xor_b32 s56, s57, 1\nv_and_b32 v201, 3, v30\n"
    "v_and_b32 v200, s56, v201\ns_mov_b32 s58, 0x480\nv_mad_u32_u24 v2, v200, s58, v2\nv_bfe_u32 v202, v0, 1, 1\ns_mov_b32 s59, 0x240\nv_mad_u32_u24 v2, v202, s59, v2\n"
    "s_add_u32 s60, s54, 0x1f80\nv_lshl_add_u32 v4, v198, 5, s60\nv_and_b32 v203, 3, v0\nv_lshl_add_u32 v4, v203, 3, v4\nv_and_b32 v204, 1, v30\n"
    "v_mad_u32_u24 v3, v204, s58, v4\ns_mul_i32 s62, s48, 0x2800\ns_add_u32 s61, s62, 0x18000\nv_lshl_add_u32 v7, v58, 5, s61\nv_lshl_add_u32 v7, v56, 4, v7\n"
    "s_add_u32 s63, s62, 0x19800\nv_lshl_add_u32 v8, v58, 6, s63\nv_lshrrev_b32 v207, 1, v0\nv_and_b32 v206, 2, v207\nv_xor_b32 v205, v56, v206\nv_lshl_add_u32 v8, v205, 4, v8\n"
    "v_lshlrev_b32 v209, 4, v0\nv_and_b32 v211, 16, v0\nv_lshlrev_b32 v210, 1, v211\nv_xor_b32 v208, v209, v210\nv_add_u32 v9, 0x18800, v208\nv_and_b32 v212, s57, v0\n"
    "s_lshl3_add_u32 s64, s48, 0x27fd0\nv_lshl_add_u32 v11, v212, 2, s64\ns_lshl_b32 s67, s56, 8\ns_add_u32 s66, s67, s47\ns_add_u32 s65, s66, 0x17800\n"
    "v_lshl_add_u32 v10, v54, 3, s65\nv_mad_u32_u24 v10, v59, s49, v10\nv_add_u32 v22, 8192, v20\nv_add_u32 v23, 16384, v20\nv_add_u32 v24, 24576, v20\n"
    "v_add_u32 v25, 32768, v20\nv_add_u32 v26, 40960, v20\nv_add_u32 v27, 49152, v20\nv_add_u32 v28, 57344, v20\ns_waitcnt vmcnt(0)\ns_sleep 9\ns_barrier\n"
    "global_load_dwordx4 v[240:243], v21, s[32:33] sc1\nglobal_load_dwordx4 v[208:211], v20, s[32:33] offset:-4096 sc1\n"
    "global_load_dwordx4 v[212:215], v22, s[32:33] offset:-4096 sc1\nglobal_load_dwordx4 v[216:219], v23, s[32:33] offset:-4096 sc1\n"
    "global_load_dwordx4 v[220:223], v24, s[32:33] offset:-4096 sc1\nglobal_load_dwordx4 v[224:227], v25, s[32:33] offset:-4096 sc1\n"
    "global_load_dwordx4 v[228:231], v26, s[32:33] offset:-4096 sc1\nglobal_load_dwordx4 v[232:235], v27, s[32:33] offset:-4096 sc1\n"
    "global_load_dwordx4 v[236:239], v28, s[32:33] offset:-4096 sc1\ns_cmp_eq_u32 s6, 0\ns_cbranch_scc0 .LL35\nv_mov_b32 v60, 163216\nds_read_b64 v[60:61], v60\n"
    "s_waitcnt lgkmcnt(0)\nv_readfirstlane_b32 s88, v60\nv_readfirstlane_b32 s90, v61\n.LL35:\nv_mov_b64 v[60:61], 0\nv_mov_b64 v[62:63], 0\ns_cmp_eq_u32 s3, 3\n"
    "s_cbranch_scc1 .LL36\ns_cmp_eq_u32 s3, 2\ns_cselect_b32 exec_hi, 3, -1\nds_write_b128 v18, v[60:63]\ns_mov_b64 exec, -1\n.LL36:\nds_read_b64 v[64:65], v19 offset:896\n"
    "ds_read_b64 v[66:67], v19 offset:904\nds_read_b64 v[68:69], v19 offset:864\nds_read_b64 v[70:71], v19 offset:872\nds_read_b64 v[72:73], v19 offset:832\n"
    "ds_read_b64 v[74:75], v19 offset:840\nds_read_b64 v[76:77], v19 offset:800\nds_read_b64 v[78:79], v19 offset:808\nds_read_b64 v[80:81], v19 offset:704\n"
    "ds_read_b64 v[82:83], v19 offset:712\nds_read_b64 v[84:85], v19 offset:672\nds_read_b64 v[86:87], v19 offset:680\nds_read_b64 v[88:89], v19 offset:640\n"
    "ds_read_b64 v[90:91], v19 offset:648\nds_read_b64 v[92:93], v19 offset:608\nds_read_b64 v[94:95], v19 offset:616\nds_read_b64 v[96:97], v10 offset:512\n"
    "ds_read_b64 v[98:99], v10 offset:520\nds_read_b64 v[100:101], v10 offset:480\nds_read_b64 v[102:103], v10 offset:488\nds_read_b64 v[104:105], v10 offset:448\n"
    "ds_read_b64 v[106:107], v10 offset:456\nds_read_b64 v[108:109], v10 offset:416\nds_read_b64 v[110:111], v10 offset:424\nds_read_b64 v[112:113], v10 offset:320\n"
    "ds_read_b64 v[114:115], v10 offset:328\nds_read_b64 v[116:117], v10 offset:288\nds_read_b64 v[118:119], v10 offset:296\nds_read_b64 v[120:121], v10 offset:256\n"
    "ds_read_b64 v[122:123], v10 offset:264\nds_read_b64 v[124:125], v10 offset:224\nds_read_b64 v[126:127], v10 offset:232\ns_waitcnt lgkmcnt(0)\ns_mov_b32 s84, 0x3b000000\n"
    "s_mov_b32 s86, 0x3727c5ac\ns_mov_b32 s80, 0x10000\ns_waitcnt vmcnt(8)\n.Lln37:\nv_max3_u32 v60, v240, v241, v242\nv_max_u32 v60, v60, v243\nv_cmp_eq_u32 s[82:83], -1, v60\n"
    "v_lshlrev_b32 v32, 16, v240\nv_and_b32 v33, 0xffff0000, v240\nv_lshlrev_b32 v34, 16, v241\nv_and_b32 v35, 0xffff0000, v241\nv_lshlrev_b32 v36, 16, v242\n"
    "v_and_b32 v37, 0xffff0000, v242\nv_lshlrev_b32 v38, 16, v243\nv_and_b32 v39, 0xffff0000, v243\ns_waitcnt vmcnt(4)\nv_pk_add_f32 v[48:49], v[208:209], v[216:217]\n"
    "v_pk_add_f32 v[50:51], v[210:211], v[218:219]\nv_pk_add_f32 v[52:53], v[212:213], v[220:221]\nv_pk_add_f32 v[54:55], v[214:215], v[222:223]\ns_waitcnt vmcnt(1)\n"
    "v_pk_add_f32 v[56:57], v[224:225], v[232:233]\nv_pk_add_f32 v[58:59], v[226:227], v[234:235]\nv_pk_add_f32 v[48:49], v[48:49], v[56:57]\n"
    "v_pk_add_f32 v[50:51], v[50:51], v[58:59]\nv_pk_mul_f32 v[48:49], v[48:49], s[84:85] op_sel_hi:[1,0]\nv_pk_mul_f32 v[50:51], v[50:51], s[84:85] op_sel_hi:[1,0]\n"
    "s_waitcnt vmcnt(0)\nv_pk_add_f32 v[56:57], v[228:229], v[236:237]\nv_pk_add_f32 v[58:59], v[230:231], v[238:239]\nv_pk_add_f32 v[52:53], v[52:53], v[56:57]\n"
    "v_pk_add_f32 v[54:55], v[54:55], v[58:59]\nv_pk_mul_f32 v[56:57], v[48:49], v[48:49]\nv_pk_mul_f32 v[58:59], v[50:51], v[50:51]\n"
    "v_pk_fma_f32 v[52:53], v[52:53], s[84:85], v[56:57] op_sel_hi:[1,0,1] neg_lo:[0,0,1] neg_hi:[0,0,1]\n"
    "v_pk_fma_f32 v[54:55], v[54:55], s[84:85], v[58:59] op_sel_hi:[1,0,1] neg_lo:[0,0,1] neg_hi:[0,0,1]\nv_cvt_pk_bf16_f32 v48, 0, v48\nv_cvt_pk_bf16_f32 v49, 0, v49\n"
    "v_pk_add_f32 v[56:57], v[52:53], v[54:55]\nv_cvt_pk_bf16_f32 v50, 0, v50\nv_cvt_pk_bf16_f32 v51, 0, v51\nv_cmp_u_f32 vcc, v56, v57\nv_max_f32 v52, 0, v52\n"
    "v_max_f32 v53, 0, v53\nv_max_f32 v54, 0, v54\nv_max_f32 v55, 0, v55\ns_or_b64 vcc, vcc, s[82:83]\ns_cbranch_vccnz .Lls39\n.Llo38:\nv_cvt_pk_bf16_f32 v52, 0, v52\n"
    "v_cvt_pk_bf16_f32 v53, 0, v53\nv_cvt_pk_bf16_f32 v54, 0, v54\nv_cvt_pk_bf16_f32 v55, 0, v55\nv_pk_add_f32 v[52:53], v[52:53], s[86:87] op_sel_hi:[1,0]\n"
    "v_pk_add_f32 v[54:55], v[54:55], s[86:87] op_sel_hi:[1,0]\nv_pk_add_f32 v[244:245], v[32:33], v[48:49] op_sel:[0,0] op_sel_hi:[1,0] neg_lo:[0,1] neg_hi:[0,1]\n"
    "v_pk_add_f32 v[246:247], v[34:35], v[48:49] op_sel:[0,1] op_sel_hi:[1,1] neg_lo:[0,1] neg_hi:[0,1]\nv_cvt_pk_bf16_f32 v52, 0, v52\nv_cvt_pk_bf16_f32 v53, 0, v53\n"
    "v_cvt_pk_bf16_f32 v54, 0, v54\nv_cvt_pk_bf16_f32 v55, 0, v55\nv_sqrt_f32 v52, v52\n"
    "v_pk_add_f32 v[248:249], v[36:37], v[50:51] op_sel:[0,0] op_sel_hi:[1,0] neg_lo:[0,1] neg_hi:[0,1]\nv_sqrt_f32 v53, v53\n"
    "v_pk_add_f32 v[250:251], v[38:39], v[50:51] op_sel:[0,1] op_sel_hi:[1,1] neg_lo:[0,1] neg_hi:[0,1]\nv_sqrt_f32 v54, v54\nv_cvt_pk_bf16_f32 v244, 0, v244\n"
    "v_sqrt_f32 v55, v55\nv_cvt_pk_bf16_f32 v245, 0, v245\nv_cvt_pk_bf16_f32 v52, 0, v52\nv_cvt_pk_bf16_f32 v53, 0, v53\nv_cvt_pk_bf16_f32 v54, 0, v54\n"
    "v_cvt_pk_bf16_f32 v55, 0, v55\nv_rcp_f32 v52, v52\nv_cvt_pk_bf16_f32 v246, 0, v246\nv_rcp_f32 v53, v53\nv_cvt_pk_bf16_f32 v247, 0, v247\nv_rcp_f32 v54, v54\n"
    "v_cvt_pk_bf16_f32 v248, 0, v248\nv_rcp_f32 v55, v55\nv_cvt_pk_bf16_f32 v249, 0, v249\nv_cvt_pk_bf16_f32 v250, 0, v250\nv_cvt_pk_bf16_f32 v251, 0, v251\n"
    "v_pk_mul_f32 v[244:245], v[244:245], v[52:53] op_sel:[0,0] op_sel_hi:[1,0]\nv_pk_mul_f32 v[246:247], v[246:247], v[52:53] op_sel:[0,1] op_sel_hi:[1,1]\n"
    "v_pk_mul_f32 v[248:249], v[248:249], v[54:55] op_sel:[0,0] op_sel_hi:[1,0]\nv_pk_mul_f32 v[250:251], v[250:251], v[54:55] op_sel:[0,1] op_sel_hi:[1,1]\n"
    "v_cvt_pk_bf16_f32 v244, 0, v244\nv_cvt_pk_bf16_f32 v246, 0, v246\nv_cvt_pk_bf16_f32 v248, 0, v248\nv_cvt_pk_bf16_f32 v250, 0, v250\nv_cvt_pk_f16_f32 v56, v244, v246\n"
    "v_cvt_pk_f16_f32 v57, v248, v250\nv_cvt_pk_bf16_f32 v245, 0, v245\nv_cvt_pk_bf16_f32 v247, 0, v247\nds_write_b64 v14, v[56:57]\nv_cvt_pk_bf16_f32 v249, 0, v249\n"
    "v_cvt_pk_bf16_f32 v251, 0, v251\nv_cvt_pk_f16_f32 v58, v245, v247\nv_cvt_pk_f16_f32 v59, v249, v251\nds_write_b64 v14, v[58:59] offset:6480\ns_waitcnt lgkmcnt(0)\n"
    "s_barrier\nds_read_b32 v48, v5\nds_read_b32 v49, v5 offset:6480\nds_read_b64 v[50:51], v6\nds_read_b64 v[52:53], v6 offset:6480\nv_mul_f32 v244, s96, v244\n"
    "v_mul_f32 v245, s97, v245\nv_mul_f32 v246, s96, v246\nv_mul_f32 v247, s97, v247\nv_mul_f32 v248, s96, v248\nv_mul_f32 v249, s97, v249\nv_mul_f32 v250, s96, v250\n"
    "v_mul_f32 v251, s97, v251\nv_cvt_pk_bf16_f32 v244, 0, v244\nv_cvt_pk_bf16_f32 v245, 0, v245\nv_cvt_pk_bf16_f32 v246, 0, v246\nv_cvt_pk_bf16_f32 v247, 0, v247\n"
    "v_cvt_pk_bf16_f32 v248, 0, v248\nv_cvt_pk_bf16_f32 v249, 0, v249\nv_cvt_pk_bf16_f32 v250, 0, v250\nv_cvt_pk_bf16_f32 v251, 0, v251\ns_waitcnt lgkmcnt(0)\n"
    "v_cvt_f32_f16_sdwa v24, v48 dst_sel:DWORD dst_unused:UNUSED_PAD src0_sel:WORD_1\nv_cvt_f32_f16_sdwa v26, v50 dst_sel:DWORD dst_unused:UNUSED_PAD src0_sel:WORD_1\n"
    "v_cvt_f32_f16 v25, v50\nv_cvt_f32_f16 v27, v51\nv_cvt_f32_f16_sdwa v28, v49 dst_sel:DWORD dst_unused:UNUSED_PAD src0_sel:WORD_1\n"
    "v_cvt_f32_f16_sdwa v30, v52 dst_sel:DWORD dst_unused:UNUSED_PAD src0_sel:WORD_1\nv_cvt_f32_f16 v29, v52\nv_cvt_f32_f16 v31, v53\nv_mul_f32_e64 v24, -s92, v24\n"
    "v_mul_f32_e64 v25, -s92, v25\nv_mul_f32_e64 v26, -s92, v26\nv_mul_f32_e64 v27, -s92, v27\nv_mul_f32_e64 v28, -s94, v28\nv_mul_f32_e64 v29, -s94, v29\n"
    "v_mul_f32_e64 v30, -s94, v30\nv_mul_f32_e64 v31, -s94, v31\ns_cmp_eq_u32 s6, 0\ns_cbranch_scc0 .LL49\nv_and_b32 v56, 63, v0\ns_lshl_b32 s16, s41, 13\n"
    "s_lshl_b32 s17, s3, 11\ns_add_u32 s15, s16, s17\ns_add_u32 s14, s15, 0x800000\nv_lshl_add_u32 v48, v56, 4, s14\ns_add_u32 s18, s15, 0xa00000\n"
    "v_lshl_add_u32 v49, v56, 4, s18\ns_add_u32 s19, s15, 0x1200000\nv_lshl_add_u32 v50, v56, 4, s19\nv_and_b32 v57, 15, v0\ns_lshl_b32 s21, s2, 8\n"
    "s_add_u32 s20, s21, 0xc00000\nv_lshl_add_u32 v51, v57, 4, s20\nv_mov_b32 v52, -1\nv_mov_b32 v53, -1\nv_mov_b32 v54, -1\nv_mov_b32 v55, -1\n"
    "global_store_dwordx4 v48, v[52:55], s[32:33] offset:0 sc1\nglobal_store_dwordx4 v48, v[52:55], s[32:33] offset:1024 sc1\n"
    "global_store_dwordx4 v49, v[52:55], s[32:33] offset:0 sc1\nglobal_store_dwordx4 v49, v[52:55], s[32:33] offset:1024 sc1\n"
    "global_store_dwordx4 v50, v[52:55], s[32:33] offset:0 sc1\nglobal_store_dwordx4 v50, v[52:55], s[32:33] offset:1024 sc1\ns_cmp_eq_u32 s3, 2\ns_cbranch_scc0 .LL49\n"
    "global_store_dwordx4 v51, v[52:55], s[32:33] sc1\n.LL49:\ns_mov_b32 s80, 0x100000\n.Lsl54:\nds_read_b32 v22, v11\ns_waitcnt lgkmcnt(0)\nv_cmp_gt_i32 vcc, v13, v22\n"
    "s_cbranch_vccz .Lsr53\ns_sleep 1\ns_sub_u32 s80, s80, 1\ns_cbranch_scc0 .Lsl54\n.Lsr53:\ns_setprio 2\ns_bitcmp1_b32 s3, 0\ns_cbranch_scc1 .LL50\n"
    "ds_read_b64_tr_b16 v[48:49], v2 offset:1984\nds_read_b64_tr_b16 v[50:51], v2 offset:2048\nds_read_b64_tr_b16 v[52:53], v2 offset:1728\n"
    "ds_read_b64_tr_b16 v[54:55], v2 offset:1792\nds_read_b64_tr_b16 v[56:57], v2 offset:1408\nds_read_b64_tr_b16 v[58:59], v2 offset:1472\n"
    "ds_read_b64_tr_b16 v[60:61], v2 offset:1152\nds_read_b64_tr_b16 v[62:63], v2 offset:1216\ns_waitcnt lgkmcnt(6)\nv_mfma_f32_32x32x16_f16 v[196:211], v[48:51], v[64:67], 0\n"
    "ds_read_b64_tr_b16 v[48:49], v2 offset:832\nds_read_b64_tr_b16 v[50:51], v2 offset:896\ns_waitcnt lgkmcnt(6)\n"
    "v_mfma_f32_32x32x16_f16 v[196:211], v[52:55], v[68:71], v[196:211]\nds_read_b64_tr_b16 v[52:53], v2 offset:576\nds_read_b64_tr_b16 v[54:55], v2 offset:640\n"
    "s_waitcnt lgkmcnt(6)\nv_mfma_f32_32x32x16_f16 v[196:211], v[56:59], v[72:75], v[196:211]\nds_read_b64_tr_b16 v[56:57], v2 offset:6016\n"
    "ds_read_b64_tr_b16 v[58:59], v2 offset:6080\ns_waitcnt lgkmcnt(6)\nv_mfma_f32_32x32x16_f16 v[196:211], v[60:63], v[76:79], v[196:211]\n"
    "ds_read_b64_tr_b16 v[60:61], v2 offset:5760\nds_read_b64_tr_b16 v[62:63], v2 offset:5824\ns_waitcnt lgkmcnt(6)\n"
    "v_mfma_f32_32x32x16_f16 v[196:211], v[48:51], v[96:99], v[196:211]\nds_read_b64_tr_b16 v[48:49], v2 offset:5440\nds_read_b64_tr_b16 v[50:51], v2 offset:5504\n"
    "s_waitcnt lgkmcnt(6)\nv_mfma_f32_32x32x16_f16 v[196:211], v[52:55], v[100:103], v[196:211]\nds_read_b64_tr_b16 v[52:53], v2 offset:5184\n"
    "ds_read_b64_tr_b16 v[54:55], v2 offset:5248\ns_waitcnt lgkmcnt(6)\nv_mfma_f32_32x32x16_f16 v[196:211], v[56:59], v[80:83], v[196:211]\n"
    "ds_read_b64_tr_b16 v[56:57], v2 offset:4864\nds_read_b64_tr_b16 v[58:59], v2 offset:4928\ns_waitcnt lgkmcnt(6)\n"
    "v_mfma_f32_32x32x16_f16 v[196:211], v[60:63], v[84:87], v[196:211]\nds_read_b64_tr_b16 v[60:61], v2 offset:4608\nds_read_b64_tr_b16 v[62:63], v2 offset:4672\n"
    "s_mov_b64 exec, 1\nds_write_b32 v12, v13 offset:64\ns_mov_b64 exec, -1\ns_waitcnt lgkmcnt(7)\nv_mfma_f32_32x32x16_f16 v[196:211], v[48:51], v[88:91], v[196:211]\n"
    "s_waitcnt lgkmcnt(5)\nv_mfma_f32_32x32x16_f16 v[196:211], v[52:55], v[92:95], v[196:211]\ns_waitcnt lgkmcnt(3)\n"
    "v_mfma_f32_32x32x16_f16 v[196:211], v[56:59], v[112:115], v[196:211]\ns_waitcnt lgkmcnt(1)\nv_mfma_f32_32x32x16_f16 v[196:211], v[60:63], v[116:119], v[196:211]\ns_nop 11\n"
    "ds_write_b128 v7, v[196:199] offset:2048\nds_write_b128 v7, v[200:203] offset:3072\nds_write_b128 v7, v[204:207] offset:4096\nds_write_b128 v7, v[208:211] offset:5120\n"
    "s_branch .LL51\n.LL50:\nds_read_b64_tr_b16 v[48:49], v3 offset:512\nds_read_b64_tr_b16 v[50:51], v3 offset:640\nds_read_b64_tr_b16 v[52:53], v3 offset:0\n"
    "ds_read_b64_tr_b16 v[54:55], v3 offset:128\nds_read_b64_tr_b16 v[56:57], v4 offset:512\nds_read_b64_tr_b16 v[58:59], v4 offset:640\n"
    "ds_read_b64_tr_b16 v[60:61], v4 offset:0\nds_read_b64_tr_b16 v[62:63], v4 offset:128\ns_waitcnt lgkmcnt(6)\nv_mfma_f32_32x32x16_f16 v[228:243], v[48:51], v[96:99], 0\n"
    "ds_read_b64_tr_b16 v[48:49], v3 offset:2816\nds_read_b64_tr_b16 v[50:51], v3 offset:2944\ns_waitcnt lgkmcnt(6)\n"
    "v_mfma_f32_32x32x16_f16 v[228:243], v[52:55], v[100:103], v[228:243]\nds_read_b64_tr_b16 v[52:53], v3 offset:2304\nds_read_b64_tr_b16 v[54:55], v3 offset:2432\n"
    "s_waitcnt lgkmcnt(6)\nv_mfma_f32_32x32x16_f16 v[212:227], v[56:59], v[104:107], 0\nds_read_b64_tr_b16 v[56:57], v4 offset:2816\n"
    "ds_read_b64_tr_b16 v[58:59], v4 offset:2944\ns_waitcnt lgkmcnt(6)\nv_mfma_f32_32x32x16_f16 v[212:227], v[60:63], v[108:111], v[212:227]\n"
    "ds_read_b64_tr_b16 v[60:61], v4 offset:2304\nds_read_b64_tr_b16 v[62:63], v4 offset:2432\ns_waitcnt lgkmcnt(6)\n"
    "v_mfma_f32_32x32x16_f16 v[228:243], v[48:51], v[112:115], v[228:243]\nds_read_b64_tr_b16 v[48:49], v2 offset:832\nds_read_b64_tr_b16 v[50:51], v2 offset:896\n"
    "s_waitcnt lgkmcnt(6)\nv_mfma_f32_32x32x16_f16 v[228:243], v[52:55], v[116:119], v[228:243]\nds_read_b64_tr_b16 v[52:53], v2 offset:576\n"
    "ds_read_b64_tr_b16 v[54:55], v2 offset:640\ns_waitcnt lgkmcnt(6)\nv_mfma_f32_32x32x16_f16 v[212:227], v[56:59], v[120:123], v[212:227]\n"
    "ds_read_b64_tr_b16 v[56:57], v2 offset:256\nds_read_b64_tr_b16 v[58:59], v2 offset:320\ns_waitcnt lgkmcnt(6)\n"
    "v_mfma_f32_32x32x16_f16 v[212:227], v[60:63], v[124:127], v[212:227]\nds_read_b64_tr_b16 v[60:61], v2 offset:0\nds_read_b64_tr_b16 v[62:63], v2 offset:64\n"
    "s_waitcnt lgkmcnt(6)\nv_mfma_f32_32x32x16_f16 v[196:211], v[48:51], v[64:67], 0\nds_read_b64_tr_b16 v[48:49], v2 offset:4864\nds_read_b64_tr_b16 v[50:51], v2 offset:4928\n"
    "s_waitcnt lgkmcnt(6)\nv_mfma_f32_32x32x16_f16 v[196:211], v[52:55], v[68:71], v[196:211]\nds_read_b64_tr_b16 v[52:53], v2 offset:4608\n"
    "ds_read_b64_tr_b16 v[54:55], v2 offset:4672\ns_waitcnt lgkmcnt(6)\nv_mfma_f32_32x32x16_f16 v[196:211], v[56:59], v[72:75], v[196:211]\n"
    "ds_read_b64_tr_b16 v[56:57], v2 offset:4288\nds_read_b64_tr_b16 v[58:59], v2 offset:4352\ns_waitcnt lgkmcnt(6)\n"
    "v_mfma_f32_32x32x16_f16 v[196:211], v[60:63], v[76:79], v[196:211]\nds_read_b64_tr_b16 v[60:61], v2 offset:4032\nds_read_b64_tr_b16 v[62:63], v2 offset:4096\n"
    "s_mov_b64 exec, 1\nds_write_b32 v12, v13 offset:64\ns_mov_b64 exec, -1\ns_waitcnt lgkmcnt(7)\nv_mfma_f32_32x32x16_f16 v[196:211], v[48:51], v[80:83], v[196:211]\n"
    "v_add_f32 v236, v236, v220\nv_add_f32 v237, v237, v221\nv_add_f32 v238, v238, v222\nv_add_f32 v239, v239, v223\ns_waitcnt lgkmcnt(5)\n"
    "v_mfma_f32_32x32x16_f16 v[196:211], v[52:55], v[84:87], v[196:211]\nv_add_f32 v240, v240, v224\nv_add_f32 v241, v241, v225\nv_add_f32 v242, v242, v226\n"
    "v_add_f32 v243, v243, v227\ns_waitcnt lgkmcnt(3)\nv_mfma_f32_32x32x16_f16 v[196:211], v[56:59], v[88:91], v[196:211]\nv_xor_b32 v22, 32, v8\ns_waitcnt lgkmcnt(1)\n"
    "v_mfma_f32_32x32x16_f16 v[196:211], v[60:63], v[92:95], v[196:211]\nds_write_b128 v8, v[228:231] offset:0\nds_write_b128 v22, v[232:235] offset:0\n"
    "ds_write_b128 v8, v[236:239] offset:2048\nds_write_b128 v22, v[240:243] offset:2048\ns_nop 7\nds_write_b128 v7, v[196:199]\nds_write_b128 v7, v[200:203] offset:1024\n"
    ".LL51:\ns_setprio 0\ns_waitcnt vmcnt(0)\ns_waitcnt lgkmcnt(0)\ns_barrier\nds_read_b128 v[48:51], v15\nds_read_b128 v[52:55], v15 offset:12672\nds_read_b128 v[56:59], v16\n"
    "ds_read_b128 v[196:199], v9\nds_read_b128 v[60:63], v16 offset:10240\nds_read_b128 v[200:203], v9 offset:10240\ns_waitcnt lgkmcnt(4)\n"
    "v_pk_add_f32 v[24:25], v[24:25], v[48:49]\nv_pk_add_f32 v[26:27], v[26:27], v[50:51]\nv_pk_add_f32 v[28:29], v[28:29], v[52:53]\nv_pk_add_f32 v[30:31], v[30:31], v[54:55]\n"
    "s_waitcnt lgkmcnt(2)\nv_pk_add_f32 v[56:57], v[56:57], v[196:197]\nv_pk_add_f32 v[58:59], v[58:59], v[198:199]\ns_waitcnt lgkmcnt(0)\n"
    "v_pk_add_f32 v[60:61], v[60:61], v[200:201]\nv_pk_add_f32 v[62:63], v[62:63], v[202:203]\nv_pk_add_f32 v[24:25], v[24:25], v[56:57]\n"
    "v_pk_add_f32 v[26:27], v[26:27], v[58:59]\nv_pk_add_f32 v[28:29], v[28:29], v[60:61]\nv_pk_add_f32 v[30:31], v[30:31], v[62:63]\n"
    "v_pk_mul_f32 v[24:25], v[24:25], s[88:89] op_sel_hi:[1,0]\nv_pk_mul_f32 v[26:27], v[26:27], s[88:89] op_sel_hi:[1,0]\n"
    "v_pk_mul_f32 v[28:29], v[28:29], s[90:91] op_sel_hi:[1,0]\nv_pk_mul_f32 v[30:31], v[30:31], s[90:91] op_sel_hi:[1,0]\nv_cvt_pk_bf16_f32 v48, 0, v24\n"
    "v_cvt_pk_bf16_f32 v49, 0, v28\nv_cvt_pk_bf16_f32 v50, 0, v25\nv_cvt_pk_bf16_f32 v51, 0, v29\nv_cvt_pk_bf16_f32 v52, 0, v26\nv_cvt_pk_bf16_f32 v53, 0, v30\n"
    "v_cvt_pk_bf16_f32 v54, 0, v27\nv_cvt_pk_bf16_f32 v55, 0, v31\nv_pk_add_f32 v[244:245], v[244:245], v[48:49]\nv_pk_add_f32 v[246:247], v[246:247], v[50:51]\n"
    "v_pk_add_f32 v[248:249], v[248:249], v[52:53]\nv_pk_add_f32 v[250:251], v[250:251], v[54:55]\nv_cvt_pk_bf16_f32 v244, 0, v244\nv_cvt_pk_bf16_f32 v245, 0, v245\n"
    "v_cvt_pk_bf16_f32 v246, 0, v246\nv_cvt_pk_bf16_f32 v247, 0, v247\nv_cvt_pk_bf16_f32 v248, 0, v248\nv_cvt_pk_bf16_f32 v249, 0, v249\nv_cvt_pk_bf16_f32 v250, 0, v250\n"
    "v_cvt_pk_bf16_f32 v251, 0, v251\nv_pk_add_f32 v[32:33], v[32:33], v[244:245]\nv_pk_add_f32 v[34:35], v[34:35], v[246:247]\nv_pk_add_f32 v[36:37], v[36:37], v[248:249]\n"
    "v_pk_add_f32 v[38:39], v[38:39], v[250:251]\ns_cmp_eq_u32 s6, 0\ns_cbranch_scc0 .LL56\ns_mov_b32 s81, 0xfffefffe\ns_mov_b32 vcc_lo, 0x55555555\n"
    "s_mov_b32 vcc_hi, 0x55555555\nv_cvt_pk_bf16_f32 v54, v33, v35\nv_cvt_pk_bf16_f32 v55, v37, v39\nv_cvt_pk_bf16_f32 v52, v32, v34\nv_cvt_pk_bf16_f32 v53, v36, v38\n"
    "v_pk_min_u16 v54, v54, s81\nv_pk_min_u16 v55, v55, s81\nv_pk_min_u16 v52, v52, s81\nv_pk_min_u16 v53, v53, s81\ns_nop 0\n"
    "v_cndmask_b32_dpp v48, v54, v52, vcc quad_perm:[1,0,3,2] row_mask:0xf bank_mask:0xf\nv_cndmask_b32_dpp v49, v55, v53, vcc quad_perm:[1,0,3,2] row_mask:0xf bank_mask:0xf\n"
    "s_not_b64 vcc, vcc\nv_cndmask_b32_dpp v50, v52, v54, vcc quad_perm:[1,0,3,2] row_mask:0xf bank_mask:0xf\n"
    "v_cndmask_b32_dpp v51, v53, v55, vcc quad_perm:[1,0,3,2] row_mask:0xf bank_mask:0xf\nglobal_store_dwordx4 v17, v[48:51], s[32:33] sc1\ns_branch .LH213\n.LL56:\n"
    "v_cvt_pk_bf16_f32 v48, v32, v33\nv_cvt_pk_bf16_f32 v49, v34, v35\nv_cvt_pk_bf16_f32 v50, v36, v37\nv_cvt_pk_bf16_f32 v51, v38, v39\nv_min_u32 v48, -2, v48\n"
    "v_min_u32 v49, -2, v49\nv_min_u32 v50, -2, v50\nv_min_u32 v51, -2, v51\nglobal_store_dwordx4 v17, v[48:51], s[32:33] sc1\nv_and_b32 v4, 7, v0\ns_lshl_b32 s15, s41, 13\n"
    "s_add_u32 s14, s15, 0x1200000\nv_lshl_add_u32 v2, v4, 10, s14\nv_lshrrev_b32 v5, 3, v0\nv_lshl_add_u32 v2, v5, 2, v2\ns_lshl_b32 s17, s36, 16\ns_lshl_b32 s18, s35, 7\n"
    "s_add_u32 s16, s17, s18\nv_lshl_add_u32 v3, v5, 10, s16\nv_lshl_add_u32 v3, v4, 4, v3\ns_mov_b32 s80, 0x10000\ns_waitcnt vmcnt(0)\ns_sleep 12\n.LL57:\n"
    "global_load_dword v52, v2, s[32:33] offset:0 sc1\nglobal_load_dword v53, v2, s[32:33] offset:256 sc1\nglobal_load_dword v54, v2, s[32:33] offset:512 sc1\n"
    "global_load_dword v55, v2, s[32:33] offset:768 sc1\ns_waitcnt vmcnt(0)\nv_max3_u32 v4, v52, v53, v54\nv_max_u32 v4, v4, v55\nv_cmp_eq_u32 vcc, -1, v4\n"
    "s_cbranch_vccz .LL58\ns_sub_u32 s80, s80, 1\ns_cbranch_scc0 .LL57\n.LL58:\nglobal_store_dwordx4 v3, v[52:55], s[30:31] sc1\ns_endpgm\n.LH213:\ns_mov_b32 s6, 1\n"
    "s_mov_b32 s7, 8388608\nv_and_b32 v196, 63, v0\nv_lshlrev_b32 v26, 4, v196\nv_bfe_u32 v197, v0, 1, 5\nv_lshl_add_u32 v27, v197, 6, 32\nv_and_b32 v198, 1, v0\n"
    "v_lshl_add_u32 v27, v198, 4, v27\ns_mov_b32 s14, 0x1020\nv_lshl_add_u32 v9, v197, 6, s14\nv_lshl_add_u32 v9, v198, 4, v9\nv_bfe_u32 v200, v0, 4, 2\n"
    "v_bfe_u32 v201, v0, 2, 2\nv_lshl_add_u32 v199, v200, 3, v201\ns_lshl_b32 s16, s3, 12\ns_add_u32 s15, s16, 0x24000\nv_lshl_add_u32 v29, v199, 5, s15\nv_and_b32 v202, 3, v0\n"
    "v_lshl_add_u32 v29, v202, 3, v29\ns_lshl_b32 s18, s3, 6\ns_add_u32 s17, s18, 0x27c80\nv_lshl_add_u32 v40, v200, 4, s17\ns_bfe_u32 s22, s3, 0x10001\n"
    "s_lshl_b32 s21, s22, 15\ns_lshl_b32 s23, s3, 13\ns_add_u32 s20, s21, s23\ns_add_u32 s19, s20, 0x8000\nv_lshl_add_u32 v30, v199, 6, s19\nv_lshl_add_u32 v30, v202, 3, v30\n"
    "s_and_b32 s24, s3, 3\nv_xor_b32 v204, s24, v200\nv_and_b32 v203, 1, v204\nv_lshl_add_u32 v30, v203, 5, v30\ns_add_u32 s47, s3, 1\ns_bfe_u32 s46, s47, 0x10001\n"
    "s_lshl_b32 s45, s46, 15\ns_add_u32 s44, s45, s23\ns_add_u32 s25, s44, 0x8000\nv_lshl_add_u32 v31, v199, 6, s25\nv_lshl_add_u32 v31, v202, 3, v31\ns_and_b32 s48, s47, 3\n"
    "v_xor_b32 v206, s48, v200\nv_and_b32 v205, 1, v206\nv_lshl_add_u32 v31, v205, 5, v31\ns_add_u32 s53, s3, 2\ns_bfe_u32 s52, s53, 0x10001\ns_lshl_b32 s51, s52, 15\n"
    "s_add_u32 s50, s51, s23\ns_add_u32 s49, s50, 0x8000\nv_lshl_add_u32 v32, v199, 6, s49\nv_lshl_add_u32 v32, v202, 3, v32\ns_and_b32 s54, s53, 3\nv_xor_b32 v208, s54, v200\n"
    "v_and_b32 v207, 1, v208\nv_lshl_add_u32 v32, v207, 5, v32\ns_add_u32 s59, s3, 3\ns_bfe_u32 s58, s59, 0x10001\ns_lshl_b32 s57, s58, 15\ns_add_u32 s56, s57, s23\n"
    "s_add_u32 s55, s56, 0x8000\nv_lshl_add_u32 v33, v199, 6, s55\nv_lshl_add_u32 v33, v202, 3, v33\ns_and_b32 s60, s59, 3\nv_xor_b32 v210, s60, v200\nv_and_b32 v209, 1, v210\n"
    "v_lshl_add_u32 v33, v209, 5, v33\nds_read_b128 v[248:251], v40\ns_lshl2_add_u32 s15, s37, s3\ns_lshl_b32 s14, s15, 15\ns_add_u32 s62, s14, 0x600000\n"
    "s_mul_i32 s16, s3, 0x3000\ns_add_u32 s64, s16, 0x18000\ns_lshl_b32 s21, s37, 2\ns_max_u32 s20, s21, 1\ns_add_u32 s19, s20, -1\ns_lshl_b32 s18, s19, 15\n"
    "s_add_u32 s17, s18, 0x600000\ns_lshl_b32 s22, s3, 13\ns_add_u32 s60, s17, s22\ns_lshl_b32 s24, s3, 12\ns_add_u32 s23, s24, 0x24000\ns_add_u32 s65, s23, 0xfffffc00\n"
    "s_and_b32 s66, s42, 7\ns_lshl_b32 s25, s66, 12\ns_add_u32 s44, s62, s25\ns_add_u32 s69, s42, 1\ns_and_b32 s68, s69, 7\ns_lshl_b32 s67, s68, 12\ns_add_u32 s45, s62, s67\n"
    "s_add_u32 s72, s42, 2\ns_and_b32 s71, s72, 7\ns_lshl_b32 s70, s71, 12\ns_add_u32 s46, s62, s70\ns_add_u32 s75, s42, 3\ns_and_b32 s74, s75, 7\ns_lshl_b32 s73, s74, 12\n"
    "s_add_u32 s47, s62, s73\ns_add_u32 s78, s42, 4\ns_and_b32 s77, s78, 7\ns_lshl_b32 s76, s77, 12\ns_add_u32 s48, s62, s76\ns_add_u32 s16, s42, 5\ns_and_b32 s15, s16, 7\n"
    "s_lshl_b32 s14, s15, 12\ns_add_u32 s49, s62, s14\ns_add_u32 s19, s42, 6\ns_and_b32 s18, s19, 7\ns_lshl_b32 s17, s18, 12\ns_add_u32 s50, s62, s17\ns_add_u32 s22, s42, 7\n"
    "s_and_b32 s21, s22, 7\ns_lshl_b32 s20, s21, 12\ns_add_u32 s51, s62, s20\ns_add_u32 s60, s60, s32\ns_addc_u32 s61, s33, 0\nds_read_b64_tr_b16 v[64:65], v30 offset:0\n"
    "ds_read_b64_tr_b16 v[66:67], v30 offset:256\nds_read_b64_tr_b16 v[68:69], v30 offset:2048\nds_read_b64_tr_b16 v[70:71], v30 offset:2304\n"
    "ds_read_b64_tr_b16 v[72:73], v30 offset:4096\nds_read_b64_tr_b16 v[74:75], v30 offset:4352\nds_read_b64_tr_b16 v[76:77], v30 offset:6144\n"
    "ds_read_b64_tr_b16 v[78:79], v30 offset:6400\nds_read_b64_tr_b16 v[80:81], v31 offset:0\nds_read_b64_tr_b16 v[82:83], v31 offset:256\n"
    "ds_read_b64_tr_b16 v[84:85], v31 offset:2048\nds_read_b64_tr_b16 v[86:87], v31 offset:2304\nds_read_b64_tr_b16 v[88:89], v31 offset:4096\n"
    "ds_read_b64_tr_b16 v[90:91], v31 offset:4352\nds_read_b64_tr_b16 v[92:93], v31 offset:6144\nds_read_b64_tr_b16 v[94:95], v31 offset:6400\n"
    "ds_read_b64_tr_b16 v[96:97], v32 offset:0\nds_read_b64_tr_b16 v[98:99], v32 offset:256\nds_read_b64_tr_b16 v[100:101], v32 offset:2048\n"
    "ds_read_b64_tr_b16 v[102:103], v32 offset:2304\nds_read_b64_tr_b16 v[104:105], v32 offset:4096\nds_read_b64_tr_b16 v[106:107], v32 offset:4352\n"
    "ds_read_b64_tr_b16 v[108:109], v32 offset:6144\nds_read_b64_tr_b16 v[110:111], v32 offset:6400\nds_read_b64_tr_b16 v[112:113], v33 offset:0\n"
    "ds_read_b64_tr_b16 v[114:115], v33 offset:256\nds_read_b64_tr_b16 v[116:117], v33 offset:2048\nds_read_b64_tr_b16 v[118:119], v33 offset:2304\n"
    "ds_read_b64_tr_b16 v[120:121], v33 offset:4096\nds_read_b64_tr_b16 v[122:123], v33 offset:4352\nds_read_b64_tr_b16 v[124:125], v33 offset:6144\n"
    "ds_read_b64_tr_b16 v[126:127], v33 offset:6400\ns_waitcnt vmcnt(0) lgkmcnt(0)\ns_sleep 8\ns_setprio 3\ns_barrier\ns_add_u32 m0, s64, 0\ns_add_u32 s52, s32, s44\n"
    "s_addc_u32 s53, s33, 0\nglobal_load_lds_dwordx4 v26, s[52:53] offset:0 sc1\nglobal_load_lds_dwordx4 v26, s[52:53] offset:1024 sc1\n"
    "global_load_lds_dwordx4 v26, s[52:53] offset:2048 sc1\nglobal_load_lds_dwordx4 v26, s[52:53] offset:3072 sc1\ns_add_u32 m0, s64, 4096\ns_add_u32 s52, s32, s45\n"
    "s_addc_u32 s53, s33, 0\nglobal_load_lds_dwordx4 v26, s[52:53] offset:0 sc1\nglobal_load_lds_dwordx4 v26, s[52:53] offset:1024 sc1\n"
    "global_load_lds_dwordx4 v26, s[52:53] offset:2048 sc1\nglobal_load_lds_dwordx4 v26, s[52:53] offset:3072 sc1\nv_mov_b64 v[10:11], 0\nv_mov_b64 v[12:13], 0\n"
    "v_mov_b64 v[14:15], 0\nv_mov_b64 v[16:17], 0\nv_mov_b64 v[18:19], 0\nv_mov_b64 v[20:21], 0\nv_mov_b64 v[22:23], 0\nv_mov_b64 v[24:25], 0\nv_add_u32 v1, 8388608, v1\n"
    "v_and_b32 v196, 63, v0\ns_lshl_b32 s15, s3, 12\ns_add_u32 s14, s15, 0x24000\nv_lshl_add_u32 v28, v196, 4, s14\ns_add_u32 s19, s3, 3\ns_and_b32 s18, s19, 3\n"
    "s_lshl_b32 s17, s18, 12\ns_add_u32 s16, s17, 0x24000\nv_lshl_add_u32 v34, v196, 4, s16\ns_add_u32 s23, s3, 2\ns_and_b32 s22, s23, 3\ns_lshl_b32 s21, s22, 12\n"
    "s_add_u32 s20, s21, 0x24400\nv_lshl_add_u32 v35, v196, 4, s20\ns_add_u32 s67, s3, 1\ns_and_b32 s66, s67, 3\ns_lshl_b32 s25, s66, 12\ns_add_u32 s24, s25, 0x24800\n"
    "v_lshl_add_u32 v36, v196, 4, s24\ns_lshl_b32 s71, s35, 15\ns_and_b32 s73, s36, 3\ns_lshl_b32 s72, s73, 13\ns_add_u32 s70, s71, s72\ns_lshl_b32 s74, s3, 11\n"
    "s_add_u32 s69, s70, s74\ns_add_u32 s68, s69, 0xd00000\nv_lshl_add_u32 v37, v196, 2, s68\nv_bfe_u32 v196, v0, 4, 2\ns_max_u32 s20, s37, 1\ns_add_u32 s19, s20, -1\n"
    "s_lshr_b32 s18, s19, 2\ns_and_b32 s21, s35, 1\ns_lshl1_add_u32 s17, s18, s21\ns_lshl_b32 s16, s17, 15\ns_and_b32 s23, s19, 3\ns_lshl_b32 s22, s23, 13\n"
    "s_add_u32 s15, s16, s22\ns_add_u32 s14, s15, 0xd01840\nv_lshl_add_u32 v38, v196, 9, s14\nv_and_b32 v197, 15, v0\nv_lshl_add_u32 v38, v197, 2, v38\nv_and_b32 v198, 63, v0\n"
    "s_lshl_b32 s25, s3, 12\ns_add_u32 s24, s25, 0x24c00\nv_lshl_add_u32 v39, v198, 3, s24\ns_lshl_b32 s67, s3, 5\ns_add_u32 s66, s67, 0x18040\n"
    "v_lshl_add_u32 v41, v196, 3, s66\ns_mov_b32 s68, 0xd0\nv_mad_u32_u24 v41, v197, s68, v41\nv_and_b32 v199, 31, v0\ns_mov_b32 s69, 0x27f80\nv_lshl_add_u32 v42, v199, 2, s69\n"
    "v_mov_b32 v43, 2\ns_lshl2_add_u32 s72, s40, s3\ns_lshl_b32 s71, s72, 15\ns_add_u32 s70, s71, 0xe05000\nv_lshl_add_u32 v4, v198, 4, s70\ns_add_u32 s73, s71, 0xe07000\n"
    "v_lshl_add_u32 v5, v198, 4, s73\ns_setprio 0\ns_waitcnt vmcnt(4)\ns_waitcnt lgkmcnt(0)\ns_barrier\ns_add_u32 m0, s64, 8192\ns_add_u32 s52, s32, s46\n"
    "s_addc_u32 s53, s33, 0\nglobal_load_lds_dwordx4 v26, s[52:53] offset:0 sc1\nglobal_load_lds_dwordx4 v26, s[52:53] offset:1024 sc1\n"
    "global_load_lds_dwordx4 v26, s[52:53] offset:2048 sc1\nglobal_load_lds_dwordx4 v26, s[52:53] offset:3072 sc1\ns_add_u32 m0, s65, 1024\ns_nop 0\n"
    "global_load_lds_dwordx4 v27, s[60:61] offset:0 sc1\ns_add_u32 m0, s65, 0\ns_nop 0\nglobal_load_lds_dwordx4 v27, s[60:61] offset:2048 sc1\ns_add_u32 m0, s65, 3072\ns_nop 0\n"
    "global_load_lds_dwordx4 v9, s[60:61] offset:0 sc1\ns_add_u32 m0, s65, 2048\ns_nop 0\nglobal_load_lds_dwordx4 v9, s[60:61] offset:2048 sc1\ns_waitcnt vmcnt(8)\n"
    "s_waitcnt lgkmcnt(0)\ns_barrier\ns_add_u32 m0, s64, 0\ns_add_u32 s52, s32, s47\ns_addc_u32 s53, s33, 0\nglobal_load_lds_dwordx4 v26, s[52:53] offset:0 sc1\n"
    "global_load_lds_dwordx4 v26, s[52:53] offset:1024 sc1\nglobal_load_lds_dwordx4 v26, s[52:53] offset:2048 sc1\nglobal_load_lds_dwordx4 v26, s[52:53] offset:3072 sc1\n"
    "s_waitcnt vmcnt(8)\ns_waitcnt lgkmcnt(0)\ns_barrier\ns_add_u32 m0, s64, 4096\ns_add_u32 s52, s32, s48\ns_addc_u32 s53, s33, 0\n"
    "global_load_lds_dwordx4 v26, s[52:53] offset:0 sc1\nglobal_load_lds_dwordx4 v26, s[52:53] offset:1024 sc1\nglobal_load_lds_dwordx4 v26, s[52:53] offset:2048 sc1\n"
    "global_load_lds_dwordx4 v26, s[52:53] offset:3072 sc1\ns_waitcnt vmcnt(8)\nds_read_b64_tr_b16 v[212:213], v29 offset:0\nds_read_b64_tr_b16 v[214:215], v29 offset:128\n"
    "ds_read_b64_tr_b16 v[216:217], v29 offset:1024\nds_read_b64_tr_b16 v[218:219], v29 offset:1152\nds_read_b64_tr_b16 v[220:221], v29 offset:2048\n"
    "ds_read_b64_tr_b16 v[222:223], v29 offset:2176\nds_read_b64_tr_b16 v[224:225], v29 offset:3072\nds_read_b64_tr_b16 v[226:227], v29 offset:3200\ns_waitcnt vmcnt(4)\n"
    "s_waitcnt lgkmcnt(0)\ns_barrier\ns_add_u32 m0, s64, 8192\ns_add_u32 s52, s32, s49\ns_addc_u32 s53, s33, 0\nglobal_load_lds_dwordx4 v26, s[52:53] offset:0 sc1\n"
    "global_load_lds_dwordx4 v26, s[52:53] offset:1024 sc1\nglobal_load_lds_dwordx4 v26, s[52:53] offset:2048 sc1\nglobal_load_lds_dwordx4 v26, s[52:53] offset:3072 sc1\n"
    "v_pk_maximum3_f16 v62, v212, v213, v214\nv_pk_maximum3_f16 v63, v215, v216, v217\nv_pk_maximum3_f16 v62, v62, v218, v219\nv_pk_maximum3_f16 v63, v63, v220, v221\n"
    "v_pk_maximum3_f16 v62, v62, v222, v223\nv_pk_maximum3_f16 v63, v63, v224, v225\nv_pk_maximum3_f16 v62, v62, v226, v227\nv_pk_maximum3_f16 v62, v62, v63, v63\n"
    "v_cmp_u_f16_sdwa vcc, v62, v62 src0_sel:WORD_0 src1_sel:WORD_1\ns_cbranch_vccnz .Lxs59\n.Lxr60:\ns_waitcnt vmcnt(4)\ns_waitcnt lgkmcnt(0)\ns_barrier\ns_add_u32 m0, s64, 0\n"
    "s_add_u32 s52, s32, s50\ns_addc_u32 s53, s33, 0\nglobal_load_lds_dwordx4 v26, s[52:53] offset:0 sc1\nglobal_load_lds_dwordx4 v26, s[52:53] offset:1024 sc1\n"
    "global_load_lds_dwordx4 v26, s[52:53] offset:2048 sc1\nglobal_load_lds_dwordx4 v26, s[52:53] offset:3072 sc1\nglobal_load_dword v228, v37, s[32:33] offset:0 sc1\n"
    "global_load_dword v229, v37, s[32:33] offset:256 sc1\nglobal_load_dword v230, v37, s[32:33] offset:512 sc1\nglobal_load_dword v231, v37, s[32:33] offset:768 sc1\n"
    "global_load_dword v232, v37, s[32:33] offset:1024 sc1\nglobal_load_dword v233, v37, s[32:33] offset:1280 sc1\nglobal_load_dword v234, v37, s[32:33] offset:1536 sc1\n"
    "global_load_dword v235, v37, s[32:33] offset:1792 sc1\nglobal_load_dword v236, v38, s[32:33] offset:0 sc1\nglobal_load_dword v237, v38, s[32:33] offset:128 sc1\n"
    "global_load_dword v238, v38, s[32:33] offset:256 sc1\nglobal_load_dword v239, v38, s[32:33] offset:384 sc1\nv_mfma_f32_16x16x32_bf16 v[46:49], v[64:67], v[212:215], 0\n"
    "v_mfma_f32_16x16x32_bf16 v[50:53], v[80:83], v[212:215], 0\nv_mfma_f32_16x16x32_bf16 v[54:57], v[96:99], v[212:215], 0\n"
    "v_mfma_f32_16x16x32_bf16 v[58:61], v[112:115], v[212:215], 0\nv_mfma_f32_16x16x32_bf16 v[46:49], v[68:71], v[216:219], v[46:49]\n"
    "v_mfma_f32_16x16x32_bf16 v[50:53], v[84:87], v[216:219], v[50:53]\nv_mfma_f32_16x16x32_bf16 v[54:57], v[100:103], v[216:219], v[54:57]\n"
    "v_mfma_f32_16x16x32_bf16 v[58:61], v[116:119], v[216:219], v[58:61]\ns_waitcnt vmcnt(16)\ns_waitcnt lgkmcnt(0)\ns_barrier\ns_add_u32 m0, s64, 4096\n"
    "s_add_u32 s52, s32, s51\ns_addc_u32 s53, s33, 0\nglobal_load_lds_dwordx4 v26, s[52:53] offset:0 sc1\nglobal_load_lds_dwordx4 v26, s[52:53] offset:1024 sc1\n"
    "global_load_lds_dwordx4 v26, s[52:53] offset:2048 sc1\nglobal_load_lds_dwordx4 v26, s[52:53] offset:3072 sc1\n"
    "v_mfma_f32_16x16x32_bf16 v[46:49], v[72:75], v[220:223], v[46:49]\nv_mfma_f32_16x16x32_bf16 v[50:53], v[88:91], v[220:223], v[50:53]\n"
    "v_mfma_f32_16x16x32_bf16 v[54:57], v[104:107], v[220:223], v[54:57]\nv_mfma_f32_16x16x32_bf16 v[58:61], v[120:123], v[220:223], v[58:61]\n"
    "v_mfma_f32_16x16x32_bf16 v[46:49], v[76:79], v[224:227], v[46:49]\nv_mfma_f32_16x16x32_bf16 v[50:53], v[92:95], v[224:227], v[50:53]\n"
    "v_mfma_f32_16x16x32_bf16 v[54:57], v[108:111], v[224:227], v[54:57]\nv_mfma_f32_16x16x32_bf16 v[58:61], v[124:127], v[224:227], v[58:61]\ns_waitcnt vmcnt(16)\n"
    "s_waitcnt lgkmcnt(0)\ns_barrier\ns_nop 2\nds_write_b128 v28, v[50:53] offset:0\nds_write_b128 v28, v[54:57] offset:1024\nds_write_b128 v28, v[58:61] offset:2048\n"
    "s_waitcnt vmcnt(0)\ns_waitcnt lgkmcnt(0)\ns_barrier\ns_mov_b32 s80, 0x10000\n.Lpl64:\nv_add_f32 v212, v228, v229\nv_add_f32 v213, v230, v231\nv_add_f32 v214, v232, v233\n"
    "v_add_f32 v215, v234, v235\nv_add_f32 v216, v236, v238\nv_add_f32 v217, v237, v239\nv_add_f32 v212, v212, v213\nv_add_f32 v214, v214, v215\n"
    "v_permlane16_swap_b32 v216, v217\nv_add_f32 v213, v212, v214\nv_add_f32 v212, v212, v214\nv_add_f32 v216, v216, v217\nv_cmp_u_f32 vcc, v212, v216\ns_cbranch_vccnz .Lps62\n"
    ".Lpr63:\ns_mov_b32 s86, 0x3b000000\nv_permlane32_swap_b32 v212, v213\nv_mul_f32 v212, s86, v212\nv_mul_f32 v214, v212, v212\nv_fma_f32 v213, v213, s86, -v214\n"
    "v_max_f32 v213, 0, v213\nv_cvt_pk_bf16_f32 v213, 0, v213\nv_add_f32 v213, 0x3727c5ac, v213\nv_cvt_pk_bf16_f32 v213, 0, v213\nv_sqrt_f32 v213, v213\ns_nop 0\n"
    "v_cvt_pk_bf16_f32 v213, 0, v213\nv_rcp_f32 v213, v213\nv_cvt_pk_bf16_f32 v212, -v212, -v212\ns_mov_b32 exec_lo, 0\nv_mov_b32 v212, 0\ns_mov_b64 exec, -1\n"
    "ds_write_b64 v39, v[212:213]\ns_waitcnt lgkmcnt(0)\ns_barrier\ns_setprio 0\nds_read_b128 v[196:199], v34\nds_read_b128 v[200:203], v35\nds_read_b128 v[204:207], v36\n"
    "ds_write_b32 v42, v43\nv_mov_b32 v217, v216\nglobal_load_dwordx4 v[64:67], v4, s[32:33] offset:-4096 sc1\nglobal_load_dwordx4 v[68:71], v4, s[32:33] offset:-3072 sc1\n"
    "global_load_dwordx4 v[72:75], v4, s[32:33] offset:-2048 sc1\nglobal_load_dwordx4 v[76:79], v4, s[32:33] offset:-1024 sc1\n"
    "global_load_dwordx4 v[80:83], v4, s[32:33] offset:0 sc1\nglobal_load_dwordx4 v[84:87], v4, s[32:33] offset:1024 sc1\n"
    "global_load_dwordx4 v[88:91], v4, s[32:33] offset:2048 sc1\nglobal_load_dwordx4 v[92:95], v4, s[32:33] offset:3072 sc1\nv_permlane32_swap_b32 v216, v217\n"
    "v_add_f32 v216, v216, v217\nv_mov_b32 v217, v216\ns_nop 1\nv_permlane16_swap_b32 v216, v217\nv_mul_f32 v216, s86, v216\nv_mul_f32 v214, v216, v216\n"
    "v_fma_f32 v217, v217, s86, -v214\nv_max_f32 v217, 0, v217\nv_cvt_pk_bf16_f32 v217, 0, v217\nv_add_f32 v217, 0x3727c5ac, v217\nv_cvt_pk_bf16_f32 v217, 0, v217\n"
    "v_sqrt_f32 v217, v217\ns_nop 0\nv_cvt_pk_bf16_f32 v217, 0, v217\nv_rcp_f32 v217, v217\nv_cvt_pk_bf16_f32 v216, 0, v216\ns_waitcnt lgkmcnt(0)\nv_add_f32 v46, v46, v196\n"
    "v_add_f32 v47, v47, v197\nv_add_f32 v48, v48, v198\nv_add_f32 v49, v49, v199\nv_add_f32 v200, v200, v204\nv_add_f32 v201, v201, v205\nv_add_f32 v202, v202, v206\n"
    "v_add_f32 v203, v203, v207\nv_add_f32 v46, v46, v200\nv_add_f32 v47, v47, v201\nv_add_f32 v48, v48, v202\nv_add_f32 v49, v49, v203\nv_fma_f32 v46, -v216, v248, v46\n"
    "v_fma_f32 v47, -v216, v249, v47\nv_fma_f32 v48, -v216, v250, v48\nv_fma_f32 v49, -v216, v251, v49\nv_mul_f32 v46, v46, v217\nv_mul_f32 v47, v47, v217\n"
    "v_mul_f32 v48, v48, v217\nv_mul_f32 v49, v49, v217\nv_cvt_pk_bf16_f32 v46, v46, v47\nv_cvt_pk_bf16_f32 v47, v48, v49\nds_write_b64 v41, v[46:47]\nv_mov_b32 v62, 0\n"
    "v_mov_b32 v63, 0\ns_branch .LHL12\n.LG1:\ns_waitcnt lgkmcnt(0)\ns_mov_b32 s86, 16777216\ns_load_dword s34, s[32:33], s86\ns_mov_b32 s6, 0\ns_mov_b32 s7, 0\n"
    "s_and_b32 s35, s2, 7\ns_lshr_b32 s36, s2, 3\ns_bfe_u32 s18, s2, 0x20001\ns_bfe_u32 s19, s2, 0x20003\ns_lshl2_add_u32 s37, s18, s19\ns_and_b32 s20, s2, 1\n"
    "s_lshr_b32 s21, s2, 5\ns_lshl3_add_u32 s38, s20, s21\ns_bfe_u32 s22, s2, 0x30003\ns_lshl3_add_u32 s39, s35, s22\ns_lshr_b32 s40, s2, 6\ns_lshl_b32 s23, s35, 5\n"
    "s_add_u32 s41, s23, s36\ns_lshl1_add_u32 s24, s19, s21\ns_and_b32 s42, s24, 7\ns_lshl_b32 s86, s41, 2\ns_load_dword s96, s[26:27], s86\ns_load_dword s95, s[28:29], s86\n"
    "v_mov_b32 v64, 0\nv_mov_b32 v65, 0xf149f2ca\nv_bfe_u32 v68, v0, 4, 2\nv_lshlrev_b32 v67, 2, v68\ns_add_u32 s19, s37, s3\ns_cmp_eq_u32 s19, 0\ns_cselect_b32 s18, 8, 0\n"
    "v_and_b32 v71, 7, v0\nv_add_u32 v70, 1, v71\nv_max_u32 v69, s18, v70\nv_sub_u32 v66, v67, v69\nv_add_u32 v73, 8, v71\nv_sub_u32 v72, v73, v69\nv_cmp_le_u32 vcc, v66, v72\n"
    "s_nop 1\nv_cndmask_b32 v2, v65, v64, vcc\nv_add_u32 v75, 1, v67\nv_sub_u32 v74, v75, v69\nv_cmp_le_u32 vcc, v74, v72\ns_nop 1\nv_cndmask_b32 v3, v65, v64, vcc\n"
    "v_add_u32 v77, 2, v67\nv_sub_u32 v76, v77, v69\nv_cmp_le_u32 vcc, v76, v72\ns_nop 1\nv_cndmask_b32 v4, v65, v64, vcc\nv_add_u32 v79, 3, v67\nv_sub_u32 v78, v79, v69\n"
    "v_cmp_le_u32 vcc, v78, v72\ns_nop 1\nv_cndmask_b32 v5, v65, v64, vcc\ns_mul_i32 s21, s3, 0xd00\ns_add_u32 s20, s21, 0x18040\nv_lshl_add_u32 v6, v68, 4, s20\n"
    "v_and_b32 v81, 15, v0\nv_max_u32 v80, 1, v81\ns_mov_b32 s22, 0x1a0\nv_mad_u32_u24 v6, v80, s22, v6\ns_add_u32 s23, s21, 0x18d00\nv_lshl_add_u32 v7, v68, 4, s23\n"
    "v_mad_u32_u24 v7, v71, s22, v7\nv_and_b32 v82, 3, v0\ns_add_u32 s24, s21, 0x18080\nv_lshl_add_u32 v8, v82, 3, s24\nv_bfe_u32 v85, v0, 2, 2\nv_add_u32 v84, v67, v85\n"
    "v_max_u32 v83, 1, v84\nv_mad_u32_u24 v8, v83, s22, v8\nv_bfe_u32 v86, v0, 3, 1\ns_lshr_b32 s47, s3, 1\ns_lshl2_add_u32 s46, s37, s47\ns_lshl_b32 s45, s46, 15\n"
    "s_lshl_b32 s48, s38, 11\ns_add_u32 s44, s45, s48\ns_and_b32 s50, s3, 1\ns_lshl_b32 s49, s50, 8\ns_add_u32 s25, s44, s49\nv_lshl_add_u32 v9, v86, 10, s25\n"
    "v_bfe_u32 v87, v0, 5, 1\nv_lshl_add_u32 v9, v87, 9, v9\nv_bfe_u32 v88, v0, 4, 1\nv_lshl_add_u32 v9, v88, 4, v9\nv_lshl_add_u32 v9, v71, 5, v9\nv_mov_b64 v[10:11], 0\n"
    "v_mov_b64 v[12:13], 0\nv_mov_b64 v[14:15], 0\nv_mov_b64 v[16:17], 0\nv_mov_b64 v[18:19], 0\nv_mov_b64 v[20:21], 0\nv_mov_b64 v[22:23], 0\nv_mov_b64 v[24:25], 0\n"
    "v_bfe_u32 v65, v0, 4, 1\nv_bfe_u32 v66, v0, 5, 1\nv_xor_b32 v64, v65, v66\nv_bfe_u32 v69, v0, 2, 2\nv_lshl_add_u32 v68, v66, 3, v69\nv_lshlrev_b32 v67, 6, v68\n"
    "v_lshl_add_u32 v26, v64, 5, v67\nv_and_b32 v70, 3, v0\nv_lshl_add_u32 v26, v70, 3, v26\ns_mul_i32 s19, s3, 0x1a00\ns_add_u32 s18, s19, 0x18d00\n"
    "v_lshl_add_u32 v27, v66, 3, s18\nv_and_b32 v71, 31, v0\ns_mov_b32 s20, 0xd0\nv_mad_u32_u24 v27, v71, s20, v27\ns_mov_b32 s21, 0x27c00\nv_lshl_add_u32 v28, v71, 2, s21\n"
    "s_mul_i32 s23, s3, 0x3000\ns_add_u32 s22, s23, 0x18000\nv_lshl_add_u32 v29, v71, 7, s22\nv_bfe_u32 v73, v0, 1, 3\nv_xor_b32 v72, v66, v73\nv_lshl_add_u32 v29, v72, 4, v29\n"
    "s_and_b32 s44, s41, 3\ns_lshr_b32 s47, s41, 2\ns_lshl_b32 s46, s47, 7\ns_add_u32 s45, s46, 0x1002000\ns_lshl3_add_u32 s25, s44, s45\ns_lshl2_add_u32 s24, s3, s25\n"
    "v_mov_b32 v30, s24\nv_and_b32 v74, 0x7f, v0\ns_mov_b32 s48, 0x23800\nv_lshl_add_u32 v31, v74, 3, s48\ns_mov_b32 s49, 0x23000\nv_lshl_add_u32 v32, v74, 4, s49\n"
    "v_and_b32 v75, 63, v0\ns_lshr_b32 s52, s3, 1\ns_lshl_b32 s51, s52, 8\ns_add_u32 s50, s51, 0x20000\nv_lshl_add_u32 v33, v75, 2, s50\ns_lshl_b32 s54, s41, 11\n"
    "s_add_u32 s53, s54, 0xe00000\nv_lshl_add_u32 v38, v0, 4, s53\ns_add_u32 s55, s23, 0x1a000\nv_add_u32 v39, s55, v26\ns_mul_i32 s57, s3, 0x2200\ns_add_u32 s56, s57, 0x1f400\n"
    "v_lshl_add_u32 v188, v75, 3, s56\ns_lshl_b32 s59, s39, 9\ns_add_u32 s58, s59, 0x1008000\nv_lshl_add_u32 v187, v70, 7, s58\nv_mov_b32 v191, 0x10000\n"
    "s_lshl_b32 s62, s41, 13\ns_lshl_b32 s63, s3, 11\ns_add_u32 s61, s62, s63\ns_add_u32 s60, s61, 0x200000\nv_lshl_add_u32 v192, v75, 4, s60\ns_lshl_b32 s66, s2, 8\n"
    "s_lshl_b32 s67, s3, 6\ns_add_u32 s65, s66, s67\ns_add_u32 s64, s65, 0x400000\nv_lshl_add_u32 v193, v70, 4, s64\ns_and_b32 s70, s2, 3\ns_lshr_b32 s73, s2, 2\n"
    "s_lshl_b32 s72, s73, 7\ns_add_u32 s71, s72, 0x1004000\ns_lshl4_add_u32 s69, s70, s71\ns_lshl2_add_u32 s68, s3, s69\nv_mov_b32 v194, s68\ns_add_u32 s74, s61, 0x600000\n"
    "v_lshl_add_u32 v200, v75, 4, s74\ns_lshl_b32 s77, s41, 10\ns_lshl_b32 s78, s3, 8\ns_add_u32 s76, s77, s78\ns_add_u32 s75, s76, 0xd00000\nv_lshl_add_u32 v201, v75, 4, s75\n"
    "v_and_b32 v76, 15, v0\ns_add_u32 s79, s63, 0x1004000\nv_lshl_add_u32 v202, v76, 7, s79\nv_bfe_u32 v77, v0, 4, 2\nv_lshl_add_u32 v202, v77, 4, v202\nv_mov_b32 v196, -1\n"
    "v_mov_b32 v197, -1\nv_mov_b32 v198, -1\nv_mov_b32 v199, -1\ns_lshl_b32 s19, s39, 15\ns_add_u32 s18, s19, 0x1000\nv_lshl_add_u32 v182, v0, 4, s18\n"
    "s_add_u32 s20, s18, 0x2000\nv_lshl_add_u32 v183, v0, 4, s20\ns_add_u32 s21, s18, 0x4000\nv_lshl_add_u32 v184, v0, 4, s21\ns_add_u32 s22, s18, 0x6000\n"
    "v_lshl_add_u32 v185, v0, 4, s22\ns_mov_b32 s23, 0x1f600\nv_lshl_add_u32 v186, v0, 4, s23\nv_bfe_u32 v64, v0, 4, 2\ns_mul_i32 s25, s3, 0x1760\ns_add_u32 s24, s25, 0x18000\n"
    "v_lshl_add_u32 v189, v64, 4, s24\nv_and_b32 v65, 15, v0\ns_mov_b32 s44, 0x220\nv_mad_u32_u24 v189, v65, s44, v189\ns_lshr_b32 s47, s3, 1\ns_mul_i32 s46, s47, 0x4400\n"
    "s_add_u32 s45, s46, 0x1f400\nv_lshl_add_u32 v190, v64, 4, s45\nv_mad_u32_u24 v190, v65, s44, v190\ns_cmp_eq_u32 s3, 3\ns_cbranch_scc0 .LL67\nv_mov_b32 v64, 0\n"
    "v_lshlrev_b32 v65, 2, v0\nv_add_u32 v65, 162048, v65\ns_mov_b32 exec_hi, 0\nds_write_b32 v65, v64\ns_mov_b64 exec, 15\nv_mov_b32 v64, 0x3f803f80\ns_mov_b32 exec_lo, -1\n"
    "s_mov_b32 exec_hi, 15\nds_write_b32 v65, v64 offset:880\ns_mov_b64 exec, -1\n.LL67:\ns_and_b32 s54, s42, 7\ns_lshl_b32 s44, s54, 12\ns_add_u32 s56, s42, 1\n"
    "s_and_b32 s55, s56, 7\ns_lshl_b32 s45, s55, 12\ns_add_u32 s58, s42, 2\ns_and_b32 s57, s58, 7\ns_lshl_b32 s46, s57, 12\ns_add_u32 s60, s42, 3\ns_and_b32 s59, s60, 7\n"
    "s_lshl_b32 s47, s59, 12\ns_add_u32 s62, s42, 4\ns_and_b32 s61, s62, 7\ns_lshl_b32 s48, s61, 12\ns_add_u32 s64, s42, 5\ns_and_b32 s63, s64, 7\ns_lshl_b32 s49, s63, 12\n"
    "s_add_u32 s66, s42, 6\ns_and_b32 s65, s66, 7\ns_lshl_b32 s50, s65, 12\ns_add_u32 s68, s42, 7\ns_and_b32 s67, s68, 7\ns_lshl_b32 s51, s67, 12\ns_mov_b32 s81, 0x3b000000\n"
    "s_mov_b32 s84, -1\ns_mov_b32 s85, 0\nv_mov_b32 v143, 0x3f803f80\nv_add_u32 v116, s44, v26\nv_add_u32 v118, 0x10000, v116\nv_mov_b32 v112, 0\nv_mov_b32 v113, 0\n"
    "v_mov_b32 v114, 0\nv_mov_b32 v115, 0\nv_mov_b32 v121, 0\nv_mov_b32 v122, 0\nv_mov_b32 v123, 0\nv_mov_b32 v125, 0\nv_mov_b32 v126, 0\nv_mov_b32 v127, 0\nv_mov_b32 v129, 0\n"
    "v_mov_b32 v130, 0\nv_mov_b32 v131, 0\nv_mov_b32 v133, 0\nv_mov_b32 v134, 0\nv_mov_b32 v135, 0\nv_xor_b32 v176, 32, v29\nv_xor_b32 v177, 64, v29\nv_xor_b32 v178, 0x60, v29\n"
    "s_waitcnt lgkmcnt(0)\ns_lshl_b32 s97, s96, 16\ns_and_b32 s96, s96, 0xffff0000\ns_cmp_lt_u32 s3, 2\ns_cselect_b32 s96, s97, s96\ns_mov_b32 s97, 0x3fb8aa3b\n"
    "v_mov_b32 v66, s96\nv_mul_f32 v67, s97, v66\nv_fma_f32 v68, s97, v66, -v67\nv_exp_f32 v67, v67\nv_fmac_f32 v68, 0x32a57060, v66\nv_mul_f32 v68, 0x3f317218, v68\n"
    "v_fmac_f32 v67, v67, v68\nv_cvt_pk_bf16_f32 v67, 0, v67\nv_mul_f32 v66, s97, v67\nv_mul_f32 v68, 0x3e22f983, v67\nv_readfirstlane_b32 s92, v67\n"
    "v_readfirstlane_b32 s93, v66\nv_readfirstlane_b32 s94, v68\ns_and_b32 s97, s95, 0xffff0000\ns_lshl_b32 s96, s95, 16\ns_add_u32 s34, s34, 1\nv_mov_b32 v180, s34\n"
    "s_cmp_lt_u32 s3, 2\ns_cbranch_scc0 .LL66\ns_waitcnt vmcnt(0)\nds_read_b64_tr_b16 v[244:245], v39\nds_read_b64_tr_b16 v[246:247], v39 offset:256\ns_waitcnt lgkmcnt(0)\n"
    "global_store_dwordx4 v38, v[244:247], s[32:33] sc1\n.LL66:\ns_barrier\nds_read_b128 v[144:147], v29 offset:0\nds_read_b128 v[148:151], v176 offset:0\n"
    "ds_read_b128 v[152:155], v177 offset:0\nds_read_b128 v[156:159], v178 offset:0\nds_read_b64_tr_b16 v[44:45], v116 offset:0\nds_read_b64_tr_b16 v[46:47], v116 offset:256\n"
    "ds_read_b64_tr_b16 v[48:49], v116 offset:1024\nds_read_b64_tr_b16 v[50:51], v116 offset:1280\nds_read_b64_tr_b16 v[52:53], v116 offset:2048\n"
    "ds_read_b64_tr_b16 v[54:55], v116 offset:2304\nds_read_b64_tr_b16 v[56:57], v116 offset:3072\nds_read_b64_tr_b16 v[58:59], v116 offset:3328\n"
    "ds_read_b64_tr_b16 v[60:61], v116 offset:32768\nds_read_b64_tr_b16 v[62:63], v116 offset:33024\ns_waitcnt lgkmcnt(13)\nv_dot2c_f32_bf16 v112, v143, v144\n"
    "v_dot2c_f32_bf16 v113, v144, v144\nv_dot2c_f32_bf16 v114, v143, v145\nv_dot2c_f32_bf16 v115, v145, v145\nv_dot2c_f32_bf16 v112, v143, v146\n"
    "v_dot2c_f32_bf16 v113, v146, v146\nv_dot2c_f32_bf16 v114, v143, v147\nv_dot2c_f32_bf16 v115, v147, v147\ns_waitcnt lgkmcnt(12)\nv_dot2c_f32_bf16 v112, v143, v148\n"
    "v_dot2c_f32_bf16 v113, v148, v148\nv_dot2c_f32_bf16 v114, v143, v149\nv_dot2c_f32_bf16 v115, v149, v149\nv_dot2c_f32_bf16 v112, v143, v150\n"
    "v_dot2c_f32_bf16 v113, v150, v150\nv_dot2c_f32_bf16 v114, v143, v151\nv_dot2c_f32_bf16 v115, v151, v151\ns_waitcnt lgkmcnt(11)\nv_dot2c_f32_bf16 v112, v143, v152\n"
    "v_dot2c_f32_bf16 v113, v152, v152\nv_dot2c_f32_bf16 v114, v143, v153\nv_dot2c_f32_bf16 v115, v153, v153\nv_dot2c_f32_bf16 v112, v143, v154\n"
    "v_dot2c_f32_bf16 v113, v154, v154\nv_dot2c_f32_bf16 v114, v143, v155\nv_dot2c_f32_bf16 v115, v155, v155\ns_waitcnt lgkmcnt(10)\nv_dot2c_f32_bf16 v112, v143, v156\n"
    "v_dot2c_f32_bf16 v113, v156, v156\nv_dot2c_f32_bf16 v114, v143, v157\nv_dot2c_f32_bf16 v115, v157, v157\nv_dot2c_f32_bf16 v112, v143, v158\n"
    "v_dot2c_f32_bf16 v113, v158, v158\nv_dot2c_f32_bf16 v114, v143, v159\nv_dot2c_f32_bf16 v115, v159, v159\ns_waitcnt lgkmcnt(8)\n"
    "v_mfma_f32_32x32x16_bf16 v[64:79], v[44:47], v[144:147], 0\nds_read_b64_tr_b16 v[44:45], v116 offset:33792\nds_read_b64_tr_b16 v[46:47], v116 offset:34048\n"
    "v_add_u32 v117, s45, v26\nv_add_u32 v119, 0x10000, v117\ns_waitcnt lgkmcnt(8)\nv_mfma_f32_32x32x16_bf16 v[64:79], v[48:51], v[148:151], v[64:79]\n"
    "ds_read_b64_tr_b16 v[48:49], v116 offset:34816\nds_read_b64_tr_b16 v[50:51], v116 offset:35072\ns_waitcnt lgkmcnt(8)\n"
    "v_mfma_f32_32x32x16_bf16 v[64:79], v[52:55], v[152:155], v[64:79]\nds_read_b64_tr_b16 v[52:53], v116 offset:35840\nds_read_b64_tr_b16 v[54:55], v116 offset:36096\n"
    "s_waitcnt lgkmcnt(8)\nv_mfma_f32_32x32x16_bf16 v[64:79], v[56:59], v[156:159], v[64:79]\nds_read_b64_tr_b16 v[56:57], v118 offset:0\n"
    "ds_read_b64_tr_b16 v[58:59], v118 offset:256\ns_waitcnt lgkmcnt(8)\nv_mfma_f32_32x32x16_bf16 v[80:95], v[60:63], v[144:147], 0\n"
    "ds_read_b64_tr_b16 v[60:61], v118 offset:1024\nds_read_b64_tr_b16 v[62:63], v118 offset:1280\ns_waitcnt lgkmcnt(8)\n"
    "v_mfma_f32_32x32x16_bf16 v[80:95], v[44:47], v[148:151], v[80:95]\nds_read_b64_tr_b16 v[44:45], v118 offset:2048\nds_read_b64_tr_b16 v[46:47], v118 offset:2304\n"
    "s_waitcnt lgkmcnt(8)\nv_mfma_f32_32x32x16_bf16 v[80:95], v[48:51], v[152:155], v[80:95]\nds_read_b64_tr_b16 v[48:49], v118 offset:3072\n"
    "ds_read_b64_tr_b16 v[50:51], v118 offset:3328\ns_barrier\ns_waitcnt lgkmcnt(8)\nv_mfma_f32_32x32x16_bf16 v[80:95], v[52:55], v[156:159], v[80:95]\n"
    "ds_read_b128 v[160:163], v29 offset:4096\nds_read_b64_tr_b16 v[52:53], v117 offset:0\nds_read_b64_tr_b16 v[54:55], v117 offset:256\ns_waitcnt lgkmcnt(9)\n"
    "v_mfma_f32_32x32x16_bf16 v[96:111], v[56:59], v[144:147], 0\nds_read_b128 v[164:167], v176 offset:4096\nds_read_b64_tr_b16 v[56:57], v117 offset:1024\n"
    "ds_read_b64_tr_b16 v[58:59], v117 offset:1280\ns_waitcnt lgkmcnt(10)\nv_mfma_f32_32x32x16_bf16 v[96:111], v[60:63], v[148:151], v[96:111]\n"
    "ds_read_b128 v[168:171], v177 offset:4096\nds_read_b64_tr_b16 v[60:61], v117 offset:2048\nds_read_b64_tr_b16 v[62:63], v117 offset:2304\ns_waitcnt lgkmcnt(11)\n"
    "v_mfma_f32_32x32x16_bf16 v[96:111], v[44:47], v[152:155], v[96:111]\nds_read_b128 v[172:175], v178 offset:4096\nds_read_b64_tr_b16 v[44:45], v117 offset:3072\n"
    "ds_read_b64_tr_b16 v[46:47], v117 offset:3328\ns_waitcnt lgkmcnt(12)\nv_mfma_f32_32x32x16_bf16 v[96:111], v[48:51], v[156:159], v[96:111]\n"
    "ds_read_b64_tr_b16 v[48:49], v117 offset:32768\nds_read_b64_tr_b16 v[50:51], v117 offset:33024\ns_waitcnt lgkmcnt(13)\nv_dot2c_f32_bf16 v112, v143, v160\n"
    "v_dot2c_f32_bf16 v113, v160, v160\nv_dot2c_f32_bf16 v114, v143, v161\nv_dot2c_f32_bf16 v115, v161, v161\nv_dot2c_f32_bf16 v112, v143, v162\n"
    "v_dot2c_f32_bf16 v113, v162, v162\nv_dot2c_f32_bf16 v114, v143, v163\nv_dot2c_f32_bf16 v115, v163, v163\ns_waitcnt lgkmcnt(10)\nv_dot2c_f32_bf16 v112, v143, v164\n"
    "v_dot2c_f32_bf16 v113, v164, v164\nv_dot2c_f32_bf16 v114, v143, v165\nv_dot2c_f32_bf16 v115, v165, v165\nv_dot2c_f32_bf16 v112, v143, v166\n"
    "v_dot2c_f32_bf16 v113, v166, v166\nv_dot2c_f32_bf16 v114, v143, v167\nv_dot2c_f32_bf16 v115, v167, v167\ns_waitcnt lgkmcnt(7)\nv_dot2c_f32_bf16 v112, v143, v168\n"
    "v_dot2c_f32_bf16 v113, v168, v168\nv_dot2c_f32_bf16 v114, v143, v169\nv_dot2c_f32_bf16 v115, v169, v169\nv_dot2c_f32_bf16 v112, v143, v170\n"
    "v_dot2c_f32_bf16 v113, v170, v170\nv_dot2c_f32_bf16 v114, v143, v171\nv_dot2c_f32_bf16 v115, v171, v171\ns_waitcnt lgkmcnt(4)\nv_dot2c_f32_bf16 v112, v143, v172\n"
    "v_dot2c_f32_bf16 v113, v172, v172\nv_dot2c_f32_bf16 v114, v143, v173\nv_dot2c_f32_bf16 v115, v173, v173\nv_dot2c_f32_bf16 v112, v143, v174\n"
    "v_dot2c_f32_bf16 v113, v174, v174\nv_dot2c_f32_bf16 v114, v143, v175\nv_dot2c_f32_bf16 v115, v175, v175\nv_mfma_f32_32x32x16_bf16 v[64:79], v[52:55], v[160:163], v[64:79]\n"
    "ds_read_b64_tr_b16 v[52:53], v117 offset:33792\nds_read_b64_tr_b16 v[54:55], v117 offset:34048\nv_add_u32 v116, s46, v26\nv_add_u32 v118, 0x10000, v116\n"
    "v_mfma_f32_32x32x16_bf16 v[64:79], v[56:59], v[164:167], v[64:79]\nds_read_b64_tr_b16 v[56:57], v117 offset:34816\nds_read_b64_tr_b16 v[58:59], v117 offset:35072\n"
    "v_mfma_f32_32x32x16_bf16 v[64:79], v[60:63], v[168:171], v[64:79]\nds_read_b64_tr_b16 v[60:61], v117 offset:35840\nds_read_b64_tr_b16 v[62:63], v117 offset:36096\n"
    "s_waitcnt lgkmcnt(8)\nv_mfma_f32_32x32x16_bf16 v[64:79], v[44:47], v[172:175], v[64:79]\nds_read_b64_tr_b16 v[44:45], v119 offset:0\n"
    "ds_read_b64_tr_b16 v[46:47], v119 offset:256\ns_waitcnt lgkmcnt(8)\nv_mfma_f32_32x32x16_bf16 v[80:95], v[48:51], v[160:163], v[80:95]\n"
    "ds_read_b64_tr_b16 v[48:49], v119 offset:1024\nds_read_b64_tr_b16 v[50:51], v119 offset:1280\ns_waitcnt lgkmcnt(8)\n"
    "v_mfma_f32_32x32x16_bf16 v[80:95], v[52:55], v[164:167], v[80:95]\nds_read_b64_tr_b16 v[52:53], v119 offset:2048\nds_read_b64_tr_b16 v[54:55], v119 offset:2304\n"
    "s_waitcnt lgkmcnt(8)\nv_mfma_f32_32x32x16_bf16 v[80:95], v[56:59], v[168:171], v[80:95]\nds_read_b64_tr_b16 v[56:57], v119 offset:3072\n"
    "ds_read_b64_tr_b16 v[58:59], v119 offset:3328\ns_barrier\ns_waitcnt lgkmcnt(8)\nv_mfma_f32_32x32x16_bf16 v[80:95], v[60:63], v[172:175], v[80:95]\n"
    "ds_read_b128 v[144:147], v29 offset:8192\nds_read_b64_tr_b16 v[60:61], v116 offset:0\nds_read_b64_tr_b16 v[62:63], v116 offset:256\ns_waitcnt lgkmcnt(9)\n"
    "v_mfma_f32_32x32x16_bf16 v[96:111], v[44:47], v[160:163], v[96:111]\nds_read_b128 v[148:151], v176 offset:8192\nds_read_b64_tr_b16 v[44:45], v116 offset:1024\n"
    "ds_read_b64_tr_b16 v[46:47], v116 offset:1280\ns_waitcnt lgkmcnt(10)\nv_mfma_f32_32x32x16_bf16 v[96:111], v[48:51], v[164:167], v[96:111]\n"
    "ds_read_b128 v[152:155], v177 offset:8192\nds_read_b64_tr_b16 v[48:49], v116 offset:2048\nds_read_b64_tr_b16 v[50:51], v116 offset:2304\ns_waitcnt lgkmcnt(11)\n"
    "v_mfma_f32_32x32x16_bf16 v[96:111], v[52:55], v[168:171], v[96:111]\nds_read_b128 v[156:159], v178 offset:8192\nds_read_b64_tr_b16 v[52:53], v116 offset:3072\n"
    "ds_read_b64_tr_b16 v[54:55], v116 offset:3328\ns_waitcnt lgkmcnt(12)\nv_mfma_f32_32x32x16_bf16 v[96:111], v[56:59], v[172:175], v[96:111]\n"
    "ds_read_b64_tr_b16 v[56:57], v116 offset:32768\nds_read_b64_tr_b16 v[58:59], v116 offset:33024\ns_waitcnt lgkmcnt(13)\nv_dot2c_f32_bf16 v112, v143, v144\n"
    "v_dot2c_f32_bf16 v113, v144, v144\nv_dot2c_f32_bf16 v114, v143, v145\nv_dot2c_f32_bf16 v115, v145, v145\nv_dot2c_f32_bf16 v112, v143, v146\n"
    "v_dot2c_f32_bf16 v113, v146, v146\nv_dot2c_f32_bf16 v114, v143, v147\nv_dot2c_f32_bf16 v115, v147, v147\ns_waitcnt lgkmcnt(10)\nv_dot2c_f32_bf16 v112, v143, v148\n"
    "v_dot2c_f32_bf16 v113, v148, v148\nv_dot2c_f32_bf16 v114, v143, v149\nv_dot2c_f32_bf16 v115, v149, v149\nv_dot2c_f32_bf16 v112, v143, v150\n"
    "v_dot2c_f32_bf16 v113, v150, v150\nv_dot2c_f32_bf16 v114, v143, v151\nv_dot2c_f32_bf16 v115, v151, v151\ns_waitcnt lgkmcnt(7)\nv_dot2c_f32_bf16 v112, v143, v152\n"
    "v_dot2c_f32_bf16 v113, v152, v152\nv_dot2c_f32_bf16 v114, v143, v153\nv_dot2c_f32_bf16 v115, v153, v153\nv_dot2c_f32_bf16 v112, v143, v154\n"
    "v_dot2c_f32_bf16 v113, v154, v154\nv_dot2c_f32_bf16 v114, v143, v155\nv_dot2c_f32_bf16 v115, v155, v155\ns_waitcnt lgkmcnt(4)\nv_dot2c_f32_bf16 v112, v143, v156\n"
    "v_dot2c_f32_bf16 v113, v156, v156\nv_dot2c_f32_bf16 v114, v143, v157\nv_dot2c_f32_bf16 v115, v157, v157\nv_dot2c_f32_bf16 v112, v143, v158\n"
    "v_dot2c_f32_bf16 v113, v158, v158\nv_dot2c_f32_bf16 v114, v143, v159\nv_dot2c_f32_bf16 v115, v159, v159\nv_mfma_f32_32x32x16_bf16 v[64:79], v[60:63], v[144:147], v[64:79]\n"
    "ds_read_b64_tr_b16 v[60:61], v116 offset:33792\nds_read_b64_tr_b16 v[62:63], v116 offset:34048\nv_add_u32 v117, s47, v26\nv_add_u32 v119, 0x10000, v117\n"
    "v_mfma_f32_32x32x16_bf16 v[64:79], v[44:47], v[148:151], v[64:79]\nds_read_b64_tr_b16 v[44:45], v116 offset:34816\nds_read_b64_tr_b16 v[46:47], v116 offset:35072\n"
    "v_mfma_f32_32x32x16_bf16 v[64:79], v[48:51], v[152:155], v[64:79]\nds_read_b64_tr_b16 v[48:49], v116 offset:35840\nds_read_b64_tr_b16 v[50:51], v116 offset:36096\n"
    "s_waitcnt lgkmcnt(8)\nv_mfma_f32_32x32x16_bf16 v[64:79], v[52:55], v[156:159], v[64:79]\nds_read_b64_tr_b16 v[52:53], v118 offset:0\n"
    "ds_read_b64_tr_b16 v[54:55], v118 offset:256\ns_waitcnt lgkmcnt(8)\nv_mfma_f32_32x32x16_bf16 v[80:95], v[56:59], v[144:147], v[80:95]\n"
    "ds_read_b64_tr_b16 v[56:57], v118 offset:1024\nds_read_b64_tr_b16 v[58:59], v118 offset:1280\ns_waitcnt lgkmcnt(8)\n"
    "v_mfma_f32_32x32x16_bf16 v[80:95], v[60:63], v[148:151], v[80:95]\nds_read_b64_tr_b16 v[60:61], v118 offset:2048\nds_read_b64_tr_b16 v[62:63], v118 offset:2304\n"
    "s_waitcnt lgkmcnt(8)\nv_mfma_f32_32x32x16_bf16 v[80:95], v[44:47], v[152:155], v[80:95]\nds_read_b64_tr_b16 v[44:45], v118 offset:3072\n"
    "ds_read_b64_tr_b16 v[46:47], v118 offset:3328\ns_barrier\ns_waitcnt lgkmcnt(8)\nv_mfma_f32_32x32x16_bf16 v[80:95], v[48:51], v[156:159], v[80:95]\n"
    "ds_read_b128 v[160:163], v29 offset:0\nds_read_b64_tr_b16 v[48:49], v117 offset:0\nds_read_b64_tr_b16 v[50:51], v117 offset:256\ns_waitcnt lgkmcnt(9)\n"
    "v_mfma_f32_32x32x16_bf16 v[96:111], v[52:55], v[144:147], v[96:111]\nds_read_b128 v[164:167], v176 offset:0\nds_read_b64_tr_b16 v[52:53], v117 offset:1024\n"
    "ds_read_b64_tr_b16 v[54:55], v117 offset:1280\ns_waitcnt lgkmcnt(10)\nv_mfma_f32_32x32x16_bf16 v[96:111], v[56:59], v[148:151], v[96:111]\n"
    "ds_read_b128 v[168:171], v177 offset:0\nds_read_b64_tr_b16 v[56:57], v117 offset:2048\nds_read_b64_tr_b16 v[58:59], v117 offset:2304\ns_waitcnt lgkmcnt(11)\n"
    "v_mfma_f32_32x32x16_bf16 v[96:111], v[60:63], v[152:155], v[96:111]\nds_read_b128 v[172:175], v178 offset:0\nds_read_b64_tr_b16 v[60:61], v117 offset:3072\n"
    "ds_read_b64_tr_b16 v[62:63], v117 offset:3328\ns_waitcnt lgkmcnt(12)\nv_mfma_f32_32x32x16_bf16 v[96:111], v[44:47], v[156:159], v[96:111]\n"
    "ds_read_b64_tr_b16 v[44:45], v117 offset:32768\nds_read_b64_tr_b16 v[46:47], v117 offset:33024\ns_waitcnt lgkmcnt(13)\nv_dot2c_f32_bf16 v112, v143, v160\n"
    "v_dot2c_f32_bf16 v113, v160, v160\nv_dot2c_f32_bf16 v114, v143, v161\nv_dot2c_f32_bf16 v115, v161, v161\nv_dot2c_f32_bf16 v112, v143, v162\n"
    "v_dot2c_f32_bf16 v113, v162, v162\nv_dot2c_f32_bf16 v114, v143, v163\nv_dot2c_f32_bf16 v115, v163, v163\ns_waitcnt lgkmcnt(10)\nv_dot2c_f32_bf16 v112, v143, v164\n"
    "v_dot2c_f32_bf16 v113, v164, v164\nv_dot2c_f32_bf16 v114, v143, v165\nv_dot2c_f32_bf16 v115, v165, v165\nv_dot2c_f32_bf16 v112, v143, v166\n"
    "v_dot2c_f32_bf16 v113, v166, v166\nv_dot2c_f32_bf16 v114, v143, v167\nv_dot2c_f32_bf16 v115, v167, v167\ns_waitcnt lgkmcnt(7)\nv_dot2c_f32_bf16 v112, v143, v168\n"
    "v_dot2c_f32_bf16 v113, v168, v168\nv_dot2c_f32_bf16 v114, v143, v169\nv_dot2c_f32_bf16 v115, v169, v169\nv_dot2c_f32_bf16 v112, v143, v170\n"
    "v_dot2c_f32_bf16 v113, v170, v170\nv_dot2c_f32_bf16 v114, v143, v171\nv_dot2c_f32_bf16 v115, v171, v171\ns_waitcnt lgkmcnt(4)\nv_dot2c_f32_bf16 v112, v143, v172\n"
    "v_dot2c_f32_bf16 v113, v172, v172\nv_dot2c_f32_bf16 v114, v143, v173\nv_dot2c_f32_bf16 v115, v173, v173\nv_dot2c_f32_bf16 v112, v143, v174\n"
    "v_dot2c_f32_bf16 v113, v174, v174\nv_dot2c_f32_bf16 v114, v143, v175\nv_dot2c_f32_bf16 v115, v175, v175\nv_mfma_f32_32x32x16_bf16 v[64:79], v[48:51], v[160:163], v[64:79]\n"
    "ds_read_b64_tr_b16 v[48:49], v117 offset:33792\nds_read_b64_tr_b16 v[50:51], v117 offset:34048\nv_add_u32 v116, s48, v26\nv_add_u32 v118, 0x10000, v116\n"
    "v_mfma_f32_32x32x16_bf16 v[64:79], v[52:55], v[164:167], v[64:79]\nds_read_b64_tr_b16 v[52:53], v117 offset:34816\nds_read_b64_tr_b16 v[54:55], v117 offset:35072\n"
    "s_cmp_lt_u32 s3, 2\ns_cbranch_scc0 .LL65\ns_waitcnt vmcnt(0)\ns_mov_b64 exec, 1\nglobal_store_dword v30, v180, s[32:33] sc1\ns_mov_b64 exec, -1\n.LL65:\n"
    "v_mfma_f32_32x32x16_bf16 v[64:79], v[56:59], v[168:171], v[64:79]\nds_read_b64_tr_b16 v[56:57], v117 offset:35840\nds_read_b64_tr_b16 v[58:59], v117 offset:36096\n"
    "s_waitcnt lgkmcnt(8)\nv_mfma_f32_32x32x16_bf16 v[64:79], v[60:63], v[172:175], v[64:79]\nds_read_b64_tr_b16 v[60:61], v119 offset:0\n"
    "ds_read_b64_tr_b16 v[62:63], v119 offset:256\ns_waitcnt lgkmcnt(8)\nv_mfma_f32_32x32x16_bf16 v[80:95], v[44:47], v[160:163], v[80:95]\n"
    "ds_read_b64_tr_b16 v[44:45], v119 offset:1024\nds_read_b64_tr_b16 v[46:47], v119 offset:1280\ns_waitcnt lgkmcnt(8)\n"
    "v_mfma_f32_32x32x16_bf16 v[80:95], v[48:51], v[164:167], v[80:95]\nds_read_b64_tr_b16 v[48:49], v119 offset:2048\nds_read_b64_tr_b16 v[50:51], v119 offset:2304\n"
    "s_waitcnt lgkmcnt(8)\nv_mfma_f32_32x32x16_bf16 v[80:95], v[52:55], v[168:171], v[80:95]\nds_read_b64_tr_b16 v[52:53], v119 offset:3072\n"
    "ds_read_b64_tr_b16 v[54:55], v119 offset:3328\ns_barrier\ns_waitcnt lgkmcnt(8)\nv_mfma_f32_32x32x16_bf16 v[80:95], v[56:59], v[172:175], v[80:95]\n"
    "ds_read_b128 v[144:147], v29 offset:4096\nds_read_b64_tr_b16 v[56:57], v116 offset:0\nds_read_b64_tr_b16 v[58:59], v116 offset:256\ns_waitcnt lgkmcnt(9)\n"
    "v_mfma_f32_32x32x16_bf16 v[96:111], v[60:63], v[160:163], v[96:111]\nds_read_b128 v[148:151], v176 offset:4096\nds_read_b64_tr_b16 v[60:61], v116 offset:1024\n"
    "ds_read_b64_tr_b16 v[62:63], v116 offset:1280\ns_waitcnt lgkmcnt(10)\nv_mfma_f32_32x32x16_bf16 v[96:111], v[44:47], v[164:167], v[96:111]\n"
    "ds_read_b128 v[152:155], v177 offset:4096\nds_read_b64_tr_b16 v[44:45], v116 offset:2048\nds_read_b64_tr_b16 v[46:47], v116 offset:2304\ns_waitcnt lgkmcnt(11)\n"
    "v_mfma_f32_32x32x16_bf16 v[96:111], v[48:51], v[168:171], v[96:111]\nds_read_b128 v[156:159], v178 offset:4096\nds_read_b64_tr_b16 v[48:49], v116 offset:3072\n"
    "ds_read_b64_tr_b16 v[50:51], v116 offset:3328\ns_waitcnt lgkmcnt(12)\nv_mfma_f32_32x32x16_bf16 v[96:111], v[52:55], v[172:175], v[96:111]\n"
    "ds_read_b64_tr_b16 v[52:53], v116 offset:32768\nds_read_b64_tr_b16 v[54:55], v116 offset:33024\ns_waitcnt lgkmcnt(13)\nv_dot2c_f32_bf16 v112, v143, v144\n"
    "v_dot2c_f32_bf16 v113, v144, v144\nv_dot2c_f32_bf16 v114, v143, v145\nv_dot2c_f32_bf16 v115, v145, v145\nv_dot2c_f32_bf16 v112, v143, v146\n"
    "v_dot2c_f32_bf16 v113, v146, v146\nv_dot2c_f32_bf16 v114, v143, v147\nv_dot2c_f32_bf16 v115, v147, v147\ns_waitcnt lgkmcnt(10)\nv_dot2c_f32_bf16 v112, v143, v148\n"
    "v_dot2c_f32_bf16 v113, v148, v148\nv_dot2c_f32_bf16 v114, v143, v149\nv_dot2c_f32_bf16 v115, v149, v149\nv_dot2c_f32_bf16 v112, v143, v150\n"
    "v_dot2c_f32_bf16 v113, v150, v150\nv_dot2c_f32_bf16 v114, v143, v151\nv_dot2c_f32_bf16 v115, v151, v151\ns_waitcnt lgkmcnt(7)\nv_dot2c_f32_bf16 v112, v143, v152\n"
    "v_dot2c_f32_bf16 v113, v152, v152\nv_dot2c_f32_bf16 v114, v143, v153\nv_dot2c_f32_bf16 v115, v153, v153\nv_dot2c_f32_bf16 v112, v143, v154\n"
    "v_dot2c_f32_bf16 v113, v154, v154\nv_dot2c_f32_bf16 v114, v143, v155\nv_dot2c_f32_bf16 v115, v155, v155\ns_waitcnt lgkmcnt(4)\nv_dot2c_f32_bf16 v112, v143, v156\n"
    "v_dot2c_f32_bf16 v113, v156, v156\nv_dot2c_f32_bf16 v114, v143, v157\nv_dot2c_f32_bf16 v115, v157, v157\nv_dot2c_f32_bf16 v112, v143, v158\n"
    "v_dot2c_f32_bf16 v113, v158, v158\nv_dot2c_f32_bf16 v114, v143, v159\nv_dot2c_f32_bf16 v115, v159, v159\nv_mfma_f32_32x32x16_bf16 v[64:79], v[56:59], v[144:147], v[64:79]\n"
    "ds_read_b64_tr_b16 v[56:57], v116 offset:33792\nds_read_b64_tr_b16 v[58:59], v116 offset:34048\nv_add_u32 v117, s49, v26\nv_add_u32 v119, 0x10000, v117\n"
    "v_mfma_f32_32x32x16_bf16 v[64:79], v[60:63], v[148:151], v[64:79]\nds_read_b64_tr_b16 v[60:61], v116 offset:34816\nds_read_b64_tr_b16 v[62:63], v116 offset:35072\n"
    "v_mfma_f32_32x32x16_bf16 v[64:79], v[44:47], v[152:155], v[64:79]\nds_read_b64_tr_b16 v[44:45], v116 offset:35840\nds_read_b64_tr_b16 v[46:47], v116 offset:36096\n"
    "s_waitcnt lgkmcnt(8)\nv_mfma_f32_32x32x16_bf16 v[64:79], v[48:51], v[156:159], v[64:79]\nds_read_b64_tr_b16 v[48:49], v118 offset:0\n"
    "ds_read_b64_tr_b16 v[50:51], v118 offset:256\ns_waitcnt lgkmcnt(8)\nv_mfma_f32_32x32x16_bf16 v[80:95], v[52:55], v[144:147], v[80:95]\n"
    "ds_read_b64_tr_b16 v[52:53], v118 offset:1024\nds_read_b64_tr_b16 v[54:55], v118 offset:1280\ns_waitcnt lgkmcnt(8)\n"
    "v_mfma_f32_32x32x16_bf16 v[80:95], v[56:59], v[148:151], v[80:95]\nds_read_b64_tr_b16 v[56:57], v118 offset:2048\nds_read_b64_tr_b16 v[58:59], v118 offset:2304\n"
    "s_waitcnt lgkmcnt(8)\nv_mfma_f32_32x32x16_bf16 v[80:95], v[60:63], v[152:155], v[80:95]\nds_read_b64_tr_b16 v[60:61], v118 offset:3072\n"
    "ds_read_b64_tr_b16 v[62:63], v118 offset:3328\ns_barrier\ns_waitcnt lgkmcnt(8)\nv_mfma_f32_32x32x16_bf16 v[80:95], v[44:47], v[156:159], v[80:95]\n"
    "ds_read_b128 v[160:163], v29 offset:8192\nds_read_b64_tr_b16 v[44:45], v117 offset:0\nds_read_b64_tr_b16 v[46:47], v117 offset:256\ns_waitcnt lgkmcnt(9)\n"
    "v_mfma_f32_32x32x16_bf16 v[96:111], v[48:51], v[144:147], v[96:111]\nds_read_b128 v[164:167], v176 offset:8192\nds_read_b64_tr_b16 v[48:49], v117 offset:1024\n"
    "ds_read_b64_tr_b16 v[50:51], v117 offset:1280\ns_waitcnt lgkmcnt(10)\nv_mfma_f32_32x32x16_bf16 v[96:111], v[52:55], v[148:151], v[96:111]\n"
    "ds_read_b128 v[168:171], v177 offset:8192\nds_read_b64_tr_b16 v[52:53], v117 offset:2048\nds_read_b64_tr_b16 v[54:55], v117 offset:2304\ns_waitcnt lgkmcnt(11)\n"
    "v_mfma_f32_32x32x16_bf16 v[96:111], v[56:59], v[152:155], v[96:111]\nds_read_b128 v[172:175], v178 offset:8192\nds_read_b64_tr_b16 v[56:57], v117 offset:3072\n"
    "ds_read_b64_tr_b16 v[58:59], v117 offset:3328\ns_waitcnt lgkmcnt(12)\nv_mfma_f32_32x32x16_bf16 v[96:111], v[60:63], v[156:159], v[96:111]\n"
    "ds_read_b64_tr_b16 v[60:61], v117 offset:32768\nds_read_b64_tr_b16 v[62:63], v117 offset:33024\ns_waitcnt lgkmcnt(13)\nv_dot2c_f32_bf16 v112, v143, v160\n"
    "v_dot2c_f32_bf16 v113, v160, v160\nv_dot2c_f32_bf16 v114, v143, v161\nv_dot2c_f32_bf16 v115, v161, v161\nv_dot2c_f32_bf16 v112, v143, v162\n"
    "v_dot2c_f32_bf16 v113, v162, v162\nv_dot2c_f32_bf16 v114, v143, v163\nv_dot2c_f32_bf16 v115, v163, v163\ns_waitcnt lgkmcnt(10)\nv_dot2c_f32_bf16 v112, v143, v164\n"
    "v_dot2c_f32_bf16 v113, v164, v164\nv_dot2c_f32_bf16 v114, v143, v165\nv_dot2c_f32_bf16 v115, v165, v165\nv_dot2c_f32_bf16 v112, v143, v166\n"
    "v_dot2c_f32_bf16 v113, v166, v166\nv_dot2c_f32_bf16 v114, v143, v167\nv_dot2c_f32_bf16 v115, v167, v167\ns_waitcnt lgkmcnt(7)\nv_dot2c_f32_bf16 v112, v143, v168\n"
    "v_dot2c_f32_bf16 v113, v168, v168\nv_dot2c_f32_bf16 v114, v143, v169\nv_dot2c_f32_bf16 v115, v169, v169\nv_dot2c_f32_bf16 v112, v143, v170\n"
    "v_dot2c_f32_bf16 v113, v170, v170\nv_dot2c_f32_bf16 v114, v143, v171\nv_dot2c_f32_bf16 v115, v171, v171\ns_waitcnt lgkmcnt(4)\nv_dot2c_f32_bf16 v112, v143, v172\n"
    "v_dot2c_f32_bf16 v113, v172, v172\nv_dot2c_f32_bf16 v114, v143, v173\nv_dot2c_f32_bf16 v115, v173, v173\nv_dot2c_f32_bf16 v112, v143, v174\n"
    "v_dot2c_f32_bf16 v113, v174, v174\nv_dot2c_f32_bf16 v114, v143, v175\nv_dot2c_f32_bf16 v115, v175, v175\nv_mfma_f32_32x32x16_bf16 v[64:79], v[44:47], v[160:163], v[64:79]\n"
    "ds_read_b64_tr_b16 v[44:45], v117 offset:33792\nds_read_b64_tr_b16 v[46:47], v117 offset:34048\nv_add_u32 v116, s50, v26\nv_add_u32 v118, 0x10000, v116\n"
    "v_mfma_f32_32x32x16_bf16 v[64:79], v[48:51], v[164:167], v[64:79]\nds_read_b64_tr_b16 v[48:49], v117 offset:34816\nds_read_b64_tr_b16 v[50:51], v117 offset:35072\n"
    "v_mfma_f32_32x32x16_bf16 v[64:79], v[52:55], v[168:171], v[64:79]\nds_read_b64_tr_b16 v[52:53], v117 offset:35840\nds_read_b64_tr_b16 v[54:55], v117 offset:36096\n"
    "s_waitcnt lgkmcnt(8)\nv_mfma_f32_32x32x16_bf16 v[64:79], v[56:59], v[172:175], v[64:79]\nds_read_b64_tr_b16 v[56:57], v119 offset:0\n"
    "ds_read_b64_tr_b16 v[58:59], v119 offset:256\ns_waitcnt lgkmcnt(8)\nv_mfma_f32_32x32x16_bf16 v[80:95], v[60:63], v[160:163], v[80:95]\n"
    "ds_read_b64_tr_b16 v[60:61], v119 offset:1024\nds_read_b64_tr_b16 v[62:63], v119 offset:1280\ns_waitcnt lgkmcnt(8)\n"
    "v_mfma_f32_32x32x16_bf16 v[80:95], v[44:47], v[164:167], v[80:95]\nds_read_b64_tr_b16 v[44:45], v119 offset:2048\nds_read_b64_tr_b16 v[46:47], v119 offset:2304\n"
    "s_waitcnt lgkmcnt(8)\nv_mfma_f32_32x32x16_bf16 v[80:95], v[48:51], v[168:171], v[80:95]\nds_read_b64_tr_b16 v[48:49], v119 offset:3072\n"
    "ds_read_b64_tr_b16 v[50:51], v119 offset:3328\ns_barrier\ns_waitcnt lgkmcnt(8)\nv_mfma_f32_32x32x16_bf16 v[80:95], v[52:55], v[172:175], v[80:95]\n"
    "ds_read_b128 v[144:147], v29 offset:0\nds_read_b64_tr_b16 v[52:53], v116 offset:0\nds_read_b64_tr_b16 v[54:55], v116 offset:256\ns_waitcnt lgkmcnt(9)\n"
    "v_mfma_f32_32x32x16_bf16 v[96:111], v[56:59], v[160:163], v[96:111]\nds_read_b128 v[148:151], v176 offset:0\nds_read_b64_tr_b16 v[56:57], v116 offset:1024\n"
    "ds_read_b64_tr_b16 v[58:59], v116 offset:1280\ns_waitcnt lgkmcnt(10)\nv_mfma_f32_32x32x16_bf16 v[96:111], v[60:63], v[164:167], v[96:111]\n"
    "ds_read_b128 v[152:155], v177 offset:0\nds_read_b64_tr_b16 v[60:61], v116 offset:2048\nds_read_b64_tr_b16 v[62:63], v116 offset:2304\ns_waitcnt lgkmcnt(11)\n"
    "v_mfma_f32_32x32x16_bf16 v[96:111], v[44:47], v[168:171], v[96:111]\nds_read_b128 v[156:159], v178 offset:0\nds_read_b64_tr_b16 v[44:45], v116 offset:3072\n"
    "ds_read_b64_tr_b16 v[46:47], v116 offset:3328\ns_waitcnt lgkmcnt(12)\nv_mfma_f32_32x32x16_bf16 v[96:111], v[48:51], v[172:175], v[96:111]\n"
    "ds_read_b64_tr_b16 v[48:49], v116 offset:32768\nds_read_b64_tr_b16 v[50:51], v116 offset:33024\ns_waitcnt lgkmcnt(13)\nv_dot2c_f32_bf16 v112, v143, v144\n"
    "v_dot2c_f32_bf16 v113, v144, v144\nv_dot2c_f32_bf16 v114, v143, v145\nv_dot2c_f32_bf16 v115, v145, v145\nv_dot2c_f32_bf16 v112, v143, v146\n"
    "v_dot2c_f32_bf16 v113, v146, v146\nv_dot2c_f32_bf16 v114, v143, v147\nv_dot2c_f32_bf16 v115, v147, v147\ns_waitcnt lgkmcnt(10)\nv_dot2c_f32_bf16 v112, v143, v148\n"
    "v_dot2c_f32_bf16 v113, v148, v148\nv_dot2c_f32_bf16 v114, v143, v149\nv_dot2c_f32_bf16 v115, v149, v149\nv_dot2c_f32_bf16 v112, v143, v150\n"
    "v_dot2c_f32_bf16 v113, v150, v150\nv_dot2c_f32_bf16 v114, v143, v151\nv_dot2c_f32_bf16 v115, v151, v151\ns_waitcnt lgkmcnt(7)\nv_dot2c_f32_bf16 v112, v143, v152\n"
    "v_dot2c_f32_bf16 v113, v152, v152\nv_dot2c_f32_bf16 v114, v143, v153\nv_dot2c_f32_bf16 v115, v153, v153\nv_dot2c_f32_bf16 v112, v143, v154\n"
    "v_dot2c_f32_bf16 v113, v154, v154\nv_dot2c_f32_bf16 v114, v143, v155\nv_dot2c_f32_bf16 v115, v155, v155\ns_waitcnt lgkmcnt(4)\nv_dot2c_f32_bf16 v112, v143, v156\n"
    "v_dot2c_f32_bf16 v113, v156, v156\nv_dot2c_f32_bf16 v114, v143, v157\nv_dot2c_f32_bf16 v115, v157, v157\nv_dot2c_f32_bf16 v112, v143, v158\n"
    "v_dot2c_f32_bf16 v113, v158, v158\nv_dot2c_f32_bf16 v114, v143, v159\nv_dot2c_f32_bf16 v115, v159, v159\nv_mfma_f32_32x32x16_bf16 v[64:79], v[52:55], v[144:147], v[64:79]\n"
    "ds_read_b64_tr_b16 v[52:53], v116 offset:33792\nds_read_b64_tr_b16 v[54:55], v116 offset:34048\nv_add_u32 v117, s51, v26\nv_add_u32 v119, 0x10000, v117\n"
    "v_mfma_f32_32x32x16_bf16 v[64:79], v[56:59], v[148:151], v[64:79]\nds_read_b64_tr_b16 v[56:57], v116 offset:34816\nds_read_b64_tr_b16 v[58:59], v116 offset:35072\n"
    "v_mfma_f32_32x32x16_bf16 v[64:79], v[60:63], v[152:155], v[64:79]\nds_read_b64_tr_b16 v[60:61], v116 offset:35840\nds_read_b64_tr_b16 v[62:63], v116 offset:36096\n"
    "s_waitcnt lgkmcnt(8)\nv_mfma_f32_32x32x16_bf16 v[64:79], v[44:47], v[156:159], v[64:79]\nds_read_b64_tr_b16 v[44:45], v118 offset:0\n"
    "ds_read_b64_tr_b16 v[46:47], v118 offset:256\ns_waitcnt lgkmcnt(8)\nv_mfma_f32_32x32x16_bf16 v[80:95], v[48:51], v[144:147], v[80:95]\n"
    "ds_read_b64_tr_b16 v[48:49], v118 offset:1024\nds_read_b64_tr_b16 v[50:51], v118 offset:1280\ns_waitcnt lgkmcnt(8)\n"
    "v_mfma_f32_32x32x16_bf16 v[80:95], v[52:55], v[148:151], v[80:95]\nds_read_b64_tr_b16 v[52:53], v118 offset:2048\nds_read_b64_tr_b16 v[54:55], v118 offset:2304\n"
    "s_waitcnt lgkmcnt(8)\nv_mfma_f32_32x32x16_bf16 v[80:95], v[56:59], v[152:155], v[80:95]\nds_read_b64_tr_b16 v[56:57], v118 offset:3072\n"
    "ds_read_b64_tr_b16 v[58:59], v118 offset:3328\ns_waitcnt lgkmcnt(8)\nv_mfma_f32_32x32x16_bf16 v[80:95], v[60:63], v[156:159], v[80:95]\ns_waitcnt lgkmcnt(6)\n"
    "v_mfma_f32_32x32x16_bf16 v[96:111], v[44:47], v[144:147], v[96:111]\ns_waitcnt lgkmcnt(4)\nv_mfma_f32_32x32x16_bf16 v[96:111], v[48:51], v[148:151], v[96:111]\n"
    "s_waitcnt lgkmcnt(2)\nv_mfma_f32_32x32x16_bf16 v[96:111], v[52:55], v[152:155], v[96:111]\ns_waitcnt lgkmcnt(0)\n"
    "v_mfma_f32_32x32x16_bf16 v[96:111], v[56:59], v[156:159], v[96:111]\ns_barrier\nds_read_b128 v[160:163], v29 offset:4096\nds_read_b128 v[164:167], v176 offset:4096\n"
    "ds_read_b128 v[168:171], v177 offset:4096\nds_read_b128 v[172:175], v178 offset:4096\nds_read_b64_tr_b16 v[60:61], v117 offset:0\n"
    "ds_read_b64_tr_b16 v[62:63], v117 offset:256\nds_read_b64_tr_b16 v[44:45], v117 offset:1024\nds_read_b64_tr_b16 v[46:47], v117 offset:1280\n"
    "ds_read_b64_tr_b16 v[48:49], v117 offset:2048\nds_read_b64_tr_b16 v[50:51], v117 offset:2304\nds_read_b64_tr_b16 v[52:53], v117 offset:3072\n"
    "ds_read_b64_tr_b16 v[54:55], v117 offset:3328\nds_read_b64_tr_b16 v[56:57], v117 offset:32768\nds_read_b64_tr_b16 v[58:59], v117 offset:33024\ns_waitcnt lgkmcnt(13)\n"
    "v_dot2c_f32_bf16 v112, v143, v160\nv_dot2c_f32_bf16 v113, v160, v160\nv_dot2c_f32_bf16 v114, v143, v161\nv_dot2c_f32_bf16 v115, v161, v161\n"
    "v_dot2c_f32_bf16 v112, v143, v162\nv_dot2c_f32_bf16 v113, v162, v162\nv_dot2c_f32_bf16 v114, v143, v163\nv_dot2c_f32_bf16 v115, v163, v163\ns_waitcnt lgkmcnt(12)\n"
    "v_dot2c_f32_bf16 v112, v143, v164\nv_dot2c_f32_bf16 v113, v164, v164\nv_dot2c_f32_bf16 v114, v143, v165\nv_dot2c_f32_bf16 v115, v165, v165\n"
    "v_dot2c_f32_bf16 v112, v143, v166\nv_dot2c_f32_bf16 v113, v166, v166\nv_dot2c_f32_bf16 v114, v143, v167\nv_dot2c_f32_bf16 v115, v167, v167\ns_waitcnt lgkmcnt(11)\n"
    "v_dot2c_f32_bf16 v112, v143, v168\nv_dot2c_f32_bf16 v113, v168, v168\nv_dot2c_f32_bf16 v114, v143, v169\nv_dot2c_f32_bf16 v115, v169, v169\n"
    "v_dot2c_f32_bf16 v112, v143, v170\nv_dot2c_f32_bf16 v113, v170, v170\nv_dot2c_f32_bf16 v114, v143, v171\nv_dot2c_f32_bf16 v115, v171, v171\ns_waitcnt lgkmcnt(10)\n"
    "v_dot2c_f32_bf16 v112, v143, v172\nv_dot2c_f32_bf16 v113, v172, v172\nv_dot2c_f32_bf16 v114, v143, v173\nv_dot2c_f32_bf16 v115, v173, v173\n"
    "v_dot2c_f32_bf16 v112, v143, v174\nv_dot2c_f32_bf16 v113, v174, v174\nv_dot2c_f32_bf16 v114, v143, v175\nv_dot2c_f32_bf16 v115, v175, v175\ns_waitcnt lgkmcnt(8)\n"
    "v_mfma_f32_32x32x16_bf16 v[64:79], v[60:63], v[160:163], v[64:79]\nds_read_b64_tr_b16 v[60:61], v117 offset:33792\nds_read_b64_tr_b16 v[62:63], v117 offset:34048\n"
    "v_add_f32 v112, v112, v114\nv_add_f32 v113, v113, v115\ns_waitcnt lgkmcnt(8)\nv_mfma_f32_32x32x16_bf16 v[64:79], v[44:47], v[164:167], v[64:79]\n"
    "ds_read_b64_tr_b16 v[44:45], v117 offset:34816\nds_read_b64_tr_b16 v[46:47], v117 offset:35072\nv_permlane32_swap_b32 v112, v113\nv_add_f32 v138, v112, v113\n"
    "v_add_f32 v137, v112, v113\ns_waitcnt lgkmcnt(8)\nv_mfma_f32_32x32x16_bf16 v[64:79], v[48:51], v[168:171], v[64:79]\nds_read_b64_tr_b16 v[48:49], v117 offset:35840\n"
    "ds_read_b64_tr_b16 v[50:51], v117 offset:36096\nv_permlane32_swap_b32 v138, v137\nv_mul_f32 v138, s81, v138\nv_mul_f32 v112, v138, v138\ns_waitcnt lgkmcnt(8)\n"
    "v_mfma_f32_32x32x16_bf16 v[64:79], v[52:55], v[172:175], v[64:79]\nds_read_b64_tr_b16 v[52:53], v119 offset:0\nds_read_b64_tr_b16 v[54:55], v119 offset:256\n"
    "v_fma_f32 v137, v137, s81, -v112\nv_max_f32 v137, 0, v137\nv_cvt_pk_bf16_f32 v137, 0, v137\ns_waitcnt lgkmcnt(8)\n"
    "v_mfma_f32_32x32x16_bf16 v[80:95], v[56:59], v[160:163], v[80:95]\nds_read_b64_tr_b16 v[56:57], v119 offset:1024\nds_read_b64_tr_b16 v[58:59], v119 offset:1280\n"
    "v_add_f32 v137, 0x3727c5ac, v137\nv_cvt_pk_bf16_f32 v137, 0, v137\ns_waitcnt lgkmcnt(8)\nv_mfma_f32_32x32x16_bf16 v[80:95], v[60:63], v[164:167], v[80:95]\n"
    "ds_read_b64_tr_b16 v[60:61], v119 offset:2048\nds_read_b64_tr_b16 v[62:63], v119 offset:2304\nv_sqrt_f32 v137, v137\nv_cvt_pk_bf16_f32 v138, -v138, -v138\ns_barrier\n"
    "ds_read_b32 v124, v28 offset:0\nds_read_b32 v128, v28 offset:128\nds_read_b32 v132, v28 offset:256\ns_waitcnt lgkmcnt(11)\n"
    "v_mfma_f32_32x32x16_bf16 v[80:95], v[44:47], v[168:171], v[80:95]\nds_read_b64_tr_b16 v[44:45], v119 offset:3072\nds_read_b64_tr_b16 v[46:47], v119 offset:3328\n"
    "v_cndmask_b32 v120, 0, v138, s[84:85]\nv_cvt_pk_bf16_f32 v137, 0, v137\nv_rcp_f32 v137, v137\ns_waitcnt lgkmcnt(11)\n"
    "v_mfma_f32_32x32x16_bf16 v[80:95], v[48:51], v[172:175], v[80:95]\ns_waitcnt lgkmcnt(0)\nv_cvt_pk_bf16_f32 v140, 0, v124\nv_cvt_pk_bf16_f32 v141, 0, v128\n"
    "v_cvt_pk_bf16_f32 v142, 0, v132\nv_mfma_f32_32x32x16_bf16 v[96:111], v[52:55], v[160:163], v[96:111]\nv_sub_f32 v124, v124, v140\nv_sub_f32 v128, v128, v141\n"
    "v_sub_f32 v132, v132, v142\nv_cvt_pk_bf16_f32 v124, v140, v124\nv_cvt_pk_bf16_f32 v128, v141, v128\nv_cvt_pk_bf16_f32 v132, v142, v132\n"
    "v_mfma_f32_32x32x16_bf16 v[64:79], v[124:127], v[120:123], v[64:79]\nv_mfma_f32_32x32x16_bf16 v[80:95], v[128:131], v[120:123], v[80:95]\n"
    "v_mfma_f32_32x32x16_bf16 v[96:111], v[56:59], v[164:167], v[96:111]\ns_nop 9\nv_mul_f32 v64, v64, v137\nv_mul_f32 v65, v65, v137\nv_mul_f32 v66, v66, v137\n"
    "v_mul_f32 v67, v67, v137\nv_cvt_pk_bf16_f32 v64, v64, v65\nv_cvt_pk_bf16_f32 v65, v66, v67\nds_write_b64 v27, v[64:65] offset:0\n"
    "v_mfma_f32_32x32x16_bf16 v[96:111], v[60:63], v[168:171], v[96:111]\nv_mul_f32 v68, v68, v137\nv_mul_f32 v69, v69, v137\nv_mul_f32 v70, v70, v137\n"
    "v_mul_f32 v71, v71, v137\nv_cvt_pk_bf16_f32 v68, v68, v69\nv_cvt_pk_bf16_f32 v69, v70, v71\nds_write_b64 v27, v[68:69] offset:16\n"
    "v_mfma_f32_32x32x16_bf16 v[96:111], v[44:47], v[172:175], v[96:111]\nv_mul_f32 v72, v72, v137\nv_mul_f32 v73, v73, v137\nv_mul_f32 v74, v74, v137\n"
    "v_mul_f32 v75, v75, v137\nv_cvt_pk_bf16_f32 v72, v72, v73\nv_cvt_pk_bf16_f32 v73, v74, v75\nds_write_b64 v27, v[72:73] offset:32\n"
    "v_mfma_f32_32x32x16_bf16 v[96:111], v[132:135], v[120:123], v[96:111]\nv_mul_f32 v76, v76, v137\nv_mul_f32 v77, v77, v137\nv_mul_f32 v78, v78, v137\n"
    "v_mul_f32 v79, v79, v137\nv_cvt_pk_bf16_f32 v76, v76, v77\nv_cvt_pk_bf16_f32 v77, v78, v79\nds_write_b64 v27, v[76:77] offset:48\n"
    "v_pk_mul_f32 v[80:81], v[80:81], v[136:137] op_sel:[0,1]\nv_pk_mul_f32 v[82:83], v[82:83], v[136:137] op_sel:[0,1]\nv_cvt_pk_bf16_f32 v80, v80, v81\n"
    "v_cvt_pk_bf16_f32 v81, v82, v83\nds_write_b64 v27, v[80:81] offset:64\nv_pk_mul_f32 v[84:85], v[84:85], v[136:137] op_sel:[0,1]\n"
    "v_pk_mul_f32 v[86:87], v[86:87], v[136:137] op_sel:[0,1]\nv_cvt_pk_bf16_f32 v84, v84, v85\nv_cvt_pk_bf16_f32 v85, v86, v87\nds_write_b64 v27, v[84:85] offset:80\n"
    "v_pk_mul_f32 v[88:89], v[88:89], v[136:137] op_sel:[0,1]\nv_pk_mul_f32 v[90:91], v[90:91], v[136:137] op_sel:[0,1]\nv_cvt_pk_bf16_f32 v88, v88, v89\n"
    "v_cvt_pk_bf16_f32 v89, v90, v91\nds_write_b64 v27, v[88:89] offset:96\nv_pk_mul_f32 v[92:93], v[92:93], v[136:137] op_sel:[0,1]\n"
    "v_pk_mul_f32 v[94:95], v[94:95], v[136:137] op_sel:[0,1]\nv_cvt_pk_bf16_f32 v92, v92, v93\nv_cvt_pk_bf16_f32 v93, v94, v95\nds_write_b64 v27, v[92:93] offset:112\n"
    "v_pk_mul_f32 v[96:97], v[96:97], v[136:137] op_sel:[0,1]\nv_pk_mul_f32 v[98:99], v[98:99], v[136:137] op_sel:[0,1]\nv_cvt_pk_bf16_f32 v96, v96, v97\n"
    "v_cvt_pk_bf16_f32 v97, v98, v99\nds_write_b64 v27, v[96:97] offset:128\nv_pk_mul_f32 v[100:101], v[100:101], v[136:137] op_sel:[0,1]\n"
    "v_pk_mul_f32 v[102:103], v[102:103], v[136:137] op_sel:[0,1]\nv_cvt_pk_bf16_f32 v100, v100, v101\nv_cvt_pk_bf16_f32 v101, v102, v103\n"
    "ds_write_b64 v27, v[100:101] offset:144\nv_pk_mul_f32 v[104:105], v[104:105], v[136:137] op_sel:[0,1]\nv_pk_mul_f32 v[106:107], v[106:107], v[136:137] op_sel:[0,1]\n"
    "v_cvt_pk_bf16_f32 v104, v104, v105\nv_cvt_pk_bf16_f32 v105, v106, v107\nds_write_b64 v27, v[104:105] offset:160\n"
    "v_pk_mul_f32 v[108:109], v[108:109], v[136:137] op_sel:[0,1]\nv_pk_mul_f32 v[110:111], v[110:111], v[136:137] op_sel:[0,1]\nv_cvt_pk_bf16_f32 v108, v108, v109\n"
    "v_cvt_pk_bf16_f32 v109, v110, v111\nds_write_b64 v27, v[108:109] offset:176\n.LGL68:\ns_waitcnt lgkmcnt(0)\ns_barrier\nds_read_b128 v[64:67], v6\n"
    "ds_read_b128 v[68:71], v6 offset:208\ns_mov_b32 exec_lo, 0x00ff00ff\ns_mov_b32 exec_hi, 0xff00ff00\nds_read_b128 v[10:13], v7\nds_read_b128 v[14:17], v7 offset:208\n"
    "s_mov_b64 exec, -1\nds_read_b64_tr_b16 v[72:73], v8 offset:0\nds_read_b64_tr_b16 v[74:75], v8 offset:32\nds_read_b64_tr_b16 v[76:77], v8 offset:208\n"
    "ds_read_b64_tr_b16 v[78:79], v8 offset:240\ns_mov_b32 s81, 0x3eb8aa3b\ns_waitcnt lgkmcnt(5)\nv_mfma_f32_16x16x32_bf16 v[80:83], v[64:67], v[10:13], v[2:5]\n"
    "s_waitcnt lgkmcnt(4)\nv_mfma_f32_16x16x32_bf16 v[84:87], v[68:71], v[14:17], v[2:5]\ns_nop 5\nv_cvt_pk_bf16_f32 v80, 0, v80\nv_cvt_pk_bf16_f32 v81, 0, v81\n"
    "v_cvt_pk_bf16_f32 v82, 0, v82\nv_cvt_pk_bf16_f32 v83, 0, v83\nv_cvt_pk_bf16_f32 v84, 0, v84\nv_cvt_pk_bf16_f32 v85, 0, v85\nv_cvt_pk_bf16_f32 v86, 0, v86\n"
    "v_cvt_pk_bf16_f32 v87, 0, v87\nv_max_f32 v88, v80, v81\nv_max_f32 v89, v84, v85\nv_max3_f32 v88, v88, v82, v83\nv_max3_f32 v89, v89, v86, v87\ns_nop 1\n"
    "v_permlane16_swap_b32 v88, v89\nv_max_f32 v90, v88, v89\nv_max_f32 v89, v88, v89\ns_nop 1\nv_permlane32_swap_b32 v90, v89\nv_max_f32 v90, v90, v89\n"
    "v_mul_f32 v88, 0xbeb8aa3b, v90\nv_mul_f32 v89, 0xbeb8aa3b, v90\ns_nop 1\nv_permlane16_swap_b32 v88, v89\nv_fma_f32 v80, v80, s81, v88\nv_fma_f32 v81, v81, s81, v88\n"
    "v_fma_f32 v82, v82, s81, v88\nv_fma_f32 v83, v83, s81, v88\nv_fma_f32 v84, v84, s81, v89\nv_fma_f32 v85, v85, s81, v89\nv_fma_f32 v86, v86, s81, v89\n"
    "v_fma_f32 v87, v87, s81, v89\nv_exp_f32 v80, v80\nv_exp_f32 v81, v81\nv_exp_f32 v82, v82\nv_exp_f32 v83, v83\nv_exp_f32 v84, v84\nv_exp_f32 v85, v85\nv_exp_f32 v86, v86\n"
    "v_exp_f32 v87, v87\nv_add_f32 v88, v80, v81\nv_add_f32 v91, v82, v83\nv_add_f32 v89, v84, v85\nv_add_f32 v92, v86, v87\nv_add_f32 v88, v88, v91\nv_add_f32 v89, v89, v92\n"
    "s_nop 1\nv_permlane16_swap_b32 v88, v89\nv_add_f32 v90, v88, v89\nv_add_f32 v89, v88, v89\ns_nop 1\nv_permlane32_swap_b32 v90, v89\nv_add_f32 v90, v90, v89\n"
    "v_rcp_f32 v88, v90\nv_rcp_f32 v89, v90\ns_nop 1\nv_permlane16_swap_b32 v88, v89\nv_mul_f32 v80, v80, v88\nv_mul_f32 v81, v81, v88\nv_mul_f32 v82, v82, v88\n"
    "v_mul_f32 v83, v83, v88\nv_mul_f32 v84, v84, v89\nv_mul_f32 v85, v85, v89\nv_mul_f32 v86, v86, v89\nv_mul_f32 v87, v87, v89\ns_mov_b32 exec_lo, 0x00ff00ff\n"
    "s_mov_b32 exec_hi, 0x00ff00ff\nv_cvt_pk_bf16_f32 v18, v80, v81\nv_cvt_pk_bf16_f32 v19, v82, v83\nv_cvt_pk_bf16_f32 v22, v84, v85\nv_cvt_pk_bf16_f32 v23, v86, v87\n"
    "s_not_b64 exec, exec\nv_cvt_pk_bf16_f32 v20, v80, v81\nv_cvt_pk_bf16_f32 v21, v82, v83\nv_cvt_pk_bf16_f32 v24, v84, v85\nv_cvt_pk_bf16_f32 v25, v86, v87\n"
    "s_mov_b64 exec, -1\ns_waitcnt lgkmcnt(0)\nv_mfma_f32_16x16x32_bf16 v[64:67], v[72:75], v[18:21], 0\nv_mfma_f32_16x16x32_bf16 v[68:71], v[76:79], v[22:25], 0\ns_nop 6\n"
    "v_cvt_pk_bf16_f32 v80, v64, v65\nv_cvt_pk_bf16_f32 v81, v66, v67\nv_cvt_pk_bf16_f32 v82, v68, v69\nv_cvt_pk_bf16_f32 v83, v70, v71\nv_min_u32 v80, -2, v80\n"
    "v_min_u32 v82, -2, v82\nv_min_u32 v81, -2, v81\nv_min_u32 v83, -2, v83\nv_permlane16_swap_b32 v80, v82\ns_nop 0\nv_permlane16_swap_b32 v81, v83\n"
    "global_store_dwordx4 v9, v[80:83], s[32:33] sc1\ns_cmp_eq_u32 s6, 0\ns_cbranch_scc1 .LL70\ns_lshl_b32 s15, s39, 15\ns_add_u32 s14, s15, 0x801000\n"
    "v_lshl_add_u32 v182, v0, 4, s14\ns_add_u32 s16, s15, 0x803000\nv_lshl_add_u32 v183, v0, 4, s16\ns_add_u32 s17, s15, 0x805000\nv_lshl_add_u32 v184, v0, 4, s17\n"
    "s_add_u32 s18, s15, 0x807000\nv_lshl_add_u32 v185, v0, 4, s18\ns_mov_b32 s19, 0x1f600\nv_lshl_add_u32 v186, v0, 4, s19\ns_barrier\ns_waitcnt vmcnt(0)\ns_sleep 18\n"
    "global_load_dwordx4 v[208:211], v182, s[32:33] offset:-4096 sc1\nglobal_load_dwordx4 v[212:215], v182, s[32:33] offset:0 sc1\n"
    "global_load_dwordx4 v[216:219], v183, s[32:33] offset:-4096 sc1\nglobal_load_dwordx4 v[220:223], v183, s[32:33] offset:0 sc1\n"
    "global_load_dwordx4 v[224:227], v184, s[32:33] offset:-4096 sc1\nglobal_load_dwordx4 v[228:231], v184, s[32:33] offset:0 sc1\n"
    "global_load_dwordx4 v[232:235], v185, s[32:33] offset:-4096 sc1\nglobal_load_dwordx4 v[236:239], v185, s[32:33] offset:0 sc1\ns_branch .LL71\n.LL70:\n"
    "ds_read_b32 v242, v33\nds_read_b32 v243, v33 offset:512\nds_read_b64 v[244:245], v31\nds_read_b128 v[246:249], v32\nds_read_b64 v[250:251], v32 offset:16\n"
    "global_load_dwordx4 v[22:25], v187, s[32:33] sc1\ns_bitcmp1_b32 s3, 0\ns_cselect_b32 s82, 0x43000000, 0\ns_mov_b32 s86, 0x41000000\ns_waitcnt lgkmcnt(0)\ns_barrier\n"
    "v_lshlrev_b32 v112, 16, v242\nv_and_b32 v113, 0xffff0000, v242\nv_lshlrev_b32 v114, 16, v243\nv_and_b32 v115, 0xffff0000, v243\nv_mul_f32 v112, s93, v112\n"
    "v_mul_f32 v113, s93, v113\nv_mul_f32 v114, s94, v114\nv_mul_f32 v115, s94, v115\nv_mul_f32 v132, s82, v112\nv_mul_f32 v133, s82, v113\nv_mul_f32 v134, s86, v112\n"
    "v_mul_f32 v135, s86, v113\nv_mul_f32 v117, s82, v114\nv_mul_f32 v119, s82, v115\nv_mul_f32 v121, s86, v114\nv_mul_f32 v123, s86, v115\nv_exp_f32 v132, v132\n"
    "v_exp_f32 v133, v133\nv_exp_f32 v134, v134\nv_exp_f32 v135, v135\nv_fract_f32 v117, v117\nv_fract_f32 v119, v119\nv_fract_f32 v121, v121\nv_fract_f32 v123, v123\n"
    "v_cos_f32 v116, v117\nv_cos_f32 v118, v119\nv_cos_f32 v120, v121\nv_cos_f32 v122, v123\nv_sin_f32 v117, v117\nv_sin_f32 v119, v119\nv_sin_f32 v121, v121\n"
    "v_sin_f32 v123, v123\nv_mul_f32 v116, v116, v132\nv_mul_f32 v118, v118, v133\nv_mul_f32 v120, v120, v134\nv_mul_f32 v122, v122, v135\nv_mul_f32 v117, v117, v132\n"
    "v_mul_f32 v119, v119, v133\nv_mul_f32 v121, v121, v134\nv_mul_f32 v123, v123, v135\ns_lshr_b32 s83, s3, 1\ns_mul_i32 s83, s83, 0x6000600\ns_mov_b32 s84, 0xffff\n"
    "v_add_u32 v244, s83, v244\nv_add_u32 v245, s83, v245\nv_add_u32 v246, s83, v246\nv_add_u32 v247, s83, v247\nv_add_u32 v248, s83, v248\nv_add_u32 v249, s83, v249\n"
    "v_add_u32 v250, s83, v250\nv_add_u32 v251, s83, v251\nv_cvt_pk_f16_f32 v128, v116, v117\nv_cvt_pk_f16_f32 v129, v118, v119\n"
    "v_pk_mul_f32 v[124:125], v[116:117], v[120:121] op_sel_hi:[0,1]\nv_pk_mul_f32 v[126:127], v[118:119], v[122:123] op_sel_hi:[0,1]\nds_write_b64 v188, v[128:129] offset:0\n"
    "v_pk_fma_f32 v[116:117], v[116:117], v[120:121], v[124:125] op_sel:[1,1,0] op_sel_hi:[1,0,1] neg_lo:[0,1,0]\n"
    "v_pk_fma_f32 v[118:119], v[118:119], v[122:123], v[126:127] op_sel:[1,1,0] op_sel_hi:[1,0,1] neg_lo:[0,1,0]\nv_cvt_pk_f16_f32 v130, v116, v117\n"
    "v_cvt_pk_f16_f32 v131, v118, v119\nv_pk_mul_f32 v[124:125], v[116:117], v[120:121] op_sel_hi:[0,1]\nv_pk_mul_f32 v[126:127], v[118:119], v[122:123] op_sel_hi:[0,1]\n"
    "ds_write_b64 v188, v[130:131] offset:544\nv_pk_fma_f32 v[116:117], v[116:117], v[120:121], v[124:125] op_sel:[1,1,0] op_sel_hi:[1,0,1] neg_lo:[0,1,0]\n"
    "v_pk_fma_f32 v[118:119], v[118:119], v[122:123], v[126:127] op_sel:[1,1,0] op_sel_hi:[1,0,1] neg_lo:[0,1,0]\nv_cvt_pk_f16_f32 v128, v116, v117\n"
    "v_cvt_pk_f16_f32 v129, v118, v119\nv_pk_mul_f32 v[124:125], v[116:117], v[120:121] op_sel_hi:[0,1]\nv_pk_mul_f32 v[126:127], v[118:119], v[122:123] op_sel_hi:[0,1]\n"
    "ds_write_b64 v188, v[128:129] offset:1088\nv_pk_fma_f32 v[116:117], v[116:117], v[120:121], v[124:125] op_sel:[1,1,0] op_sel_hi:[1,0,1] neg_lo:[0,1,0]\n"
    "v_pk_fma_f32 v[118:119], v[118:119], v[122:123], v[126:127] op_sel:[1,1,0] op_sel_hi:[1,0,1] neg_lo:[0,1,0]\nv_cvt_pk_f16_f32 v130, v116, v117\n"
    "v_cvt_pk_f16_f32 v131, v118, v119\nv_pk_mul_f32 v[124:125], v[116:117], v[120:121] op_sel_hi:[0,1]\nv_pk_mul_f32 v[126:127], v[118:119], v[122:123] op_sel_hi:[0,1]\n"
    "ds_write_b64 v188, v[130:131] offset:1632\nv_pk_fma_f32 v[116:117], v[116:117], v[120:121], v[124:125] op_sel:[1,1,0] op_sel_hi:[1,0,1] neg_lo:[0,1,0]\n"
    "v_pk_fma_f32 v[118:119], v[118:119], v[122:123], v[126:127] op_sel:[1,1,0] op_sel_hi:[1,0,1] neg_lo:[0,1,0]\nv_cvt_pk_f16_f32 v128, v116, v117\n"
    "v_cvt_pk_f16_f32 v129, v118, v119\nv_pk_mul_f32 v[124:125], v[116:117], v[120:121] op_sel_hi:[0,1]\nv_pk_mul_f32 v[126:127], v[118:119], v[122:123] op_sel_hi:[0,1]\n"
    "ds_write_b64 v188, v[128:129] offset:2176\nv_pk_fma_f32 v[116:117], v[116:117], v[120:121], v[124:125] op_sel:[1,1,0] op_sel_hi:[1,0,1] neg_lo:[0,1,0]\n"
    "v_pk_fma_f32 v[118:119], v[118:119], v[122:123], v[126:127] op_sel:[1,1,0] op_sel_hi:[1,0,1] neg_lo:[0,1,0]\nv_cvt_pk_f16_f32 v130, v116, v117\n"
    "v_cvt_pk_f16_f32 v131, v118, v119\nv_pk_mul_f32 v[124:125], v[116:117], v[120:121] op_sel_hi:[0,1]\nv_pk_mul_f32 v[126:127], v[118:119], v[122:123] op_sel_hi:[0,1]\n"
    "ds_write_b64 v188, v[130:131] offset:2720\nv_pk_fma_f32 v[116:117], v[116:117], v[120:121], v[124:125] op_sel:[1,1,0] op_sel_hi:[1,0,1] neg_lo:[0,1,0]\n"
    "v_pk_fma_f32 v[118:119], v[118:119], v[122:123], v[126:127] op_sel:[1,1,0] op_sel_hi:[1,0,1] neg_lo:[0,1,0]\nv_cvt_pk_f16_f32 v128, v116, v117\n"
    "v_cvt_pk_f16_f32 v129, v118, v119\nv_pk_mul_f32 v[124:125], v[116:117], v[120:121] op_sel_hi:[0,1]\nv_pk_mul_f32 v[126:127], v[118:119], v[122:123] op_sel_hi:[0,1]\n"
    "ds_write_b64 v188, v[128:129] offset:3264\nv_pk_fma_f32 v[116:117], v[116:117], v[120:121], v[124:125] op_sel:[1,1,0] op_sel_hi:[1,0,1] neg_lo:[0,1,0]\n"
    "v_pk_fma_f32 v[118:119], v[118:119], v[122:123], v[126:127] op_sel:[1,1,0] op_sel_hi:[1,0,1] neg_lo:[0,1,0]\nv_cvt_pk_f16_f32 v130, v116, v117\n"
    "v_cvt_pk_f16_f32 v131, v118, v119\nv_pk_mul_f32 v[124:125], v[116:117], v[120:121] op_sel_hi:[0,1]\nv_pk_mul_f32 v[126:127], v[118:119], v[122:123] op_sel_hi:[0,1]\n"
    "ds_write_b64 v188, v[130:131] offset:3808\nv_pk_fma_f32 v[116:117], v[116:117], v[120:121], v[124:125] op_sel:[1,1,0] op_sel_hi:[1,0,1] neg_lo:[0,1,0]\n"
    "v_pk_fma_f32 v[118:119], v[118:119], v[122:123], v[126:127] op_sel:[1,1,0] op_sel_hi:[1,0,1] neg_lo:[0,1,0]\nv_cvt_pk_f16_f32 v128, v116, v117\n"
    "v_cvt_pk_f16_f32 v129, v118, v119\nv_pk_mul_f32 v[124:125], v[116:117], v[120:121] op_sel_hi:[0,1]\nv_pk_mul_f32 v[126:127], v[118:119], v[122:123] op_sel_hi:[0,1]\n"
    "ds_write_b64 v188, v[128:129] offset:4352\nv_pk_fma_f32 v[116:117], v[116:117], v[120:121], v[124:125] op_sel:[1,1,0] op_sel_hi:[1,0,1] neg_lo:[0,1,0]\n"
    "v_pk_fma_f32 v[118:119], v[118:119], v[122:123], v[126:127] op_sel:[1,1,0] op_sel_hi:[1,0,1] neg_lo:[0,1,0]\nv_cvt_pk_f16_f32 v130, v116, v117\n"
    "v_cvt_pk_f16_f32 v131, v118, v119\nv_pk_mul_f32 v[124:125], v[116:117], v[120:121] op_sel_hi:[0,1]\nv_pk_mul_f32 v[126:127], v[118:119], v[122:123] op_sel_hi:[0,1]\n"
    "ds_write_b64 v188, v[130:131] offset:4896\nv_pk_fma_f32 v[116:117], v[116:117], v[120:121], v[124:125] op_sel:[1,1,0] op_sel_hi:[1,0,1] neg_lo:[0,1,0]\n"
    "v_pk_fma_f32 v[118:119], v[118:119], v[122:123], v[126:127] op_sel:[1,1,0] op_sel_hi:[1,0,1] neg_lo:[0,1,0]\nv_cvt_pk_f16_f32 v128, v116, v117\n"
    "v_cvt_pk_f16_f32 v129, v118, v119\nv_pk_mul_f32 v[124:125], v[116:117], v[120:121] op_sel_hi:[0,1]\nv_pk_mul_f32 v[126:127], v[118:119], v[122:123] op_sel_hi:[0,1]\n"
    "ds_write_b64 v188, v[128:129] offset:5440\nv_pk_fma_f32 v[116:117], v[116:117], v[120:121], v[124:125] op_sel:[1,1,0] op_sel_hi:[1,0,1] neg_lo:[0,1,0]\n"
    "v_pk_fma_f32 v[118:119], v[118:119], v[122:123], v[126:127] op_sel:[1,1,0] op_sel_hi:[1,0,1] neg_lo:[0,1,0]\nv_cvt_pk_f16_f32 v130, v116, v117\n"
    "v_cvt_pk_f16_f32 v131, v118, v119\nv_pk_mul_f32 v[124:125], v[116:117], v[120:121] op_sel_hi:[0,1]\nv_pk_mul_f32 v[126:127], v[118:119], v[122:123] op_sel_hi:[0,1]\n"
    "ds_write_b64 v188, v[130:131] offset:5984\nv_pk_fma_f32 v[116:117], v[116:117], v[120:121], v[124:125] op_sel:[1,1,0] op_sel_hi:[1,0,1] neg_lo:[0,1,0]\n"
    "v_pk_fma_f32 v[118:119], v[118:119], v[122:123], v[126:127] op_sel:[1,1,0] op_sel_hi:[1,0,1] neg_lo:[0,1,0]\nv_cvt_pk_f16_f32 v128, v116, v117\n"
    "v_cvt_pk_f16_f32 v129, v118, v119\nv_pk_mul_f32 v[124:125], v[116:117], v[120:121] op_sel_hi:[0,1]\nv_pk_mul_f32 v[126:127], v[118:119], v[122:123] op_sel_hi:[0,1]\n"
    "ds_write_b64 v188, v[128:129] offset:6528\nv_pk_fma_f32 v[116:117], v[116:117], v[120:121], v[124:125] op_sel:[1,1,0] op_sel_hi:[1,0,1] neg_lo:[0,1,0]\n"
    "v_pk_fma_f32 v[118:119], v[118:119], v[122:123], v[126:127] op_sel:[1,1,0] op_sel_hi:[1,0,1] neg_lo:[0,1,0]\nv_cvt_pk_f16_f32 v130, v116, v117\n"
    "v_cvt_pk_f16_f32 v131, v118, v119\nv_pk_mul_f32 v[124:125], v[116:117], v[120:121] op_sel_hi:[0,1]\nv_pk_mul_f32 v[126:127], v[118:119], v[122:123] op_sel_hi:[0,1]\n"
    "ds_write_b64 v188, v[130:131] offset:7072\nv_pk_fma_f32 v[116:117], v[116:117], v[120:121], v[124:125] op_sel:[1,1,0] op_sel_hi:[1,0,1] neg_lo:[0,1,0]\n"
    "v_pk_fma_f32 v[118:119], v[118:119], v[122:123], v[126:127] op_sel:[1,1,0] op_sel_hi:[1,0,1] neg_lo:[0,1,0]\nv_cvt_pk_f16_f32 v128, v116, v117\n"
    "v_cvt_pk_f16_f32 v129, v118, v119\nv_pk_mul_f32 v[124:125], v[116:117], v[120:121] op_sel_hi:[0,1]\nv_pk_mul_f32 v[126:127], v[118:119], v[122:123] op_sel_hi:[0,1]\n"
    "ds_write_b64 v188, v[128:129] offset:7616\nv_pk_fma_f32 v[116:117], v[116:117], v[120:121], v[124:125] op_sel:[1,1,0] op_sel_hi:[1,0,1] neg_lo:[0,1,0]\n"
    "v_pk_fma_f32 v[118:119], v[118:119], v[122:123], v[126:127] op_sel:[1,1,0] op_sel_hi:[1,0,1] neg_lo:[0,1,0]\nv_cvt_pk_f16_f32 v130, v116, v117\n"
    "v_cvt_pk_f16_f32 v131, v118, v119\nds_write_b64 v188, v[130:131] offset:8160\ns_waitcnt vmcnt(0)\ns_mov_b32 s80, 0x10000\n.Ltp72:\nv_xor_b32 v22, s34, v22\n"
    "v_xor_b32 v23, s34, v23\nv_xor_b32 v24, s34, v24\nv_xor_b32 v25, s34, v25\nv_or3_b32 v22, v22, v23, v24\nv_or_b32 v22, v22, v25\nv_cmp_ne_u32 vcc, 0, v22\n"
    "s_cbranch_vccz .Ltq73\ns_sub_u32 s80, s80, 1\ns_cbranch_scc1 .Ltq73\ns_sleep 2\nglobal_load_dwordx4 v[22:25], v187, s[32:33] sc1\ns_waitcnt vmcnt(0)\ns_branch .Ltp72\n"
    ".Ltq73:\nglobal_load_dwordx4 v[208:211], v182, s[32:33] offset:-4096 sc1\nglobal_load_dwordx4 v[212:215], v182, s[32:33] offset:0 sc1\n"
    "global_load_dwordx4 v[216:219], v183, s[32:33] offset:-4096 sc1\nglobal_load_dwordx4 v[220:223], v183, s[32:33] offset:0 sc1\n"
    "global_load_dwordx4 v[224:227], v184, s[32:33] offset:-4096 sc1\nglobal_load_dwordx4 v[228:231], v184, s[32:33] offset:0 sc1\n"
    "global_load_dwordx4 v[232:235], v185, s[32:33] offset:-4096 sc1\nglobal_load_dwordx4 v[236:239], v185, s[32:33] offset:0 sc1\ns_waitcnt lgkmcnt(0)\ns_barrier\n"
    "ds_read_b128 v[64:67], v189 offset:0\nds_read_b128 v[68:71], v190 offset:0\nds_read_b128 v[72:75], v189 offset:64\nds_read_b128 v[76:79], v190 offset:64\n"
    "ds_read_b128 v[80:83], v189 offset:128\nds_read_b128 v[84:87], v190 offset:128\nds_read_b128 v[88:91], v189 offset:192\nds_read_b128 v[92:95], v190 offset:192\n"
    "ds_read_b128 v[96:99], v189 offset:256\nds_read_b128 v[100:103], v190 offset:256\ns_waitcnt lgkmcnt(8)\nv_mfma_f32_16x16x32_f16 v[104:107], v[64:67], v[68:71], 0\n"
    "ds_read_b128 v[64:67], v189 offset:320\nds_read_b128 v[68:71], v190 offset:320\ns_waitcnt lgkmcnt(8)\nv_mfma_f32_16x16x32_f16 v[104:107], v[72:75], v[76:79], v[104:107]\n"
    "ds_read_b128 v[72:75], v189 offset:384\nds_read_b128 v[76:79], v190 offset:384\ns_waitcnt lgkmcnt(8)\nv_mfma_f32_16x16x32_f16 v[104:107], v[80:83], v[84:87], v[104:107]\n"
    "ds_read_b128 v[80:83], v189 offset:448\nds_read_b128 v[84:87], v190 offset:448\ns_waitcnt lgkmcnt(8)\nv_mfma_f32_16x16x32_f16 v[104:107], v[88:91], v[92:95], v[104:107]\n"
    "s_waitcnt lgkmcnt(6)\nv_mfma_f32_16x16x32_f16 v[104:107], v[96:99], v[100:103], v[104:107]\ns_waitcnt lgkmcnt(4)\n"
    "v_mfma_f32_16x16x32_f16 v[104:107], v[64:67], v[68:71], v[104:107]\ns_waitcnt lgkmcnt(2)\nv_mfma_f32_16x16x32_f16 v[104:107], v[72:75], v[76:79], v[104:107]\n"
    "s_waitcnt lgkmcnt(0)\nv_mfma_f32_16x16x32_f16 v[104:107], v[80:83], v[84:87], v[104:107]\nv_and_or_b32 v88, v244, s84, v191\nv_alignbit_b32 v89, 1, v244, 16\n"
    "v_and_or_b32 v90, v245, s84, v191\nv_alignbit_b32 v91, 1, v245, 16\ns_nop 3\nv_cvt_pk_f16_f32 v104, v104, v105\nv_cvt_pk_f16_f32 v105, v106, v107\n"
    "ds_write_b16 v88, v104 offset:59392\nds_write_b16_d16_hi v89, v104 offset:59392\nds_write_b16 v90, v105 offset:59392\nds_write_b16_d16_hi v91, v105 offset:59392\n"
    "s_waitcnt lgkmcnt(0)\ns_barrier\n.LL71:\ns_mov_b32 s80, 0x10000\n.LL74:\ns_waitcnt vmcnt(7)\nds_write_b128 v186, v[208:211] offset:0\nv_max3_u32 v22, v208, v209, 0\n"
    "v_max3_u32 v22, v210, v211, v22\ns_waitcnt vmcnt(6)\nds_write_b128 v186, v[212:215] offset:4096\nv_max3_u32 v22, v212, v213, v22\nv_max3_u32 v22, v214, v215, v22\n"
    "s_waitcnt vmcnt(5)\nds_write_b128 v186, v[216:219] offset:8192\nv_max3_u32 v22, v216, v217, v22\nv_max3_u32 v22, v218, v219, v22\ns_waitcnt vmcnt(4)\n"
    "ds_write_b128 v186, v[220:223] offset:12288\nv_max3_u32 v22, v220, v221, v22\nv_max3_u32 v22, v222, v223, v22\ns_waitcnt vmcnt(3)\n"
    "ds_write_b128 v186, v[224:227] offset:16384\nv_max3_u32 v22, v224, v225, v22\nv_max3_u32 v22, v226, v227, v22\ns_waitcnt vmcnt(2)\n"
    "ds_write_b128 v186, v[228:231] offset:20480\nv_max3_u32 v22, v228, v229, v22\nv_max3_u32 v22, v230, v231, v22\ns_waitcnt vmcnt(1)\n"
    "ds_write_b128 v186, v[232:235] offset:24576\nv_max3_u32 v22, v232, v233, v22\nv_max3_u32 v22, v234, v235, v22\ns_waitcnt vmcnt(0)\n"
    "ds_write_b128 v186, v[236:239] offset:28672\nv_max3_u32 v22, v236, v237, v22\nv_max3_u32 v22, v238, v239, v22\nv_cmp_eq_u32 vcc, -1, v22\ns_cbranch_vccnz .LL76\n.LL75:\n"
    "s_waitcnt lgkmcnt(0)\ns_barrier\ns_cmp_eq_u32 s6, 0\ns_cbranch_scc0 .LL87\nglobal_store_dwordx4 v200, v[196:199], s[32:33] sc1\n"
    "global_store_dwordx4 v200, v[196:199], s[32:33] offset:1024 sc1\ns_mov_b64 exec, 0xffff\nglobal_store_dwordx4 v201, v[196:199], s[32:33] sc1\ns_mov_b64 exec, -1\n"
    "global_load_dwordx4 v[22:25], v202, s[32:33] sc1\nv_and_or_b32 v64, v246, s84, v191\nv_alignbit_b32 v65, 1, v246, 16\nv_and_or_b32 v66, v247, s84, v191\n"
    "v_alignbit_b32 v67, 1, v247, 16\nv_and_or_b32 v68, v248, s84, v191\nv_alignbit_b32 v69, 1, v248, 16\nv_and_or_b32 v70, v249, s84, v191\nv_alignbit_b32 v71, 1, v249, 16\n"
    "v_and_or_b32 v72, v250, s84, v191\nv_alignbit_b32 v73, 1, v250, 16\nv_and_or_b32 v74, v251, s84, v191\nv_alignbit_b32 v75, 1, v251, 16\nds_read_u16 v64, v64 offset:59392\n"
    "ds_read_u16 v65, v65 offset:59392\nds_read_u16 v66, v66 offset:59392\nds_read_u16 v67, v67 offset:59392\nds_read_u16 v68, v68 offset:59392\n"
    "ds_read_u16 v69, v69 offset:59392\nds_read_u16 v70, v70 offset:59392\nds_read_u16 v71, v71 offset:59392\nds_read_u16 v72, v72 offset:59392\n"
    "ds_read_u16 v73, v73 offset:59392\nds_read_u16 v74, v74 offset:59392\nds_read_u16 v75, v75 offset:59392\ns_waitcnt lgkmcnt(0)\nv_lshl_or_b32 v40, v65, 16, v64\n"
    "v_lshl_or_b32 v41, v67, 16, v66\nv_lshl_or_b32 v42, v69, 16, v68\nv_lshl_or_b32 v43, v71, 16, v70\nv_lshl_or_b32 v44, v73, 16, v72\nv_lshl_or_b32 v45, v75, 16, v74\n"
    ".LL87:\nv_and_b32 v3, 0x7f, v0\ns_lshr_b32 s16, s3, 1\ns_mul_i32 s15, s16, 0x2100\ns_add_u32 s14, s15, 0x18000\nv_lshl_add_u32 v2, v3, 4, s14\n"
    "s_lshl2_add_u32 s17, s3, 0x27fb0\nv_mov_b32 v6, s17\nv_mov_b32 v7, 2\nv_alignbit_b32 v48, v41, v40, 16\nv_alignbit_b32 v49, v42, v41, 16\nv_alignbit_b32 v50, v43, v42, 16\n"
    "v_alignbit_b32 v51, v44, v43, 16\nv_mov_b32 v52, v41\nv_mov_b32 v53, v42\nv_mov_b32 v54, v43\nv_mov_b32 v55, v44\nv_alignbit_b32 v56, v42, v41, 16\n"
    "v_alignbit_b32 v57, v43, v42, 16\nv_alignbit_b32 v58, v44, v43, 16\nv_alignbit_b32 v59, v45, v44, 16\nds_write_b128 v2, v[40:43]\nds_write_b128 v2, v[48:51] offset:2112\n"
    "ds_write_b128 v2, v[52:55] offset:4224\nds_write_b128 v2, v[56:59] offset:6336\ns_cmp_eq_u32 s6, 0\ns_cbranch_scc0 .LL85\ns_waitcnt vmcnt(0)\ns_mov_b32 s80, 0x10000\n"
    ".Ltp88:\nv_xor_b32 v22, s34, v22\nv_xor_b32 v23, s34, v23\nv_xor_b32 v24, s34, v24\nv_xor_b32 v25, s34, v25\nv_or3_b32 v22, v22, v23, v24\nv_or_b32 v22, v22, v25\n"
    "v_cmp_ne_u32 vcc, 0, v22\ns_cbranch_vccz .Ltq89\ns_sub_u32 s80, s80, 1\ns_cbranch_scc1 .Ltq89\ns_sleep 2\nglobal_load_dwordx4 v[22:25], v202, s[32:33] sc1\n"
    "s_waitcnt vmcnt(0)\ns_branch .Ltp88\n.Ltq89:\ns_mov_b64 exec, 1\nds_write_b32 v6, v7\ns_mov_b64 exec, -1\n.LL85:\ns_add_u32 s14, s7, 0x401000\n"
    "v_lshl_add_u32 v20, v0, 4, s14\ns_lshl_b32 s17, s41, 13\ns_add_u32 s16, s7, s17\ns_add_u32 s15, s16, 0x200000\nv_lshl_add_u32 v21, v0, 4, s15\nv_and_b32 v22, 7, v0\n"
    "s_mov_b32 s18, 0x1d550\nv_lshl_add_u32 v14, v22, 3, s18\nv_lshrrev_b32 v23, 3, v0\ns_mov_b32 s19, 0x50\nv_mad_u32_u24 v14, v23, s19, v14\ns_mov_b32 s20, 0x20300\n"
    "v_lshl_add_u32 v15, v23, 7, s20\nv_xor_b32 v25, v0, v23\nv_and_b32 v24, 7, v25\nv_lshl_add_u32 v15, v24, 4, v15\ns_mov_b32 s21, 0x17800\nv_lshl_add_u32 v16, v0, 4, s21\n"
    "s_lshl_b32 s23, s41, 7\ns_add_u32 s22, s23, 0x600000\nv_lshl_add_u32 v26, v23, 15, s22\nv_and_b32 v27, 1, v0\nv_lshl_add_u32 v26, v27, 6, v26\nv_bfe_u32 v28, v0, 1, 2\n"
    "v_lshl_add_u32 v26, v28, 4, v26\nv_lshrrev_b32 v30, 4, v0\ns_lshl_b32 s44, s35, 18\ns_lshl_b32 s45, s36, 8\ns_add_u32 s25, s44, s45\ns_add_u32 s24, s25, 0x1200000\n"
    "v_lshl_add_u32 v29, v30, 13, s24\nv_and_b32 v31, 15, v0\nv_lshl_add_u32 v29, v31, 4, v29\ns_cmp_eq_u32 s6, 0\ns_cselect_b64 vcc, -1, 0\nv_cndmask_b32 v17, v29, v26, vcc\n"
    "v_and_b32 v46, 0xff, v0\nv_mov_b32 v48, 0x1d000\nv_mov_b32 v50, 0x1fdb0\nv_mov_b32 v52, 0x20b10\nv_mov_b32 v53, 0x21870\nv_cmp_gt_u32 vcc, 0xa1, v46\ns_nop 1\n"
    "v_cndmask_b32 v51, v53, v52, vcc\nv_cmp_gt_u32 vcc, 0x7b, v46\ns_nop 1\nv_cndmask_b32 v49, v51, v50, vcc\nv_cmp_gt_u32 vcc, 0x55, v46\ns_nop 1\n"
    "v_cndmask_b32 v47, v49, v48, vcc\nv_lshl_add_u32 v18, v46, 4, v47\nv_bfe_u32 v56, v0, 5, 1\nv_and_b32 v58, 31, v0\nv_sub_u32 v57, 0x3ff, v58\n"
    "v_lshl_add_u32 v55, v56, 3, v57\nv_lshrrev_b32 v54, 2, v55\ns_lshr_b32 s48, s3, 1\ns_mul_i32 s47, s48, 0x2100\ns_add_u32 s46, s47, 0x17800\n"
    "v_lshl_add_u32 v19, v54, 3, s46\nv_and_b32 v59, 3, v55\ns_mov_b32 s49, 0x840\nv_mad_u32_u24 v19, v59, s49, v19\ns_lshl2_add_u32 s50, s3, 0x27f80\nv_mov_b32 v12, s50\n"
    "s_add_u32 s51, s6, 12\nv_mov_b32 v13, s51\ns_mul_i32 s54, s48, 0x1950\ns_and_b32 s56, s3, 1\ns_mul_i32 s55, s56, 0xa00\ns_add_u32 s53, s54, s55\n"
    "s_add_u32 s52, s53, 0x1d000\nv_lshl_add_u32 v2, v56, 4, s52\nv_mad_u32_u24 v2, v58, s19, v2\ns_mul_i32 s59, s48, 0x3180\ns_lshl_b32 s60, s56, 12\ns_add_u32 s58, s59, s60\n"
    "s_add_u32 s57, s58, 0x20300\nv_lshl_add_u32 v3, v58, 7, s57\nv_xor_b32 v60, v56, v22\nv_lshl_add_u32 v3, v60, 4, v3\ns_lshl3_add_u32 s61, s48, 0x27fc0\n"
    "v_lshl_add_u32 v11, v27, 2, s61\nv_and_b32 v63, 63, v0\nv_add_u32 v62, 3, v63\nv_and_b32 v61, 3, v62\ns_add_u32 s62, s54, 0x1d500\nv_lshl_add_u32 v4, v61, 4, s62\n"
    "v_lshrrev_b32 v196, 2, v62\nv_mad_u32_u24 v4, v196, s19, v4\nv_add_u32 v198, 2, v63\nv_and_b32 v197, 3, v198\ns_add_u32 s63, s54, 0x1d508\nv_lshl_add_u32 v5, v197, 4, s63\n"
    "v_lshrrev_b32 v199, 2, v198\nv_mad_u32_u24 v5, v199, s19, v5\nv_and_b32 v200, 3, v0\ns_add_u32 s64, s54, 0x1d550\nv_lshl_add_u32 v6, v200, 4, s64\n"
    "v_bfe_u32 v201, v0, 2, 4\nv_mad_u32_u24 v6, v201, s19, v6\nv_add_u32 v204, 1, v63\nv_and_b32 v203, 31, v204\ns_add_u32 s65, s59, 0x20540\n"
    "v_lshl_add_u32 v202, v203, 4, s65\nv_lshrrev_b32 v205, 5, v204\ns_mov_b32 s66, 0x240\nv_mad_u32_u24 v202, v205, s66, v202\nv_and_b32 v207, 63, v204\n"
    "s_add_u32 s67, s59, 0x22280\nv_lshl_add_u32 v206, v207, 4, s67\nv_lshrrev_b32 v208, 6, v204\ns_mov_b32 s68, 0x480\nv_mad_u32_u24 v206, v208, s68, v206\n"
    "s_cmp_eq_u32 s56, 0\ns_cselect_b64 vcc, -1, 0\nv_cndmask_b32 v7, v206, v202, vcc\nv_and_b32 v210, 31, v198\nv_lshl_add_u32 v209, v210, 4, s65\nv_lshrrev_b32 v211, 5, v198\n"
    "v_mad_u32_u24 v209, v211, s66, v209\ns_add_u32 s69, s59, 0x22b80\nv_lshl_add_u32 v212, v63, 4, s69\ns_cmp_eq_u32 s56, 0\ns_cselect_b64 vcc, -1, 0\n"
    "v_cndmask_b32 v8, v212, v209, vcc\ns_add_u32 s71, s59, 0x20300\ns_mov_b32 s73, 0xfffffb40\ns_cmp_eq_u32 s56, 0\ns_cselect_b32 s72, s73, 0x1f00\ns_add_u32 s70, s71, s72\n"
    "v_mov_b32 v213, s70\nv_cmp_gt_u32 vcc, 64, v204\ns_nop 1\nv_cndmask_b32 v9, v213, v7, vcc\ns_add_u32 s75, s71, 0x440\ns_add_u32 s74, s75, 0xfffff700\nv_mov_b32 v214, s74\n"
    "v_cmp_gt_u32 vcc, 64, v198\ns_nop 1\nv_cndmask_b32 v10, v214, v209, vcc\nv_add_u32 v22, 8192, v20\nv_add_u32 v23, 16384, v20\nv_add_u32 v24, 24576, v20\n"
    "v_add_u32 v25, 32768, v20\nv_add_u32 v26, 40960, v20\nv_add_u32 v27, 49152, v20\nv_add_u32 v28, 57344, v20\ns_waitcnt lgkmcnt(0)\ns_barrier\ns_or_b32 s81, s2, s3\n"
    "s_or_b32 s81, s81, s6\ns_cbranch_scc1 .LL104\nv_mov_b32 v60, 16777216\nv_mov_b32 v61, s34\ns_mov_b64 exec, 1\nglobal_store_dword v60, v61, s[32:33] sc1\n"
    "s_mov_b64 exec, -1\n.LL104:\nglobal_load_dwordx4 v[240:243], v21, s[32:33] sc1\nglobal_load_dwordx4 v[208:211], v20, s[32:33] offset:-4096 sc1\n"
    "global_load_dwordx4 v[212:215], v22, s[32:33] offset:-4096 sc1\nglobal_load_dwordx4 v[216:219], v23, s[32:33] offset:-4096 sc1\n"
    "global_load_dwordx4 v[220:223], v24, s[32:33] offset:-4096 sc1\nglobal_load_dwordx4 v[224:227], v25, s[32:33] offset:-4096 sc1\n"
    "global_load_dwordx4 v[228:231], v26, s[32:33] offset:-4096 sc1\nglobal_load_dwordx4 v[232:235], v27, s[32:33] offset:-4096 sc1\n"
    "global_load_dwordx4 v[236:239], v28, s[32:33] offset:-4096 sc1\ns_cmp_eq_u32 s6, 0\ns_cbranch_scc0 .LL90\nv_mov_b32 v60, 163216\nds_read_b64 v[60:61], v60\n"
    "s_waitcnt lgkmcnt(0)\nv_readfirstlane_b32 s88, v60\nv_readfirstlane_b32 s90, v61\n.LL90:\nv_mov_b64 v[60:61], 0\nv_mov_b64 v[62:63], 0\ns_cmp_eq_u32 s3, 3\n"
    "s_cbranch_scc1 .LL91\ns_cmp_eq_u32 s3, 2\ns_cselect_b32 exec_hi, 3, -1\nds_write_b128 v18, v[60:63]\ns_mov_b64 exec, -1\n.LL91:\nds_read_b64 v[64:65], v19 offset:2016\n"
    "ds_read_b64 v[66:67], v19 offset:2024\nds_read_b64 v[68:69], v19 offset:1984\nds_read_b64 v[70:71], v19 offset:1992\nds_read_b64 v[72:73], v19 offset:1952\n"
    "ds_read_b64 v[74:75], v19 offset:1960\nds_read_b64 v[76:77], v19 offset:1920\nds_read_b64 v[78:79], v19 offset:1928\nds_read_b64 v[80:81], v19 offset:1888\n"
    "ds_read_b64 v[82:83], v19 offset:1896\nds_read_b64 v[84:85], v19 offset:1856\nds_read_b64 v[86:87], v19 offset:1864\nds_read_b64 v[88:89], v19 offset:1824\n"
    "ds_read_b64 v[90:91], v19 offset:1832\nds_read_b64 v[92:93], v19 offset:1792\nds_read_b64 v[94:95], v19 offset:1800\nds_read_b64 v[96:97], v19 offset:1760\n"
    "ds_read_b64 v[98:99], v19 offset:1768\nds_read_b64 v[100:101], v19 offset:1728\nds_read_b64 v[102:103], v19 offset:1736\nds_read_b64 v[104:105], v19 offset:1696\n"
    "ds_read_b64 v[106:107], v19 offset:1704\nds_read_b64 v[108:109], v19 offset:1664\nds_read_b64 v[110:111], v19 offset:1672\nds_read_b64 v[112:113], v19 offset:1632\n"
    "ds_read_b64 v[114:115], v19 offset:1640\nds_read_b64 v[116:117], v19 offset:1600\nds_read_b64 v[118:119], v19 offset:1608\nds_read_b64 v[120:121], v19 offset:1568\n"
    "ds_read_b64 v[122:123], v19 offset:1576\nds_read_b64 v[124:125], v19 offset:1536\nds_read_b64 v[126:127], v19 offset:1544\nds_read_b64 v[128:129], v19 offset:1504\n"
    "ds_read_b64 v[130:131], v19 offset:1512\nds_read_b64 v[132:133], v19 offset:1472\nds_read_b64 v[134:135], v19 offset:1480\nds_read_b64 v[136:137], v19 offset:1440\n"
    "ds_read_b64 v[138:139], v19 offset:1448\nds_read_b64 v[140:141], v19 offset:1408\nds_read_b64 v[142:143], v19 offset:1416\nds_read_b64 v[144:145], v19 offset:1376\n"
    "ds_read_b64 v[146:147], v19 offset:1384\nds_read_b64 v[148:149], v19 offset:1344\nds_read_b64 v[150:151], v19 offset:1352\nds_read_b64 v[152:153], v19 offset:1312\n"
    "ds_read_b64 v[154:155], v19 offset:1320\nds_read_b64 v[156:157], v19 offset:1280\nds_read_b64 v[158:159], v19 offset:1288\nds_read_b64 v[160:161], v19 offset:1248\n"
    "ds_read_b64 v[162:163], v19 offset:1256\nds_read_b64 v[164:165], v19 offset:1216\nds_read_b64 v[166:167], v19 offset:1224\nds_read_b64 v[168:169], v19 offset:1184\n"
    "ds_read_b64 v[170:171], v19 offset:1192\nds_read_b64 v[172:173], v19 offset:1152\nds_read_b64 v[174:175], v19 offset:1160\nds_read_b64 v[176:177], v19 offset:1120\n"
    "ds_read_b64 v[178:179], v19 offset:1128\nds_read_b64 v[180:181], v19 offset:1088\nds_read_b64 v[182:183], v19 offset:1096\nds_read_b64 v[184:185], v19 offset:1056\n"
    "ds_read_b64 v[186:187], v19 offset:1064\nds_read_b64 v[188:189], v19 offset:1024\nds_read_b64 v[190:191], v19 offset:1032\nds_read_b64 v[192:193], v19 offset:992\n"
    "ds_read_b64 v[194:195], v19 offset:1000\nds_read_b64 v[196:197], v19 offset:960\nds_read_b64 v[198:199], v19 offset:968\nds_read_b64 v[200:201], v19 offset:928\n"
    "ds_read_b64 v[202:203], v19 offset:936\ns_waitcnt lgkmcnt(0)\ns_mov_b32 s84, 0x3b000000\ns_mov_b32 s86, 0x3727c5ac\ns_mov_b32 s80, 0x10000\ns_waitcnt vmcnt(8)\n.Lln92:\n"
    "v_max3_u32 v60, v240, v241, v242\nv_max_u32 v60, v60, v243\nv_cmp_eq_u32 s[82:83], -1, v60\nv_lshlrev_b32 v32, 16, v240\nv_and_b32 v33, 0xffff0000, v240\n"
    "v_lshlrev_b32 v34, 16, v241\nv_and_b32 v35, 0xffff0000, v241\nv_lshlrev_b32 v36, 16, v242\nv_and_b32 v37, 0xffff0000, v242\nv_lshlrev_b32 v38, 16, v243\n"
    "v_and_b32 v39, 0xffff0000, v243\ns_waitcnt vmcnt(4)\nv_pk_add_f32 v[48:49], v[208:209], v[216:217]\nv_pk_add_f32 v[50:51], v[210:211], v[218:219]\n"
    "v_pk_add_f32 v[52:53], v[212:213], v[220:221]\nv_pk_add_f32 v[54:55], v[214:215], v[222:223]\ns_waitcnt vmcnt(1)\nv_pk_add_f32 v[56:57], v[224:225], v[232:233]\n"
    "v_pk_add_f32 v[58:59], v[226:227], v[234:235]\nv_pk_add_f32 v[48:49], v[48:49], v[56:57]\nv_pk_add_f32 v[50:51], v[50:51], v[58:59]\n"
    "v_pk_mul_f32 v[48:49], v[48:49], s[84:85] op_sel_hi:[1,0]\nv_pk_mul_f32 v[50:51], v[50:51], s[84:85] op_sel_hi:[1,0]\ns_waitcnt vmcnt(0)\n"
    "v_pk_add_f32 v[56:57], v[228:229], v[236:237]\nv_pk_add_f32 v[58:59], v[230:231], v[238:239]\nv_pk_add_f32 v[52:53], v[52:53], v[56:57]\n"
    "v_pk_add_f32 v[54:55], v[54:55], v[58:59]\nv_pk_mul_f32 v[56:57], v[48:49], v[48:49]\nv_pk_mul_f32 v[58:59], v[50:51], v[50:51]\n"
    "v_pk_fma_f32 v[52:53], v[52:53], s[84:85], v[56:57] op_sel_hi:[1,0,1] neg_lo:[0,0,1] neg_hi:[0,0,1]\n"
    "v_pk_fma_f32 v[54:55], v[54:55], s[84:85], v[58:59] op_sel_hi:[1,0,1] neg_lo:[0,0,1] neg_hi:[0,0,1]\nv_cvt_pk_bf16_f32 v48, 0, v48\nv_cvt_pk_bf16_f32 v49, 0, v49\n"
    "v_pk_add_f32 v[56:57], v[52:53], v[54:55]\nv_cvt_pk_bf16_f32 v50, 0, v50\nv_cvt_pk_bf16_f32 v51, 0, v51\nv_cmp_u_f32 vcc, v56, v57\nv_max_f32 v52, 0, v52\n"
    "v_max_f32 v53, 0, v53\nv_max_f32 v54, 0, v54\nv_max_f32 v55, 0, v55\ns_or_b64 vcc, vcc, s[82:83]\ns_cbranch_vccnz .Lls94\n.Llo93:\nv_cvt_pk_bf16_f32 v52, 0, v52\n"
    "v_cvt_pk_bf16_f32 v53, 0, v53\nv_cvt_pk_bf16_f32 v54, 0, v54\nv_cvt_pk_bf16_f32 v55, 0, v55\nv_pk_add_f32 v[52:53], v[52:53], s[86:87] op_sel_hi:[1,0]\n"
    "v_pk_add_f32 v[54:55], v[54:55], s[86:87] op_sel_hi:[1,0]\nv_pk_add_f32 v[244:245], v[32:33], v[48:49] op_sel:[0,0] op_sel_hi:[1,0] neg_lo:[0,1] neg_hi:[0,1]\n"
    "v_pk_add_f32 v[246:247], v[34:35], v[48:49] op_sel:[0,1] op_sel_hi:[1,1] neg_lo:[0,1] neg_hi:[0,1]\nv_cvt_pk_bf16_f32 v52, 0, v52\nv_cvt_pk_bf16_f32 v53, 0, v53\n"
    "v_cvt_pk_bf16_f32 v54, 0, v54\nv_cvt_pk_bf16_f32 v55, 0, v55\nv_sqrt_f32 v52, v52\n"
    "v_pk_add_f32 v[248:249], v[36:37], v[50:51] op_sel:[0,0] op_sel_hi:[1,0] neg_lo:[0,1] neg_hi:[0,1]\nv_sqrt_f32 v53, v53\n"
    "v_pk_add_f32 v[250:251], v[38:39], v[50:51] op_sel:[0,1] op_sel_hi:[1,1] neg_lo:[0,1] neg_hi:[0,1]\nv_sqrt_f32 v54, v54\nv_cvt_pk_bf16_f32 v244, 0, v244\n"
    "v_sqrt_f32 v55, v55\nv_cvt_pk_bf16_f32 v245, 0, v245\nv_cvt_pk_bf16_f32 v52, 0, v52\nv_cvt_pk_bf16_f32 v53, 0, v53\nv_cvt_pk_bf16_f32 v54, 0, v54\n"
    "v_cvt_pk_bf16_f32 v55, 0, v55\nv_rcp_f32 v52, v52\nv_cvt_pk_bf16_f32 v246, 0, v246\nv_rcp_f32 v53, v53\nv_cvt_pk_bf16_f32 v247, 0, v247\nv_rcp_f32 v54, v54\n"
    "v_cvt_pk_bf16_f32 v248, 0, v248\nv_rcp_f32 v55, v55\nv_cvt_pk_bf16_f32 v249, 0, v249\nv_cvt_pk_bf16_f32 v250, 0, v250\nv_cvt_pk_bf16_f32 v251, 0, v251\n"
    "v_pk_mul_f32 v[244:245], v[244:245], v[52:53] op_sel:[0,0] op_sel_hi:[1,0]\nv_pk_mul_f32 v[246:247], v[246:247], v[52:53] op_sel:[0,1] op_sel_hi:[1,1]\n"
    "v_pk_mul_f32 v[248:249], v[248:249], v[54:55] op_sel:[0,0] op_sel_hi:[1,0]\nv_pk_mul_f32 v[250:251], v[250:251], v[54:55] op_sel:[0,1] op_sel_hi:[1,1]\n"
    "v_cvt_pk_bf16_f32 v244, 0, v244\nv_cvt_pk_bf16_f32 v246, 0, v246\nv_cvt_pk_bf16_f32 v248, 0, v248\nv_cvt_pk_bf16_f32 v250, 0, v250\nv_cvt_pk_f16_f32 v56, v244, v246\n"
    "v_cvt_pk_f16_f32 v57, v248, v250\nv_cvt_pk_bf16_f32 v245, 0, v245\nv_cvt_pk_bf16_f32 v247, 0, v247\nds_write_b64 v14, v[56:57]\nv_cvt_pk_bf16_f32 v249, 0, v249\n"
    "v_cvt_pk_bf16_f32 v251, 0, v251\nv_cvt_pk_f16_f32 v58, v245, v247\nv_cvt_pk_f16_f32 v59, v249, v251\nds_write_b64 v14, v[58:59] offset:6480\ns_waitcnt lgkmcnt(0)\n"
    "s_barrier\ns_bitcmp1_b32 s3, 0\ns_cbranch_scc1 .LL105\nds_read_b128 v[220:223], v2 offset:1392\nds_read_b64 v[48:49], v5 offset:0\nds_read_b128 v[50:53], v4 offset:0\n"
    "ds_read_b64 v[54:55], v6 offset:0\nds_read_b128 v[224:227], v2 offset:1360\nds_read_b128 v[228:231], v2 offset:1312\nds_read_b128 v[232:235], v2 offset:1280\n"
    "ds_read_b64 v[56:57], v5 offset:1280\nds_read_b128 v[58:61], v4 offset:1280\nds_read_b64 v[62:63], v6 offset:1280\ns_waitcnt lgkmcnt(9)\n"
    "v_mfma_f32_32x32x16_f16 v[204:219], v[64:67], v[220:223], 0\nds_read_b128 v[220:223], v2 offset:1232\ns_waitcnt lgkmcnt(7)\nv_alignbit_b32 v236, v50, v49, 16\n"
    "v_alignbit_b32 v237, v51, v50, 16\nv_alignbit_b32 v238, v52, v51, 16\nv_alignbit_b32 v239, v53, v52, 16\nv_alignbit_b32 v240, v54, v53, 16\ns_waitcnt lgkmcnt(6)\n"
    "v_mfma_f32_32x32x16_f16 v[204:219], v[68:71], v[224:227], v[204:219]\nds_read_b128 v[224:227], v2 offset:1200\nv_pk_add_f16 v64, v49, v236\nv_pk_add_f16 v65, v50, v237\n"
    "v_pk_add_f16 v66, v51, v238\nv_pk_add_f16 v67, v52, v239\nv_pk_add_f16 v68, v53, v240\ns_waitcnt lgkmcnt(6)\n"
    "v_mfma_f32_32x32x16_f16 v[204:219], v[72:75], v[228:231], v[204:219]\nds_read_b128 v[228:231], v2 offset:1152\nv_pk_add_f16 v72, v236, v65\nv_pk_add_f16 v73, v237, v66\n"
    "v_pk_add_f16 v74, v238, v67\nv_pk_add_f16 v75, v239, v68\nds_write_b64 v7, v[72:73] offset:4040\ns_waitcnt lgkmcnt(7)\n"
    "v_mfma_f32_32x32x16_f16 v[204:219], v[76:79], v[232:235], v[204:219]\nds_read_b128 v[232:235], v2 offset:1120\nv_pk_add_f16 v76, v64, v51\nv_pk_add_f16 v77, v65, v52\n"
    "v_pk_add_f16 v78, v66, v53\nv_pk_add_f16 v79, v67, v54\nds_write_b64 v8, v[74:75] offset:4032\ns_waitcnt lgkmcnt(5)\n"
    "v_mfma_f32_32x32x16_f16 v[204:219], v[80:83], v[220:223], v[204:219]\nds_read_b128 v[220:223], v2 offset:1072\nds_read_b64 v[48:49], v5 offset:2560\n"
    "ds_read_b128 v[50:53], v4 offset:2560\nds_read_b64 v[54:55], v6 offset:2560\nv_pk_add_f16 v76, v76, v65\nv_pk_add_f16 v77, v77, v66\ns_waitcnt lgkmcnt(8)\n"
    "v_mfma_f32_32x32x16_f16 v[204:219], v[84:87], v[224:227], v[204:219]\nds_read_b128 v[224:227], v2 offset:1040\nv_pk_add_f16 v78, v78, v67\nv_pk_add_f16 v79, v79, v68\n"
    "ds_write_b128 v7, v[76:79] offset:0\nv_alignbit_b32 v236, v58, v57, 16\nv_alignbit_b32 v237, v59, v58, 16\ns_waitcnt lgkmcnt(9)\n"
    "v_mfma_f32_32x32x16_f16 v[204:219], v[88:91], v[228:231], v[204:219]\nds_read_b128 v[228:231], v2 offset:992\nv_alignbit_b32 v238, v60, v59, 16\n"
    "v_alignbit_b32 v239, v61, v60, 16\nv_alignbit_b32 v240, v62, v61, 16\nv_pk_add_f16 v64, v57, v236\nv_pk_add_f16 v65, v58, v237\ns_waitcnt lgkmcnt(8)\n"
    "v_mfma_f32_32x32x16_f16 v[204:219], v[92:95], v[232:235], v[204:219]\nds_read_b128 v[232:235], v2 offset:960\nv_pk_add_f16 v66, v59, v238\nv_pk_add_f16 v67, v60, v239\n"
    "v_pk_add_f16 v68, v61, v240\nv_pk_add_f16 v72, v236, v65\nv_pk_add_f16 v73, v237, v66\ns_waitcnt lgkmcnt(7)\n"
    "v_mfma_f32_32x32x16_f16 v[204:219], v[96:99], v[220:223], v[204:219]\nds_read_b128 v[220:223], v2 offset:912\nv_pk_add_f16 v74, v238, v67\nv_pk_add_f16 v75, v239, v68\n"
    "ds_write_b64 v7, v[72:73] offset:5192\nv_pk_add_f16 v76, v64, v59\nv_pk_add_f16 v77, v65, v60\ns_waitcnt lgkmcnt(5)\n"
    "v_mfma_f32_32x32x16_f16 v[204:219], v[100:103], v[224:227], v[204:219]\nds_read_b128 v[224:227], v2 offset:880\nv_pk_add_f16 v78, v66, v61\nv_pk_add_f16 v79, v67, v62\n"
    "ds_write_b64 v8, v[74:75] offset:5184\nv_pk_add_f16 v76, v76, v65\nv_pk_add_f16 v77, v77, v66\ns_waitcnt lgkmcnt(5)\n"
    "v_mfma_f32_32x32x16_f16 v[204:219], v[104:107], v[228:231], v[204:219]\nds_read_b128 v[228:231], v2 offset:832\nv_pk_add_f16 v78, v78, v67\nv_pk_add_f16 v79, v79, v68\n"
    "ds_write_b128 v7, v[76:79] offset:1152\nv_alignbit_b32 v236, v50, v49, 16\nv_alignbit_b32 v237, v51, v50, 16\ns_waitcnt lgkmcnt(6)\n"
    "v_mfma_f32_32x32x16_f16 v[204:219], v[108:111], v[232:235], v[204:219]\nds_read_b128 v[232:235], v2 offset:800\nv_alignbit_b32 v238, v52, v51, 16\n"
    "v_alignbit_b32 v239, v53, v52, 16\nv_alignbit_b32 v240, v54, v53, 16\nv_pk_add_f16 v64, v49, v236\nv_pk_add_f16 v65, v50, v237\ns_waitcnt lgkmcnt(6)\n"
    "v_mfma_f32_32x32x16_f16 v[204:219], v[112:115], v[220:223], v[204:219]\nds_read_b128 v[220:223], v2 offset:752\nv_pk_add_f16 v66, v51, v238\nv_pk_add_f16 v67, v52, v239\n"
    "v_pk_add_f16 v68, v53, v240\nv_pk_add_f16 v72, v236, v65\nv_pk_add_f16 v73, v237, v66\ns_waitcnt lgkmcnt(5)\n"
    "v_mfma_f32_32x32x16_f16 v[204:219], v[116:119], v[224:227], v[204:219]\nds_read_b128 v[224:227], v2 offset:720\nv_pk_add_f16 v74, v238, v67\nv_pk_add_f16 v75, v239, v68\n"
    "ds_write_b64 v9, v[72:73] offset:6344\nv_pk_add_f16 v76, v64, v51\nv_pk_add_f16 v77, v65, v52\ns_waitcnt lgkmcnt(5)\n"
    "v_mfma_f32_32x32x16_f16 v[204:219], v[120:123], v[228:231], v[204:219]\nds_read_b128 v[228:231], v2 offset:672\nv_pk_add_f16 v78, v66, v53\nv_pk_add_f16 v79, v67, v54\n"
    "ds_write_b64 v10, v[74:75] offset:6336\nv_pk_add_f16 v76, v76, v65\nv_pk_add_f16 v77, v77, v66\ns_waitcnt lgkmcnt(5)\n"
    "v_mfma_f32_32x32x16_f16 v[204:219], v[124:127], v[232:235], v[204:219]\nds_read_b128 v[232:235], v2 offset:640\nv_pk_add_f16 v78, v78, v67\nv_pk_add_f16 v79, v79, v68\n"
    "ds_write_b128 v9, v[76:79] offset:2304\ns_mov_b64 exec, 1\nds_write_b32 v12, v13 offset:80\ns_mov_b64 exec, -1\ns_waitcnt lgkmcnt(7)\n"
    "v_mfma_f32_32x32x16_f16 v[204:219], v[128:131], v[220:223], v[204:219]\nds_read_b128 v[220:223], v2 offset:592\nv_mul_f32 v244, s96, v244\nv_mul_f32 v245, s97, v245\n"
    "v_mul_f32 v246, s96, v246\nv_mul_f32 v247, s97, v247\nv_mul_f32 v248, s96, v248\ns_waitcnt lgkmcnt(7)\n"
    "v_mfma_f32_32x32x16_f16 v[204:219], v[132:135], v[224:227], v[204:219]\nds_read_b128 v[224:227], v2 offset:560\nv_mul_f32 v249, s97, v249\nv_mul_f32 v250, s96, v250\n"
    "v_mul_f32 v251, s97, v251\nv_cvt_pk_bf16_f32 v244, 0, v244\nv_cvt_pk_bf16_f32 v245, 0, v245\ns_waitcnt lgkmcnt(6)\n"
    "v_mfma_f32_32x32x16_f16 v[204:219], v[136:139], v[228:231], v[204:219]\nds_read_b128 v[228:231], v2 offset:512\nv_cvt_pk_bf16_f32 v246, 0, v246\n"
    "v_cvt_pk_bf16_f32 v247, 0, v247\nv_cvt_pk_bf16_f32 v248, 0, v248\nv_cvt_pk_bf16_f32 v249, 0, v249\nv_cvt_pk_bf16_f32 v250, 0, v250\ns_waitcnt lgkmcnt(5)\n"
    "v_mfma_f32_32x32x16_f16 v[204:219], v[140:143], v[232:235], v[204:219]\nds_read_b128 v[232:235], v2 offset:480\nv_cvt_pk_bf16_f32 v251, 0, v251\nv_xor_b32 v238, 32, v3\n"
    "v_xor_b32 v239, 64, v3\nv_xor_b32 v240, 0x60, v3\nv_mov_b32 v24, 0\ns_waitcnt lgkmcnt(3)\nv_mfma_f32_32x32x16_f16 v[204:219], v[144:147], v[220:223], v[204:219]\n"
    "ds_read_b128 v[220:223], v2 offset:432\nv_mov_b32 v25, 0\nv_mov_b32 v26, 0\nv_mov_b32 v27, 0\nv_mov_b32 v28, 0\nv_mov_b32 v29, 0\ns_waitcnt lgkmcnt(3)\n"
    "v_mfma_f32_32x32x16_f16 v[204:219], v[148:151], v[224:227], v[204:219]\nds_read_b128 v[224:227], v2 offset:400\nv_mov_b32 v30, 0\nv_mov_b32 v31, 0\ns_waitcnt lgkmcnt(3)\n"
    "v_mfma_f32_32x32x16_f16 v[204:219], v[152:155], v[228:231], v[204:219]\nds_read_b128 v[228:231], v2 offset:352\ns_waitcnt lgkmcnt(3)\n"
    "v_mfma_f32_32x32x16_f16 v[204:219], v[156:159], v[232:235], v[204:219]\nds_read_b128 v[232:235], v2 offset:320\ns_waitcnt lgkmcnt(3)\n"
    "v_mfma_f32_32x32x16_f16 v[204:219], v[160:163], v[220:223], v[204:219]\nds_read_b128 v[220:223], v2 offset:272\ns_waitcnt lgkmcnt(3)\n"
    "v_mfma_f32_32x32x16_f16 v[204:219], v[164:167], v[224:227], v[204:219]\nds_read_b128 v[224:227], v2 offset:240\ns_waitcnt lgkmcnt(3)\n"
    "v_mfma_f32_32x32x16_f16 v[204:219], v[168:171], v[228:231], v[204:219]\nds_read_b128 v[228:231], v2 offset:192\ns_waitcnt lgkmcnt(3)\n"
    "v_mfma_f32_32x32x16_f16 v[204:219], v[172:175], v[232:235], v[204:219]\nds_read_b128 v[232:235], v2 offset:160\ns_waitcnt lgkmcnt(3)\n"
    "v_mfma_f32_32x32x16_f16 v[204:219], v[176:179], v[220:223], v[204:219]\nds_read_b128 v[220:223], v2 offset:112\ns_waitcnt lgkmcnt(3)\n"
    "v_mfma_f32_32x32x16_f16 v[204:219], v[180:183], v[224:227], v[204:219]\nds_read_b128 v[224:227], v2 offset:80\ns_waitcnt lgkmcnt(3)\n"
    "v_mfma_f32_32x32x16_f16 v[204:219], v[184:187], v[228:231], v[204:219]\nds_read_b128 v[228:231], v2 offset:32\ns_waitcnt lgkmcnt(3)\n"
    "v_mfma_f32_32x32x16_f16 v[204:219], v[188:191], v[232:235], v[204:219]\ns_waitcnt lgkmcnt(2)\nv_mfma_f32_32x32x16_f16 v[204:219], v[192:195], v[220:223], v[204:219]\n"
    "ds_read_b32 v241, v11\ns_waitcnt lgkmcnt(2)\nv_mfma_f32_32x32x16_f16 v[204:219], v[196:199], v[224:227], v[204:219]\ns_waitcnt lgkmcnt(1)\n"
    "v_mfma_f32_32x32x16_f16 v[204:219], v[200:203], v[228:231], v[204:219]\ns_branch .LL106\n.LL105:\nds_read_b128 v[220:223], v2 offset:1392\n"
    "ds_read_b64 v[48:49], v5 offset:0\nds_read_b128 v[50:53], v4 offset:0\nds_read_b64 v[54:55], v6 offset:0\nds_read_b128 v[224:227], v2 offset:1360\n"
    "ds_read_b128 v[228:231], v2 offset:1312\nds_read_b128 v[232:235], v2 offset:1280\nds_read_b64 v[56:57], v5 offset:1280\nds_read_b128 v[58:61], v4 offset:1280\n"
    "ds_read_b64 v[62:63], v6 offset:1280\ns_waitcnt lgkmcnt(9)\nv_mfma_f32_32x32x16_f16 v[204:219], v[64:67], v[220:223], 0\nds_read_b128 v[220:223], v2 offset:1232\n"
    "s_waitcnt lgkmcnt(7)\nv_alignbit_b32 v236, v49, v48, 16\nv_alignbit_b32 v237, v50, v49, 16\nv_alignbit_b32 v238, v51, v50, 16\nv_alignbit_b32 v239, v52, v51, 16\n"
    "v_alignbit_b32 v240, v53, v52, 16\ns_waitcnt lgkmcnt(6)\nv_mfma_f32_32x32x16_f16 v[204:219], v[68:71], v[224:227], v[204:219]\nds_read_b128 v[224:227], v2 offset:1200\n"
    "v_alignbit_b32 v241, v54, v53, 16\nv_alignbit_b32 v242, v55, v54, 16\nv_pk_add_f16 v64, v48, v236\nv_pk_add_f16 v65, v49, v237\nv_pk_add_f16 v66, v50, v238\n"
    "s_waitcnt lgkmcnt(6)\nv_mfma_f32_32x32x16_f16 v[204:219], v[72:75], v[228:231], v[204:219]\nds_read_b128 v[228:231], v2 offset:1152\nv_pk_add_f16 v67, v51, v239\n"
    "v_pk_add_f16 v68, v52, v240\nv_pk_add_f16 v69, v53, v241\nv_pk_add_f16 v70, v54, v242\nv_pk_add_f16 v71, v66, v67\ns_waitcnt lgkmcnt(6)\n"
    "v_mfma_f32_32x32x16_f16 v[204:219], v[76:79], v[232:235], v[204:219]\nds_read_b128 v[232:235], v2 offset:1120\nv_pk_add_f16 v243, v68, v69\nv_pk_add_f16 v76, v64, v52\n"
    "v_pk_add_f16 v77, v65, v53\nv_pk_add_f16 v78, v66, v54\nv_pk_add_f16 v79, v67, v55\ns_waitcnt lgkmcnt(3)\n"
    "v_mfma_f32_32x32x16_f16 v[204:219], v[80:83], v[220:223], v[204:219]\nds_read_b128 v[220:223], v2 offset:1072\nv_pk_add_f16 v80, v65, v71\nv_pk_add_f16 v81, v71, v68\n"
    "v_pk_add_f16 v82, v67, v243\nv_pk_add_f16 v83, v243, v70\nv_pk_add_f16 v72, v80, v236\ns_waitcnt lgkmcnt(3)\n"
    "v_mfma_f32_32x32x16_f16 v[204:219], v[84:87], v[224:227], v[204:219]\nds_read_b128 v[224:227], v2 offset:1040\nv_pk_add_f16 v73, v81, v237\nv_pk_add_f16 v74, v82, v238\n"
    "v_pk_add_f16 v75, v83, v239\nds_write_b128 v8, v[72:75] offset:0\nv_pk_add_f16 v76, v76, v80\ns_waitcnt lgkmcnt(4)\n"
    "v_mfma_f32_32x32x16_f16 v[204:219], v[88:91], v[228:231], v[204:219]\nds_read_b128 v[228:231], v2 offset:992\nv_pk_add_f16 v77, v77, v81\nv_pk_add_f16 v78, v78, v82\n"
    "v_pk_add_f16 v79, v79, v83\nds_write_b128 v7, v[76:79] offset:0\nv_alignbit_b32 v236, v57, v56, 16\ns_waitcnt lgkmcnt(5)\n"
    "v_mfma_f32_32x32x16_f16 v[204:219], v[92:95], v[232:235], v[204:219]\nds_read_b128 v[232:235], v2 offset:960\nv_alignbit_b32 v237, v58, v57, 16\n"
    "v_alignbit_b32 v238, v59, v58, 16\nv_alignbit_b32 v239, v60, v59, 16\nv_alignbit_b32 v240, v61, v60, 16\nv_alignbit_b32 v241, v62, v61, 16\ns_waitcnt lgkmcnt(5)\n"
    "v_mfma_f32_32x32x16_f16 v[204:219], v[96:99], v[220:223], v[204:219]\nds_read_b128 v[220:223], v2 offset:912\nv_alignbit_b32 v242, v63, v62, 16\n"
    "v_pk_add_f16 v64, v56, v236\nv_pk_add_f16 v65, v57, v237\nv_pk_add_f16 v66, v58, v238\nv_pk_add_f16 v67, v59, v239\ns_waitcnt lgkmcnt(5)\n"
    "v_mfma_f32_32x32x16_f16 v[204:219], v[100:103], v[224:227], v[204:219]\nds_read_b128 v[224:227], v2 offset:880\nv_pk_add_f16 v68, v60, v240\nv_pk_add_f16 v69, v61, v241\n"
    "v_pk_add_f16 v70, v62, v242\nv_pk_add_f16 v71, v66, v67\nv_pk_add_f16 v243, v68, v69\ns_waitcnt lgkmcnt(4)\n"
    "v_mfma_f32_32x32x16_f16 v[204:219], v[104:107], v[228:231], v[204:219]\nds_read_b128 v[228:231], v2 offset:832\nv_pk_add_f16 v76, v64, v60\nv_pk_add_f16 v77, v65, v61\n"
    "v_pk_add_f16 v78, v66, v62\nv_pk_add_f16 v79, v67, v63\nv_pk_add_f16 v80, v65, v71\ns_waitcnt lgkmcnt(3)\n"
    "v_mfma_f32_32x32x16_f16 v[204:219], v[108:111], v[232:235], v[204:219]\nds_read_b128 v[232:235], v2 offset:800\nv_pk_add_f16 v81, v71, v68\nv_pk_add_f16 v82, v67, v243\n"
    "v_pk_add_f16 v83, v243, v70\nv_pk_add_f16 v72, v80, v236\nv_pk_add_f16 v73, v81, v237\ns_waitcnt lgkmcnt(3)\n"
    "v_mfma_f32_32x32x16_f16 v[204:219], v[112:115], v[220:223], v[204:219]\nds_read_b128 v[220:223], v2 offset:752\nv_pk_add_f16 v74, v82, v238\nv_pk_add_f16 v75, v83, v239\n"
    "ds_write_b128 v8, v[72:75] offset:1152\nv_pk_add_f16 v76, v76, v80\nv_pk_add_f16 v77, v77, v81\ns_waitcnt lgkmcnt(4)\n"
    "v_mfma_f32_32x32x16_f16 v[204:219], v[116:119], v[224:227], v[204:219]\nds_read_b128 v[224:227], v2 offset:720\nv_pk_add_f16 v78, v78, v82\nv_pk_add_f16 v79, v79, v83\n"
    "ds_write_b128 v9, v[76:79] offset:1152\ns_mov_b64 exec, 1\nds_write_b32 v12, v13 offset:80\ns_mov_b64 exec, -1\ns_waitcnt lgkmcnt(6)\n"
    "v_mfma_f32_32x32x16_f16 v[204:219], v[120:123], v[228:231], v[204:219]\nds_read_b128 v[228:231], v2 offset:672\nv_mul_f32 v244, s96, v244\nv_mul_f32 v245, s97, v245\n"
    "v_mul_f32 v246, s96, v246\nv_mul_f32 v247, s97, v247\nv_mul_f32 v248, s96, v248\ns_waitcnt lgkmcnt(6)\n"
    "v_mfma_f32_32x32x16_f16 v[204:219], v[124:127], v[232:235], v[204:219]\nds_read_b128 v[232:235], v2 offset:640\nv_mul_f32 v249, s97, v249\nv_mul_f32 v250, s96, v250\n"
    "v_mul_f32 v251, s97, v251\nv_cvt_pk_bf16_f32 v244, 0, v244\nv_cvt_pk_bf16_f32 v245, 0, v245\ns_waitcnt lgkmcnt(6)\n"
    "v_mfma_f32_32x32x16_f16 v[204:219], v[128:131], v[220:223], v[204:219]\nds_read_b128 v[220:223], v2 offset:592\nv_cvt_pk_bf16_f32 v246, 0, v246\n"
    "v_cvt_pk_bf16_f32 v247, 0, v247\nv_cvt_pk_bf16_f32 v248, 0, v248\nv_cvt_pk_bf16_f32 v249, 0, v249\nv_cvt_pk_bf16_f32 v250, 0, v250\ns_waitcnt lgkmcnt(5)\n"
    "v_mfma_f32_32x32x16_f16 v[204:219], v[132:135], v[224:227], v[204:219]\nds_read_b128 v[224:227], v2 offset:560\nv_cvt_pk_bf16_f32 v251, 0, v251\nv_xor_b32 v238, 32, v3\n"
    "v_xor_b32 v239, 64, v3\nv_xor_b32 v240, 0x60, v3\nv_mov_b32 v24, 0\ns_waitcnt lgkmcnt(3)\nv_mfma_f32_32x32x16_f16 v[204:219], v[136:139], v[228:231], v[204:219]\n"
    "ds_read_b128 v[228:231], v2 offset:512\nv_mov_b32 v25, 0\nv_mov_b32 v26, 0\nv_mov_b32 v27, 0\nv_mov_b32 v28, 0\nv_mov_b32 v29, 0\ns_waitcnt lgkmcnt(3)\n"
    "v_mfma_f32_32x32x16_f16 v[204:219], v[140:143], v[232:235], v[204:219]\nds_read_b128 v[232:235], v2 offset:480\nv_mov_b32 v30, 0\nv_mov_b32 v31, 0\ns_waitcnt lgkmcnt(3)\n"
    "v_mfma_f32_32x32x16_f16 v[204:219], v[144:147], v[220:223], v[204:219]\nds_read_b128 v[220:223], v2 offset:432\ns_waitcnt lgkmcnt(3)\n"
    "v_mfma_f32_32x32x16_f16 v[204:219], v[148:151], v[224:227], v[204:219]\nds_read_b128 v[224:227], v2 offset:400\ns_waitcnt lgkmcnt(3)\n"
    "v_mfma_f32_32x32x16_f16 v[204:219], v[152:155], v[228:231], v[204:219]\nds_read_b128 v[228:231], v2 offset:352\ns_waitcnt lgkmcnt(3)\n"
    "v_mfma_f32_32x32x16_f16 v[204:219], v[156:159], v[232:235], v[204:219]\nds_read_b128 v[232:235], v2 offset:320\ns_waitcnt lgkmcnt(3)\n"
    "v_mfma_f32_32x32x16_f16 v[204:219], v[160:163], v[220:223], v[204:219]\nds_read_b128 v[220:223], v2 offset:272\ns_waitcnt lgkmcnt(3)\n"
    "v_mfma_f32_32x32x16_f16 v[204:219], v[164:167], v[224:227], v[204:219]\nds_read_b128 v[224:227], v2 offset:240\ns_waitcnt lgkmcnt(3)\n"
    "v_mfma_f32_32x32x16_f16 v[204:219], v[168:171], v[228:231], v[204:219]\nds_read_b128 v[228:231], v2 offset:192\ns_waitcnt lgkmcnt(3)\n"
    "v_mfma_f32_32x32x16_f16 v[204:219], v[172:175], v[232:235], v[204:219]\nds_read_b128 v[232:235], v2 offset:160\ns_waitcnt lgkmcnt(3)\n"
    "v_mfma_f32_32x32x16_f16 v[204:219], v[176:179], v[220:223], v[204:219]\nds_read_b128 v[220:223], v2 offset:112\ns_waitcnt lgkmcnt(3)\n"
    "v_mfma_f32_32x32x16_f16 v[204:219], v[180:183], v[224:227], v[204:219]\nds_read_b128 v[224:227], v2 offset:80\ns_waitcnt lgkmcnt(3)\n"
    "v_mfma_f32_32x32x16_f16 v[204:219], v[184:187], v[228:231], v[204:219]\nds_read_b128 v[228:231], v2 offset:32\ns_waitcnt lgkmcnt(3)\n"
    "v_mfma_f32_32x32x16_f16 v[204:219], v[188:191], v[232:235], v[204:219]\ns_waitcnt lgkmcnt(2)\nv_mfma_f32_32x32x16_f16 v[204:219], v[192:195], v[220:223], v[204:219]\n"
    "ds_read_b32 v241, v11\ns_waitcnt lgkmcnt(2)\nv_mfma_f32_32x32x16_f16 v[204:219], v[196:199], v[224:227], v[204:219]\ns_waitcnt lgkmcnt(1)\n"
    "v_mfma_f32_32x32x16_f16 v[204:219], v[200:203], v[228:231], v[204:219]\n.LL106:\ns_waitcnt lgkmcnt(0)\nv_cmp_gt_i32 vcc, v13, v241\ns_cbranch_vccnz .Lfs107\n.Lfr108:\n"
    "s_nop 8\nds_write_b128 v3, v[204:207]\nds_write_b128 v238, v[208:211]\nds_write_b128 v239, v[212:215]\nds_write_b128 v240, v[216:219]\ns_waitcnt lgkmcnt(0)\ns_barrier\n"
    "ds_read_b128 v[48:51], v15\nds_read_b128 v[52:55], v15 offset:12672\ns_cmp_lt_u32 s3, 2\ns_cbranch_scc1 .LL110\nds_read_b128 v[24:27], v16\n"
    "ds_read_b128 v[28:31], v16 offset:10240\n.LL110:\ns_waitcnt lgkmcnt(0)\nv_pk_add_f32 v[24:25], v[24:25], v[48:49]\nv_pk_add_f32 v[26:27], v[26:27], v[50:51]\n"
    "v_pk_add_f32 v[28:29], v[28:29], v[52:53]\nv_pk_add_f32 v[30:31], v[30:31], v[54:55]\nv_pk_mul_f32 v[24:25], v[24:25], s[88:89] op_sel_hi:[1,0]\n"
    "v_pk_mul_f32 v[26:27], v[26:27], s[88:89] op_sel_hi:[1,0]\nv_pk_mul_f32 v[28:29], v[28:29], s[90:91] op_sel_hi:[1,0]\n"
    "v_pk_mul_f32 v[30:31], v[30:31], s[90:91] op_sel_hi:[1,0]\nv_cvt_pk_bf16_f32 v48, 0, v24\nv_cvt_pk_bf16_f32 v49, 0, v28\nv_cvt_pk_bf16_f32 v50, 0, v25\n"
    "v_cvt_pk_bf16_f32 v51, 0, v29\nv_cvt_pk_bf16_f32 v52, 0, v26\nv_cvt_pk_bf16_f32 v53, 0, v30\nv_cvt_pk_bf16_f32 v54, 0, v27\nv_cvt_pk_bf16_f32 v55, 0, v31\n"
    "v_pk_add_f32 v[244:245], v[244:245], v[48:49]\nv_pk_add_f32 v[246:247], v[246:247], v[50:51]\nv_pk_add_f32 v[248:249], v[248:249], v[52:53]\n"
    "v_pk_add_f32 v[250:251], v[250:251], v[54:55]\nv_cvt_pk_bf16_f32 v244, 0, v244\nv_cvt_pk_bf16_f32 v245, 0, v245\nv_cvt_pk_bf16_f32 v246, 0, v246\n"
    "v_cvt_pk_bf16_f32 v247, 0, v247\nv_cvt_pk_bf16_f32 v248, 0, v248\nv_cvt_pk_bf16_f32 v249, 0, v249\nv_cvt_pk_bf16_f32 v250, 0, v250\nv_cvt_pk_bf16_f32 v251, 0, v251\n"
    "v_pk_add_f32 v[32:33], v[32:33], v[244:245]\nv_pk_add_f32 v[34:35], v[34:35], v[246:247]\nv_pk_add_f32 v[36:37], v[36:37], v[248:249]\n"
    "v_pk_add_f32 v[38:39], v[38:39], v[250:251]\ns_cmp_eq_u32 s6, 0\ns_cbranch_scc0 .LL111\ns_mov_b32 s81, 0xfffefffe\ns_mov_b32 vcc_lo, 0x55555555\n"
    "s_mov_b32 vcc_hi, 0x55555555\nv_cvt_pk_bf16_f32 v54, v33, v35\nv_cvt_pk_bf16_f32 v55, v37, v39\nv_cvt_pk_bf16_f32 v52, v32, v34\nv_cvt_pk_bf16_f32 v53, v36, v38\n"
    "v_pk_min_u16 v54, v54, s81\nv_pk_min_u16 v55, v55, s81\nv_pk_min_u16 v52, v52, s81\nv_pk_min_u16 v53, v53, s81\ns_nop 0\n"
    "v_cndmask_b32_dpp v48, v54, v52, vcc quad_perm:[1,0,3,2] row_mask:0xf bank_mask:0xf\nv_cndmask_b32_dpp v49, v55, v53, vcc quad_perm:[1,0,3,2] row_mask:0xf bank_mask:0xf\n"
    "s_not_b64 vcc, vcc\nv_cndmask_b32_dpp v50, v52, v54, vcc quad_perm:[1,0,3,2] row_mask:0xf bank_mask:0xf\n"
    "v_cndmask_b32_dpp v51, v53, v55, vcc quad_perm:[1,0,3,2] row_mask:0xf bank_mask:0xf\nglobal_store_dwordx4 v17, v[48:51], s[32:33] sc1\ns_branch .LG269\n.LL111:\n"
    "v_cvt_pk_bf16_f32 v48, v32, v33\nv_cvt_pk_bf16_f32 v49, v34, v35\nv_cvt_pk_bf16_f32 v50, v36, v37\nv_cvt_pk_bf16_f32 v51, v38, v39\nv_min_u32 v48, -2, v48\n"
    "v_min_u32 v49, -2, v49\nv_min_u32 v50, -2, v50\nv_min_u32 v51, -2, v51\nglobal_store_dwordx4 v17, v[48:51], s[32:33] sc1\nv_and_b32 v4, 7, v0\ns_lshl_b32 s15, s41, 13\n"
    "s_add_u32 s14, s15, 0x1200000\nv_lshl_add_u32 v2, v4, 10, s14\nv_lshrrev_b32 v5, 3, v0\nv_lshl_add_u32 v2, v5, 2, v2\ns_lshl_b32 s17, s36, 16\ns_lshl_b32 s18, s35, 7\n"
    "s_add_u32 s16, s17, s18\nv_lshl_add_u32 v3, v5, 10, s16\nv_lshl_add_u32 v3, v4, 4, v3\ns_mov_b32 s80, 0x10000\ns_waitcnt vmcnt(0)\ns_sleep 12\n.LL112:\n"
    "global_load_dword v52, v2, s[32:33] offset:0 sc1\nglobal_load_dword v53, v2, s[32:33] offset:256 sc1\nglobal_load_dword v54, v2, s[32:33] offset:512 sc1\n"
    "global_load_dword v55, v2, s[32:33] offset:768 sc1\ns_waitcnt vmcnt(0)\nv_max3_u32 v4, v52, v53, v54\nv_max_u32 v4, v4, v55\nv_cmp_eq_u32 vcc, -1, v4\n"
    "s_cbranch_vccz .LL113\ns_sub_u32 s80, s80, 1\ns_cbranch_scc0 .LL112\n.LL113:\nglobal_store_dwordx4 v3, v[52:55], s[30:31] sc1\ns_endpgm\n.LG269:\ns_mov_b32 s6, 1\n"
    "s_mov_b32 s7, 8388608\nv_bfe_u32 v65, v0, 4, 1\nv_bfe_u32 v66, v0, 5, 1\nv_xor_b32 v64, v65, v66\nv_bfe_u32 v69, v0, 2, 2\nv_lshl_add_u32 v68, v66, 3, v69\n"
    "v_lshlrev_b32 v67, 6, v68\nv_lshl_add_u32 v26, v64, 5, v67\nv_and_b32 v70, 3, v0\nv_lshl_add_u32 v26, v70, 3, v26\ns_mul_i32 s15, s3, 0x1a00\ns_add_u32 s14, s15, 0x18d00\n"
    "v_lshl_add_u32 v27, v66, 3, s14\nv_and_b32 v71, 31, v0\ns_mov_b32 s16, 0xd0\nv_mad_u32_u24 v27, v71, s16, v27\ns_mov_b32 s17, 0x27c00\nv_lshl_add_u32 v28, v71, 2, s17\n"
    "s_mul_i32 s19, s3, 0x3000\ns_add_u32 s18, s19, 0x18000\nv_add_u32 v29, s18, v67\nv_lshl_add_u32 v29, v65, 5, v29\nv_lshl_add_u32 v29, v70, 3, v29\nv_and_b32 v72, 63, v0\n"
    "s_lshl_b32 s23, s35, 15\ns_and_b32 s25, s36, 3\ns_lshl_b32 s24, s25, 13\ns_add_u32 s22, s23, s24\ns_lshl_b32 s44, s3, 11\ns_add_u32 s21, s22, s44\n"
    "s_add_u32 s20, s21, 0xd00000\nv_lshl_add_u32 v34, v72, 2, s20\nv_lshlrev_b32 v35, 4, v72\ns_lshl_b32 s46, s3, 12\ns_add_u32 s45, s46, 0x24c00\n"
    "v_lshl_add_u32 v36, v72, 3, s45\ns_and_b32 s54, s42, 7\ns_lshl_b32 s44, s54, 12\ns_add_u32 s56, s42, 1\ns_and_b32 s55, s56, 7\ns_lshl_b32 s45, s55, 12\n"
    "s_add_u32 s58, s42, 2\ns_and_b32 s57, s58, 7\ns_lshl_b32 s46, s57, 12\ns_add_u32 s60, s42, 3\ns_and_b32 s59, s60, 7\ns_lshl_b32 s47, s59, 12\ns_add_u32 s62, s42, 4\n"
    "s_and_b32 s61, s62, 7\ns_lshl_b32 s48, s61, 12\ns_add_u32 s64, s42, 5\ns_and_b32 s63, s64, 7\ns_lshl_b32 s49, s63, 12\ns_add_u32 s66, s42, 6\ns_and_b32 s65, s66, 7\n"
    "s_lshl_b32 s50, s65, 12\ns_add_u32 s68, s42, 7\ns_and_b32 s67, s68, 7\ns_lshl_b32 s51, s67, 12\ns_mov_b32 s81, 0x3b000000\ns_mov_b32 s84, -1\ns_mov_b32 s85, 0\n"
    "v_mov_b32 v143, 0x3f803f80\nv_add_u32 v116, s44, v26\nv_add_u32 v118, 0x10000, v116\nv_mov_b32 v112, 0\nv_mov_b32 v113, 0\nv_mov_b32 v114, 0\nv_mov_b32 v115, 0\n"
    "v_mov_b32 v121, 0\nv_mov_b32 v122, 0\nv_mov_b32 v123, 0\nv_mov_b32 v125, 0\nv_mov_b32 v126, 0\nv_mov_b32 v127, 0\nv_mov_b32 v129, 0\nv_mov_b32 v130, 0\nv_mov_b32 v131, 0\n"
    "v_mov_b32 v133, 0\nv_mov_b32 v134, 0\nv_mov_b32 v135, 0\nds_read_b32 v124, v28 offset:0\nds_read_b32 v128, v28 offset:128\nds_read_b32 v132, v28 offset:256\n"
    "s_waitcnt lgkmcnt(0)\nv_cvt_pk_bf16_f32 v140, 0, v124\nv_cvt_pk_bf16_f32 v141, 0, v128\nv_cvt_pk_bf16_f32 v142, 0, v132\nv_sub_f32 v124, v124, v140\n"
    "v_sub_f32 v128, v128, v141\nv_sub_f32 v132, v132, v142\nv_cvt_pk_bf16_f32 v124, v140, v124\nv_cvt_pk_bf16_f32 v128, v141, v128\nv_cvt_pk_bf16_f32 v132, v142, v132\n"
    "s_lshr_b32 s86, s44, 4\nv_add_u32 v34, s86, v34\ns_lshl2_add_u32 s15, s37, s3\ns_lshl_b32 s14, s15, 15\ns_add_u32 s82, s14, 0x600000\ns_mul_i32 s16, s3, 0x3000\n"
    "s_add_u32 s87, s16, 0x18000\ns_add_u32 s82, s82, s32\ns_addc_u32 s83, s33, 0\nds_read_b64_tr_b16 v[192:193], v116 offset:0\nds_read_b64_tr_b16 v[194:195], v116 offset:256\n"
    "ds_read_b64_tr_b16 v[196:197], v116 offset:1024\nds_read_b64_tr_b16 v[198:199], v116 offset:1280\nds_read_b64_tr_b16 v[200:201], v116 offset:2048\n"
    "ds_read_b64_tr_b16 v[202:203], v116 offset:2304\nds_read_b64_tr_b16 v[204:205], v116 offset:3072\nds_read_b64_tr_b16 v[206:207], v116 offset:3328\n"
    "ds_read_b64_tr_b16 v[208:209], v116 offset:32768\nds_read_b64_tr_b16 v[210:211], v116 offset:33024\nds_read_b64_tr_b16 v[212:213], v116 offset:33792\n"
    "ds_read_b64_tr_b16 v[214:215], v116 offset:34048\ns_waitcnt vmcnt(0)\ns_barrier\nv_mov_b32 v64, 0\nv_mov_b32 v65, 0xf149f2ca\nv_bfe_u32 v68, v0, 4, 2\n"
    "v_lshlrev_b32 v67, 2, v68\ns_add_u32 s15, s37, s3\ns_cmp_eq_u32 s15, 0\ns_cselect_b32 s14, 8, 0\nv_and_b32 v71, 7, v0\nv_add_u32 v70, 1, v71\nv_max_u32 v69, s14, v70\n"
    "v_sub_u32 v66, v67, v69\nv_add_u32 v73, 8, v71\nv_sub_u32 v72, v73, v69\nv_cmp_le_u32 vcc, v66, v72\ns_nop 1\nv_cndmask_b32 v2, v65, v64, vcc\nv_add_u32 v75, 1, v67\n"
    "v_sub_u32 v74, v75, v69\nv_cmp_le_u32 vcc, v74, v72\ns_nop 1\nv_cndmask_b32 v3, v65, v64, vcc\nv_add_u32 v77, 2, v67\nv_sub_u32 v76, v77, v69\nv_cmp_le_u32 vcc, v76, v72\n"
    "s_nop 1\nv_cndmask_b32 v4, v65, v64, vcc\nv_add_u32 v79, 3, v67\nv_sub_u32 v78, v79, v69\nv_cmp_le_u32 vcc, v78, v72\ns_nop 1\nv_cndmask_b32 v5, v65, v64, vcc\n"
    "s_mul_i32 s17, s3, 0xd00\ns_add_u32 s16, s17, 0x18040\nv_lshl_add_u32 v6, v68, 4, s16\nv_and_b32 v81, 15, v0\nv_max_u32 v80, 1, v81\ns_mov_b32 s18, 0x1a0\n"
    "v_mad_u32_u24 v6, v80, s18, v6\ns_add_u32 s19, s17, 0x18d00\nv_lshl_add_u32 v7, v68, 4, s19\nv_mad_u32_u24 v7, v71, s18, v7\nv_and_b32 v82, 3, v0\n"
    "s_add_u32 s20, s17, 0x18080\nv_lshl_add_u32 v8, v82, 3, s20\nv_bfe_u32 v85, v0, 2, 2\nv_add_u32 v84, v67, v85\nv_max_u32 v83, 1, v84\nv_mad_u32_u24 v8, v83, s18, v8\n"
    "v_bfe_u32 v86, v0, 3, 1\ns_lshr_b32 s54, s3, 1\ns_lshl2_add_u32 s25, s37, s54\ns_lshl_b32 s24, s25, 15\ns_lshl_b32 s55, s38, 11\ns_add_u32 s23, s24, s55\n"
    "s_and_b32 s57, s3, 1\ns_lshl_b32 s56, s57, 8\ns_add_u32 s22, s23, s56\ns_add_u32 s21, s22, 0x800000\nv_lshl_add_u32 v9, v86, 10, s21\nv_bfe_u32 v87, v0, 5, 1\n"
    "v_lshl_add_u32 v9, v87, 9, v9\nv_bfe_u32 v88, v0, 4, 1\nv_lshl_add_u32 v9, v88, 4, v9\nv_lshl_add_u32 v9, v71, 5, v9\nv_mov_b64 v[10:11], 0\nv_mov_b64 v[12:13], 0\n"
    "v_mov_b64 v[14:15], 0\nv_mov_b64 v[16:17], 0\nv_mov_b64 v[18:19], 0\nv_mov_b64 v[20:21], 0\nv_mov_b64 v[22:23], 0\nv_mov_b64 v[24:25], 0\ns_barrier\n"
    "ds_read_b64_tr_b16 v[144:145], v29 offset:0\nds_read_b64_tr_b16 v[146:147], v29 offset:256\nds_read_b64_tr_b16 v[148:149], v29 offset:1024\n"
    "ds_read_b64_tr_b16 v[150:151], v29 offset:1280\nds_read_b64_tr_b16 v[152:153], v29 offset:2048\nds_read_b64_tr_b16 v[154:155], v29 offset:2304\n"
    "ds_read_b64_tr_b16 v[156:157], v29 offset:3072\nds_read_b64_tr_b16 v[158:159], v29 offset:3328\ns_waitcnt lgkmcnt(0)\nv_pk_maximum3_f16 v138, v144, v145, v146\n"
    "v_pk_maximum3_f16 v139, v147, v148, v149\nv_pk_maximum3_f16 v138, v138, v150, v151\nv_pk_maximum3_f16 v139, v139, v152, v153\nv_pk_maximum3_f16 v138, v138, v154, v155\n"
    "v_pk_maximum3_f16 v139, v139, v156, v157\nv_pk_maximum3_f16 v138, v138, v158, v159\nv_pk_maximum3_f16 v138, v138, v139, v139\n"
    "v_cmp_u_f16_sdwa vcc, v138, v138 src0_sel:WORD_0 src1_sel:WORD_1\ns_cbranch_vccnz .Lxs114\n.Lxr115:\nv_dot2c_f32_bf16 v112, v143, v144\nv_dot2c_f32_bf16 v113, v144, v144\n"
    "v_dot2c_f32_bf16 v114, v143, v145\nv_dot2c_f32_bf16 v115, v145, v145\nv_dot2c_f32_bf16 v112, v143, v146\nv_dot2c_f32_bf16 v113, v146, v146\n"
    "v_dot2c_f32_bf16 v114, v143, v147\nv_dot2c_f32_bf16 v115, v147, v147\nv_dot2c_f32_bf16 v112, v143, v148\nv_dot2c_f32_bf16 v113, v148, v148\n"
    "v_dot2c_f32_bf16 v114, v143, v149\nv_dot2c_f32_bf16 v115, v149, v149\nv_dot2c_f32_bf16 v112, v143, v150\nv_dot2c_f32_bf16 v113, v150, v150\n"
    "v_dot2c_f32_bf16 v114, v143, v151\nv_dot2c_f32_bf16 v115, v151, v151\nv_dot2c_f32_bf16 v112, v143, v152\nv_dot2c_f32_bf16 v113, v152, v152\n"
    "v_dot2c_f32_bf16 v114, v143, v153\nv_dot2c_f32_bf16 v115, v153, v153\nv_dot2c_f32_bf16 v112, v143, v154\nv_dot2c_f32_bf16 v113, v154, v154\n"
    "v_dot2c_f32_bf16 v114, v143, v155\nv_dot2c_f32_bf16 v115, v155, v155\nv_dot2c_f32_bf16 v112, v143, v156\nv_dot2c_f32_bf16 v113, v156, v156\n"
    "v_dot2c_f32_bf16 v114, v143, v157\nv_dot2c_f32_bf16 v115, v157, v157\nv_dot2c_f32_bf16 v112, v143, v158\nv_dot2c_f32_bf16 v113, v158, v158\n"
    "v_dot2c_f32_bf16 v114, v143, v159\nv_dot2c_f32_bf16 v115, v159, v159\nv_mfma_f32_32x32x16_bf16 v[64:79], v[192:195], v[144:147], 0\n"
    "ds_read_b64_tr_b16 v[192:193], v116 offset:34816\nds_read_b64_tr_b16 v[194:195], v116 offset:35072\nv_add_f32 v112, v112, v114\nv_add_f32 v113, v113, v115\n"
    "v_add_u32 v117, s45, v26\nv_add_u32 v119, 0x10000, v117\nv_permlane32_swap_b32 v112, v113\nv_mfma_f32_32x32x16_bf16 v[64:79], v[196:199], v[148:151], v[64:79]\n"
    "ds_read_b64_tr_b16 v[196:197], v116 offset:35840\nds_read_b64_tr_b16 v[198:199], v116 offset:36096\nv_add_f32 v112, v112, v113\nv_min_u32 v112, -2, v112\n"
    "global_store_dword v34, v112, s[32:33] sc1\ns_barrier\nv_mfma_f32_32x32x16_bf16 v[64:79], v[200:203], v[152:155], v[64:79]\nds_read_b64_tr_b16 v[160:161], v29 offset:4096\n"
    "ds_read_b64_tr_b16 v[162:163], v29 offset:4352\nds_read_b64_tr_b16 v[200:201], v118 offset:0\nds_read_b64_tr_b16 v[202:203], v118 offset:256\n"
    "v_mfma_f32_32x32x16_bf16 v[64:79], v[204:207], v[156:159], v[64:79]\nds_read_b64_tr_b16 v[164:165], v29 offset:5120\nds_read_b64_tr_b16 v[166:167], v29 offset:5376\n"
    "ds_read_b64_tr_b16 v[204:205], v118 offset:1024\nds_read_b64_tr_b16 v[206:207], v118 offset:1280\nv_mfma_f32_32x32x16_bf16 v[80:95], v[208:211], v[144:147], 0\n"
    "ds_read_b64_tr_b16 v[168:169], v29 offset:6144\nds_read_b64_tr_b16 v[170:171], v29 offset:6400\nds_read_b64_tr_b16 v[208:209], v118 offset:2048\n"
    "ds_read_b64_tr_b16 v[210:211], v118 offset:2304\nv_mfma_f32_32x32x16_bf16 v[80:95], v[212:215], v[148:151], v[80:95]\nds_read_b64_tr_b16 v[172:173], v29 offset:7168\n"
    "ds_read_b64_tr_b16 v[174:175], v29 offset:7424\nds_read_b64_tr_b16 v[212:213], v118 offset:3072\nds_read_b64_tr_b16 v[214:215], v118 offset:3328\ns_waitcnt lgkmcnt(15)\n"
    "v_mfma_f32_32x32x16_bf16 v[80:95], v[192:195], v[152:155], v[80:95]\nds_read_b64_tr_b16 v[192:193], v117 offset:0\nds_read_b64_tr_b16 v[194:195], v117 offset:256\n"
    "v_mfma_f32_32x32x16_bf16 v[80:95], v[196:199], v[156:159], v[80:95]\nds_read_b64_tr_b16 v[196:197], v117 offset:1024\nds_read_b64_tr_b16 v[198:199], v117 offset:1280\n"
    "s_waitcnt lgkmcnt(15)\nv_mfma_f32_32x32x16_bf16 v[96:111], v[200:203], v[144:147], 0\nds_read_b64_tr_b16 v[200:201], v117 offset:2048\n"
    "ds_read_b64_tr_b16 v[202:203], v117 offset:2304\ns_waitcnt lgkmcnt(8)\nv_pk_maximum3_f16 v138, v160, v161, v162\nv_pk_maximum3_f16 v139, v163, v164, v165\n"
    "v_pk_maximum3_f16 v138, v138, v166, v167\nv_mfma_f32_32x32x16_bf16 v[96:111], v[204:207], v[148:151], v[96:111]\nds_read_b64_tr_b16 v[204:205], v117 offset:3072\n"
    "ds_read_b64_tr_b16 v[206:207], v117 offset:3328\nv_pk_maximum3_f16 v139, v139, v168, v169\nv_pk_maximum3_f16 v138, v138, v170, v171\n"
    "v_pk_maximum3_f16 v139, v139, v172, v173\nv_mfma_f32_32x32x16_bf16 v[96:111], v[208:211], v[152:155], v[96:111]\nds_read_b64_tr_b16 v[208:209], v117 offset:32768\n"
    "ds_read_b64_tr_b16 v[210:211], v117 offset:33024\nv_pk_maximum3_f16 v138, v138, v174, v175\nv_pk_maximum3_f16 v138, v138, v139, v139\n"
    "v_cmp_u_f16_sdwa vcc, v138, v138 src0_sel:WORD_0 src1_sel:WORD_1\ns_waitcnt lgkmcnt(10)\nv_mfma_f32_32x32x16_bf16 v[96:111], v[212:215], v[156:159], v[96:111]\n"
    "ds_read_b64_tr_b16 v[212:213], v117 offset:33792\nds_read_b64_tr_b16 v[214:215], v117 offset:34048\ns_cbranch_vccnz .Lxs117\n.Lxr118:\ns_waitcnt lgkmcnt(10)\n"
    "v_mfma_f32_32x32x16_bf16 v[64:79], v[192:195], v[160:163], v[64:79]\nds_read_b64_tr_b16 v[192:193], v117 offset:34816\nds_read_b64_tr_b16 v[194:195], v117 offset:35072\n"
    "v_add_u32 v116, s46, v26\nv_add_u32 v118, 0x10000, v116\ns_waitcnt lgkmcnt(10)\nv_mfma_f32_32x32x16_bf16 v[64:79], v[196:199], v[164:167], v[64:79]\n"
    "ds_read_b64_tr_b16 v[196:197], v117 offset:35840\nds_read_b64_tr_b16 v[198:199], v117 offset:36096\ns_barrier\ns_waitcnt lgkmcnt(10)\n"
    "v_mfma_f32_32x32x16_bf16 v[64:79], v[200:203], v[168:171], v[64:79]\nds_read_b64_tr_b16 v[144:145], v29 offset:8192\nds_read_b64_tr_b16 v[146:147], v29 offset:8448\n"
    "ds_read_b64_tr_b16 v[200:201], v119 offset:0\nds_read_b64_tr_b16 v[202:203], v119 offset:256\ns_waitcnt lgkmcnt(12)\n"
    "v_mfma_f32_32x32x16_bf16 v[64:79], v[204:207], v[172:175], v[64:79]\nds_read_b64_tr_b16 v[148:149], v29 offset:9216\nds_read_b64_tr_b16 v[150:151], v29 offset:9472\n"
    "ds_read_b64_tr_b16 v[204:205], v119 offset:1024\nds_read_b64_tr_b16 v[206:207], v119 offset:1280\ns_waitcnt lgkmcnt(14)\n"
    "v_mfma_f32_32x32x16_bf16 v[80:95], v[208:211], v[160:163], v[80:95]\nds_read_b64_tr_b16 v[152:153], v29 offset:10240\nds_read_b64_tr_b16 v[154:155], v29 offset:10496\n"
    "ds_read_b64_tr_b16 v[208:209], v119 offset:2048\nds_read_b64_tr_b16 v[210:211], v119 offset:2304\ns_waitcnt lgkmcnt(15)\n"
    "v_mfma_f32_32x32x16_bf16 v[80:95], v[212:215], v[164:167], v[80:95]\nds_read_b64_tr_b16 v[156:157], v29 offset:11264\nds_read_b64_tr_b16 v[158:159], v29 offset:11520\n"
    "ds_read_b64_tr_b16 v[212:213], v119 offset:3072\nds_read_b64_tr_b16 v[214:215], v119 offset:3328\ns_waitcnt lgkmcnt(15)\n"
    "v_mfma_f32_32x32x16_bf16 v[80:95], v[192:195], v[168:171], v[80:95]\nds_read_b64_tr_b16 v[192:193], v116 offset:0\nds_read_b64_tr_b16 v[194:195], v116 offset:256\n"
    "v_mfma_f32_32x32x16_bf16 v[80:95], v[196:199], v[172:175], v[80:95]\nds_read_b64_tr_b16 v[196:197], v116 offset:1024\nds_read_b64_tr_b16 v[198:199], v116 offset:1280\n"
    "s_waitcnt lgkmcnt(15)\nv_mfma_f32_32x32x16_bf16 v[96:111], v[200:203], v[160:163], v[96:111]\nds_read_b64_tr_b16 v[200:201], v116 offset:2048\n"
    "ds_read_b64_tr_b16 v[202:203], v116 offset:2304\ns_waitcnt lgkmcnt(8)\nv_pk_maximum3_f16 v138, v144, v145, v146\nv_pk_maximum3_f16 v139, v147, v148, v149\n"
    "v_pk_maximum3_f16 v138, v138, v150, v151\nv_mfma_f32_32x32x16_bf16 v[96:111], v[204:207], v[164:167], v[96:111]\nds_read_b64_tr_b16 v[204:205], v116 offset:3072\n"
    "ds_read_b64_tr_b16 v[206:207], v116 offset:3328\nv_pk_maximum3_f16 v139, v139, v152, v153\nv_pk_maximum3_f16 v138, v138, v154, v155\n"
    "v_pk_maximum3_f16 v139, v139, v156, v157\nv_mfma_f32_32x32x16_bf16 v[96:111], v[208:211], v[168:171], v[96:111]\nds_read_b64_tr_b16 v[208:209], v116 offset:32768\n"
    "ds_read_b64_tr_b16 v[210:211], v116 offset:33024\nv_pk_maximum3_f16 v138, v138, v158, v159\nv_pk_maximum3_f16 v138, v138, v139, v139\n"
    "v_cmp_u_f16_sdwa vcc, v138, v138 src0_sel:WORD_0 src1_sel:WORD_1\ns_waitcnt lgkmcnt(10)\nv_mfma_f32_32x32x16_bf16 v[96:111], v[212:215], v[172:175], v[96:111]\n"
    "ds_read_b64_tr_b16 v[212:213], v116 offset:33792\nds_read_b64_tr_b16 v[214:215], v116 offset:34048\ns_cbranch_vccnz .Lxs120\n.Lxr121:\ns_waitcnt lgkmcnt(10)\n"
    "v_mfma_f32_32x32x16_bf16 v[64:79], v[192:195], v[144:147], v[64:79]\nds_read_b64_tr_b16 v[192:193], v116 offset:34816\nds_read_b64_tr_b16 v[194:195], v116 offset:35072\n"
    "v_add_u32 v117, s47, v26\nv_add_u32 v119, 0x10000, v117\ns_waitcnt lgkmcnt(10)\nv_mfma_f32_32x32x16_bf16 v[64:79], v[196:199], v[148:151], v[64:79]\n"
    "ds_read_b64_tr_b16 v[196:197], v116 offset:35840\nds_read_b64_tr_b16 v[198:199], v116 offset:36096\ns_barrier\ns_waitcnt lgkmcnt(10)\n"
    "v_mfma_f32_32x32x16_bf16 v[64:79], v[200:203], v[152:155], v[64:79]\nds_read_b64_tr_b16 v[160:161], v29 offset:0\nds_read_b64_tr_b16 v[162:163], v29 offset:256\n"
    "ds_read_b64_tr_b16 v[200:201], v118 offset:0\nds_read_b64_tr_b16 v[202:203], v118 offset:256\ns_waitcnt lgkmcnt(12)\n"
    "v_mfma_f32_32x32x16_bf16 v[64:79], v[204:207], v[156:159], v[64:79]\nds_read_b64_tr_b16 v[164:165], v29 offset:1024\nds_read_b64_tr_b16 v[166:167], v29 offset:1280\n"
    "ds_read_b64_tr_b16 v[204:205], v118 offset:1024\nds_read_b64_tr_b16 v[206:207], v118 offset:1280\ns_waitcnt lgkmcnt(14)\n"
    "v_mfma_f32_32x32x16_bf16 v[80:95], v[208:211], v[144:147], v[80:95]\nds_read_b64_tr_b16 v[168:169], v29 offset:2048\nds_read_b64_tr_b16 v[170:171], v29 offset:2304\n"
    "ds_read_b64_tr_b16 v[208:209], v118 offset:2048\nds_read_b64_tr_b16 v[210:211], v118 offset:2304\ns_waitcnt lgkmcnt(15)\n"
    "v_mfma_f32_32x32x16_bf16 v[80:95], v[212:215], v[148:151], v[80:95]\nds_read_b64_tr_b16 v[172:173], v29 offset:3072\nds_read_b64_tr_b16 v[174:175], v29 offset:3328\n"
    "ds_read_b64_tr_b16 v[212:213], v118 offset:3072\nds_read_b64_tr_b16 v[214:215], v118 offset:3328\ns_waitcnt lgkmcnt(15)\n"
    "v_mfma_f32_32x32x16_bf16 v[80:95], v[192:195], v[152:155], v[80:95]\nds_read_b64_tr_b16 v[192:193], v117 offset:0\nds_read_b64_tr_b16 v[194:195], v117 offset:256\n"
    "v_mfma_f32_32x32x16_bf16 v[80:95], v[196:199], v[156:159], v[80:95]\nds_read_b64_tr_b16 v[196:197], v117 offset:1024\nds_read_b64_tr_b16 v[198:199], v117 offset:1280\n"
    "s_waitcnt lgkmcnt(15)\nv_mfma_f32_32x32x16_bf16 v[96:111], v[200:203], v[144:147], v[96:111]\nds_read_b64_tr_b16 v[200:201], v117 offset:2048\n"
    "ds_read_b64_tr_b16 v[202:203], v117 offset:2304\ns_waitcnt lgkmcnt(8)\nv_pk_maximum3_f16 v138, v160, v161, v162\nv_pk_maximum3_f16 v139, v163, v164, v165\n"
    "v_pk_maximum3_f16 v138, v138, v166, v167\nv_mfma_f32_32x32x16_bf16 v[96:111], v[204:207], v[148:151], v[96:111]\nds_read_b64_tr_b16 v[204:205], v117 offset:3072\n"
    "ds_read_b64_tr_b16 v[206:207], v117 offset:3328\nv_pk_maximum3_f16 v139, v139, v168, v169\nv_pk_maximum3_f16 v138, v138, v170, v171\n"
    "v_pk_maximum3_f16 v139, v139, v172, v173\nv_mfma_f32_32x32x16_bf16 v[96:111], v[208:211], v[152:155], v[96:111]\nds_read_b64_tr_b16 v[208:209], v117 offset:32768\n"
    "ds_read_b64_tr_b16 v[210:211], v117 offset:33024\nv_pk_maximum3_f16 v138, v138, v174, v175\nv_pk_maximum3_f16 v138, v138, v139, v139\n"
    "v_cmp_u_f16_sdwa vcc, v138, v138 src0_sel:WORD_0 src1_sel:WORD_1\ns_waitcnt lgkmcnt(10)\nv_mfma_f32_32x32x16_bf16 v[96:111], v[212:215], v[156:159], v[96:111]\n"
    "ds_read_b64_tr_b16 v[212:213], v117 offset:33792\nds_read_b64_tr_b16 v[214:215], v117 offset:34048\ns_cbranch_vccnz .Lxs123\n.Lxr124:\ns_waitcnt lgkmcnt(10)\n"
    "v_mfma_f32_32x32x16_bf16 v[64:79], v[192:195], v[160:163], v[64:79]\nds_read_b64_tr_b16 v[192:193], v117 offset:34816\nds_read_b64_tr_b16 v[194:195], v117 offset:35072\n"
    "v_add_u32 v116, s48, v26\nv_add_u32 v118, 0x10000, v116\ns_waitcnt lgkmcnt(10)\nv_mfma_f32_32x32x16_bf16 v[64:79], v[196:199], v[164:167], v[64:79]\n"
    "ds_read_b64_tr_b16 v[196:197], v117 offset:35840\nds_read_b64_tr_b16 v[198:199], v117 offset:36096\ns_barrier\ns_waitcnt lgkmcnt(10)\n"
    "v_mfma_f32_32x32x16_bf16 v[64:79], v[200:203], v[168:171], v[64:79]\nds_read_b64_tr_b16 v[144:145], v29 offset:4096\nds_read_b64_tr_b16 v[146:147], v29 offset:4352\n"
    "ds_read_b64_tr_b16 v[200:201], v119 offset:0\nds_read_b64_tr_b16 v[202:203], v119 offset:256\ns_waitcnt lgkmcnt(12)\n"
    "v_mfma_f32_32x32x16_bf16 v[64:79], v[204:207], v[172:175], v[64:79]\nds_read_b64_tr_b16 v[148:149], v29 offset:5120\nds_read_b64_tr_b16 v[150:151], v29 offset:5376\n"
    "ds_read_b64_tr_b16 v[204:205], v119 offset:1024\nds_read_b64_tr_b16 v[206:207], v119 offset:1280\ns_waitcnt lgkmcnt(14)\n"
    "v_mfma_f32_32x32x16_bf16 v[80:95], v[208:211], v[160:163], v[80:95]\nds_read_b64_tr_b16 v[152:153], v29 offset:6144\nds_read_b64_tr_b16 v[154:155], v29 offset:6400\n"
    "ds_read_b64_tr_b16 v[208:209], v119 offset:2048\nds_read_b64_tr_b16 v[210:211], v119 offset:2304\ns_waitcnt lgkmcnt(15)\n"
    "v_mfma_f32_32x32x16_bf16 v[80:95], v[212:215], v[164:167], v[80:95]\nds_read_b64_tr_b16 v[156:157], v29 offset:7168\nds_read_b64_tr_b16 v[158:159], v29 offset:7424\n"
    "ds_read_b64_tr_b16 v[212:213], v119 offset:3072\nds_read_b64_tr_b16 v[214:215], v119 offset:3328\ns_waitcnt lgkmcnt(15)\n"
    "v_mfma_f32_32x32x16_bf16 v[80:95], v[192:195], v[168:171], v[80:95]\nds_read_b64_tr_b16 v[192:193], v116 offset:0\nds_read_b64_tr_b16 v[194:195], v116 offset:256\n"
    "v_mfma_f32_32x32x16_bf16 v[80:95], v[196:199], v[172:175], v[80:95]\nds_read_b64_tr_b16 v[196:197], v116 offset:1024\nds_read_b64_tr_b16 v[198:199], v116 offset:1280\n"
    "s_waitcnt lgkmcnt(15)\nv_mfma_f32_32x32x16_bf16 v[96:111], v[200:203], v[160:163], v[96:111]\nds_read_b64_tr_b16 v[200:201], v116 offset:2048\n"
    "ds_read_b64_tr_b16 v[202:203], v116 offset:2304\ns_waitcnt lgkmcnt(8)\nv_pk_maximum3_f16 v138, v144, v145, v146\nv_pk_maximum3_f16 v139, v147, v148, v149\n"
    "v_pk_maximum3_f16 v138, v138, v150, v151\nv_mfma_f32_32x32x16_bf16 v[96:111], v[204:207], v[164:167], v[96:111]\nds_read_b64_tr_b16 v[204:205], v116 offset:3072\n"
    "ds_read_b64_tr_b16 v[206:207], v116 offset:3328\nv_pk_maximum3_f16 v139, v139, v152, v153\nv_pk_maximum3_f16 v138, v138, v154, v155\n"
    "v_pk_maximum3_f16 v139, v139, v156, v157\nv_mfma_f32_32x32x16_bf16 v[96:111], v[208:211], v[168:171], v[96:111]\nds_read_b64_tr_b16 v[208:209], v116 offset:32768\n"
    "ds_read_b64_tr_b16 v[210:211], v116 offset:33024\nv_pk_maximum3_f16 v138, v138, v158, v159\nv_pk_maximum3_f16 v138, v138, v139, v139\n"
    "v_cmp_u_f16_sdwa vcc, v138, v138 src0_sel:WORD_0 src1_sel:WORD_1\ns_waitcnt lgkmcnt(10)\nv_mfma_f32_32x32x16_bf16 v[96:111], v[212:215], v[172:175], v[96:111]\n"
    "ds_read_b64_tr_b16 v[212:213], v116 offset:33792\nds_read_b64_tr_b16 v[214:215], v116 offset:34048\ns_cbranch_vccnz .Lxs126\n.Lxr127:\ns_waitcnt lgkmcnt(10)\n"
    "v_mfma_f32_32x32x16_bf16 v[64:79], v[192:195], v[144:147], v[64:79]\nds_read_b64_tr_b16 v[192:193], v116 offset:34816\nds_read_b64_tr_b16 v[194:195], v116 offset:35072\n"
    "v_add_u32 v117, s49, v26\nv_add_u32 v119, 0x10000, v117\ns_waitcnt lgkmcnt(10)\nv_mfma_f32_32x32x16_bf16 v[64:79], v[196:199], v[148:151], v[64:79]\n"
    "ds_read_b64_tr_b16 v[196:197], v116 offset:35840\nds_read_b64_tr_b16 v[198:199], v116 offset:36096\ns_barrier\ns_waitcnt lgkmcnt(10)\n"
    "v_mfma_f32_32x32x16_bf16 v[64:79], v[200:203], v[152:155], v[64:79]\nds_read_b64_tr_b16 v[160:161], v29 offset:8192\nds_read_b64_tr_b16 v[162:163], v29 offset:8448\n"
    "ds_read_b64_tr_b16 v[200:201], v118 offset:0\nds_read_b64_tr_b16 v[202:203], v118 offset:256\ns_waitcnt lgkmcnt(12)\n"
    "v_mfma_f32_32x32x16_bf16 v[64:79], v[204:207], v[156:159], v[64:79]\nds_read_b64_tr_b16 v[164:165], v29 offset:9216\nds_read_b64_tr_b16 v[166:167], v29 offset:9472\n"
    "ds_read_b64_tr_b16 v[204:205], v118 offset:1024\nds_read_b64_tr_b16 v[206:207], v118 offset:1280\ns_waitcnt lgkmcnt(14)\n"
    "v_mfma_f32_32x32x16_bf16 v[80:95], v[208:211], v[144:147], v[80:95]\nds_read_b64_tr_b16 v[168:169], v29 offset:10240\nds_read_b64_tr_b16 v[170:171], v29 offset:10496\n"
    "ds_read_b64_tr_b16 v[208:209], v118 offset:2048\nds_read_b64_tr_b16 v[210:211], v118 offset:2304\ns_waitcnt lgkmcnt(15)\n"
    "v_mfma_f32_32x32x16_bf16 v[80:95], v[212:215], v[148:151], v[80:95]\nds_read_b64_tr_b16 v[172:173], v29 offset:11264\nds_read_b64_tr_b16 v[174:175], v29 offset:11520\n"
    "ds_read_b64_tr_b16 v[212:213], v118 offset:3072\nds_read_b64_tr_b16 v[214:215], v118 offset:3328\ns_waitcnt lgkmcnt(15)\n"
    "v_mfma_f32_32x32x16_bf16 v[80:95], v[192:195], v[152:155], v[80:95]\nds_read_b64_tr_b16 v[192:193], v117 offset:0\nds_read_b64_tr_b16 v[194:195], v117 offset:256\n"
    "v_mfma_f32_32x32x16_bf16 v[80:95], v[196:199], v[156:159], v[80:95]\nds_read_b64_tr_b16 v[196:197], v117 offset:1024\nds_read_b64_tr_b16 v[198:199], v117 offset:1280\n"
    "s_waitcnt lgkmcnt(15)\nv_mfma_f32_32x32x16_bf16 v[96:111], v[200:203], v[144:147], v[96:111]\nds_read_b64_tr_b16 v[200:201], v117 offset:2048\n"
    "ds_read_b64_tr_b16 v[202:203], v117 offset:2304\ns_waitcnt lgkmcnt(8)\nv_pk_maximum3_f16 v138, v160, v161, v162\nv_pk_maximum3_f16 v139, v163, v164, v165\n"
    "v_pk_maximum3_f16 v138, v138, v166, v167\nv_mfma_f32_32x32x16_bf16 v[96:111], v[204:207], v[148:151], v[96:111]\nds_read_b64_tr_b16 v[204:205], v117 offset:3072\n"
    "ds_read_b64_tr_b16 v[206:207], v117 offset:3328\nv_pk_maximum3_f16 v139, v139, v168, v169\nv_pk_maximum3_f16 v138, v138, v170, v171\n"
    "v_pk_maximum3_f16 v139, v139, v172, v173\nv_mfma_f32_32x32x16_bf16 v[96:111], v[208:211], v[152:155], v[96:111]\nds_read_b64_tr_b16 v[208:209], v117 offset:32768\n"
    "ds_read_b64_tr_b16 v[210:211], v117 offset:33024\nv_pk_maximum3_f16 v138, v138, v174, v175\nv_pk_maximum3_f16 v138, v138, v139, v139\n"
    "v_cmp_u_f16_sdwa vcc, v138, v138 src0_sel:WORD_0 src1_sel:WORD_1\ns_waitcnt lgkmcnt(10)\nv_mfma_f32_32x32x16_bf16 v[96:111], v[212:215], v[156:159], v[96:111]\n"
    "ds_read_b64_tr_b16 v[212:213], v117 offset:33792\nds_read_b64_tr_b16 v[214:215], v117 offset:34048\ns_cbranch_vccnz .Lxs129\n.Lxr130:\ns_waitcnt lgkmcnt(10)\n"
    "v_mfma_f32_32x32x16_bf16 v[64:79], v[192:195], v[160:163], v[64:79]\nds_read_b64_tr_b16 v[192:193], v117 offset:34816\nds_read_b64_tr_b16 v[194:195], v117 offset:35072\n"
    "v_add_u32 v116, s50, v26\nv_add_u32 v118, 0x10000, v116\ns_waitcnt lgkmcnt(10)\nv_mfma_f32_32x32x16_bf16 v[64:79], v[196:199], v[164:167], v[64:79]\n"
    "ds_read_b64_tr_b16 v[196:197], v117 offset:35840\nds_read_b64_tr_b16 v[198:199], v117 offset:36096\ns_barrier\ns_waitcnt lgkmcnt(10)\n"
    "v_mfma_f32_32x32x16_bf16 v[64:79], v[200:203], v[168:171], v[64:79]\nds_read_b64_tr_b16 v[144:145], v29 offset:0\nds_read_b64_tr_b16 v[146:147], v29 offset:256\n"
    "ds_read_b64_tr_b16 v[200:201], v119 offset:0\nds_read_b64_tr_b16 v[202:203], v119 offset:256\ns_waitcnt lgkmcnt(12)\n"
    "v_mfma_f32_32x32x16_bf16 v[64:79], v[204:207], v[172:175], v[64:79]\nds_read_b64_tr_b16 v[148:149], v29 offset:1024\nds_read_b64_tr_b16 v[150:151], v29 offset:1280\n"
    "ds_read_b64_tr_b16 v[204:205], v119 offset:1024\nds_read_b64_tr_b16 v[206:207], v119 offset:1280\ns_waitcnt lgkmcnt(14)\n"
    "v_mfma_f32_32x32x16_bf16 v[80:95], v[208:211], v[160:163], v[80:95]\nds_read_b64_tr_b16 v[152:153], v29 offset:2048\nds_read_b64_tr_b16 v[154:155], v29 offset:2304\n"
    "ds_read_b64_tr_b16 v[208:209], v119 offset:2048\nds_read_b64_tr_b16 v[210:211], v119 offset:2304\ns_waitcnt lgkmcnt(15)\n"
    "v_mfma_f32_32x32x16_bf16 v[80:95], v[212:215], v[164:167], v[80:95]\nds_read_b64_tr_b16 v[156:157], v29 offset:3072\nds_read_b64_tr_b16 v[158:159], v29 offset:3328\n"
    "ds_read_b64_tr_b16 v[212:213], v119 offset:3072\nds_read_b64_tr_b16 v[214:215], v119 offset:3328\ns_waitcnt lgkmcnt(15)\n"
    "v_mfma_f32_32x32x16_bf16 v[80:95], v[192:195], v[168:171], v[80:95]\nds_read_b64_tr_b16 v[192:193], v116 offset:0\nds_read_b64_tr_b16 v[194:195], v116 offset:256\n"
    "v_mfma_f32_32x32x16_bf16 v[80:95], v[196:199], v[172:175], v[80:95]\nds_read_b64_tr_b16 v[196:197], v116 offset:1024\nds_read_b64_tr_b16 v[198:199], v116 offset:1280\n"
    "s_waitcnt lgkmcnt(15)\nv_mfma_f32_32x32x16_bf16 v[96:111], v[200:203], v[160:163], v[96:111]\nds_read_b64_tr_b16 v[200:201], v116 offset:2048\n"
    "ds_read_b64_tr_b16 v[202:203], v116 offset:2304\ns_waitcnt lgkmcnt(8)\nv_pk_maximum3_f16 v138, v144, v145, v146\nv_pk_maximum3_f16 v139, v147, v148, v149\n"
    "v_pk_maximum3_f16 v138, v138, v150, v151\nv_mfma_f32_32x32x16_bf16 v[96:111], v[204:207], v[164:167], v[96:111]\nds_read_b64_tr_b16 v[204:205], v116 offset:3072\n"
    "ds_read_b64_tr_b16 v[206:207], v116 offset:3328\nv_pk_maximum3_f16 v139, v139, v152, v153\nv_pk_maximum3_f16 v138, v138, v154, v155\n"
    "v_pk_maximum3_f16 v139, v139, v156, v157\nv_mfma_f32_32x32x16_bf16 v[96:111], v[208:211], v[168:171], v[96:111]\nds_read_b64_tr_b16 v[208:209], v116 offset:32768\n"
    "ds_read_b64_tr_b16 v[210:211], v116 offset:33024\nv_pk_maximum3_f16 v138, v138, v158, v159\nv_pk_maximum3_f16 v138, v138, v139, v139\n"
    "v_cmp_u_f16_sdwa vcc, v138, v138 src0_sel:WORD_0 src1_sel:WORD_1\ns_waitcnt lgkmcnt(10)\nv_mfma_f32_32x32x16_bf16 v[96:111], v[212:215], v[172:175], v[96:111]\n"
    "ds_read_b64_tr_b16 v[212:213], v116 offset:33792\nds_read_b64_tr_b16 v[214:215], v116 offset:34048\ns_cbranch_vccnz .Lxs132\n.Lxr133:\ns_waitcnt lgkmcnt(10)\n"
    "v_mfma_f32_32x32x16_bf16 v[64:79], v[192:195], v[144:147], v[64:79]\nds_read_b64_tr_b16 v[192:193], v116 offset:34816\nds_read_b64_tr_b16 v[194:195], v116 offset:35072\n"
    "v_add_u32 v117, s51, v26\nv_add_u32 v119, 0x10000, v117\ns_waitcnt lgkmcnt(10)\nv_mfma_f32_32x32x16_bf16 v[64:79], v[196:199], v[148:151], v[64:79]\n"
    "ds_read_b64_tr_b16 v[196:197], v116 offset:35840\nds_read_b64_tr_b16 v[198:199], v116 offset:36096\ns_barrier\ns_waitcnt lgkmcnt(10)\n"
    "v_mfma_f32_32x32x16_bf16 v[64:79], v[200:203], v[152:155], v[64:79]\nds_read_b64_tr_b16 v[160:161], v29 offset:4096\nds_read_b64_tr_b16 v[162:163], v29 offset:4352\n"
    "ds_read_b64_tr_b16 v[200:201], v118 offset:0\nds_read_b64_tr_b16 v[202:203], v118 offset:256\ns_waitcnt lgkmcnt(12)\n"
    "v_mfma_f32_32x32x16_bf16 v[64:79], v[204:207], v[156:159], v[64:79]\nds_read_b64_tr_b16 v[164:165], v29 offset:5120\nds_read_b64_tr_b16 v[166:167], v29 offset:5376\n"
    "ds_read_b64_tr_b16 v[204:205], v118 offset:1024\nds_read_b64_tr_b16 v[206:207], v118 offset:1280\ns_waitcnt lgkmcnt(14)\n"
    "v_mfma_f32_32x32x16_bf16 v[80:95], v[208:211], v[144:147], v[80:95]\nds_read_b64_tr_b16 v[168:169], v29 offset:6144\nds_read_b64_tr_b16 v[170:171], v29 offset:6400\n"
    "ds_read_b64_tr_b16 v[208:209], v118 offset:2048\nds_read_b64_tr_b16 v[210:211], v118 offset:2304\ns_waitcnt lgkmcnt(15)\n"
    "v_mfma_f32_32x32x16_bf16 v[80:95], v[212:215], v[148:151], v[80:95]\nds_read_b64_tr_b16 v[172:173], v29 offset:7168\nds_read_b64_tr_b16 v[174:175], v29 offset:7424\n"
    "ds_read_b64_tr_b16 v[212:213], v118 offset:3072\nds_read_b64_tr_b16 v[214:215], v118 offset:3328\ns_waitcnt lgkmcnt(15)\n"
    "v_mfma_f32_32x32x16_bf16 v[80:95], v[192:195], v[152:155], v[80:95]\nds_read_b64_tr_b16 v[192:193], v117 offset:0\nds_read_b64_tr_b16 v[194:195], v117 offset:256\n"
    "v_mfma_f32_32x32x16_bf16 v[80:95], v[196:199], v[156:159], v[80:95]\nds_read_b64_tr_b16 v[196:197], v117 offset:1024\nds_read_b64_tr_b16 v[198:199], v117 offset:1280\n"
    "s_waitcnt lgkmcnt(15)\nv_mfma_f32_32x32x16_bf16 v[96:111], v[200:203], v[144:147], v[96:111]\nds_read_b64_tr_b16 v[200:201], v117 offset:2048\n"
    "ds_read_b64_tr_b16 v[202:203], v117 offset:2304\ns_waitcnt lgkmcnt(8)\nv_pk_maximum3_f16 v138, v160, v161, v162\nv_pk_maximum3_f16 v139, v163, v164, v165\n"
    "v_pk_maximum3_f16 v138, v138, v166, v167\nv_mfma_f32_32x32x16_bf16 v[96:111], v[204:207], v[148:151], v[96:111]\nds_read_b64_tr_b16 v[204:205], v117 offset:3072\n"
    "ds_read_b64_tr_b16 v[206:207], v117 offset:3328\nv_pk_maximum3_f16 v139, v139, v168, v169\nv_pk_maximum3_f16 v138, v138, v170, v171\n"
    "v_pk_maximum3_f16 v139, v139, v172, v173\nv_mfma_f32_32x32x16_bf16 v[96:111], v[208:211], v[152:155], v[96:111]\nds_read_b64_tr_b16 v[208:209], v117 offset:32768\n"
    "ds_read_b64_tr_b16 v[210:211], v117 offset:33024\nv_pk_maximum3_f16 v138, v138, v174, v175\nv_pk_maximum3_f16 v138, v138, v139, v139\n"
    "v_cmp_u_f16_sdwa vcc, v138, v138 src0_sel:WORD_0 src1_sel:WORD_1\ns_waitcnt lgkmcnt(10)\nv_mfma_f32_32x32x16_bf16 v[96:111], v[212:215], v[156:159], v[96:111]\n"
    "ds_read_b64_tr_b16 v[212:213], v117 offset:33792\nds_read_b64_tr_b16 v[214:215], v117 offset:34048\ns_cbranch_vccnz .Lxs135\n.Lxr136:\ns_waitcnt lgkmcnt(10)\n"
    "v_mfma_f32_32x32x16_bf16 v[64:79], v[192:195], v[160:163], v[64:79]\nds_read_b64_tr_b16 v[192:193], v117 offset:34816\nds_read_b64_tr_b16 v[194:195], v117 offset:35072\n"
    "s_waitcnt lgkmcnt(10)\nv_mfma_f32_32x32x16_bf16 v[64:79], v[196:199], v[164:167], v[64:79]\nds_read_b64_tr_b16 v[196:197], v117 offset:35840\n"
    "ds_read_b64_tr_b16 v[198:199], v117 offset:36096\ns_barrier\nds_read_b64 v[136:137], v36\ns_waitcnt lgkmcnt(11)\n"
    "v_mfma_f32_32x32x16_bf16 v[64:79], v[200:203], v[168:171], v[64:79]\nds_read_b64_tr_b16 v[200:201], v119 offset:0\nds_read_b64_tr_b16 v[202:203], v119 offset:256\n"
    "s_waitcnt lgkmcnt(11)\nv_mfma_f32_32x32x16_bf16 v[64:79], v[204:207], v[172:175], v[64:79]\nds_read_b64_tr_b16 v[204:205], v119 offset:1024\n"
    "ds_read_b64_tr_b16 v[206:207], v119 offset:1280\ns_waitcnt lgkmcnt(11)\nv_mfma_f32_32x32x16_bf16 v[80:95], v[208:211], v[160:163], v[80:95]\n"
    "ds_read_b64_tr_b16 v[208:209], v119 offset:2048\nds_read_b64_tr_b16 v[210:211], v119 offset:2304\ns_waitcnt lgkmcnt(11)\n"
    "v_mfma_f32_32x32x16_bf16 v[80:95], v[212:215], v[164:167], v[80:95]\ns_waitcnt lgkmcnt(6)\nv_mov_b32 v120, v136\nds_read_b64_tr_b16 v[212:213], v119 offset:3072\n"
    "ds_read_b64_tr_b16 v[214:215], v119 offset:3328\nv_mfma_f32_32x32x16_bf16 v[80:95], v[192:195], v[168:171], v[80:95]\n"
    "v_mfma_f32_32x32x16_bf16 v[64:79], v[124:127], v[120:123], v[64:79]\nv_mfma_f32_32x32x16_bf16 v[80:95], v[196:199], v[172:175], v[80:95]\n"
    "v_mfma_f32_32x32x16_bf16 v[80:95], v[128:131], v[120:123], v[80:95]\ns_waitcnt lgkmcnt(6)\nv_mfma_f32_32x32x16_bf16 v[96:111], v[200:203], v[160:163], v[96:111]\n"
    "s_waitcnt lgkmcnt(4)\nv_mfma_f32_32x32x16_bf16 v[96:111], v[204:207], v[164:167], v[96:111]\ns_nop 5\nv_mul_f32 v64, v64, v137\nv_mul_f32 v65, v65, v137\n"
    "v_mul_f32 v66, v66, v137\nv_mul_f32 v67, v67, v137\nv_cvt_pk_bf16_f32 v64, v64, v65\nv_cvt_pk_bf16_f32 v65, v66, v67\nds_write_b64 v27, v[64:65] offset:0\n"
    "s_waitcnt lgkmcnt(2)\nv_mfma_f32_32x32x16_bf16 v[96:111], v[208:211], v[168:171], v[96:111]\nv_mul_f32 v68, v68, v137\nv_mul_f32 v69, v69, v137\nv_mul_f32 v70, v70, v137\n"
    "v_mul_f32 v71, v71, v137\nv_cvt_pk_bf16_f32 v68, v68, v69\nv_cvt_pk_bf16_f32 v69, v70, v71\nds_write_b64 v27, v[68:69] offset:16\ns_waitcnt lgkmcnt(0)\n"
    "v_mfma_f32_32x32x16_bf16 v[96:111], v[212:215], v[172:175], v[96:111]\nv_mul_f32 v72, v72, v137\nv_mul_f32 v73, v73, v137\nv_mul_f32 v74, v74, v137\n"
    "v_mul_f32 v75, v75, v137\nv_cvt_pk_bf16_f32 v72, v72, v73\nv_cvt_pk_bf16_f32 v73, v74, v75\nds_write_b64 v27, v[72:73] offset:32\n"
    "v_mfma_f32_32x32x16_bf16 v[96:111], v[132:135], v[120:123], v[96:111]\nv_mul_f32 v76, v76, v137\nv_mul_f32 v77, v77, v137\nv_mul_f32 v78, v78, v137\n"
    "v_mul_f32 v79, v79, v137\nv_cvt_pk_bf16_f32 v76, v76, v77\nv_cvt_pk_bf16_f32 v77, v78, v79\nds_write_b64 v27, v[76:77] offset:48\n"
    "v_pk_mul_f32 v[80:81], v[80:81], v[136:137] op_sel:[0,1]\nv_pk_mul_f32 v[82:83], v[82:83], v[136:137] op_sel:[0,1]\nv_cvt_pk_bf16_f32 v80, v80, v81\n"
    "v_cvt_pk_bf16_f32 v81, v82, v83\nds_write_b64 v27, v[80:81] offset:64\nv_pk_mul_f32 v[84:85], v[84:85], v[136:137] op_sel:[0,1]\n"
    "v_pk_mul_f32 v[86:87], v[86:87], v[136:137] op_sel:[0,1]\nv_cvt_pk_bf16_f32 v84, v84, v85\nv_cvt_pk_bf16_f32 v85, v86, v87\nds_write_b64 v27, v[84:85] offset:80\n"
    "v_pk_mul_f32 v[88:89], v[88:89], v[136:137] op_sel:[0,1]\nv_pk_mul_f32 v[90:91], v[90:91], v[136:137] op_sel:[0,1]\nv_cvt_pk_bf16_f32 v88, v88, v89\n"
    "v_cvt_pk_bf16_f32 v89, v90, v91\nds_write_b64 v27, v[88:89] offset:96\nv_pk_mul_f32 v[92:93], v[92:93], v[136:137] op_sel:[0,1]\n"
    "v_pk_mul_f32 v[94:95], v[94:95], v[136:137] op_sel:[0,1]\nv_cvt_pk_bf16_f32 v92, v92, v93\nv_cvt_pk_bf16_f32 v93, v94, v95\nds_write_b64 v27, v[92:93] offset:112\n"
    "v_pk_mul_f32 v[96:97], v[96:97], v[136:137] op_sel:[0,1]\nv_pk_mul_f32 v[98:99], v[98:99], v[136:137] op_sel:[0,1]\nv_cvt_pk_bf16_f32 v96, v96, v97\n"
    "v_cvt_pk_bf16_f32 v97, v98, v99\nds_write_b64 v27, v[96:97] offset:128\nv_pk_mul_f32 v[100:101], v[100:101], v[136:137] op_sel:[0,1]\n"
    "v_pk_mul_f32 v[102:103], v[102:103], v[136:137] op_sel:[0,1]\nv_cvt_pk_bf16_f32 v100, v100, v101\nv_cvt_pk_bf16_f32 v101, v102, v103\n"
    "ds_write_b64 v27, v[100:101] offset:144\nv_pk_mul_f32 v[104:105], v[104:105], v[136:137] op_sel:[0,1]\nv_pk_mul_f32 v[106:107], v[106:107], v[136:137] op_sel:[0,1]\n"
    "v_cvt_pk_bf16_f32 v104, v104, v105\nv_cvt_pk_bf16_f32 v105, v106, v107\nds_write_b64 v27, v[104:105] offset:160\n"
    "v_pk_mul_f32 v[108:109], v[108:109], v[136:137] op_sel:[0,1]\nv_pk_mul_f32 v[110:111], v[110:111], v[136:137] op_sel:[0,1]\nv_cvt_pk_bf16_f32 v108, v108, v109\n"
    "v_cvt_pk_bf16_f32 v109, v110, v111\nds_write_b64 v27, v[108:109] offset:176\ns_branch .LGL68\n.Lfs32:\ns_mov_b32 s80, 0x100000\n.Lfl34:\ns_sleep 1\nds_read_b32 v31, v30\n"
    "s_waitcnt lgkmcnt(0)\nv_cmp_gt_i32 vcc, 2, v31\ns_cbranch_vccz .Lfr33\ns_sub_u32 s80, s80, 1\ns_cbranch_scc0 .Lfl34\ns_branch .Lfr33\n.Lls39:\ns_sub_u32 s80, s80, 1\n"
    "s_cbranch_scc1 .Llo38\ns_mov_b64 s[82:83], 0\nv_max3_u32 v60, v208, v209, v210\nv_max_u32 v60, v60, v211\nv_cmp_eq_u32 vcc, -1, v60\ns_nop 0\n"
    "s_or_b64 s[82:83], s[82:83], vcc\ns_cbranch_vccz .Lag40\ns_mov_b64 exec, vcc\nglobal_load_dwordx4 v[208:211], v20, s[32:33] offset:-4096 sc1\ns_mov_b64 exec, -1\n.Lag40:\n"
    "v_max3_u32 v60, v212, v213, v214\nv_max_u32 v60, v60, v215\nv_cmp_eq_u32 vcc, -1, v60\ns_nop 0\ns_or_b64 s[82:83], s[82:83], vcc\ns_cbranch_vccz .Lag41\n"
    "s_mov_b64 exec, vcc\nglobal_load_dwordx4 v[212:215], v22, s[32:33] offset:-4096 sc1\ns_mov_b64 exec, -1\n.Lag41:\nv_max3_u32 v60, v216, v217, v218\n"
    "v_max_u32 v60, v60, v219\nv_cmp_eq_u32 vcc, -1, v60\ns_nop 0\ns_or_b64 s[82:83], s[82:83], vcc\ns_cbranch_vccz .Lag42\ns_mov_b64 exec, vcc\n"
    "global_load_dwordx4 v[216:219], v23, s[32:33] offset:-4096 sc1\ns_mov_b64 exec, -1\n.Lag42:\nv_max3_u32 v60, v220, v221, v222\nv_max_u32 v60, v60, v223\n"
    "v_cmp_eq_u32 vcc, -1, v60\ns_nop 0\ns_or_b64 s[82:83], s[82:83], vcc\ns_cbranch_vccz .Lag43\ns_mov_b64 exec, vcc\n"
    "global_load_dwordx4 v[220:223], v24, s[32:33] offset:-4096 sc1\ns_mov_b64 exec, -1\n.Lag43:\nv_max3_u32 v60, v224, v225, v226\nv_max_u32 v60, v60, v227\n"
    "v_cmp_eq_u32 vcc, -1, v60\ns_nop 0\ns_or_b64 s[82:83], s[82:83], vcc\ns_cbranch_vccz .Lag44\ns_mov_b64 exec, vcc\n"
    "global_load_dwordx4 v[224:227], v25, s[32:33] offset:-4096 sc1\ns_mov_b64 exec, -1\n.Lag44:\nv_max3_u32 v60, v228, v229, v230\nv_max_u32 v60, v60, v231\n"
    "v_cmp_eq_u32 vcc, -1, v60\ns_nop 0\ns_or_b64 s[82:83], s[82:83], vcc\ns_cbranch_vccz .Lag45\ns_mov_b64 exec, vcc\n"
    "global_load_dwordx4 v[228:231], v26, s[32:33] offset:-4096 sc1\ns_mov_b64 exec, -1\n.Lag45:\nv_max3_u32 v60, v232, v233, v234\nv_max_u32 v60, v60, v235\n"
    "v_cmp_eq_u32 vcc, -1, v60\ns_nop 0\ns_or_b64 s[82:83], s[82:83], vcc\ns_cbranch_vccz .Lag46\ns_mov_b64 exec, vcc\n"
    "global_load_dwordx4 v[232:235], v27, s[32:33] offset:-4096 sc1\ns_mov_b64 exec, -1\n.Lag46:\nv_max3_u32 v60, v236, v237, v238\nv_max_u32 v60, v60, v239\n"
    "v_cmp_eq_u32 vcc, -1, v60\ns_nop 0\ns_or_b64 s[82:83], s[82:83], vcc\ns_cbranch_vccz .Lag47\ns_mov_b64 exec, vcc\n"
    "global_load_dwordx4 v[236:239], v28, s[32:33] offset:-4096 sc1\ns_mov_b64 exec, -1\n.Lag47:\nv_max3_u32 v60, v240, v241, v242\nv_max_u32 v60, v60, v243\n"
    "v_cmp_eq_u32 vcc, -1, v60\ns_nop 0\ns_or_b64 s[82:83], s[82:83], vcc\ns_cbranch_vccz .Lag48\ns_mov_b64 exec, vcc\nglobal_load_dwordx4 v[240:243], v21, s[32:33] sc1\n"
    "s_mov_b64 exec, -1\n.Lag48:\ns_cmp_eq_u64 s[82:83], 0\ns_cbranch_scc1 .Llo38\ns_waitcnt vmcnt(0)\ns_branch .Lln37\n.Lxs59:\ns_mov_b32 s80, 0x10000\n.Lxl61:\n"
    "v_pk_max_u16 v62, v212, v213\nv_pk_max_u16 v62, v62, v214\nv_pk_max_u16 v62, v62, v215\nv_pk_max_u16 v62, v62, v216\nv_pk_max_u16 v62, v62, v217\n"
    "v_pk_max_u16 v62, v62, v218\nv_pk_max_u16 v62, v62, v219\nv_pk_max_u16 v62, v62, v220\nv_pk_max_u16 v62, v62, v221\nv_pk_max_u16 v62, v62, v222\n"
    "v_pk_max_u16 v62, v62, v223\nv_pk_max_u16 v62, v62, v224\nv_pk_max_u16 v62, v62, v225\nv_pk_max_u16 v62, v62, v226\nv_pk_max_u16 v62, v62, v227\n"
    "v_pk_max_u16 v62, v62, v62 op_sel:[0,1] op_sel_hi:[1,0]\nv_cmp_eq_u32 vcc, -1, v62\ns_cbranch_vccz .Lxr60\ns_sub_u32 s80, s80, 1\ns_cbranch_scc1 .Lxr60\ns_sleep 2\n"
    "s_add_u32 m0, s65, 1024\ns_nop 0\nglobal_load_lds_dwordx4 v27, s[60:61] offset:0 sc1\ns_add_u32 m0, s65, 0\ns_nop 0\nglobal_load_lds_dwordx4 v27, s[60:61] offset:2048 sc1\n"
    "s_add_u32 m0, s65, 3072\ns_nop 0\nglobal_load_lds_dwordx4 v9, s[60:61] offset:0 sc1\ns_add_u32 m0, s65, 2048\ns_nop 0\n"
    "global_load_lds_dwordx4 v9, s[60:61] offset:2048 sc1\ns_waitcnt vmcnt(0)\nds_read_b64_tr_b16 v[212:213], v29 offset:0\nds_read_b64_tr_b16 v[214:215], v29 offset:128\n"
    "ds_read_b64_tr_b16 v[216:217], v29 offset:1024\nds_read_b64_tr_b16 v[218:219], v29 offset:1152\nds_read_b64_tr_b16 v[220:221], v29 offset:2048\n"
    "ds_read_b64_tr_b16 v[222:223], v29 offset:2176\nds_read_b64_tr_b16 v[224:225], v29 offset:3072\nds_read_b64_tr_b16 v[226:227], v29 offset:3200\ns_waitcnt lgkmcnt(0)\n"
    "s_branch .Lxl61\n.Lps62:\nv_max3_u32 v62, v228, v229, v230\nv_max3_u32 v62, v62, v231, v232\nv_max3_u32 v62, v62, v233, v234\nv_max3_u32 v62, v62, v235, v236\n"
    "v_max3_u32 v62, v62, v237, v238\nv_max_u32 v62, v62, v239\nv_cmp_eq_u32 vcc, -1, v62\ns_cbranch_vccz .Lpr63\ns_sub_u32 s80, s80, 1\ns_cbranch_scc1 .Lpr63\n"
    "global_load_dword v228, v37, s[32:33] offset:0 sc1\nglobal_load_dword v229, v37, s[32:33] offset:256 sc1\nglobal_load_dword v230, v37, s[32:33] offset:512 sc1\n"
    "global_load_dword v231, v37, s[32:33] offset:768 sc1\nglobal_load_dword v232, v37, s[32:33] offset:1024 sc1\nglobal_load_dword v233, v37, s[32:33] offset:1280 sc1\n"
    "global_load_dword v234, v37, s[32:33] offset:1536 sc1\nglobal_load_dword v235, v37, s[32:33] offset:1792 sc1\nglobal_load_dword v236, v38, s[32:33] offset:0 sc1\n"
    "global_load_dword v237, v38, s[32:33] offset:128 sc1\nglobal_load_dword v238, v38, s[32:33] offset:256 sc1\nglobal_load_dword v239, v38, s[32:33] offset:384 sc1\n"
    "s_waitcnt vmcnt(0)\ns_branch .Lpl64\n.LL76:\ns_sub_u32 s80, s80, 1\ns_cbranch_scc1 .LL75\ns_waitcnt lgkmcnt(0)\nv_max3_u32 v22, v208, v209, v210\nv_max_u32 v22, v22, v211\n"
    "v_cmp_eq_u32 vcc, -1, v22\ns_nop 0\ns_or_b64 s[82:83], s[82:83], vcc\ns_cbranch_vccz .Lag77\ns_mov_b64 exec, vcc\n"
    "global_load_dwordx4 v[208:211], v182, s[32:33] offset:-4096 sc1\ns_mov_b64 exec, -1\n.Lag77:\nv_max3_u32 v22, v212, v213, v214\nv_max_u32 v22, v22, v215\n"
    "v_cmp_eq_u32 vcc, -1, v22\ns_nop 0\ns_or_b64 s[82:83], s[82:83], vcc\ns_cbranch_vccz .Lag78\ns_mov_b64 exec, vcc\n"
    "global_load_dwordx4 v[212:215], v182, s[32:33] offset:0 sc1\ns_mov_b64 exec, -1\n.Lag78:\nv_max3_u32 v22, v216, v217, v218\nv_max_u32 v22, v22, v219\n"
    "v_cmp_eq_u32 vcc, -1, v22\ns_nop 0\ns_or_b64 s[82:83], s[82:83], vcc\ns_cbranch_vccz .Lag79\ns_mov_b64 exec, vcc\n"
    "global_load_dwordx4 v[216:219], v183, s[32:33] offset:-4096 sc1\ns_mov_b64 exec, -1\n.Lag79:\nv_max3_u32 v22, v220, v221, v222\nv_max_u32 v22, v22, v223\n"
    "v_cmp_eq_u32 vcc, -1, v22\ns_nop 0\ns_or_b64 s[82:83], s[82:83], vcc\ns_cbranch_vccz .Lag80\ns_mov_b64 exec, vcc\n"
    "global_load_dwordx4 v[220:223], v183, s[32:33] offset:0 sc1\ns_mov_b64 exec, -1\n.Lag80:\nv_max3_u32 v22, v224, v225, v226\nv_max_u32 v22, v22, v227\n"
    "v_cmp_eq_u32 vcc, -1, v22\ns_nop 0\ns_or_b64 s[82:83], s[82:83], vcc\ns_cbranch_vccz .Lag81\ns_mov_b64 exec, vcc\n"
    "global_load_dwordx4 v[224:227], v184, s[32:33] offset:-4096 sc1\ns_mov_b64 exec, -1\n.Lag81:\nv_max3_u32 v22, v228, v229, v230\nv_max_u32 v22, v22, v231\n"
    "v_cmp_eq_u32 vcc, -1, v22\ns_nop 0\ns_or_b64 s[82:83], s[82:83], vcc\ns_cbranch_vccz .Lag82\ns_mov_b64 exec, vcc\n"
    "global_load_dwordx4 v[228:231], v184, s[32:33] offset:0 sc1\ns_mov_b64 exec, -1\n.Lag82:\nv_max3_u32 v22, v232, v233, v234\nv_max_u32 v22, v22, v235\n"
    "v_cmp_eq_u32 vcc, -1, v22\ns_nop 0\ns_or_b64 s[82:83], s[82:83], vcc\ns_cbranch_vccz .Lag83\ns_mov_b64 exec, vcc\n"
    "global_load_dwordx4 v[232:235], v185, s[32:33] offset:-4096 sc1\ns_mov_b64 exec, -1\n.Lag83:\nv_max3_u32 v22, v236, v237, v238\nv_max_u32 v22, v22, v239\n"
    "v_cmp_eq_u32 vcc, -1, v22\ns_nop 0\ns_or_b64 s[82:83], s[82:83], vcc\ns_cbranch_vccz .Lag84\ns_mov_b64 exec, vcc\n"
    "global_load_dwordx4 v[236:239], v185, s[32:33] offset:0 sc1\ns_mov_b64 exec, -1\n.Lag84:\ns_waitcnt vmcnt(0)\ns_branch .LL74\n.Lls94:\ns_sub_u32 s80, s80, 1\n"
    "s_cbranch_scc1 .Llo93\ns_mov_b64 s[82:83], 0\nv_max3_u32 v60, v208, v209, v210\nv_max_u32 v60, v60, v211\nv_cmp_eq_u32 vcc, -1, v60\ns_nop 0\n"
    "s_or_b64 s[82:83], s[82:83], vcc\ns_cbranch_vccz .Lag95\ns_mov_b64 exec, vcc\nglobal_load_dwordx4 v[208:211], v20, s[32:33] offset:-4096 sc1\ns_mov_b64 exec, -1\n.Lag95:\n"
    "v_max3_u32 v60, v212, v213, v214\nv_max_u32 v60, v60, v215\nv_cmp_eq_u32 vcc, -1, v60\ns_nop 0\ns_or_b64 s[82:83], s[82:83], vcc\ns_cbranch_vccz .Lag96\n"
    "s_mov_b64 exec, vcc\nglobal_load_dwordx4 v[212:215], v22, s[32:33] offset:-4096 sc1\ns_mov_b64 exec, -1\n.Lag96:\nv_max3_u32 v60, v216, v217, v218\n"
    "v_max_u32 v60, v60, v219\nv_cmp_eq_u32 vcc, -1, v60\ns_nop 0\ns_or_b64 s[82:83], s[82:83], vcc\ns_cbranch_vccz .Lag97\ns_mov_b64 exec, vcc\n"
    "global_load_dwordx4 v[216:219], v23, s[32:33] offset:-4096 sc1\ns_mov_b64 exec, -1\n.Lag97:\nv_max3_u32 v60, v220, v221, v222\nv_max_u32 v60, v60, v223\n"
    "v_cmp_eq_u32 vcc, -1, v60\ns_nop 0\ns_or_b64 s[82:83], s[82:83], vcc\ns_cbranch_vccz .Lag98\ns_mov_b64 exec, vcc\n"
    "global_load_dwordx4 v[220:223], v24, s[32:33] offset:-4096 sc1\ns_mov_b64 exec, -1\n.Lag98:\nv_max3_u32 v60, v224, v225, v226\nv_max_u32 v60, v60, v227\n"
    "v_cmp_eq_u32 vcc, -1, v60\ns_nop 0\ns_or_b64 s[82:83], s[82:83], vcc\ns_cbranch_vccz .Lag99\ns_mov_b64 exec, vcc\n"
    "global_load_dwordx4 v[224:227], v25, s[32:33] offset:-4096 sc1\ns_mov_b64 exec, -1\n.Lag99:\nv_max3_u32 v60, v228, v229, v230\nv_max_u32 v60, v60, v231\n"
    "v_cmp_eq_u32 vcc, -1, v60\ns_nop 0\ns_or_b64 s[82:83], s[82:83], vcc\ns_cbranch_vccz .Lag100\ns_mov_b64 exec, vcc\n"
    "global_load_dwordx4 v[228:231], v26, s[32:33] offset:-4096 sc1\ns_mov_b64 exec, -1\n.Lag100:\nv_max3_u32 v60, v232, v233, v234\nv_max_u32 v60, v60, v235\n"
    "v_cmp_eq_u32 vcc, -1, v60\ns_nop 0\ns_or_b64 s[82:83], s[82:83], vcc\ns_cbranch_vccz .Lag101\ns_mov_b64 exec, vcc\n"
    "global_load_dwordx4 v[232:235], v27, s[32:33] offset:-4096 sc1\ns_mov_b64 exec, -1\n.Lag101:\nv_max3_u32 v60, v236, v237, v238\nv_max_u32 v60, v60, v239\n"
    "v_cmp_eq_u32 vcc, -1, v60\ns_nop 0\ns_or_b64 s[82:83], s[82:83], vcc\ns_cbranch_vccz .Lag102\ns_mov_b64 exec, vcc\n"
    "global_load_dwordx4 v[236:239], v28, s[32:33] offset:-4096 sc1\ns_mov_b64 exec, -1\n.Lag102:\nv_max3_u32 v60, v240, v241, v242\nv_max_u32 v60, v60, v243\n"
    "v_cmp_eq_u32 vcc, -1, v60\ns_nop 0\ns_or_b64 s[82:83], s[82:83], vcc\ns_cbranch_vccz .Lag103\ns_mov_b64 exec, vcc\nglobal_load_dwordx4 v[240:243], v21, s[32:33] sc1\n"
    "s_mov_b64 exec, -1\n.Lag103:\ns_cmp_eq_u64 s[82:83], 0\ns_cbranch_scc1 .Llo93\ns_waitcnt vmcnt(0)\ns_branch .Lln92\n.Lfs107:\ns_mov_b32 s80, 0x100000\n.Lfl109:\ns_sleep 1\n"
    "ds_read_b32 v241, v11\ns_waitcnt lgkmcnt(0)\nv_cmp_gt_i32 vcc, v13, v241\ns_cbranch_vccz .Lfr108\ns_sub_u32 s80, s80, 1\ns_cbranch_scc0 .Lfl109\ns_branch .Lfr108\n"
    ".Lxs114:\ns_mov_b32 s80, 0x10000\n.Lxl116:\nv_pk_max_u16 v138, v144, v145\nv_pk_max_u16 v138, v138, v146\nv_pk_max_u16 v138, v138, v147\nv_pk_max_u16 v138, v138, v148\n"
    "v_pk_max_u16 v138, v138, v149\nv_pk_max_u16 v138, v138, v150\nv_pk_max_u16 v138, v138, v151\nv_pk_max_u16 v138, v138, v152\nv_pk_max_u16 v138, v138, v153\n"
    "v_pk_max_u16 v138, v138, v154\nv_pk_max_u16 v138, v138, v155\nv_pk_max_u16 v138, v138, v156\nv_pk_max_u16 v138, v138, v157\nv_pk_max_u16 v138, v138, v158\n"
    "v_pk_max_u16 v138, v138, v159\nv_pk_max_u16 v138, v138, v138 op_sel:[0,1] op_sel_hi:[1,0]\nv_cmp_eq_u32 vcc, -1, v138\ns_cbranch_vccz .Lxr115\ns_sub_u32 s80, s80, 1\n"
    "s_cbranch_scc1 .Lxr115\ns_sleep 2\ns_add_u32 m0, s87, 0\ns_add_u32 s52, s82, s44\ns_addc_u32 s53, s83, 0\nglobal_load_lds_dwordx4 v35, s[52:53] offset:0 sc1\n"
    "global_load_lds_dwordx4 v35, s[52:53] offset:1024 sc1\nglobal_load_lds_dwordx4 v35, s[52:53] offset:2048 sc1\nglobal_load_lds_dwordx4 v35, s[52:53] offset:3072 sc1\n"
    "s_waitcnt vmcnt(0)\nds_read_b64_tr_b16 v[144:145], v29 offset:0\nds_read_b64_tr_b16 v[146:147], v29 offset:256\nds_read_b64_tr_b16 v[148:149], v29 offset:1024\n"
    "ds_read_b64_tr_b16 v[150:151], v29 offset:1280\nds_read_b64_tr_b16 v[152:153], v29 offset:2048\nds_read_b64_tr_b16 v[154:155], v29 offset:2304\n"
    "ds_read_b64_tr_b16 v[156:157], v29 offset:3072\nds_read_b64_tr_b16 v[158:159], v29 offset:3328\ns_waitcnt lgkmcnt(0)\ns_branch .Lxl116\n.Lxs117:\ns_mov_b32 s80, 0x10000\n"
    ".Lxl119:\nv_pk_max_u16 v138, v160, v161\nv_pk_max_u16 v138, v138, v162\nv_pk_max_u16 v138, v138, v163\nv_pk_max_u16 v138, v138, v164\nv_pk_max_u16 v138, v138, v165\n"
    "v_pk_max_u16 v138, v138, v166\nv_pk_max_u16 v138, v138, v167\nv_pk_max_u16 v138, v138, v168\nv_pk_max_u16 v138, v138, v169\nv_pk_max_u16 v138, v138, v170\n"
    "v_pk_max_u16 v138, v138, v171\nv_pk_max_u16 v138, v138, v172\nv_pk_max_u16 v138, v138, v173\nv_pk_max_u16 v138, v138, v174\nv_pk_max_u16 v138, v138, v175\n"
    "v_pk_max_u16 v138, v138, v138 op_sel:[0,1] op_sel_hi:[1,0]\nv_cmp_eq_u32 vcc, -1, v138\ns_cbranch_vccz .Lxr118\ns_sub_u32 s80, s80, 1\ns_cbranch_scc1 .Lxr118\ns_sleep 2\n"
    "s_add_u32 m0, s87, 4096\ns_add_u32 s52, s82, s45\ns_addc_u32 s53, s83, 0\nglobal_load_lds_dwordx4 v35, s[52:53] offset:0 sc1\n"
    "global_load_lds_dwordx4 v35, s[52:53] offset:1024 sc1\nglobal_load_lds_dwordx4 v35, s[52:53] offset:2048 sc1\nglobal_load_lds_dwordx4 v35, s[52:53] offset:3072 sc1\n"
    "s_waitcnt vmcnt(0)\nds_read_b64_tr_b16 v[160:161], v29 offset:4096\nds_read_b64_tr_b16 v[162:163], v29 offset:4352\nds_read_b64_tr_b16 v[164:165], v29 offset:5120\n"
    "ds_read_b64_tr_b16 v[166:167], v29 offset:5376\nds_read_b64_tr_b16 v[168:169], v29 offset:6144\nds_read_b64_tr_b16 v[170:171], v29 offset:6400\n"
    "ds_read_b64_tr_b16 v[172:173], v29 offset:7168\nds_read_b64_tr_b16 v[174:175], v29 offset:7424\ns_waitcnt lgkmcnt(0)\ns_branch .Lxl119\n.Lxs120:\ns_mov_b32 s80, 0x10000\n"
    ".Lxl122:\nv_pk_max_u16 v138, v144, v145\nv_pk_max_u16 v138, v138, v146\nv_pk_max_u16 v138, v138, v147\nv_pk_max_u16 v138, v138, v148\nv_pk_max_u16 v138, v138, v149\n"
    "v_pk_max_u16 v138, v138, v150\nv_pk_max_u16 v138, v138, v151\nv_pk_max_u16 v138, v138, v152\nv_pk_max_u16 v138, v138, v153\nv_pk_max_u16 v138, v138, v154\n"
    "v_pk_max_u16 v138, v138, v155\nv_pk_max_u16 v138, v138, v156\nv_pk_max_u16 v138, v138, v157\nv_pk_max_u16 v138, v138, v158\nv_pk_max_u16 v138, v138, v159\n"
    "v_pk_max_u16 v138, v138, v138 op_sel:[0,1] op_sel_hi:[1,0]\nv_cmp_eq_u32 vcc, -1, v138\ns_cbranch_vccz .Lxr121\ns_sub_u32 s80, s80, 1\ns_cbranch_scc1 .Lxr121\ns_sleep 2\n"
    "s_add_u32 m0, s87, 8192\ns_add_u32 s52, s82, s46\ns_addc_u32 s53, s83, 0\nglobal_load_lds_dwordx4 v35, s[52:53] offset:0 sc1\n"
    "global_load_lds_dwordx4 v35, s[52:53] offset:1024 sc1\nglobal_load_lds_dwordx4 v35, s[52:53] offset:2048 sc1\nglobal_load_lds_dwordx4 v35, s[52:53] offset:3072 sc1\n"
    "s_waitcnt vmcnt(0)\nds_read_b64_tr_b16 v[144:145], v29 offset:8192\nds_read_b64_tr_b16 v[146:147], v29 offset:8448\nds_read_b64_tr_b16 v[148:149], v29 offset:9216\n"
    "ds_read_b64_tr_b16 v[150:151], v29 offset:9472\nds_read_b64_tr_b16 v[152:153], v29 offset:10240\nds_read_b64_tr_b16 v[154:155], v29 offset:10496\n"
    "ds_read_b64_tr_b16 v[156:157], v29 offset:11264\nds_read_b64_tr_b16 v[158:159], v29 offset:11520\ns_waitcnt lgkmcnt(0)\ns_branch .Lxl122\n.Lxs123:\ns_mov_b32 s80, 0x10000\n"
    ".Lxl125:\nv_pk_max_u16 v138, v160, v161\nv_pk_max_u16 v138, v138, v162\nv_pk_max_u16 v138, v138, v163\nv_pk_max_u16 v138, v138, v164\nv_pk_max_u16 v138, v138, v165\n"
    "v_pk_max_u16 v138, v138, v166\nv_pk_max_u16 v138, v138, v167\nv_pk_max_u16 v138, v138, v168\nv_pk_max_u16 v138, v138, v169\nv_pk_max_u16 v138, v138, v170\n"
    "v_pk_max_u16 v138, v138, v171\nv_pk_max_u16 v138, v138, v172\nv_pk_max_u16 v138, v138, v173\nv_pk_max_u16 v138, v138, v174\nv_pk_max_u16 v138, v138, v175\n"
    "v_pk_max_u16 v138, v138, v138 op_sel:[0,1] op_sel_hi:[1,0]\nv_cmp_eq_u32 vcc, -1, v138\ns_cbranch_vccz .Lxr124\ns_sub_u32 s80, s80, 1\ns_cbranch_scc1 .Lxr124\ns_sleep 2\n"
    "s_add_u32 m0, s87, 0\ns_add_u32 s52, s82, s47\ns_addc_u32 s53, s83, 0\nglobal_load_lds_dwordx4 v35, s[52:53] offset:0 sc1\n"
    "global_load_lds_dwordx4 v35, s[52:53] offset:1024 sc1\nglobal_load_lds_dwordx4 v35, s[52:53] offset:2048 sc1\nglobal_load_lds_dwordx4 v35, s[52:53] offset:3072 sc1\n"
    "s_waitcnt vmcnt(0)\nds_read_b64_tr_b16 v[160:161], v29 offset:0\nds_read_b64_tr_b16 v[162:163], v29 offset:256\nds_read_b64_tr_b16 v[164:165], v29 offset:1024\n"
    "ds_read_b64_tr_b16 v[166:167], v29 offset:1280\nds_read_b64_tr_b16 v[168:169], v29 offset:2048\nds_read_b64_tr_b16 v[170:171], v29 offset:2304\n"
    "ds_read_b64_tr_b16 v[172:173], v29 offset:3072\nds_read_b64_tr_b16 v[174:175], v29 offset:3328\ns_waitcnt lgkmcnt(0)\ns_branch .Lxl125\n.Lxs126:\ns_mov_b32 s80, 0x10000\n"
    ".Lxl128:\nv_pk_max_u16 v138, v144, v145\nv_pk_max_u16 v138, v138, v146\nv_pk_max_u16 v138, v138, v147\nv_pk_max_u16 v138, v138, v148\nv_pk_max_u16 v138, v138, v149\n"
    "v_pk_max_u16 v138, v138, v150\nv_pk_max_u16 v138, v138, v151\nv_pk_max_u16 v138, v138, v152\nv_pk_max_u16 v138, v138, v153\nv_pk_max_u16 v138, v138, v154\n"
    "v_pk_max_u16 v138, v138, v155\nv_pk_max_u16 v138, v138, v156\nv_pk_max_u16 v138, v138, v157\nv_pk_max_u16 v138, v138, v158\nv_pk_max_u16 v138, v138, v159\n"
    "v_pk_max_u16 v138, v138, v138 op_sel:[0,1] op_sel_hi:[1,0]\nv_cmp_eq_u32 vcc, -1, v138\ns_cbranch_vccz .Lxr127\ns_sub_u32 s80, s80, 1\ns_cbranch_scc1 .Lxr127\ns_sleep 2\n"
    "s_add_u32 m0, s87, 4096\ns_add_u32 s52, s82, s48\ns_addc_u32 s53, s83, 0\nglobal_load_lds_dwordx4 v35, s[52:53] offset:0 sc1\n"
    "global_load_lds_dwordx4 v35, s[52:53] offset:1024 sc1\nglobal_load_lds_dwordx4 v35, s[52:53] offset:2048 sc1\nglobal_load_lds_dwordx4 v35, s[52:53] offset:3072 sc1\n"
    "s_waitcnt vmcnt(0)\nds_read_b64_tr_b16 v[144:145], v29 offset:4096\nds_read_b64_tr_b16 v[146:147], v29 offset:4352\nds_read_b64_tr_b16 v[148:149], v29 offset:5120\n"
    "ds_read_b64_tr_b16 v[150:151], v29 offset:5376\nds_read_b64_tr_b16 v[152:153], v29 offset:6144\nds_read_b64_tr_b16 v[154:155], v29 offset:6400\n"
    "ds_read_b64_tr_b16 v[156:157], v29 offset:7168\nds_read_b64_tr_b16 v[158:159], v29 offset:7424\ns_waitcnt lgkmcnt(0)\ns_branch .Lxl128\n.Lxs129:\ns_mov_b32 s80, 0x10000\n"
    ".Lxl131:\nv_pk_max_u16 v138, v160, v161\nv_pk_max_u16 v138, v138, v162\nv_pk_max_u16 v138, v138, v163\nv_pk_max_u16 v138, v138, v164\nv_pk_max_u16 v138, v138, v165\n"
    "v_pk_max_u16 v138, v138, v166\nv_pk_max_u16 v138, v138, v167\nv_pk_max_u16 v138, v138, v168\nv_pk_max_u16 v138, v138, v169\nv_pk_max_u16 v138, v138, v170\n"
    "v_pk_max_u16 v138, v138, v171\nv_pk_max_u16 v138, v138, v172\nv_pk_max_u16 v138, v138, v173\nv_pk_max_u16 v138, v138, v174\nv_pk_max_u16 v138, v138, v175\n"
    "v_pk_max_u16 v138, v138, v138 op_sel:[0,1] op_sel_hi:[1,0]\nv_cmp_eq_u32 vcc, -1, v138\ns_cbranch_vccz .Lxr130\ns_sub_u32 s80, s80, 1\ns_cbranch_scc1 .Lxr130\ns_sleep 2\n"
    "s_add_u32 m0, s87, 8192\ns_add_u32 s52, s82, s49\ns_addc_u32 s53, s83, 0\nglobal_load_lds_dwordx4 v35, s[52:53] offset:0 sc1\n"
    "global_load_lds_dwordx4 v35, s[52:53] offset:1024 sc1\nglobal_load_lds_dwordx4 v35, s[52:53] offset:2048 sc1\nglobal_load_lds_dwordx4 v35, s[52:53] offset:3072 sc1\n"
    "s_waitcnt vmcnt(0)\nds_read_b64_tr_b16 v[160:161], v29 offset:8192\nds_read_b64_tr_b16 v[162:163], v29 offset:8448\nds_read_b64_tr_b16 v[164:165], v29 offset:9216\n"
    "ds_read_b64_tr_b16 v[166:167], v29 offset:9472\nds_read_b64_tr_b16 v[168:169], v29 offset:10240\nds_read_b64_tr_b16 v[170:171], v29 offset:10496\n"
    "ds_read_b64_tr_b16 v[172:173], v29 offset:11264\nds_read_b64_tr_b16 v[174:175], v29 offset:11520\ns_waitcnt lgkmcnt(0)\ns_branch .Lxl131\n.Lxs132:\ns_mov_b32 s80, 0x10000\n"
    ".Lxl134:\nv_pk_max_u16 v138, v144, v145\nv_pk_max_u16 v138, v138, v146\nv_pk_max_u16 v138, v138, v147\nv_pk_max_u16 v138, v138, v148\nv_pk_max_u16 v138, v138, v149\n"
    "v_pk_max_u16 v138, v138, v150\nv_pk_max_u16 v138, v138, v151\nv_pk_max_u16 v138, v138, v152\nv_pk_max_u16 v138, v138, v153\nv_pk_max_u16 v138, v138, v154\n"
    "v_pk_max_u16 v138, v138, v155\nv_pk_max_u16 v138, v138, v156\nv_pk_max_u16 v138, v138, v157\nv_pk_max_u16 v138, v138, v158\nv_pk_max_u16 v138, v138, v159\n"
    "v_pk_max_u16 v138, v138, v138 op_sel:[0,1] op_sel_hi:[1,0]\nv_cmp_eq_u32 vcc, -1, v138\ns_cbranch_vccz .Lxr133\ns_sub_u32 s80, s80, 1\ns_cbranch_scc1 .Lxr133\ns_sleep 2\n"
    "s_add_u32 m0, s87, 0\ns_add_u32 s52, s82, s50\ns_addc_u32 s53, s83, 0\nglobal_load_lds_dwordx4 v35, s[52:53] offset:0 sc1\n"
    "global_load_lds_dwordx4 v35, s[52:53] offset:1024 sc1\nglobal_load_lds_dwordx4 v35, s[52:53] offset:2048 sc1\nglobal_load_lds_dwordx4 v35, s[52:53] offset:3072 sc1\n"
    "s_waitcnt vmcnt(0)\nds_read_b64_tr_b16 v[144:145], v29 offset:0\nds_read_b64_tr_b16 v[146:147], v29 offset:256\nds_read_b64_tr_b16 v[148:149], v29 offset:1024\n"
    "ds_read_b64_tr_b16 v[150:151], v29 offset:1280\nds_read_b64_tr_b16 v[152:153], v29 offset:2048\nds_read_b64_tr_b16 v[154:155], v29 offset:2304\n"
    "ds_read_b64_tr_b16 v[156:157], v29 offset:3072\nds_read_b64_tr_b16 v[158:159], v29 offset:3328\ns_waitcnt lgkmcnt(0)\ns_branch .Lxl134\n.Lxs135:\ns_mov_b32 s80, 0x10000\n"
    ".Lxl137:\nv_pk_max_u16 v138, v160, v161\nv_pk_max_u16 v138, v138, v162\nv_pk_max_u16 v138, v138, v163\nv_pk_max_u16 v138, v138, v164\nv_pk_max_u16 v138, v138, v165\n"
    "v_pk_max_u16 v138, v138, v166\nv_pk_max_u16 v138, v138, v167\nv_pk_max_u16 v138, v138, v168\nv_pk_max_u16 v138, v138, v169\nv_pk_max_u16 v138, v138, v170\n"
    "v_pk_max_u16 v138, v138, v171\nv_pk_max_u16 v138, v138, v172\nv_pk_max_u16 v138, v138, v173\nv_pk_max_u16 v138, v138, v174\nv_pk_max_u16 v138, v138, v175\n"
    "v_pk_max_u16 v138, v138, v138 op_sel:[0,1] op_sel_hi:[1,0]\nv_cmp_eq_u32 vcc, -1, v138\ns_cbranch_vccz .Lxr136\ns_sub_u32 s80, s80, 1\ns_cbranch_scc1 .Lxr136\ns_sleep 2\n"
    "s_add_u32 m0, s87, 4096\ns_add_u32 s52, s82, s51\ns_addc_u32 s53, s83, 0\nglobal_load_lds_dwordx4 v35, s[52:53] offset:0 sc1\n"
    "global_load_lds_dwordx4 v35, s[52:53] offset:1024 sc1\nglobal_load_lds_dwordx4 v35, s[52:53] offset:2048 sc1\nglobal_load_lds_dwordx4 v35, s[52:53] offset:3072 sc1\n"
    "s_waitcnt vmcnt(0)\nds_read_b64_tr_b16 v[160:161], v29 offset:4096\nds_read_b64_tr_b16 v[162:163], v29 offset:4352\nds_read_b64_tr_b16 v[164:165], v29 offset:5120\n"
    "ds_read_b64_tr_b16 v[166:167], v29 offset:5376\nds_read_b64_tr_b16 v[168:169], v29 offset:6144\nds_read_b64_tr_b16 v[170:171], v29 offset:6400\n"
    "ds_read_b64_tr_b16 v[172:173], v29 offset:7168\nds_read_b64_tr_b16 v[174:175], v29 offset:7424\ns_waitcnt lgkmcnt(0)\ns_branch .Lxl137\n.Lend:\n"
    :: "{s[0:1]}"(__builtin_amdgcn_kernarg_segment_ptr()), "{s2}"(blockIdx.x), "{v0}"(threadIdx.x), "{s[4:5]}"(&g_tabs),
       "i"((__attribute__((address_space(3))) char*)smem)
    : "v255", "s101", "vcc", "scc", "memory");
}

extern "C" void kernel_launch(void* const* d_in, const int* in_sizes, int n_in,
                              void* d_out, int out_size, void* d_ws, size_t ws_size,
                              hipStream_t stream) {
  Args a;
  a.x = (const u16*)d_in[0]; a.w = (const u16*)d_in[1]; a.wo = (const u16*)d_in[2];
  a.A_re = (const u16*)d_in[4]; a.A_im = (const u16*)d_in[5]; a.B_re = (const u16*)d_in[6]; a.B_im = (const u16*)d_in[7];
  a.C_re = (const u16*)d_in[8]; a.C_im = (const u16*)d_in[9]; a.log_dt = (const u16*)d_in[10]; a.Dskip = (const u16*)d_in[11];
  a.out = (u16*)d_out; a.ws = (char*)d_ws;
  k_all<<<256, 512, 0, stream>>>(a);
}
```

```cpp
#include <hip/hip_runtime.h>
#include <stdint.h>
typedef unsigned short u16;

struct Args { const u16 *x, *w, *wo, *A_re, *A_im, *B_re, *B_im, *C_re, *C_im, *log_dt, *Dskip; u16* out; char* ws; };

__global__ __launch_bounds__(512, 2) void k_all(const Args ar) {
  __shared__ __attribute__((aligned(128))) char smem[163840];
  asm volatile(
    ".pushsection .rodata\n.p2align 7\n.Ltabs:\n"
    ".short 1400,1400,1400,1400,1400,1400,1400,1400,1400,1400,1400,1400,1400,1400,1400,1400,1400,1400,1400,1400,1400,1400,1400,1400,1400,1400,1400,1400,1400,1400,1400,1400\n"
    ".short 1400,1400,1400,1400,1400,1400,1400,1400,1400,1400,1400,1400,1400,1400,1400,1400,1400,1400,1400,1400,1400,1400,1400,1400,1400,1400,1400,1400,1400,1400,1400,1400\n"
    ".short 1400,1400,1400,1400,1400,1400,1400,1400,1400,1400,1400,1400,1400,1400,1400,1400,1278,1274,1270,1266,1262,1258,1254,1250,1246,1242,1238,1234,1230,1226,1222,1218\n"
    ".short 1214,1210,1206,1202,1198,1194,1190,1186,1182,1178,1174,1170,1166,1162,1158,1154,1150,1146,1142,1138,1134,1130,1126,1122,1118,1114,1110,1106,1102,1098,1094,1090\n"
    ".short 1086,1082,1078,1074,1070,1066,1062,1058,1054,1050,1046,1042,1038,1034,1030,1026,1400,1400,1400,1400,1400,1400,1400,1400,1400,1400,1400,1400,1400,1400,1400,1400\n"
    ".short 1400,1400,1400,1400,1400,1400,1400,1400,1400,1400,1400,1400,1400,1400,1400,1400,1280,1276,1272,1268,1264,1260,1256,1252,1248,1244,1240,1236,1232,1228,1224,1220\n"
    ".short 1216,1212,1208,1204,1200,1196,1192,1188,1184,1180,1176,1172,1168,1164,1160,1156,1152,1148,1144,1140,1136,1132,1128,1124,1120,1116,1112,1108,1104,1100,1096,1092\n"
    ".short 1088,1084,1080,1076,1072,1068,1064,1060,1056,1052,1048,1044,1040,1036,1032,1028,1400,1400,1400,1400,1400,1400,1400,1400,1400,1400,1400,1400,1400,1400,1400,1400\n"
    ".short 1400,1400,1400,1400,1400,1400,1400,1400,1400,1400,1400,1400,1400,1400,1400,1400,1400,1024,1022,1018,1014,1010,1006,1002,998,994,990,986,982,978,974,970\n"
    ".short 966,962,958,954,950,946,942,938,934,930,926,922,918,914,910,906,902,898,894,890,886,882,878,874,870,866,862,858,854,850,846,842\n"
    ".short 838,834,830,826,822,818,814,810,806,802,798,794,790,786,782,778,1400,1400,1400,1400,1400,1400,1400,1400,1400,1400,1400,1400,1400,1400,1400,1400\n"
    ".short 1400,1400,1400,1400,1400,1400,1400,1400,1400,1400,1400,1400,1400,1400,1400,1400,1400,1024,1020,1016,1012,1008,1004,1000,996,992,988,984,980,976,972,968\n"
    ".short 964,960,956,952,948,944,940,936,932,928,924,920,916,912,908,904,900,896,892,888,884,880,876,872,868,864,860,856,852,848,844,840\n"
    ".short 836,832,828,824,820,816,812,808,804,800,796,792,788,784,780,776,1400,1400,1400,1400,1400,1400,1400,1400,1400,1400,1400,1400,1400,1400,1400,1400\n"
    ".short 1400,1400,1400,1400,1400,1400,1400,1400,1400,1400,1400,1400,1400,1400,1400,1400,1400,1400,774,774,774,772,772,772,772,772,770,770,770,768,768,768\n"
    ".short 768,766,764,764,764,762,760,760,760,758,756,756,756,754,752,752,752,750,748,748,748,746,744,744,744,742,740,740,740,738,736,736\n"
    ".short 736,734,732,732,732,730,728,728,728,726,724,724,724,722,720,720,720,718,716,716,716,714,712,712,712,710,708,708,708,706,704,704\n"
    ".short 704,702,700,700,700,698,696,696,696,694,692,692,692,690,688,688,688,686,684,684,684,682,680,680,680,678,676,676,676,674,672,672\n"
    ".short 672,670,668,668,668,666,664,664,664,662,660,660,660,658,656,656,656,654,652,652,652,650,648,648,648,646,644,644,644,642,640,640\n"
    ".short 640,638,636,636,636,634,632,632,632,630,628,628,628,626,624,624,624,622,620,620,620,618,616,616,616,614,612,612,612,610,608,608\n"
    ".short 608,606,604,604,604,602,600,600,600,598,596,596,596,594,592,592,592,590,588,588,588,586,584,584,584,582,580,580,580,578,576,576\n"
    ".short 576,574,572,572,572,570,568,568,568,566,564,564,564,562,560,560,560,558,556,556,556,554,552,552,552,550,548,548,548,546,544,544\n"
    ".short 544,542,540,540,540,538,536,536,536,534,532,532,532,530,528,528,528,526,524,524,524,522,520,520,520,518,516,516,516,514,512,512\n"
    ".short 510,508,506,504,502,500,498,496,494,492,490,488,486,484,482,480,478,476,474,472,470,468,466,464,462,460,458,456,454,452,450,448\n"
    ".short 446,444,442,440,438,436,434,432,430,428,426,424,422,420,418,416,414,412,410,408,406,404,402,400,398,396,394,392,390,388,386,384\n"
    ".short 382,380,378,376,374,372,370,368,366,364,362,360,358,356,354,352,350,348,346,344,342,340,338,336,334,332,330,328,326,324,322,320\n"
    ".short 318,316,314,312,310,308,306,304,302,300,298,296,294,292,290,288,286,284,282,280,278,276,274,272,270,268,266,264,262,260,258,256\n"
    ".short 254,252,250,248,246,244,242,240,238,236,234,232,230,228,226,224,222,220,218,216,214,212,210,208,206,204,202,200,198,196,194,192\n"
    ".short 190,188,186,184,182,180,178,176,174,172,170,168,166,164,162,160,158,156,154,152,150,148,146,144,142,140,138,136,134,132,130,128\n"
    ".short 126,124,122,120,118,116,114,112,110,108,106,104,102,100,98,96,94,92,90,88,86,84,82,80,78,76,74,72,70,68,66,64\n"
    ".short 62,60,58,56,54,52,50,48,46,44,42,40,38,36,34,32,30,28,26,24,22,20,18,16,14,12,10,8,6,4,2,0\n"
    ".short 1400,1400,1400,1400,1400,1400,1400,1400,1400,1400,1400,1400,1400,1400,1400,1400,1400,1400,1400,1400,1400,1400,1400,1400,1400,1400,1400,1400,1400,1400,1400,1400\n"
    ".short 0,2,4,6,16,18,20,22,32,34,36,38,48,50,52,54,64,66,68,70,80,82,84,86,96,98,100,102,112,114,116,118\n"
    ".short 128,130,132,134,144,146,148,150,160,162,164,166,176,178,180,182,192,194,196,198,208,210,212,214,224,226,228,230,240,242,244,246\n"
    ".short 8,10,12,14,24,26,28,30,40,42,44,46,56,58,60,62,72,74,76,78,88,90,92,94,104,106,108,110,120,122,124,126\n"
    ".short 136,138,140,142,152,154,156,158,168,170,172,174,184,186,188,190,200,202,204,206,216,218,220,222,232,234,236,238,248,250,252,254\n"
    ".short 514,516,518,1472,522,524,526,1474,530,532,534,1476,538,540,542,1478,546,548,550,1480,554,556,558,1482,562,564,566,1484,570,572,574,1486\n"
    ".short 578,580,582,1488,586,588,590,1490,594,596,598,1492,602,604,606,1494,610,612,614,1496,618,620,622,1498,626,628,630,1500,634,636,638,1502\n"
    ".short 1504,1504,1504,1504,1506,1506,1506,1506,1508,1508,1508,1508,1510,1510,1510,1510,1512,1512,1512,1512,1514,1514,1514,1514,1516,1516,1516,1516,1518,1518,1518,1518\n"
    ".short 1520,1520,1520,1520,1522,1522,1522,1522,1524,1524,1524,1524,1526,1526,1526,1526,1528,1528,1528,1528,1530,1530,1530,1530,1532,1532,1532,1532,1534,1534,1534,1534\n"
    ".short 512,768,896,1024,520,772,900,1026,528,776,904,1028,536,780,908,1030,544,784,912,1032,552,788,916,1034,560,792,920,1036,568,796,924,1038\n"
    ".short 576,800,928,1040,584,804,932,1042,592,808,936,1044,600,812,940,1046,608,816,944,1048,616,820,948,1050,624,824,952,1052,632,828,956,1054\n"
    ".short 1088,1152,1216,1280,1090,1154,1218,1442,1092,1156,1220,1444,1094,1158,1222,1446,1096,1160,1224,1448,1098,1162,1226,1450,1100,1164,1228,1452,1102,1166,1230,1454\n"
    ".short 1104,1168,1232,1456,1106,1170,1234,1458,1108,1172,1236,1460,1110,1174,1238,1462,1112,1176,1240,1464,1114,1178,1242,1466,1116,1180,1244,1468,1118,1182,1246,1470\n"
    ".short 770,898,1472,1472,774,902,1474,1474,778,906,1476,1476,782,910,1478,1478,786,914,1480,1480,790,918,1482,1482,794,922,1484,1484,798,926,1486,1486\n"
    ".short 802,930,1488,1488,806,934,1490,1490,810,938,1492,1492,814,942,1494,1494,818,946,1496,1496,822,950,1498,1498,826,954,1500,1500,830,958,1502,1502\n"
    ".short 1504,1504,1504,1504,1506,1506,1506,1506,1508,1508,1508,1508,1510,1510,1510,1510,1512,1512,1512,1512,1514,1514,1514,1514,1516,1516,1516,1516,1518,1518,1518,1518\n"
    ".short 1520,1520,1520,1520,1522,1522,1522,1522,1524,1524,1524,1524,1526,1526,1526,1526,1528,1528,1528,1528,1530,1530,1530,1530,1532,1532,1532,1532,1534,1534,1534,1534\n"
    ".short 256,258,260,262,272,274,276,278,288,290,292,294,304,306,308,310,320,322,324,326,336,338,340,342,352,354,356,358,368,370,372,374\n"
    ".short 384,386,388,390,400,402,404,406,416,418,420,422,432,434,436,438,448,450,452,454,464,466,468,470,480,482,484,486,496,498,500,502\n"
    ".short 264,266,268,270,280,282,284,286,296,298,300,302,312,314,316,318,328,330,332,334,344,346,348,350,360,362,364,366,376,378,380,382\n"
    ".short 392,394,396,398,408,410,412,414,424,426,428,430,440,442,444,446,456,458,460,462,472,474,476,478,488,490,492,494,504,506,508,510\n"
    ".short 642,644,646,1472,650,652,654,1474,658,660,662,1476,666,668,670,1478,674,676,678,1480,682,684,686,1482,690,692,694,1484,698,700,702,1486\n"
    ".short 706,708,710,1488,714,716,718,1490,722,724,726,1492,730,732,734,1494,738,740,742,1496,746,748,750,1498,754,756,758,1500,762,764,766,1502\n"
    ".short 1504,1504,1504,1504,1506,1506,1506,1506,1508,1508,1508,1508,1510,1510,1510,1510,1512,1512,1512,1512,1514,1514,1514,1514,1516,1516,1516,1516,1518,1518,1518,1518\n"
    ".short 1520,1520,1520,1520,1522,1522,1522,1522,1524,1524,1524,1524,1526,1526,1526,1526,1528,1528,1528,1528,1530,1530,1530,1530,1532,1532,1532,1532,1534,1534,1534,1534\n"
    ".short 640,832,960,1056,648,836,964,1058,656,840,968,1060,664,844,972,1062,672,848,976,1064,680,852,980,1066,688,856,984,1068,696,860,988,1070\n"
    ".short 704,864,992,1072,712,868,996,1074,720,872,1000,1076,728,876,1004,1078,736,880,1008,1080,744,884,1012,1082,752,888,1016,1084,760,892,1020,1086\n"
    ".short 1120,1184,1248,1440,1122,1186,1250,1442,1124,1188,1252,1444,1126,1190,1254,1446,1128,1192,1256,1448,1130,1194,1258,1450,1132,1196,1260,1452,1134,1198,1262,1454\n"
    ".short 1136,1200,1264,1456,1138,1202,1266,1458,1140,1204,1268,1460,1142,1206,1270,1462,1144,1208,1272,1464,1146,1210,1274,1466,1148,1212,1276,1468,1150,1214,1278,1470\n"
    ".short 834,962,1472,1472,838,966,1474,1474,842,970,1476,1476,846,974,1478,1478,850,978,1480,1480,854,982,1482,1482,858,986,1484,1484,862,990,1486,1486\n"
    ".short 866,994,1488,1488,870,998,1490,1490,874,1002,1492,1492,878,1006,1494,1494,882,1010,1496,1496,886,1014,1498,1498,890,1018,1500,1500,894,1022,1502,1502\n"
    ".short 1504,1504,1504,1504,1506,1506,1506,1506,1508,1508,1508,1508,1510,1510,1510,1510,1512,1512,1512,1512,1514,1514,1514,1514,1516,1516,1516,1516,1518,1518,1518,1518\n"
    ".short 1520,1520,1520,1520,1522,1522,1522,1522,1524,1524,1524,1524,1526,1526,1526,1526,1528,1528,1528,1528,1530,1530,1530,1530,1532,1532,1532,1532,1534,1534,1534,1534\n"
    ".popsection\ns_load_dwordx16 s[8:23], s[0:1], 0\ns_load_dwordx8 s[24:31], s[0:1], 64\ns_load_dwordx2 s[32:33], s[0:1], 96\nv_readfirstlane_b32 s3, v0\ns_nop 0\n"
    "s_cmpk_gt_u32 s3, 255\ns_cbranch_scc1 .LH2\ns_lshr_b32 s3, s3, 6\ns_cmp_lt_u32 s3, 2\ns_cbranch_scc0 .LG1\ns_lshr_b32 s77, s2, 3\ns_cmp_eq_u32 s3, 0\ns_cbranch_scc0 .LL4\n"
    "v_lshl_add_u32 v254, v0, 5, s77\nv_lshlrev_b32 v254, 7, v254\ns_getpc_b64 s[4:5]\ns_add_u32 s4, s4, .Ltabs@rel32@lo+4\ns_addc_u32 s5, s5, .Ltabs@rel32@hi+12\n"
    "s_getpc_b64 vcc\n.Lpc:\ns_sub_u32 vcc_lo, vcc_lo, s4\ns_add_u32 vcc_lo, vcc_lo, .Lend-.Lpc-4\nv_min_u32 v254, vcc_lo, v254\ns_mov_b64 exec, 0xffff\n"
    "global_load_dword v254, v254, s[4:5]\ns_mov_b64 exec, -1\n.LL4:\ns_and_b32 s79, s2, 7\ns_mul_i32 s78, s3, 12288\ns_add_u32 m0, s78, 106496\nv_lshrrev_b32 v252, 4, v0\n"
    "v_xor_b32 v252, v252, v0\nv_and_b32 v252, 2, v252\nv_and_or_b32 v252, v0, 1, v252\nv_lshlrev_b32 v252, 4, v252\nv_lshrrev_b32 v253, 2, v0\n"
    "v_lshl_or_b32 v252, v253, 10, v252\ns_bfe_u32 s78, s2, 0x40003\nv_lshl_or_b32 v252, s78, 15, v252\nv_lshl_or_b32 v252, s79, 7, v252\ns_lshr_b32 s78, s2, 7\n"
    "v_lshl_or_b32 v252, s78, 6, v252\ns_waitcnt lgkmcnt(0)\nglobal_load_lds_dwordx4 v252, s[12:13]\ns_branch .LG1\n.LH2:\ns_bfe_u32 s3, s3, 0x20006\nv_and_b32 v77, 63, v0\n"
    "s_bfe_u32 s86, s2, 0x20003\ns_bfe_u32 s63, s2, 0x20001\ns_lshl2_add_u32 s63, s63, s86\ns_lshr_b32 s87, s2, 5\ns_lshl1_add_u32 s58, s86, s87\ns_and_b32 s58, s58, 7\n"
    "s_mov_b32 s60, s58\ns_and_b32 s86, s2, 1\ns_lshl3_add_u32 s87, s86, s87\ns_lshl_b32 s87, s87, 6\ns_mul_i32 s86, s3, 49152\ns_add_u32 s67, s87, s86\n"
    "s_lshl2_add_u32 s86, s63, s3\ns_lshl_b32 s68, s86, 15\nv_lshrrev_b32 v80, 4, v77\nv_xor_b32 v81, v80, v77\nv_and_b32 v83, 2, v81\nv_and_or_b32 v83, v77, 1, v83\n"
    "v_lshlrev_b32 v83, 4, v83\nv_lshrrev_b32 v82, 2, v77\ns_movk_i32 s86, 0xc00\nv_mad_u32_u24 v76, v82, s86, v83\nv_and_b32 v81, 7, v81\nv_lshlrev_b32 v81, 4, v81\n"
    "v_lshrrev_b32 v83, 3, v77\nv_lshl_or_b32 v72, v83, 10, v81\nv_xor_b32 v73, 0x2040, v72\nv_add_u32 v74, 0x4000, v72\nv_add_u32 v75, 0x4000, v73\ns_mul_i32 s64, s3, 12288\n"
    "s_add_u32 s64, s64, 98304\ns_lshl_b32 s69, s3, 10\ns_waitcnt lgkmcnt(0)\ns_add_u32 s10, s10, s67\ns_addc_u32 s11, s11, 0\ns_add_u32 s70, s8, s68\ns_addc_u32 s71, s9, 0\n"
    "s_mul_i32 s86, s60, 196608\ns_add_u32 s54, s10, s86\ns_addc_u32 s55, s11, 0\ns_lshl_b32 s86, s60, 12\ns_add_u32 m0, s69, s86\ns_add_u32 s60, s60, 1\n"
    "global_load_lds_dwordx4 v76, s[54:55]\ns_add_u32 m0, m0, 31744\ns_and_b32 s60, s60, 7\nglobal_load_lds_dwordx4 v76, s[54:55] offset:1024\ns_add_u32 m0, m0, 31744\ns_nop 0\n"
    "global_load_lds_dwordx4 v76, s[54:55] offset:2048\ns_add_u32 m0, s64, 0\ns_lshl_b32 s86, s58, 7\ns_add_u32 s52, s70, s86\ns_addc_u32 s53, s71, 0\n"
    "global_load_lds_dwordx4 v72, s[52:53]\ns_add_u32 m0, s64, 1024\ns_add_u32 s58, s58, 1\nglobal_load_lds_dwordx4 v73, s[52:53]\ns_add_u32 m0, s64, 2048\n"
    "s_and_b32 s58, s58, 7\nglobal_load_lds_dwordx4 v74, s[52:53]\ns_add_u32 m0, s64, 3072\ns_nop 0\nglobal_load_lds_dwordx4 v75, s[52:53]\ns_setprio 3\ns_getpc_b64 s[4:5]\n"
    "s_add_u32 s4, s4, .Ltabs@rel32@lo+4\ns_addc_u32 s5, s5, .Ltabs@rel32@hi+12\ns_waitcnt lgkmcnt(0)\ns_mov_b32 s86, 16777216\ns_load_dword s34, s[32:33], s86\n"
    "s_mov_b32 s6, 0\ns_mov_b32 s7, 0\ns_and_b32 s35, s2, 7\ns_lshr_b32 s36, s2, 3\ns_bfe_u32 s44, s2, 0x20001\ns_bfe_u32 s45, s2, 0x20003\ns_lshl2_add_u32 s37, s44, s45\n"
    "s_and_b32 s46, s2, 1\ns_lshr_b32 s47, s2, 5\ns_lshl3_add_u32 s38, s46, s47\ns_bfe_u32 s48, s2, 0x30003\ns_lshl3_add_u32 s39, s35, s48\ns_lshr_b32 s40, s2, 6\n"
    "s_lshl_b32 s49, s35, 5\ns_add_u32 s41, s49, s36\ns_lshl1_add_u32 s50, s45, s47\ns_and_b32 s42, s50, 7\ns_lshl_b32 s86, s41, 2\ns_load_dword s96, s[26:27], s86\n"
    "s_load_dword s95, s[28:29], s86\nv_mov_b32 v96, 0\nv_mov_b32 v97, 0xf149f2ca\nv_bfe_u32 v100, v0, 4, 2\nv_lshlrev_b32 v99, 2, v100\nv_and_b32 v102, 7, v0\n"
    "v_add_u32 v101, 1, v102\nv_sub_u32 v98, v99, v101\nv_add_u32 v104, 8, v102\nv_sub_u32 v103, v104, v101\nv_cmp_le_u32 vcc, v98, v103\ns_nop 1\n"
    "v_cndmask_b32 v128, v97, v96, vcc\nv_add_u32 v106, 1, v99\nv_sub_u32 v105, v106, v101\nv_cmp_le_u32 vcc, v105, v103\ns_nop 1\nv_cndmask_b32 v129, v97, v96, vcc\n"
    "v_add_u32 v108, 2, v99\nv_sub_u32 v107, v108, v101\nv_cmp_le_u32 vcc, v107, v103\ns_nop 1\nv_cndmask_b32 v130, v97, v96, vcc\nv_add_u32 v110, 3, v99\n"
    "v_sub_u32 v109, v110, v101\nv_cmp_le_u32 vcc, v109, v103\ns_nop 1\nv_cndmask_b32 v131, v97, v96, vcc\ns_add_u32 s46, s3, 4\ns_mul_i32 s45, s46, 0xd00\n"
    "s_add_u32 s44, s45, 0x18040\nv_lshl_add_u32 v252, v100, 4, s44\nv_and_b32 v112, 15, v0\nv_max_u32 v111, 1, v112\ns_mov_b32 s47, 0x1a0\nv_mad_u32_u24 v252, v111, s47, v252\n"
    "s_add_u32 s48, s45, 0x18d00\nv_lshl_add_u32 v253, v100, 4, s48\nv_mad_u32_u24 v253, v102, s47, v253\nv_and_b32 v113, 3, v0\ns_add_u32 s49, s45, 0x18080\n"
    "v_lshl_add_u32 v254, v113, 3, s49\nv_bfe_u32 v116, v0, 2, 2\nv_add_u32 v115, v99, v116\nv_max_u32 v114, 1, v115\nv_mad_u32_u24 v254, v114, s47, v254\n"
    "v_bfe_u32 v117, v0, 3, 1\ns_lshr_b32 s59, s46, 1\ns_lshl2_add_u32 s57, s37, s59\ns_lshl_b32 s56, s57, 15\ns_lshl_b32 s65, s38, 11\ns_add_u32 s51, s56, s65\n"
    "s_and_b32 s72, s46, 1\ns_lshl_b32 s66, s72, 8\ns_add_u32 s50, s51, s66\nv_lshl_add_u32 v1, v117, 10, s50\nv_bfe_u32 v118, v0, 5, 1\nv_lshl_add_u32 v1, v118, 9, v1\n"
    "v_bfe_u32 v119, v0, 4, 1\nv_lshl_add_u32 v1, v119, 4, v1\nv_lshl_add_u32 v1, v102, 5, v1\nv_mov_b64 v[10:11], 0\nv_mov_b64 v[12:13], 0\nv_mov_b64 v[14:15], 0\n"
    "v_mov_b64 v[16:17], 0\nv_mov_b64 v[18:19], 0\nv_mov_b64 v[20:21], 0\nv_mov_b64 v[22:23], 0\nv_mov_b64 v[24:25], 0\nv_mov_b32 v96, 0x27f70\ns_lshr_b32 s45, s3, 1\n"
    "s_lshl_b32 s44, s45, 6\nv_and_b32 v99, 15, v0\ns_mov_b32 s46, 0x23c00\nv_lshl_add_u32 v98, v99, 10, s46\nv_bfe_u32 v101, v0, 4, 2\nv_xor_b32 v100, v101, v99\n"
    "v_lshl_add_u32 v98, v100, 4, v98\nv_xor_b32 v97, s44, v98\nv_cmp_gt_u32 vcc, 2, v99\ns_nop 1\nv_cndmask_b32 v26, v97, v96, vcc\nv_mov_b32 v102, 0\n"
    "v_cmp_gt_u32 vcc, 2, v99\ns_nop 1\nv_cndmask_b32 v27, -1, v102, vcc\nv_bfe_u32 v104, v0, 2, 2\nv_lshl_add_u32 v103, v101, 3, v104\ns_lshl_b32 s47, s45, 11\n"
    "v_lshl_add_u32 v29, v103, 6, s47\nv_and_b32 v105, 3, v0\nv_lshl_add_u32 v29, v105, 3, v29\nv_xor_b32 v107, s3, v101\nv_and_b32 v106, 1, v107\n"
    "v_lshl_add_u32 v29, v106, 5, v29\ns_add_u32 s49, s45, 1\ns_lshl_b32 s48, s49, 15\nv_add_u32 v28, s48, v29\ns_lshl_b32 s51, s3, 6\ns_add_u32 s50, s51, 0x27c80\n"
    "v_lshl_add_u32 v30, v101, 4, s50\ns_and_b32 s59, s3, 1\ns_lshl_b32 s57, s59, 6\ns_add_u32 s56, s57, 0x27c00\nv_lshl_add_u32 v31, v101, 4, s56\ns_lshl_b32 s66, s3, 7\n"
    "s_add_u32 s65, s66, 0x24000\nv_lshl_add_u32 v32, v99, 3, s65\ns_mov_b32 s72, 0x24000\nv_lshl_add_u32 v33, v99, 3, s72\ns_lshl_b32 s74, s3, 5\ns_add_u32 s73, s74, 0x18040\n"
    "v_lshl_add_u32 v34, v101, 3, s73\ns_mov_b32 s75, 0xd0\nv_mad_u32_u24 v34, v99, s75, v34\nv_and_b32 v108, 0xff, v0\nv_lshlrev_b32 v35, 4, v108\n"
    "s_lshl2_add_u32 s78, s40, s3\ns_lshl_b32 s77, s78, 9\ns_add_u32 s76, s77, 0x1002000\nv_lshl_add_u32 v36, v105, 7, s76\nv_and_b32 v109, 4, v0\n"
    "v_lshl_add_u32 v36, v109, 2, v36\nv_and_b32 v110, 63, v0\ns_lshl2_add_u32 s81, s37, s3\ns_lshl_b32 s80, s81, 15\ns_lshl_b32 s82, s38, 11\ns_add_u32 s79, s80, s82\n"
    "v_lshl_add_u32 v37, v110, 4, s79\ns_lshl_b32 s84, s78, 15\ns_add_u32 s83, s84, 0xe01000\nv_lshl_add_u32 v58, v110, 4, s83\ns_and_b32 s45, s38, 3\n"
    "s_lshl2_add_u32 s49, s37, s3\ns_lshl_b32 s48, s49, 9\ns_add_u32 s47, s48, 0x1008000\ns_lshr_b32 s51, s38, 2\ns_lshl_b32 s50, s51, 7\ns_add_u32 s46, s47, s50\n"
    "s_lshl2_add_u32 s44, s45, s46\nv_mov_b32 v39, s44\nv_mov_b32 v96, 0x27f70\ns_lshl_b32 s56, s3, 8\nv_and_b32 v99, 15, v0\ns_mov_b32 s57, 0x23c00\n"
    "v_lshl_add_u32 v98, v99, 10, s57\nv_bfe_u32 v101, v0, 4, 2\nv_xor_b32 v100, v101, v99\nv_lshl_add_u32 v98, v100, 4, v98\nv_xor_b32 v97, s56, v98\nv_cmp_gt_u32 vcc, 2, v99\n"
    "s_nop 1\nv_cndmask_b32 v40, v97, v96, vcc\nv_and_b32 v102, 63, v0\ns_lshl_b32 s59, s41, 9\nv_lshl_add_u32 v41, v102, 4, s59\ns_lshl_b32 s72, s41, 13\n"
    "s_lshl_b32 s73, s3, 11\ns_add_u32 s66, s72, s73\ns_add_u32 s65, s66, 0x200000\nv_lshl_add_u32 v60, v102, 4, s65\nv_and_b32 v103, 3, v0\ns_lshl_b32 s76, s2, 8\n"
    "s_lshl_b32 s77, s3, 6\ns_add_u32 s75, s76, s77\ns_add_u32 s74, s75, 0x400000\nv_lshl_add_u32 v61, v103, 4, s74\ns_and_b32 s80, s2, 3\ns_lshr_b32 s83, s2, 2\n"
    "s_lshl_b32 s82, s83, 7\ns_add_u32 s81, s82, 0x1004000\ns_lshl4_add_u32 s79, s80, s81\ns_lshl2_add_u32 s78, s3, s79\nv_mov_b32 v2, s78\ns_mul_i32 s85, s3, 0x1760\n"
    "s_add_u32 s84, s85, 0x18000\nv_lshl_add_u32 v3, v102, 3, s84\nv_and_b32 v96, 63, v0\ns_lshl2_add_u32 s46, s40, s3\ns_lshl_b32 s45, s46, 15\ns_add_u32 s44, s45, 0xe05000\n"
    "v_lshl_add_u32 v4, v96, 4, s44\ns_add_u32 s47, s45, 0xe07000\nv_lshl_add_u32 v5, v96, 4, s47\nv_and_b32 v97, 31, v0\ns_lshl_b32 s50, s39, 15\ns_lshl_b32 s51, s40, 8\n"
    "s_add_u32 s49, s50, s51\ns_lshl_b32 s56, s3, 6\ns_add_u32 s48, s49, s56\nv_lshl_add_u32 v6, v97, 10, s48\nv_bfe_u32 v98, v0, 5, 1\nv_lshl_add_u32 v6, v98, 4, v6\n"
    "s_add_u32 s57, s45, 0xe03000\nv_lshl_add_u32 v59, v96, 4, s57\ns_lshr_b32 s66, s3, 1\ns_lshl_b32 s65, s66, 8\ns_add_u32 s59, s65, 0x20000\nv_lshl_add_u32 v62, v96, 2, s59\n"
    "s_lshl2_add_u32 s72, s66, 0x27d90\nv_mov_b32 v7, s72\ns_mul_i32 s74, s66, 0x600\ns_add_u32 s73, s74, 0x1ed78\nv_mov_b32 v8, s73\ns_lshl_b32 s86, s63, 17\n"
    "s_sub_u32 s86, s86, 0x4000\ns_max_i32 s86, s86, 0\ns_lshl_b32 s73, s3, 2\ns_max_u32 s87, s73, 2\ns_add_u32 s73, s73, 4\ns_lshl_b32 s72, s87, 10\ns_add_u32 s86, s86, s72\n"
    "s_add_u32 s52, s8, s86\ns_addc_u32 s53, s9, 0\ns_add_u32 s72, s72, 146432\n.LL5:\ns_mov_b32 m0, s72\nv_xor_b32 v82, s87, v77\nv_lshlrev_b32 v82, 4, v82\n"
    "global_load_lds_dwordx4 v82, s[52:53]\ns_add_u32 s52, s52, 1024\ns_addc_u32 s53, s53, 0\ns_add_u32 s72, s72, 1024\ns_add_u32 s87, s87, 1\ns_cmp_lg_u32 s87, s73\n"
    "s_cbranch_scc1 .LL5\ns_mul_i32 s86, s60, 196608\ns_add_u32 s54, s10, s86\ns_addc_u32 s55, s11, 0\ns_lshl_b32 s86, s60, 12\ns_add_u32 m0, s69, s86\ns_add_u32 s60, s60, 1\n"
    "global_load_lds_dwordx4 v76, s[54:55]\ns_add_u32 m0, m0, 31744\ns_and_b32 s60, s60, 7\nglobal_load_lds_dwordx4 v76, s[54:55] offset:1024\ns_add_u32 m0, m0, 31744\ns_nop 0\n"
    "global_load_lds_dwordx4 v76, s[54:55] offset:2048\ns_add_u32 m0, s64, 4096\ns_lshl_b32 s86, s58, 7\ns_add_u32 s52, s70, s86\ns_addc_u32 s53, s71, 0\n"
    "global_load_lds_dwordx4 v72, s[52:53]\ns_add_u32 m0, s64, 5120\ns_add_u32 s58, s58, 1\nglobal_load_lds_dwordx4 v73, s[52:53]\ns_add_u32 m0, s64, 6144\n"
    "s_and_b32 s58, s58, 7\nglobal_load_lds_dwordx4 v74, s[52:53]\ns_add_u32 m0, s64, 7168\ns_nop 0\nglobal_load_lds_dwordx4 v75, s[52:53]\nv_mov_b32 v232, -1\n"
    "v_mov_b32 v233, -1\nv_mov_b32 v234, -1\nv_mov_b32 v235, -1\nv_bitop3_b32 v84, 64, v27, v26 bitop3:0x6a\ns_mov_b32 s61, s42\ns_mov_b32 s62, s42\ns_movk_i32 s86, 128\n"
    "v_bitop3_b32 v85, 64, v27, v40 bitop3:0x6a\nv_bitop3_b32 v86, s86, v27, v40 bitop3:0x6a\nv_bitop3_b32 v87, s86, v27, v85 bitop3:0x6a\ns_lshl_b32 s74, s3, 9\n"
    "s_add_u32 s74, s74, 131072\ns_bitcmp1_b32 s3, 0\ns_cselect_b64 s[76:77], s[16:17], s[14:15]\ns_cselect_b64 s[72:73], s[20:21], s[18:19]\n"
    "s_cselect_b64 s[78:79], s[24:25], s[22:23]\ns_bitcmp1_b32 s3, 1\ns_cselect_b64 s[76:77], s[72:73], s[76:77]\ns_waitcnt lgkmcnt(0)\ns_lshl_b32 s97, s96, 16\n"
    "s_and_b32 s96, s96, 0xffff0000\ns_cmp_lt_u32 s3, 2\ns_cselect_b32 s96, s97, s96\ns_mov_b32 s97, 0x3fb8aa3b\nv_mov_b32 v108, s96\nv_mul_f32 v109, s97, v108\n"
    "v_fma_f32 v110, s97, v108, -v109\nv_exp_f32 v109, v109\nv_fmac_f32 v110, 0x32a57060, v108\nv_mul_f32 v110, 0x3f317218, v110\nv_fmac_f32 v109, v109, v110\n"
    "v_cvt_pk_bf16_f32 v109, 0, v109\nv_mul_f32 v108, s97, v109\nv_mul_f32 v110, 0xbe22f983, v109\nv_readfirstlane_b32 s92, v109\nv_readfirstlane_b32 s93, v108\n"
    "v_readfirstlane_b32 s94, v110\ns_and_b32 s97, s95, 0xffff0000\ns_lshl_b32 s96, s95, 16\ns_lshl_b32 s87, s61, 12\ns_lshl_b32 s86, s61, 7\nv_add_u32 v120, s87, v28\n"
    "v_add_u32 v122, s87, v29\ns_add_u32 s61, s61, 1\nv_xor_b32 v121, 0x800, v120\ns_and_b32 s61, s61, 7\nv_bitop3_b32 v88, s86, v27, v26 bitop3:0x6a\n"
    "v_bitop3_b32 v89, s86, v27, v84 bitop3:0x6a\ns_waitcnt vmcnt(7) lgkmcnt(0)\ns_barrier\ns_setprio 0\nds_read_b128 v[132:135], v88\nds_read_b128 v[136:139], v89\n"
    "ds_read_b64_tr_b16 v[96:97], v120\nds_read_b64_tr_b16 v[98:99], v120 offset:256\nds_read_b64_tr_b16 v[100:101], v121\nds_read_b64_tr_b16 v[102:103], v121 offset:256\n"
    "ds_read_b64_tr_b16 v[104:105], v122\nds_read_b64_tr_b16 v[106:107], v122 offset:256\ns_setprio 0\ns_mul_i32 s86, s60, 196608\ns_add_u32 s54, s10, s86\n"
    "s_addc_u32 s55, s11, 0\ns_lshl_b32 s86, s60, 12\ns_add_u32 m0, s69, s86\ns_add_u32 s60, s60, 1\nglobal_load_lds_dwordx4 v76, s[54:55]\ns_add_u32 m0, m0, 31744\n"
    "s_and_b32 s60, s60, 7\nglobal_load_lds_dwordx4 v76, s[54:55] offset:1024\ns_add_u32 m0, m0, 31744\ns_nop 0\nglobal_load_lds_dwordx4 v76, s[54:55] offset:2048\n"
    "s_add_u32 m0, s64, 8192\ns_lshl_b32 s86, s58, 7\ns_add_u32 s52, s70, s86\ns_addc_u32 s53, s71, 0\nglobal_load_lds_dwordx4 v72, s[52:53]\ns_add_u32 m0, s64, 9216\n"
    "s_add_u32 s58, s58, 1\nglobal_load_lds_dwordx4 v73, s[52:53]\ns_add_u32 m0, s64, 10240\ns_and_b32 s58, s58, 7\nglobal_load_lds_dwordx4 v74, s[52:53]\n"
    "s_add_u32 m0, s64, 11264\ns_nop 0\nglobal_load_lds_dwordx4 v75, s[52:53]\ns_setprio 0\ns_lshl_b32 s87, s61, 12\ns_lshl_b32 s86, s61, 7\nv_add_u32 v120, s87, v28\n"
    "v_add_u32 v122, s87, v29\ns_add_u32 s61, s61, 1\nv_xor_b32 v121, 0x800, v120\ns_and_b32 s61, s61, 7\nv_bitop3_b32 v88, s86, v27, v26 bitop3:0x6a\n"
    "v_bitop3_b32 v89, s86, v27, v84 bitop3:0x6a\ns_waitcnt vmcnt(7) lgkmcnt(0)\ns_barrier\ns_setprio 2\nv_mfma_f32_16x16x32_bf16 v[64:67], v[96:99], v[132:135], 0\n"
    "v_mfma_f32_16x16x32_bf16 v[68:71], v[104:107], v[132:135], 0\nv_mfma_f32_16x16x32_bf16 v[64:67], v[100:103], v[136:139], v[64:67]\ns_setprio 0\n"
    "ds_read_b128 v[132:135], v88\nds_read_b128 v[136:139], v89\nds_read_b64_tr_b16 v[96:97], v120\nds_read_b64_tr_b16 v[98:99], v120 offset:256\n"
    "ds_read_b64_tr_b16 v[100:101], v121\nds_read_b64_tr_b16 v[102:103], v121 offset:256\nds_read_b64_tr_b16 v[104:105], v122\nds_read_b64_tr_b16 v[106:107], v122 offset:256\n"
    "s_setprio 0\ns_mul_i32 s86, s60, 196608\ns_add_u32 s54, s10, s86\ns_addc_u32 s55, s11, 0\ns_lshl_b32 s86, s60, 12\ns_add_u32 m0, s69, s86\ns_add_u32 s60, s60, 1\n"
    "global_load_lds_dwordx4 v76, s[54:55]\ns_add_u32 m0, m0, 31744\ns_and_b32 s60, s60, 7\nglobal_load_lds_dwordx4 v76, s[54:55] offset:1024\ns_add_u32 m0, m0, 31744\ns_nop 0\n"
    "global_load_lds_dwordx4 v76, s[54:55] offset:2048\ns_add_u32 m0, s64, 0\ns_lshl_b32 s86, s58, 7\ns_add_u32 s52, s70, s86\ns_addc_u32 s53, s71, 0\n"
    "global_load_lds_dwordx4 v72, s[52:53]\ns_add_u32 m0, s64, 1024\ns_add_u32 s58, s58, 1\nglobal_load_lds_dwordx4 v73, s[52:53]\ns_add_u32 m0, s64, 2048\n"
    "s_and_b32 s58, s58, 7\nglobal_load_lds_dwordx4 v74, s[52:53]\ns_add_u32 m0, s64, 3072\ns_nop 0\nglobal_load_lds_dwordx4 v75, s[52:53]\ns_setprio 0\n"
    "ds_read_b128 v[196:199], v40\nds_read_b128 v[200:203], v85\nds_read_b128 v[204:207], v86\nds_read_b128 v[208:211], v87\ns_lshl_b32 s87, s61, 12\ns_lshl_b32 s86, s61, 7\n"
    "v_add_u32 v120, s87, v28\nv_add_u32 v122, s87, v29\ns_add_u32 s61, s61, 1\nv_xor_b32 v121, 0x800, v120\ns_and_b32 s61, s61, 7\nv_bitop3_b32 v88, s86, v27, v26 bitop3:0x6a\n"
    "v_bitop3_b32 v89, s86, v27, v84 bitop3:0x6a\ns_waitcnt vmcnt(7) lgkmcnt(0)\ns_barrier\ns_setprio 2\nv_mfma_f32_16x16x32_bf16 v[64:67], v[96:99], v[132:135], v[64:67]\n"
    "v_mfma_f32_16x16x32_bf16 v[68:71], v[104:107], v[132:135], v[68:71]\nv_mfma_f32_16x16x32_bf16 v[64:67], v[100:103], v[136:139], v[64:67]\n"
    "v_mfma_f32_16x16x32_bf16 v[92:95], v[196:199], v[196:199], 0\nv_mfma_f32_16x16x32_bf16 v[92:95], v[200:203], v[200:203], v[92:95]\n"
    "v_mfma_f32_16x16x32_bf16 v[92:95], v[204:207], v[204:207], v[92:95]\nv_mfma_f32_16x16x32_bf16 v[92:95], v[208:211], v[208:211], v[92:95]\ns_setprio 0\n"
    "ds_read_b128 v[132:135], v88\nds_read_b128 v[136:139], v89\nds_read_b64_tr_b16 v[96:97], v120\nds_read_b64_tr_b16 v[98:99], v120 offset:256\n"
    "ds_read_b64_tr_b16 v[100:101], v121\nds_read_b64_tr_b16 v[102:103], v121 offset:256\nds_read_b64_tr_b16 v[104:105], v122\nds_read_b64_tr_b16 v[106:107], v122 offset:256\n"
    "s_setprio 0\ns_mul_i32 s86, s60, 196608\ns_add_u32 s54, s10, s86\ns_addc_u32 s55, s11, 0\ns_lshl_b32 s86, s60, 12\ns_add_u32 m0, s69, s86\ns_add_u32 s60, s60, 1\n"
    "global_load_lds_dwordx4 v76, s[54:55]\ns_add_u32 m0, m0, 31744\ns_and_b32 s60, s60, 7\nglobal_load_lds_dwordx4 v76, s[54:55] offset:1024\ns_add_u32 m0, m0, 31744\ns_nop 0\n"
    "global_load_lds_dwordx4 v76, s[54:55] offset:2048\ns_add_u32 m0, s64, 4096\ns_lshl_b32 s86, s58, 7\ns_add_u32 s52, s70, s86\ns_addc_u32 s53, s71, 0\n"
    "global_load_lds_dwordx4 v72, s[52:53]\ns_add_u32 m0, s64, 5120\ns_add_u32 s58, s58, 1\nglobal_load_lds_dwordx4 v73, s[52:53]\ns_add_u32 m0, s64, 6144\n"
    "s_and_b32 s58, s58, 7\nglobal_load_lds_dwordx4 v74, s[52:53]\ns_add_u32 m0, s64, 7168\ns_nop 0\nglobal_load_lds_dwordx4 v75, s[52:53]\ns_setprio 0\n"
    "s_lshl_b32 s87, s61, 12\ns_lshl_b32 s86, s61, 7\nv_add_u32 v120, s87, v28\nv_add_u32 v122, s87, v29\ns_add_u32 s61, s61, 1\nv_xor_b32 v121, 0x800, v120\n"
    "s_and_b32 s61, s61, 7\nv_bitop3_b32 v88, s86, v27, v26 bitop3:0x6a\nv_bitop3_b32 v89, s86, v27, v84 bitop3:0x6a\ns_waitcnt vmcnt(7) lgkmcnt(0)\ns_barrier\ns_setprio 2\n"
    "v_mfma_f32_16x16x32_bf16 v[64:67], v[96:99], v[132:135], v[64:67]\nv_mfma_f32_16x16x32_bf16 v[68:71], v[104:107], v[132:135], v[68:71]\n"
    "v_mfma_f32_16x16x32_bf16 v[64:67], v[100:103], v[136:139], v[64:67]\ns_setprio 0\nds_read_b128 v[132:135], v88\nds_read_b128 v[136:139], v89\n"
    "ds_read_b64_tr_b16 v[96:97], v120\nds_read_b64_tr_b16 v[98:99], v120 offset:256\nds_read_b64_tr_b16 v[100:101], v121\nds_read_b64_tr_b16 v[102:103], v121 offset:256\n"
    "ds_read_b64_tr_b16 v[104:105], v122\nds_read_b64_tr_b16 v[106:107], v122 offset:256\ns_setprio 0\ns_mul_i32 s86, s60, 196608\ns_add_u32 s54, s10, s86\n"
    "s_addc_u32 s55, s11, 0\ns_lshl_b32 s86, s60, 12\ns_add_u32 m0, s69, s86\ns_add_u32 s60, s60, 1\nglobal_load_lds_dwordx4 v76, s[54:55]\ns_add_u32 m0, m0, 31744\n"
    "s_and_b32 s60, s60, 7\nglobal_load_lds_dwordx4 v76, s[54:55] offset:1024\ns_add_u32 m0, m0, 31744\ns_nop 0\nglobal_load_lds_dwordx4 v76, s[54:55] offset:2048\n"
    "s_add_u32 m0, s64, 8192\ns_lshl_b32 s86, s58, 7\ns_add_u32 s52, s70, s86\ns_addc_u32 s53, s71, 0\nglobal_load_lds_dwordx4 v72, s[52:53]\ns_add_u32 m0, s64, 9216\n"
    "s_add_u32 s58, s58, 1\nglobal_load_lds_dwordx4 v73, s[52:53]\ns_add_u32 m0, s64, 10240\ns_and_b32 s58, s58, 7\nglobal_load_lds_dwordx4 v74, s[52:53]\n"
    "s_add_u32 m0, s64, 11264\ns_nop 0\nglobal_load_lds_dwordx4 v75, s[52:53]\ns_setprio 0\ns_lshl_b32 s87, s61, 12\ns_lshl_b32 s86, s61, 7\nv_add_u32 v120, s87, v28\n"
    "v_add_u32 v122, s87, v29\ns_add_u32 s61, s61, 1\nv_xor_b32 v121, 0x800, v120\ns_and_b32 s61, s61, 7\nv_bitop3_b32 v88, s86, v27, v26 bitop3:0x6a\n"
    "v_bitop3_b32 v89, s86, v27, v84 bitop3:0x6a\ns_waitcnt vmcnt(7) lgkmcnt(0)\ns_barrier\ns_setprio 2\nv_mfma_f32_16x16x32_bf16 v[64:67], v[96:99], v[132:135], v[64:67]\n"
    "v_mfma_f32_16x16x32_bf16 v[68:71], v[104:107], v[132:135], v[68:71]\nv_mfma_f32_16x16x32_bf16 v[64:67], v[100:103], v[136:139], v[64:67]\ns_setprio 0\n"
    "ds_read_b128 v[132:135], v88\nds_read_b128 v[136:139], v89\nds_read_b64_tr_b16 v[96:97], v120\nds_read_b64_tr_b16 v[98:99], v120 offset:256\n"
    "ds_read_b64_tr_b16 v[100:101], v121\nds_read_b64_tr_b16 v[102:103], v121 offset:256\nds_read_b64_tr_b16 v[104:105], v122\nds_read_b64_tr_b16 v[106:107], v122 offset:256\n"
    "s_setprio 0\ns_mul_i32 s86, s60, 196608\ns_add_u32 s54, s10, s86\ns_addc_u32 s55, s11, 0\ns_lshl_b32 s86, s60, 12\ns_add_u32 m0, s69, s86\ns_add_u32 s60, s60, 1\n"
    "global_load_lds_dwordx4 v76, s[54:55]\ns_add_u32 m0, m0, 31744\ns_and_b32 s60, s60, 7\nglobal_load_lds_dwordx4 v76, s[54:55] offset:1024\ns_add_u32 m0, m0, 31744\ns_nop 0\n"
    "global_load_lds_dwordx4 v76, s[54:55] offset:2048\ns_add_u32 m0, s64, 0\ns_lshl_b32 s86, s58, 7\ns_add_u32 s52, s70, s86\ns_addc_u32 s53, s71, 0\n"
    "global_load_lds_dwordx4 v72, s[52:53]\ns_add_u32 m0, s64, 1024\ns_add_u32 s58, s58, 1\nglobal_load_lds_dwordx4 v73, s[52:53]\ns_add_u32 m0, s64, 2048\n"
    "s_and_b32 s58, s58, 7\nglobal_load_lds_dwordx4 v74, s[52:53]\ns_add_u32 m0, s64, 3072\ns_nop 0\nglobal_load_lds_dwordx4 v75, s[52:53]\ns_setprio 0\n"
    "s_lshl_b32 s87, s61, 12\ns_lshl_b32 s86, s61, 7\nv_add_u32 v120, s87, v28\nv_add_u32 v122, s87, v29\ns_add_u32 s61, s61, 1\nv_xor_b32 v121, 0x800, v120\n"
    "s_and_b32 s61, s61, 7\nv_bitop3_b32 v88, s86, v27, v26 bitop3:0x6a\nv_bitop3_b32 v89, s86, v27, v84 bitop3:0x6a\ns_waitcnt vmcnt(7) lgkmcnt(0)\ns_barrier\ns_setprio 2\n"
    "v_mfma_f32_16x16x32_bf16 v[64:67], v[96:99], v[132:135], v[64:67]\nv_mfma_f32_16x16x32_bf16 v[68:71], v[104:107], v[132:135], v[68:71]\n"
    "v_mfma_f32_16x16x32_bf16 v[64:67], v[100:103], v[136:139], v[64:67]\ns_setprio 0\nds_read_b128 v[132:135], v88\nds_read_b128 v[136:139], v89\n"
    "ds_read_b64_tr_b16 v[96:97], v120\nds_read_b64_tr_b16 v[98:99], v120 offset:256\nds_read_b64_tr_b16 v[100:101], v121\nds_read_b64_tr_b16 v[102:103], v121 offset:256\n"
    "ds_read_b64_tr_b16 v[104:105], v122\nds_read_b64_tr_b16 v[106:107], v122 offset:256\ns_setprio 0\ns_mul_i32 s86, s60, 196608\ns_add_u32 s54, s10, s86\n"
    "s_addc_u32 s55, s11, 0\ns_lshl_b32 s86, s60, 12\ns_add_u32 m0, s69, s86\ns_add_u32 s60, s60, 1\nglobal_load_lds_dwordx4 v76, s[54:55]\ns_add_u32 m0, m0, 31744\n"
    "s_and_b32 s60, s60, 7\nglobal_load_lds_dwordx4 v76, s[54:55] offset:1024\ns_add_u32 m0, m0, 31744\ns_nop 0\nglobal_load_lds_dwordx4 v76, s[54:55] offset:2048\n"
    "s_add_u32 m0, s64, 4096\ns_lshl_b32 s86, s58, 7\ns_add_u32 s52, s70, s86\ns_addc_u32 s53, s71, 0\nglobal_load_lds_dwordx4 v72, s[52:53]\ns_add_u32 m0, s64, 5120\n"
    "s_add_u32 s58, s58, 1\nglobal_load_lds_dwordx4 v73, s[52:53]\ns_add_u32 m0, s64, 6144\ns_and_b32 s58, s58, 7\nglobal_load_lds_dwordx4 v74, s[52:53]\n"
    "s_add_u32 m0, s64, 7168\ns_nop 0\nglobal_load_lds_dwordx4 v75, s[52:53]\ns_setprio 0\ns_lshl_b32 s87, s61, 12\ns_lshl_b32 s86, s61, 7\nv_add_u32 v120, s87, v28\n"
    "v_add_u32 v122, s87, v29\ns_add_u32 s61, s61, 1\nv_xor_b32 v121, 0x800, v120\ns_and_b32 s61, s61, 7\nv_bitop3_b32 v88, s86, v27, v26 bitop3:0x6a\n"
    "v_bitop3_b32 v89, s86, v27, v84 bitop3:0x6a\ns_waitcnt vmcnt(7) lgkmcnt(0)\ns_barrier\ns_setprio 2\nv_mfma_f32_16x16x32_bf16 v[64:67], v[96:99], v[132:135], v[64:67]\n"
    "v_mfma_f32_16x16x32_bf16 v[68:71], v[104:107], v[132:135], v[68:71]\nv_mfma_f32_16x16x32_bf16 v[64:67], v[100:103], v[136:139], v[64:67]\ns_setprio 0\n"
    "ds_read_b128 v[132:135], v88\nds_read_b128 v[136:139], v89\nds_read_b64_tr_b16 v[96:97], v120\nds_read_b64_tr_b16 v[98:99], v120 offset:256\n"
    "ds_read_b64_tr_b16 v[100:101], v121\nds_read_b64_tr_b16 v[102:103], v121 offset:256\nds_read_b64_tr_b16 v[104:105], v122\nds_read_b64_tr_b16 v[106:107], v122 offset:256\n"
    "s_setprio 0\nglobal_store_dwordx4 v37, v[232:235], s[32:33] offset:0 sc1\nglobal_store_dwordx4 v37, v[232:235], s[32:33] offset:1024 sc1\ns_mov_b32 exec_hi, 0\n"
    "s_cmp_lt_u32 s3, 2\ns_cbranch_scc0 .LL6\ns_add_u32 m0, s74, 2048\ns_nop 0\nglobal_load_lds_dwordx4 v41, s[78:79]\n.LL6:\ns_mov_b32 m0, s74\ns_nop 0\n"
    "global_load_lds_dwordx4 v41, s[76:77]\ns_mov_b64 exec, -1\ns_add_u32 m0, s69, 143360\ns_nop 0\nglobal_load_lds_dwordx4 v35, s[4:5]\n"
    "global_load_dwordx4 v[140:143], v36, s[32:33] sc1\nglobal_store_dwordx4 v60, v[232:235], s[32:33] offset:0 sc1\n"
    "global_store_dwordx4 v60, v[232:235], s[32:33] offset:1024 sc1\ns_mov_b64 exec, 15\nglobal_store_dwordx4 v61, v[232:235], s[32:33] sc1\ns_mov_b64 exec, -1\ns_setprio 0\n"
    "s_cmp_lt_u32 s3, 2\ns_cbranch_scc1 .LL7\ns_waitcnt vmcnt(8)\n.LL7:\ns_lshl_b32 s87, s61, 12\ns_lshl_b32 s86, s61, 7\nv_add_u32 v120, s87, v28\nv_add_u32 v122, s87, v29\n"
    "s_add_u32 s61, s61, 1\nv_xor_b32 v121, 0x800, v120\ns_and_b32 s61, s61, 7\nv_bitop3_b32 v88, s86, v27, v26 bitop3:0x6a\nv_bitop3_b32 v89, s86, v27, v84 bitop3:0x6a\n"
    "s_waitcnt vmcnt(9) lgkmcnt(0)\ns_barrier\ns_setprio 0\nv_mfma_f32_16x16x32_bf16 v[64:67], v[96:99], v[132:135], v[64:67]\n"
    "v_mfma_f32_16x16x32_bf16 v[68:71], v[104:107], v[132:135], v[68:71]\nv_mfma_f32_16x16x32_bf16 v[64:67], v[100:103], v[136:139], v[64:67]\ns_setprio 0\n"
    "ds_read_b128 v[132:135], v88\nds_read_b128 v[136:139], v89\nds_read_b64_tr_b16 v[96:97], v120\nds_read_b64_tr_b16 v[98:99], v120 offset:256\n"
    "ds_read_b64_tr_b16 v[100:101], v121\nds_read_b64_tr_b16 v[102:103], v121 offset:256\nds_read_b64_tr_b16 v[104:105], v122\nds_read_b64_tr_b16 v[106:107], v122 offset:256\n"
    "s_setprio 0\ns_setprio 0\ns_mov_b64 exec, 0xffff\nds_write_b32 v32, v93\ns_mov_b32 exec_lo, 0x100001\ns_mov_b32 exec_hi, 0x10000100\nds_write_b32 v32, v92 offset:4\n"
    "s_mov_b32 exec_lo, 0x200002\ns_mov_b32 exec_hi, 0x20000200\nds_write_b32 v32, v93 offset:4\ns_mov_b32 exec_lo, 0x400004\ns_mov_b32 exec_hi, 0x40000400\n"
    "ds_write_b32 v32, v94 offset:4\ns_mov_b32 exec_lo, 0x800008\ns_mov_b32 exec_hi, 0x80000800\nds_write_b32 v32, v95 offset:4\ns_mov_b64 exec, -1\ns_waitcnt lgkmcnt(5)\n"
    "s_setprio 0\nv_mfma_f32_16x16x32_bf16 v[64:67], v[96:99], v[132:135], v[64:67]\nv_mfma_f32_16x16x32_bf16 v[68:71], v[104:107], v[132:135], v[68:71]\n"
    "v_mfma_f32_16x16x32_bf16 v[64:67], v[100:103], v[136:139], v[64:67]\ns_add_u32 s34, s34, 1\ns_mov_b32 exec_lo, 0x20002\ns_mov_b32 exec_hi, 0x20002\ns_nop 3\n"
    "ds_add_f32 v31, v68 offset:0\nds_add_f32 v31, v69 offset:4\nds_add_f32 v31, v70 offset:8\nds_add_f32 v31, v71 offset:12\nds_write_b128 v30, v[64:67]\ns_mov_b64 exec, -1\n"
    "s_waitcnt lgkmcnt(0)\ns_barrier\ns_setprio 0\nds_read_b64 v[108:109], v33 offset:0\nds_read_b64 v[110:111], v33 offset:128\nds_read_b64 v[112:113], v33 offset:256\n"
    "ds_read_b64 v[114:115], v33 offset:384\ns_mov_b32 s86, 0x3b000000\ns_waitcnt lgkmcnt(0)\nv_pk_add_f32 v[108:109], v[108:109], v[110:111]\n"
    "v_pk_add_f32 v[112:113], v[112:113], v[114:115]\nv_pk_add_f32 v[108:109], v[108:109], v[112:113]\nv_mul_f32 v108, s86, v108\nv_mul_f32 v110, v108, v108\n"
    "v_fma_f32 v109, v109, s86, -v110\nv_max_f32 v109, 0, v109\nv_cvt_pk_bf16_f32 v109, 0, v109\nv_add_f32 v109, 0x3727c5ac, v109\nv_cvt_pk_bf16_f32 v109, 0, v109\n"
    "v_sqrt_f32 v109, v109\ns_nop 0\nv_cvt_pk_bf16_f32 v109, 0, v109\nv_rcp_f32 v109, v109\nv_cvt_pk_bf16_f32 v90, 0, -v108\ns_nop 1\n"
    "v_fmac_f32_dpp v64, v64, v90 row_newbcast:1 row_mask:0xf bank_mask:0xf\nv_fmac_f32_dpp v65, v65, v90 row_newbcast:1 row_mask:0xf bank_mask:0xf\n"
    "v_fmac_f32_dpp v66, v66, v90 row_newbcast:1 row_mask:0xf bank_mask:0xf\nv_fmac_f32_dpp v67, v67, v90 row_newbcast:1 row_mask:0xf bank_mask:0xf\nv_mul_f32 v64, v64, v109\n"
    "v_mul_f32 v65, v65, v109\nv_mul_f32 v66, v66, v109\nv_mul_f32 v67, v67, v109\nv_cvt_pk_bf16_f32 v64, v64, v65\nv_cvt_pk_bf16_f32 v65, v66, v67\nds_write_b64 v34, v[64:65]\n"
    "v_mov_b32 v9, s34\ns_waitcnt vmcnt(3)\nv_xor_b32 v140, s34, v140\nv_xor_b32 v141, s34, v141\nv_xor_b32 v142, s34, v142\nv_xor_b32 v143, s34, v143\n"
    "v_or3_b32 v140, v140, v141, v142\nv_or_b32 v140, v140, v143\nv_cmp_ne_u32 vcc, 0, v140\ns_mov_b64 exec, 1\nglobal_store_dword v39, v9, s[32:33] sc1\ns_mov_b64 exec, -1\n"
    "s_cmp_eq_u64 vcc, 0\ns_cselect_b32 s43, 1, 0\ns_cbranch_scc1 .LL8\ns_waitcnt vmcnt(0)\ns_branch .LL9\n.LL8:\n"
    "global_load_dwordx4 v[132:135], v58, s[32:33] offset:-4096 sc1\nglobal_load_dwordx4 v[136:139], v58, s[32:33] offset:-3072 sc1\n"
    "global_load_dwordx4 v[140:143], v58, s[32:33] offset:-2048 sc1\nglobal_load_dwordx4 v[144:147], v58, s[32:33] offset:-1024 sc1\n"
    "global_load_dwordx4 v[148:151], v58, s[32:33] offset:0 sc1\nglobal_load_dwordx4 v[152:155], v58, s[32:33] offset:1024 sc1\n"
    "global_load_dwordx4 v[156:159], v58, s[32:33] offset:2048 sc1\nglobal_load_dwordx4 v[160:163], v58, s[32:33] offset:3072 sc1\n.LL9:\n.LHL12:\ns_setprio 1\n"
    "s_waitcnt lgkmcnt(0)\ns_barrier\nds_read_b128 v[28:31], v252\nds_read_b128 v[32:35], v252 offset:208\ns_mov_b32 exec_lo, 0x00ff00ff\ns_mov_b32 exec_hi, 0xff00ff00\n"
    "ds_read_b128 v[10:13], v253\nds_read_b128 v[14:17], v253 offset:208\ns_mov_b64 exec, -1\nds_read_b64_tr_b16 v[36:37], v254 offset:0\n"
    "ds_read_b64_tr_b16 v[38:39], v254 offset:32\nds_read_b64_tr_b16 v[40:41], v254 offset:208\nds_read_b64_tr_b16 v[42:43], v254 offset:240\nds_read_b32 v240, v62 offset:0\n"
    "ds_read_b32 v241, v62 offset:512\nds_read_b32 v242, v62 offset:1024\nds_read_b32 v243, v62 offset:1536\nds_read_b32 v244, v62 offset:2048\n"
    "ds_read_b32 v245, v62 offset:2560\ns_cmp_eq_u32 s43, 0\ns_cbranch_scc1 .LL14\nglobal_load_dwordx4 v[96:99], v5, s[32:33] offset:-4096 sc1\n"
    "global_load_dwordx4 v[100:103], v5, s[32:33] offset:-3072 sc1\nglobal_load_dwordx4 v[104:107], v5, s[32:33] offset:-2048 sc1\n"
    "global_load_dwordx4 v[108:111], v5, s[32:33] offset:-1024 sc1\nglobal_load_dwordx4 v[112:115], v5, s[32:33] offset:0 sc1\n"
    "global_load_dwordx4 v[116:119], v5, s[32:33] offset:1024 sc1\nglobal_load_dwordx4 v[120:123], v5, s[32:33] offset:2048 sc1\n"
    "global_load_dwordx4 v[124:127], v5, s[32:33] offset:3072 sc1\n.LL14:\ns_mov_b32 s81, 0x3eb8aa3b\ns_waitcnt lgkmcnt(11)\n"
    "v_mfma_f32_16x16x32_bf16 v[44:47], v[28:31], v[10:13], v[128:131]\ns_waitcnt lgkmcnt(10)\nv_mfma_f32_16x16x32_bf16 v[48:51], v[32:35], v[14:17], v[128:131]\ns_nop 5\n"
    "v_cvt_pk_bf16_f32 v44, 0, v44\nv_cvt_pk_bf16_f32 v45, 0, v45\nv_cvt_pk_bf16_f32 v46, 0, v46\nv_cvt_pk_bf16_f32 v47, 0, v47\nv_cvt_pk_bf16_f32 v48, 0, v48\n"
    "v_cvt_pk_bf16_f32 v49, 0, v49\nv_cvt_pk_bf16_f32 v50, 0, v50\nv_cvt_pk_bf16_f32 v51, 0, v51\nv_max_f32 v52, v44, v45\nv_max_f32 v53, v48, v49\n"
    "v_max3_f32 v52, v52, v46, v47\nv_max3_f32 v53, v53, v50, v51\ns_nop 1\nv_permlane16_swap_b32 v52, v53\nv_max_f32 v54, v52, v53\nv_max_f32 v53, v52, v53\ns_nop 1\n"
    "v_permlane32_swap_b32 v54, v53\nv_max_f32 v54, v54, v53\nv_mul_f32 v52, 0xbeb8aa3b, v54\nv_mul_f32 v53, 0xbeb8aa3b, v54\ns_nop 1\nv_permlane16_swap_b32 v52, v53\n"
    "v_fma_f32 v44, v44, s81, v52\nv_fma_f32 v45, v45, s81, v52\nv_fma_f32 v46, v46, s81, v52\nv_fma_f32 v47, v47, s81, v52\nv_fma_f32 v48, v48, s81, v53\n"
    "v_fma_f32 v49, v49, s81, v53\nv_fma_f32 v50, v50, s81, v53\nv_fma_f32 v51, v51, s81, v53\nv_exp_f32 v44, v44\nv_exp_f32 v45, v45\nv_exp_f32 v46, v46\nv_exp_f32 v47, v47\n"
    "v_exp_f32 v48, v48\nv_exp_f32 v49, v49\nv_exp_f32 v50, v50\nv_exp_f32 v51, v51\nv_add_f32 v52, v44, v45\nv_add_f32 v55, v46, v47\nv_add_f32 v53, v48, v49\n"
    "v_add_f32 v56, v50, v51\nv_add_f32 v52, v52, v55\nv_add_f32 v53, v53, v56\ns_nop 1\nv_permlane16_swap_b32 v52, v53\nv_add_f32 v54, v52, v53\nv_add_f32 v53, v52, v53\n"
    "s_nop 1\nv_permlane32_swap_b32 v54, v53\nv_add_f32 v54, v54, v53\nv_rcp_f32 v52, v54\nv_rcp_f32 v53, v54\ns_nop 1\nv_permlane16_swap_b32 v52, v53\nv_mul_f32 v44, v44, v52\n"
    "v_mul_f32 v45, v45, v52\nv_mul_f32 v46, v46, v52\nv_mul_f32 v47, v47, v52\nv_mul_f32 v48, v48, v53\nv_mul_f32 v49, v49, v53\nv_mul_f32 v50, v50, v53\n"
    "v_mul_f32 v51, v51, v53\ns_mov_b32 exec_lo, 0x00ff00ff\ns_mov_b32 exec_hi, 0x00ff00ff\nv_cvt_pk_bf16_f32 v18, v44, v45\nv_cvt_pk_bf16_f32 v19, v46, v47\n"
    "v_cvt_pk_bf16_f32 v22, v48, v49\nv_cvt_pk_bf16_f32 v23, v50, v51\ns_not_b64 exec, exec\nv_cvt_pk_bf16_f32 v20, v44, v45\nv_cvt_pk_bf16_f32 v21, v46, v47\n"
    "v_cvt_pk_bf16_f32 v24, v48, v49\nv_cvt_pk_bf16_f32 v25, v50, v51\ns_mov_b64 exec, -1\ns_waitcnt lgkmcnt(0)\nv_mfma_f32_16x16x32_bf16 v[28:31], v[36:39], v[18:21], 0\n"
    "v_mfma_f32_16x16x32_bf16 v[32:35], v[40:43], v[22:25], 0\ns_nop 6\nv_cvt_pk_bf16_f32 v44, v28, v29\nv_cvt_pk_bf16_f32 v45, v30, v31\nv_cvt_pk_bf16_f32 v46, v32, v33\n"
    "v_cvt_pk_bf16_f32 v47, v34, v35\nv_min_u32 v44, -2, v44\nv_min_u32 v46, -2, v46\nv_min_u32 v45, -2, v45\nv_min_u32 v47, -2, v47\nv_permlane16_swap_b32 v44, v46\ns_nop 0\n"
    "v_permlane16_swap_b32 v45, v47\nglobal_store_dwordx4 v1, v[44:47], s[32:33] sc1\ns_setprio 0\ns_cmp_eq_u32 s6, 0\ns_cbranch_scc0 .LL17\ns_cmp_eq_u32 s43, 1\n"
    "s_cselect_b64 vcc, -1, 0\ns_waitcnt vmcnt(18)\ns_mov_b64 exec, 1\nglobal_store_dword v2, v9, s[32:33] sc1\ns_mov_b64 exec, -1\nv_lshlrev_b32 v220, 16, v244\n"
    "s_cbranch_vccz .Lcn18\nglobal_load_dwordx4 v[164:167], v59, s[32:33] offset:-4096 sc1\nglobal_load_dwordx4 v[168:171], v59, s[32:33] offset:-3072 sc1\n"
    "global_load_dwordx4 v[172:175], v59, s[32:33] offset:-2048 sc1\nglobal_load_dwordx4 v[176:179], v59, s[32:33] offset:-1024 sc1\n.Lcn18:\nv_and_b32 v221, 0xffff0000, v244\n"
    "v_lshlrev_b32 v222, 16, v245\nv_and_b32 v223, 0xffff0000, v245\nv_lshlrev_b32 v224, 16, v242\nv_and_b32 v225, 0xffff0000, v242\nv_lshlrev_b32 v226, 16, v243\n"
    "v_and_b32 v227, 0xffff0000, v243\nv_mul_f32 v212, v220, v224\nv_mul_f32 v214, v221, v225\nv_mul_f32 v213, v220, v226\nv_mul_f32 v215, v221, v227\n"
    "v_fma_f32 v212, -v222, v226, v212\nv_fma_f32 v214, -v223, v227, v214\nv_fma_f32 v213, -v222, v224, -v213\nv_fma_f32 v215, -v223, v225, -v215\ns_bitcmp1_b32 s3, 0\n"
    "s_cselect_b32 s86, 0x43800000, 1.0\ns_mov_b32 s87, 0x43810000\ns_cselect_b32 s87, 0x44010000, s87\nv_max3_f32 v236, |v212|, |v213|, |v214|\n"
    "v_max_f32_e64 v236, v236, |v215|\nv_lshlrev_b32 v196, 16, v240\nv_and_b32 v197, 0xffff0000, v240\nv_lshlrev_b32 v198, 16, v241\nv_and_b32 v199, 0xffff0000, v241\n"
    "v_mul_f32 v196, s93, v196\nv_mul_f32 v197, s93, v197\nv_mul_f32 v198, s94, v198\nv_mul_f32 v199, s94, v199\nv_mul_f32 v208, s86, v196\ns_cbranch_vccz .Lcn19\n"
    "global_load_dwordx4 v[180:183], v59, s[32:33] offset:0 sc1\nglobal_load_dwordx4 v[184:187], v59, s[32:33] offset:1024 sc1\n"
    "global_load_dwordx4 v[188:191], v59, s[32:33] offset:2048 sc1\nglobal_load_dwordx4 v[192:195], v59, s[32:33] offset:3072 sc1\n.Lcn19:\nv_mul_f32 v209, s86, v197\n"
    "v_mul_f32 v210, s87, v196\nv_mul_f32 v211, s87, v197\nv_max_f32_dpp v236, v236, v236 row_shr:1 row_mask:0xf bank_mask:0xf bound_ctrl:0\nv_mul_f32 v229, s86, v198\n"
    "v_mul_f32 v231, s86, v199\nv_mul_f32 v217, s87, v198\nv_mul_f32 v219, s87, v199\nv_max_f32_dpp v236, v236, v236 row_shr:2 row_mask:0xf bank_mask:0xf bound_ctrl:0\n"
    "s_barrier\ns_setprio 0\nv_exp_f32 v208, v208\nv_exp_f32 v209, v209\nv_exp_f32 v210, v210\nv_exp_f32 v211, v211\n"
    "v_max_f32_dpp v236, v236, v236 row_shr:4 row_mask:0xf bank_mask:0xf bound_ctrl:0\nv_fract_f32 v229, v229\nv_fract_f32 v231, v231\nv_fract_f32 v217, v217\n"
    "v_fract_f32 v219, v219\nv_max_f32_dpp v236, v236, v236 row_shr:8 row_mask:0xf bank_mask:0xf bound_ctrl:0\nv_cos_f32 v228, v229\nv_cos_f32 v230, v231\nv_cos_f32 v216, v217\n"
    "v_cos_f32 v218, v219\nv_max_f32_dpp v236, v236, v236 row_bcast:15 row_mask:0xf bank_mask:0xf bound_ctrl:0\nv_sin_f32 v229, v229\nv_sin_f32 v231, v231\n"
    "v_sin_f32 v217, v217\nv_sin_f32 v219, v219\ns_cbranch_vccz .Lcn20\nglobal_load_dwordx4 v[64:67], v4, s[32:33] offset:-4096 sc1\n"
    "global_load_dwordx4 v[68:71], v4, s[32:33] offset:-3072 sc1\nglobal_load_dwordx4 v[72:75], v4, s[32:33] offset:-2048 sc1\n"
    "global_load_dwordx4 v[76:79], v4, s[32:33] offset:-1024 sc1\n.Lcn20:\nv_max_f32_dpp v236, v236, v236 row_bcast:31 row_mask:0xf bank_mask:0xf bound_ctrl:0\n"
    "v_mul_f32 v228, v228, v208\nv_mul_f32 v230, v230, v209\nv_mul_f32 v216, v216, v210\nv_mul_f32 v218, v218, v211\nv_mul_f32 v229, v229, v208\nv_mul_f32 v231, v231, v209\n"
    "v_mul_f32 v217, v217, v210\nv_mul_f32 v219, v219, v211\nv_readlane_b32 s86, v236, 63\ns_nop 0\ns_lshr_b32 s86, s86, 23\ns_max_u32 s86, s86, 9\ns_min_u32 s86, s86, 254\n"
    "s_sub_u32 s87, 261, s86\ns_lshl_b32 s87, s87, 23\ns_sub_u32 s86, s86, 7\ns_lshl_b32 s86, s86, 23\nv_mul_f32 v212, s87, v212\nv_mul_f32 v213, s87, v213\n"
    "v_mul_f32 v214, s87, v214\nv_mul_f32 v215, s87, v215\nv_mov_b32 v236, s86\nv_mul_f32 v236, s92, v236\nv_mov_b32 v27, 0\n"
    "v_pk_mul_f32 v[200:201], v[216:217], v[212:213] op_sel_hi:[0,1]\nv_pk_mul_f32 v[202:203], v[218:219], v[214:215] op_sel_hi:[0,1]\n"
    "v_pk_fma_f32 v[216:217], v[216:217], v[212:213], v[200:201] op_sel:[1,1,0] op_sel_hi:[1,0,1] neg_lo:[0,1,0]\n"
    "v_pk_fma_f32 v[218:219], v[218:219], v[214:215], v[202:203] op_sel:[1,1,0] op_sel_hi:[1,0,1] neg_lo:[0,1,0]\ns_bitcmp1_b32 s3, 0\ns_cbranch_scc0 .LL10\n"
    "v_pk_mul_f32 v[200:201], v[212:213], v[228:229] op_sel_hi:[0,1]\nv_pk_mul_f32 v[202:203], v[214:215], v[230:231] op_sel_hi:[0,1]\n"
    "v_pk_fma_f32 v[212:213], v[212:213], v[228:229], v[200:201] op_sel:[1,1,0] op_sel_hi:[1,0,1] neg_lo:[0,1,0]\n"
    "v_pk_fma_f32 v[214:215], v[214:215], v[230:231], v[202:203] op_sel:[1,1,0] op_sel_hi:[1,0,1] neg_lo:[0,1,0]\n.LL10:\ns_cbranch_vccz .Lcn21\n"
    "global_load_dwordx4 v[80:83], v4, s[32:33] offset:0 sc1\nglobal_load_dwordx4 v[84:87], v4, s[32:33] offset:1024 sc1\n"
    "global_load_dwordx4 v[88:91], v4, s[32:33] offset:2048 sc1\nglobal_load_dwordx4 v[92:95], v4, s[32:33] offset:3072 sc1\n.Lcn21:\ns_mov_b64 exec, 1\nds_write_b32 v7, v236\n"
    "ds_write_b16 v8, v27\ns_mov_b64 exec, -1\nv_cvt_pk_f16_f32 v204, v212, v213\nv_cvt_pk_f16_f32 v205, v214, v215\n"
    "v_pk_mul_f32 v[200:201], v[212:213], v[228:229] op_sel_hi:[0,1]\nv_pk_mul_f32 v[202:203], v[214:215], v[230:231] op_sel_hi:[0,1]\nds_write_b64 v3, v[204:205] offset:0\n"
    "v_pk_fma_f32 v[212:213], v[212:213], v[228:229], v[200:201] op_sel:[1,1,0] op_sel_hi:[1,0,1] neg_lo:[0,1,0]\n"
    "v_pk_fma_f32 v[214:215], v[214:215], v[230:231], v[202:203] op_sel:[1,1,0] op_sel_hi:[1,0,1] neg_lo:[0,1,0]\nv_cvt_pk_f16_f32 v206, v212, v213\n"
    "v_cvt_pk_f16_f32 v207, v214, v215\nv_pk_mul_f32 v[200:201], v[212:213], v[228:229] op_sel_hi:[0,1]\nv_pk_mul_f32 v[202:203], v[214:215], v[230:231] op_sel_hi:[0,1]\n"
    "ds_write_b64 v3, v[206:207] offset:544\nv_pk_fma_f32 v[212:213], v[212:213], v[228:229], v[200:201] op_sel:[1,1,0] op_sel_hi:[1,0,1] neg_lo:[0,1,0]\n"
    "v_pk_fma_f32 v[214:215], v[214:215], v[230:231], v[202:203] op_sel:[1,1,0] op_sel_hi:[1,0,1] neg_lo:[0,1,0]\nv_cvt_pk_f16_f32 v204, v212, v213\n"
    "v_cvt_pk_f16_f32 v205, v214, v215\nv_pk_mul_f32 v[200:201], v[212:213], v[228:229] op_sel_hi:[0,1]\nv_pk_mul_f32 v[202:203], v[214:215], v[230:231] op_sel_hi:[0,1]\n"
    "ds_write_b64 v3, v[204:205] offset:1088\nv_pk_fma_f32 v[212:213], v[212:213], v[228:229], v[200:201] op_sel:[1,1,0] op_sel_hi:[1,0,1] neg_lo:[0,1,0]\n"
    "v_pk_fma_f32 v[214:215], v[214:215], v[230:231], v[202:203] op_sel:[1,1,0] op_sel_hi:[1,0,1] neg_lo:[0,1,0]\nv_cvt_pk_f16_f32 v206, v212, v213\n"
    "v_cvt_pk_f16_f32 v207, v214, v215\nv_pk_mul_f32 v[200:201], v[212:213], v[228:229] op_sel_hi:[0,1]\nv_pk_mul_f32 v[202:203], v[214:215], v[230:231] op_sel_hi:[0,1]\n"
    "ds_write_b64 v3, v[206:207] offset:1632\nv_pk_fma_f32 v[212:213], v[212:213], v[228:229], v[200:201] op_sel:[1,1,0] op_sel_hi:[1,0,1] neg_lo:[0,1,0]\n"
    "v_pk_fma_f32 v[214:215], v[214:215], v[230:231], v[202:203] op_sel:[1,1,0] op_sel_hi:[1,0,1] neg_lo:[0,1,0]\nv_cvt_pk_f16_f32 v204, v212, v213\n"
    "v_cvt_pk_f16_f32 v205, v214, v215\nv_pk_mul_f32 v[200:201], v[212:213], v[228:229] op_sel_hi:[0,1]\nv_pk_mul_f32 v[202:203], v[214:215], v[230:231] op_sel_hi:[0,1]\n"
    "ds_write_b64 v3, v[204:205] offset:2176\nv_pk_fma_f32 v[212:213], v[212:213], v[228:229], v[200:201] op_sel:[1,1,0] op_sel_hi:[1,0,1] neg_lo:[0,1,0]\n"
    "v_pk_fma_f32 v[214:215], v[214:215], v[230:231], v[202:203] op_sel:[1,1,0] op_sel_hi:[1,0,1] neg_lo:[0,1,0]\nv_cvt_pk_f16_f32 v206, v212, v213\n"
    "v_cvt_pk_f16_f32 v207, v214, v215\nv_pk_mul_f32 v[200:201], v[212:213], v[228:229] op_sel_hi:[0,1]\nv_pk_mul_f32 v[202:203], v[214:215], v[230:231] op_sel_hi:[0,1]\n"
    "ds_write_b64 v3, v[206:207] offset:2720\nv_pk_fma_f32 v[212:213], v[212:213], v[228:229], v[200:201] op_sel:[1,1,0] op_sel_hi:[1,0,1] neg_lo:[0,1,0]\n"
    "v_pk_fma_f32 v[214:215], v[214:215], v[230:231], v[202:203] op_sel:[1,1,0] op_sel_hi:[1,0,1] neg_lo:[0,1,0]\nv_cvt_pk_f16_f32 v204, v212, v213\n"
    "v_cvt_pk_f16_f32 v205, v214, v215\nv_pk_mul_f32 v[200:201], v[212:213], v[228:229] op_sel_hi:[0,1]\nv_pk_mul_f32 v[202:203], v[214:215], v[230:231] op_sel_hi:[0,1]\n"
    "ds_write_b64 v3, v[204:205] offset:3264\nv_pk_fma_f32 v[212:213], v[212:213], v[228:229], v[200:201] op_sel:[1,1,0] op_sel_hi:[1,0,1] neg_lo:[0,1,0]\n"
    "v_pk_fma_f32 v[214:215], v[214:215], v[230:231], v[202:203] op_sel:[1,1,0] op_sel_hi:[1,0,1] neg_lo:[0,1,0]\nv_cvt_pk_f16_f32 v206, v212, v213\n"
    "v_cvt_pk_f16_f32 v207, v214, v215\nds_write_b64 v3, v[206:207] offset:3808\ns_bitcmp1_b32 s3, 0\ns_cbranch_scc1 .LL11\n"
    "v_pk_mul_f32 v[200:201], v[228:229], v[228:229] op_sel_hi:[0,1]\nv_pk_mul_f32 v[202:203], v[230:231], v[230:231] op_sel_hi:[0,1]\n"
    "v_pk_fma_f32 v[228:229], v[228:229], v[228:229], v[200:201] op_sel:[1,1,0] op_sel_hi:[1,0,1] neg_lo:[0,1,0]\n"
    "v_pk_fma_f32 v[230:231], v[230:231], v[230:231], v[202:203] op_sel:[1,1,0] op_sel_hi:[1,0,1] neg_lo:[0,1,0]\n.LL11:\nv_cvt_pk_f16_f32 v204, v216, v217\n"
    "v_cvt_pk_f16_f32 v205, v218, v219\nv_pk_mul_f32 v[200:201], v[216:217], v[228:229] op_sel_hi:[0,1]\nv_pk_mul_f32 v[202:203], v[218:219], v[230:231] op_sel_hi:[0,1]\n"
    "ds_write_b64 v3, v[204:205] offset:4352\nv_pk_fma_f32 v[216:217], v[216:217], v[228:229], v[200:201] op_sel:[1,1,0] op_sel_hi:[1,0,1] neg_lo:[0,1,0]\n"
    "v_pk_fma_f32 v[218:219], v[218:219], v[230:231], v[202:203] op_sel:[1,1,0] op_sel_hi:[1,0,1] neg_lo:[0,1,0]\nv_cvt_pk_f16_f32 v206, v216, v217\n"
    "v_cvt_pk_f16_f32 v207, v218, v219\nv_pk_mul_f32 v[200:201], v[216:217], v[228:229] op_sel_hi:[0,1]\nv_pk_mul_f32 v[202:203], v[218:219], v[230:231] op_sel_hi:[0,1]\n"
    "ds_write_b64 v3, v[206:207] offset:4896\nv_pk_fma_f32 v[216:217], v[216:217], v[228:229], v[200:201] op_sel:[1,1,0] op_sel_hi:[1,0,1] neg_lo:[0,1,0]\n"
    "v_pk_fma_f32 v[218:219], v[218:219], v[230:231], v[202:203] op_sel:[1,1,0] op_sel_hi:[1,0,1] neg_lo:[0,1,0]\nv_cvt_pk_f16_f32 v204, v216, v217\n"
    "v_cvt_pk_f16_f32 v205, v218, v219\nds_write_b64 v3, v[204:205] offset:5440\ns_setprio 0\nglobal_load_dwordx4 v[240:243], v6, s[8:9]\n"
    "global_load_dwordx4 v[244:247], v6, s[8:9] offset:32\ns_cmp_eq_u32 s43, 1\ns_cbranch_scc1 .LL16\nv_and_b32 v10, 3, v0\ns_lshl2_add_u32 s16, s40, s3\n"
    "s_lshl_b32 s15, s16, 9\ns_add_u32 s14, s15, 0x1002000\nv_lshl_add_u32 v3, v10, 7, s14\nv_and_b32 v11, 4, v0\nv_lshl_add_u32 v3, v11, 2, v3\ns_mov_b32 s80, 0x10000\n"
    "global_load_dwordx4 v[196:199], v3, s[32:33] sc1\ns_waitcnt vmcnt(0)\n.Ltp22:\nv_xor_b32 v196, s34, v196\nv_xor_b32 v197, s34, v197\nv_xor_b32 v198, s34, v198\n"
    "v_xor_b32 v199, s34, v199\nv_or3_b32 v196, v196, v197, v198\nv_or_b32 v196, v196, v199\nv_cmp_ne_u32 vcc, 0, v196\ns_cbranch_vccz .Ltq23\ns_sub_u32 s80, s80, 1\n"
    "s_cbranch_scc1 .Ltq23\ns_sleep 2\nglobal_load_dwordx4 v[196:199], v3, s[32:33] sc1\ns_waitcnt vmcnt(0)\ns_branch .Ltp22\n.Ltq23:\n"
    "global_load_dwordx4 v[132:135], v58, s[32:33] offset:-4096 sc1\nglobal_load_dwordx4 v[136:139], v58, s[32:33] offset:-3072 sc1\n"
    "global_load_dwordx4 v[140:143], v58, s[32:33] offset:-2048 sc1\nglobal_load_dwordx4 v[144:147], v58, s[32:33] offset:-1024 sc1\n"
    "global_load_dwordx4 v[148:151], v58, s[32:33] offset:0 sc1\nglobal_load_dwordx4 v[152:155], v58, s[32:33] offset:1024 sc1\n"
    "global_load_dwordx4 v[156:159], v58, s[32:33] offset:2048 sc1\nglobal_load_dwordx4 v[160:163], v58, s[32:33] offset:3072 sc1\n"
    "global_load_dwordx4 v[164:167], v59, s[32:33] offset:-4096 sc1\nglobal_load_dwordx4 v[168:171], v59, s[32:33] offset:-3072 sc1\n"
    "global_load_dwordx4 v[172:175], v59, s[32:33] offset:-2048 sc1\nglobal_load_dwordx4 v[176:179], v59, s[32:33] offset:-1024 sc1\n"
    "global_load_dwordx4 v[180:183], v59, s[32:33] offset:0 sc1\nglobal_load_dwordx4 v[184:187], v59, s[32:33] offset:1024 sc1\n"
    "global_load_dwordx4 v[188:191], v59, s[32:33] offset:2048 sc1\nglobal_load_dwordx4 v[192:195], v59, s[32:33] offset:3072 sc1\n"
    "global_load_dwordx4 v[64:67], v4, s[32:33] offset:-4096 sc1\nglobal_load_dwordx4 v[68:71], v4, s[32:33] offset:-3072 sc1\n"
    "global_load_dwordx4 v[72:75], v4, s[32:33] offset:-2048 sc1\nglobal_load_dwordx4 v[76:79], v4, s[32:33] offset:-1024 sc1\n"
    "global_load_dwordx4 v[80:83], v4, s[32:33] offset:0 sc1\nglobal_load_dwordx4 v[84:87], v4, s[32:33] offset:1024 sc1\n"
    "global_load_dwordx4 v[88:91], v4, s[32:33] offset:2048 sc1\nglobal_load_dwordx4 v[92:95], v4, s[32:33] offset:3072 sc1\n"
    "global_load_dwordx4 v[96:99], v5, s[32:33] offset:-4096 sc1\nglobal_load_dwordx4 v[100:103], v5, s[32:33] offset:-3072 sc1\n"
    "global_load_dwordx4 v[104:107], v5, s[32:33] offset:-2048 sc1\nglobal_load_dwordx4 v[108:111], v5, s[32:33] offset:-1024 sc1\n"
    "global_load_dwordx4 v[112:115], v5, s[32:33] offset:0 sc1\nglobal_load_dwordx4 v[116:119], v5, s[32:33] offset:1024 sc1\n"
    "global_load_dwordx4 v[120:123], v5, s[32:33] offset:2048 sc1\nglobal_load_dwordx4 v[124:127], v5, s[32:33] offset:3072 sc1\ns_waitcnt vmcnt(0)\ns_mov_b32 s43, 1\n.LL16:\n"
    "s_branch .LL15\n.LL17:\ns_barrier\nv_and_b32 v10, 63, v0\ns_lshl_b32 s16, s39, 15\ns_lshl2_add_u32 s18, s40, s3\ns_lshl_b32 s17, s18, 11\ns_add_u32 s15, s16, s17\n"
    "s_add_u32 s14, s15, 0x600000\nv_lshl_add_u32 v2, v10, 4, s14\ns_lshl_b32 s20, s3, 11\ns_add_u32 s19, s20, 0x1c200\nv_lshl_add_u32 v3, v10, 4, s19\nv_bfe_u32 v12, v0, 5, 1\n"
    "v_bfe_u32 v13, v0, 2, 2\nv_lshl_add_u32 v11, v12, 2, v13\nv_lshl_add_u32 v4, v11, 6, s19\nv_bfe_u32 v14, v0, 4, 1\nv_lshl_add_u32 v4, v14, 5, v4\nv_and_b32 v15, 3, v0\n"
    "v_lshl_add_u32 v4, v15, 3, v4\nglobal_load_dwordx4 v[240:243], v2, s[32:33] offset:0 sc1\nglobal_load_dwordx4 v[244:247], v2, s[32:33] offset:1024 sc1\n"
    "s_mov_b32 s80, 0x10000\n.LL24:\ns_waitcnt vmcnt(0)\ns_mov_b64 s[82:83], 0\nv_max3_u32 v5, v240, v241, v242\nv_max_u32 v5, v5, v243\nv_cmp_eq_u32 vcc, -1, v5\ns_nop 0\n"
    "s_or_b64 s[82:83], s[82:83], vcc\ns_cbranch_vccz .Lag26\ns_mov_b64 exec, vcc\nglobal_load_dwordx4 v[240:243], v2, s[32:33] offset:0 sc1\ns_mov_b64 exec, -1\n.Lag26:\n"
    "v_max3_u32 v5, v244, v245, v246\nv_max_u32 v5, v5, v247\nv_cmp_eq_u32 vcc, -1, v5\ns_nop 0\ns_or_b64 s[82:83], s[82:83], vcc\ns_cbranch_vccz .Lag27\ns_mov_b64 exec, vcc\n"
    "global_load_dwordx4 v[244:247], v2, s[32:33] offset:1024 sc1\ns_mov_b64 exec, -1\n.Lag27:\ns_cmp_eq_u64 s[82:83], 0\ns_cbranch_scc1 .LL25\ns_sub_u32 s80, s80, 1\n"
    "s_cbranch_scc0 .LL24\ns_waitcnt vmcnt(0)\n.LL25:\nds_write_b128 v3, v[240:243]\nds_write_b128 v3, v[244:247] offset:1024\nds_read_b64_tr_b16 v[240:241], v4 offset:0\n"
    "ds_read_b64_tr_b16 v[242:243], v4 offset:512\nds_read_b64_tr_b16 v[244:245], v4 offset:1024\nds_read_b64_tr_b16 v[246:247], v4 offset:1536\ns_waitcnt lgkmcnt(0)\n.LL15:\n"
    "v_bfe_u32 v40, v0, 5, 1\ns_lshl_b32 s15, s3, 11\ns_add_u32 s14, s15, 0x1f600\nv_lshl_add_u32 v56, v40, 8, s14\nv_and_b32 v41, 31, v0\nv_lshl_add_u32 v56, v41, 2, v56\n"
    "s_add_u32 s16, s15, 0x1fa00\nv_lshl_add_u32 v57, v40, 8, s16\nv_lshl_add_u32 v57, v41, 2, v57\nv_and_b32 v42, 63, v0\nv_lshl_add_u32 v58, v42, 4, s14\n"
    "s_mov_b32 s17, 0x1f600\nv_lshl_add_u32 v59, v42, 4, s17\nv_bfe_u32 v43, v0, 3, 3\ns_lshl_b32 s23, s40, 6\ns_lshl4_add_u32 s22, s3, s23\ns_lshl_b32 s21, s22, 13\n"
    "s_add_u32 s20, s7, s21\ns_lshl_b32 s24, s39, 7\ns_add_u32 s19, s20, s24\ns_add_u32 s18, s19, 0x200000\nv_lshl_add_u32 v60, v43, 13, s18\nv_and_b32 v44, 7, v0\n"
    "v_lshl_add_u32 v60, v44, 4, v60\ns_add_u32 s25, s19, 0x210000\nv_lshl_add_u32 v61, v43, 13, s25\nv_lshl_add_u32 v61, v44, 4, v61\ns_lshl_b32 s45, s3, 8\n"
    "s_add_u32 s44, s45, 0x27600\nv_lshl_add_u32 v62, v42, 2, s44\ns_add_u32 s14, s7, 0x401000\nv_lshl_add_u32 v20, v0, 4, s14\ns_lshl_b32 s17, s41, 13\ns_add_u32 s16, s7, s17\n"
    "s_add_u32 s15, s16, 0x200000\nv_lshl_add_u32 v21, v0, 4, s15\nv_add_u32 v22, 8192, v20\nv_add_u32 v23, 16384, v20\nv_add_u32 v24, 24576, v20\nv_add_u32 v25, 32768, v20\n"
    "v_add_u32 v26, 40960, v20\nv_add_u32 v27, 49152, v20\nv_add_u32 v28, 57344, v20\nv_mov_b32 v36, 0x3f803f80\ns_waitcnt vmcnt(2) lgkmcnt(0)\ns_setprio 2\ns_barrier\n"
    "ds_read_b128 v[196:199], v59 offset:0\nds_read_b128 v[200:203], v59 offset:1024\nds_read_b128 v[204:207], v59 offset:2048\nds_read_b128 v[208:211], v59 offset:3072\n"
    "ds_read_b128 v[212:215], v59 offset:4096\nds_read_b128 v[216:219], v59 offset:5120\ns_waitcnt lgkmcnt(5)\nv_mfma_f32_32x32x16_bf16 v[2:17], v[132:135], v[196:199], 0\n"
    "ds_read_b128 v[196:199], v59 offset:6144\ns_waitcnt lgkmcnt(5)\nv_mfma_f32_32x32x16_bf16 v[2:17], v[136:139], v[200:203], v[2:17]\n"
    "ds_read_b128 v[200:203], v59 offset:7168\ns_waitcnt lgkmcnt(5)\nv_mfma_f32_32x32x16_bf16 v[2:17], v[140:143], v[204:207], v[2:17]\ns_barrier\n"
    "ds_read_b128 v[204:207], v59 offset:8192\ns_waitcnt lgkmcnt(5)\nv_mfma_f32_32x32x16_bf16 v[2:17], v[144:147], v[208:211], v[2:17]\n"
    "ds_read_b128 v[208:211], v59 offset:9216\ns_waitcnt lgkmcnt(5)\nv_mfma_f32_32x32x16_bf16 v[2:17], v[148:151], v[212:215], v[2:17]\n"
    "ds_read_b128 v[212:215], v59 offset:10240\ns_waitcnt lgkmcnt(5)\nv_mfma_f32_32x32x16_bf16 v[2:17], v[152:155], v[216:219], v[2:17]\n"
    "ds_read_b128 v[216:219], v59 offset:11264\ns_waitcnt lgkmcnt(5)\nv_mfma_f32_32x32x16_bf16 v[2:17], v[156:159], v[196:199], v[2:17]\n"
    "ds_read_b128 v[196:199], v59 offset:12288\ns_waitcnt lgkmcnt(5)\nv_mfma_f32_32x32x16_bf16 v[2:17], v[160:163], v[200:203], v[2:17]\n"
    "ds_read_b128 v[200:203], v59 offset:13312\ns_waitcnt lgkmcnt(5)\nv_mfma_f32_32x32x16_bf16 v[2:17], v[164:167], v[204:207], v[2:17]\n"
    "ds_read_b128 v[204:207], v59 offset:14336\ns_waitcnt lgkmcnt(5)\nv_mfma_f32_32x32x16_bf16 v[2:17], v[168:171], v[208:211], v[2:17]\n"
    "ds_read_b128 v[208:211], v59 offset:15360\ns_waitcnt lgkmcnt(5)\nv_mfma_f32_32x32x16_bf16 v[2:17], v[172:175], v[212:215], v[2:17]\ns_barrier\n"
    "ds_read_b128 v[212:215], v59 offset:16384\ns_waitcnt lgkmcnt(5)\nv_mfma_f32_32x32x16_bf16 v[2:17], v[176:179], v[216:219], v[2:17]\n"
    "ds_read_b128 v[216:219], v59 offset:17408\ns_waitcnt lgkmcnt(5)\nv_mfma_f32_32x32x16_bf16 v[2:17], v[180:183], v[196:199], v[2:17]\n"
    "ds_read_b128 v[196:199], v59 offset:18432\ns_waitcnt lgkmcnt(5)\nv_mfma_f32_32x32x16_bf16 v[2:17], v[184:187], v[200:203], v[2:17]\n"
    "ds_read_b128 v[200:203], v59 offset:19456\ns_waitcnt lgkmcnt(5)\nv_mfma_f32_32x32x16_bf16 v[2:17], v[188:191], v[204:207], v[2:17]\n"
    "ds_read_b128 v[204:207], v59 offset:20480\ns_waitcnt lgkmcnt(5)\nv_mfma_f32_32x32x16_bf16 v[2:17], v[192:195], v[208:211], v[2:17]\n"
    "ds_read_b128 v[208:211], v59 offset:21504\ns_waitcnt lgkmcnt(5)\nv_mfma_f32_32x32x16_bf16 v[2:17], v[64:67], v[212:215], v[2:17]\n"
    "ds_read_b128 v[212:215], v59 offset:22528\ns_waitcnt lgkmcnt(5)\nv_mfma_f32_32x32x16_bf16 v[2:17], v[68:71], v[216:219], v[2:17]\n"
    "ds_read_b128 v[216:219], v59 offset:23552\ns_waitcnt lgkmcnt(5)\nv_mfma_f32_32x32x16_bf16 v[2:17], v[72:75], v[196:199], v[2:17]\ns_barrier\n"
    "ds_read_b128 v[196:199], v59 offset:24576\ns_waitcnt lgkmcnt(5)\nv_mfma_f32_32x32x16_bf16 v[2:17], v[76:79], v[200:203], v[2:17]\n"
    "ds_read_b128 v[200:203], v59 offset:25600\ns_waitcnt lgkmcnt(5)\nv_mfma_f32_32x32x16_bf16 v[2:17], v[80:83], v[204:207], v[2:17]\n"
    "ds_read_b128 v[204:207], v59 offset:26624\ns_waitcnt lgkmcnt(5)\nv_mfma_f32_32x32x16_bf16 v[2:17], v[84:87], v[208:211], v[2:17]\n"
    "ds_read_b128 v[208:211], v59 offset:27648\ns_waitcnt lgkmcnt(5)\nv_mfma_f32_32x32x16_bf16 v[2:17], v[88:91], v[212:215], v[2:17]\n"
    "ds_read_b128 v[212:215], v59 offset:28672\ns_waitcnt lgkmcnt(5)\nv_mfma_f32_32x32x16_bf16 v[2:17], v[92:95], v[216:219], v[2:17]\n"
    "ds_read_b128 v[216:219], v59 offset:29696\ns_waitcnt lgkmcnt(5)\nv_mfma_f32_32x32x16_bf16 v[2:17], v[96:99], v[196:199], v[2:17]\n"
    "ds_read_b128 v[196:199], v59 offset:30720\ns_waitcnt lgkmcnt(5)\nv_mfma_f32_32x32x16_bf16 v[2:17], v[100:103], v[200:203], v[2:17]\n"
    "ds_read_b128 v[200:203], v59 offset:31744\ns_cmp_eq_u32 s6, 0\ns_cbranch_scc0 .LL28\ns_waitcnt vmcnt(0)\nv_permlane32_swap_b32 v240, v242\n"
    "v_permlane32_swap_b32 v241, v243\nv_permlane32_swap_b32 v244, v246\nv_permlane32_swap_b32 v245, v247\n.LL28:\ns_waitcnt lgkmcnt(5)\n"
    "v_mfma_f32_32x32x16_bf16 v[2:17], v[104:107], v[204:207], v[2:17]\ns_waitcnt lgkmcnt(4)\nv_mfma_f32_32x32x16_bf16 v[2:17], v[108:111], v[208:211], v[2:17]\n"
    "v_lshlrev_b32 v220, 16, v240\nv_and_b32 v221, 0xffff0000, v240\nv_lshlrev_b32 v222, 16, v241\nv_and_b32 v223, 0xffff0000, v241\ns_waitcnt lgkmcnt(3)\n"
    "v_mfma_f32_32x32x16_bf16 v[2:17], v[112:115], v[212:215], v[2:17]\nv_lshlrev_b32 v224, 16, v242\nv_and_b32 v225, 0xffff0000, v242\nv_lshlrev_b32 v226, 16, v243\n"
    "v_and_b32 v227, 0xffff0000, v243\ns_waitcnt lgkmcnt(2)\nv_mfma_f32_32x32x16_bf16 v[2:17], v[116:119], v[216:219], v[2:17]\nv_lshlrev_b32 v228, 16, v244\n"
    "v_and_b32 v229, 0xffff0000, v244\nv_lshlrev_b32 v230, 16, v245\nv_and_b32 v231, 0xffff0000, v245\ns_waitcnt lgkmcnt(1)\n"
    "v_mfma_f32_32x32x16_bf16 v[2:17], v[120:123], v[196:199], v[2:17]\nv_lshlrev_b32 v232, 16, v246\nv_and_b32 v233, 0xffff0000, v246\nv_lshlrev_b32 v234, 16, v247\n"
    "v_and_b32 v235, 0xffff0000, v247\ns_waitcnt lgkmcnt(0)\nv_mfma_f32_32x32x16_bf16 v[2:17], v[124:127], v[200:203], v[2:17]\ns_nop 11\nv_cvt_pk_bf16_f32 v2, 0, v2\n"
    "v_cvt_pk_bf16_f32 v3, 0, v3\nv_cvt_pk_bf16_f32 v4, 0, v4\nv_cvt_pk_bf16_f32 v5, 0, v5\nv_cvt_pk_bf16_f32 v6, 0, v6\nv_cvt_pk_bf16_f32 v7, 0, v7\n"
    "v_cvt_pk_bf16_f32 v8, 0, v8\nv_cvt_pk_bf16_f32 v9, 0, v9\nv_cvt_pk_bf16_f32 v10, 0, v10\nv_cvt_pk_bf16_f32 v11, 0, v11\nv_cvt_pk_bf16_f32 v12, 0, v12\n"
    "v_cvt_pk_bf16_f32 v13, 0, v13\nv_cvt_pk_bf16_f32 v14, 0, v14\nv_cvt_pk_bf16_f32 v15, 0, v15\nv_cvt_pk_bf16_f32 v16, 0, v16\nv_cvt_pk_bf16_f32 v17, 0, v17\n"
    "v_pk_add_f32 v[2:3], v[2:3], v[220:221]\nv_pk_add_f32 v[4:5], v[4:5], v[222:223]\nv_pk_add_f32 v[6:7], v[6:7], v[224:225]\nv_pk_add_f32 v[8:9], v[8:9], v[226:227]\n"
    "v_pk_add_f32 v[10:11], v[10:11], v[228:229]\nv_pk_add_f32 v[12:13], v[12:13], v[230:231]\nv_pk_add_f32 v[14:15], v[14:15], v[232:233]\n"
    "v_pk_add_f32 v[16:17], v[16:17], v[234:235]\nv_cvt_pk_bf16_f32 v40, v2, v3\nv_cvt_pk_bf16_f32 v41, v4, v5\nv_cvt_pk_bf16_f32 v42, v6, v7\nv_cvt_pk_bf16_f32 v43, v8, v9\n"
    "v_cvt_pk_bf16_f32 v44, v10, v11\nv_cvt_pk_bf16_f32 v45, v12, v13\nv_cvt_pk_bf16_f32 v46, v14, v15\nv_cvt_pk_bf16_f32 v47, v16, v17\nv_min_u32 v48, -2, v40\n"
    "v_min_u32 v49, -2, v41\nds_write2_b32 v56, v48, v49 offset0:0 offset1:32\nv_min_u32 v50, -2, v42\nv_min_u32 v51, -2, v43\n"
    "ds_write2_b32 v56, v50, v51 offset0:128 offset1:160\nv_min_u32 v52, -2, v44\nv_min_u32 v53, -2, v45\nds_write2_b32 v57, v52, v53 offset0:0 offset1:32\n"
    "v_min_u32 v54, -2, v46\nv_min_u32 v55, -2, v47\nds_write2_b32 v57, v54, v55 offset0:128 offset1:160\nds_read_b128 v[2:5], v58\nds_read_b128 v[6:9], v58 offset:1024\n"
    "v_dot2_f32_bf16 v32, v40, v36, 0\nv_dot2_f32_bf16 v33, v40, v40, 0\nv_dot2_f32_bf16 v34, v41, v36, 0\nv_dot2_f32_bf16 v35, v41, v41, 0\nv_dot2c_f32_bf16 v32, v36, v42\n"
    "v_dot2c_f32_bf16 v33, v42, v42\nv_dot2c_f32_bf16 v34, v36, v43\nv_dot2c_f32_bf16 v35, v43, v43\nv_dot2c_f32_bf16 v32, v36, v44\nv_dot2c_f32_bf16 v33, v44, v44\n"
    "v_dot2c_f32_bf16 v34, v36, v45\nv_dot2c_f32_bf16 v35, v45, v45\nv_dot2c_f32_bf16 v32, v36, v46\nv_dot2c_f32_bf16 v33, v46, v46\nv_dot2c_f32_bf16 v34, v36, v47\n"
    "v_dot2c_f32_bf16 v35, v47, v47\ns_nop 1\nv_add_f32 v32, v32, v34\nv_add_f32 v33, v33, v35\ns_nop 1\nv_permlane32_swap_b32 v32, v33\nv_add_f32 v32, v32, v33\n"
    "ds_write_b32 v62, v32\ns_waitcnt lgkmcnt(0)\ns_barrier\nglobal_store_dwordx4 v60, v[2:5], s[32:33] sc1\nglobal_store_dwordx4 v61, v[6:9], s[32:33] sc1\ns_setprio 0\n"
    "s_mov_b32 s14, 0x1d550\nv_lshl_add_u32 v14, v0, 3, s14\nv_lshrrev_b32 v48, 3, v0\nv_lshl_add_u32 v14, v48, 4, v14\ns_mov_b32 s15, 0x20300\nv_lshl_add_u32 v15, v48, 7, s15\n"
    "v_xor_b32 v50, v0, v48\nv_and_b32 v49, 7, v50\nv_lshl_add_u32 v15, v49, 4, v15\ns_mov_b32 s16, 0x17800\nv_lshl_add_u32 v16, v0, 4, s16\ns_lshl_b32 s18, s41, 7\n"
    "s_add_u32 s17, s18, 0x600000\nv_lshl_add_u32 v51, v48, 15, s17\nv_and_b32 v52, 1, v0\nv_lshl_add_u32 v51, v52, 6, v51\nv_bfe_u32 v53, v0, 1, 2\n"
    "v_lshl_add_u32 v51, v53, 4, v51\nv_lshrrev_b32 v55, 4, v0\ns_lshl_b32 s21, s35, 18\ns_lshl_b32 s22, s36, 8\ns_add_u32 s20, s21, s22\ns_add_u32 s19, s20, 0x1200000\n"
    "v_lshl_add_u32 v54, v55, 13, s19\nv_and_b32 v56, 15, v0\nv_lshl_add_u32 v54, v56, 4, v54\ns_cmp_eq_u32 s6, 0\ns_cselect_b64 vcc, -1, 0\nv_cndmask_b32 v17, v54, v51, vcc\n"
    "v_bfe_u32 v59, v0, 5, 1\nv_and_b32 v61, 31, v0\nv_sub_u32 v60, 0x3ff, v61\nv_lshl_add_u32 v58, v59, 3, v60\nv_lshrrev_b32 v57, 2, v58\ns_lshr_b32 s25, s3, 1\n"
    "s_mul_i32 s24, s25, 0x2100\ns_add_u32 s23, s24, 0x17800\nv_lshl_add_u32 v19, v57, 3, s23\nv_and_b32 v62, 3, v58\ns_mov_b32 s44, 0x840\nv_mad_u32_u24 v19, v62, s44, v19\n"
    "s_lshl2_add_u32 s45, s3, 0x27f80\nv_mov_b32 v12, s45\ns_add_u32 s46, s6, 12\nv_mov_b32 v13, s46\ns_mov_b32 s47, 0x1cc34\nv_lshl_add_u32 v5, v0, 3, s47\n"
    "v_add_u32 v64, 0xffffff86, v0\nv_lshrrev_b32 v63, 3, v64\nv_lshl_add_u32 v5, v63, 4, v5\ns_mov_b32 s48, 0x1cc38\nv_lshl_add_u32 v6, v0, 3, s48\n"
    "v_add_u32 v66, 0xffffff87, v0\nv_lshrrev_b32 v65, 3, v66\nv_lshl_add_u32 v6, v65, 4, v6\nv_bfe_u32 v68, v0, 2, 2\nv_lshl_add_u32 v67, v59, 3, v68\n"
    "s_mul_i32 s50, s25, 0x3180\ns_add_u32 s49, s50, 0x20300\nv_lshl_add_u32 v2, v67, 4, s49\ns_cmp_eq_u32 s6, 0\ns_cbranch_scc1 .LL44\ns_sleep 12\ns_branch .LL45\n.LL44:\n"
    "s_barrier\ns_sleep 0\n.LL45:\nv_lshl_add_u32 v2, v52, 3, v2\ns_and_b32 s52, s3, 1\ns_xor_b32 s51, s52, 1\nv_and_b32 v70, 3, v55\nv_and_b32 v69, s51, v70\n"
    "s_mov_b32 s53, 0x480\nv_mad_u32_u24 v2, v69, s53, v2\nv_bfe_u32 v71, v0, 1, 1\ns_mov_b32 s54, 0x240\nv_mad_u32_u24 v2, v71, s54, v2\ns_add_u32 s55, s49, 0x1f80\n"
    "v_lshl_add_u32 v4, v67, 5, s55\nv_and_b32 v72, 3, v0\nv_lshl_add_u32 v4, v72, 3, v4\nv_and_b32 v73, 1, v55\nv_mad_u32_u24 v3, v73, s53, v4\ns_mul_i32 s57, s25, 0x2800\n"
    "s_add_u32 s56, s57, 0x18000\nv_lshl_add_u32 v7, v61, 5, s56\nv_lshl_add_u32 v7, v59, 4, v7\ns_add_u32 s58, s57, 0x19800\nv_lshl_add_u32 v8, v61, 6, s58\n"
    "v_lshrrev_b32 v76, 1, v0\nv_and_b32 v75, 2, v76\nv_xor_b32 v74, v59, v75\nv_lshl_add_u32 v8, v74, 4, v8\nv_lshlrev_b32 v78, 4, v0\nv_and_b32 v80, 16, v0\n"
    "v_lshlrev_b32 v79, 1, v80\nv_xor_b32 v77, v78, v79\nv_add_u32 v9, 0x18800, v77\ns_lshl_b32 s59, s51, 8\nv_add_u32 v10, s59, v19\nv_and_b32 v81, 63, v0\n"
    "s_mul_i32 s62, s25, 0x1950\ns_mul_i32 s63, s52, 0x500\ns_add_u32 s61, s62, s63\ns_add_u32 s60, s61, 0x1d530\nv_lshl_add_u32 v40, v81, 4, s60\nv_add_u32 v83, 3, v81\n"
    "v_lshrrev_b32 v82, 2, v83\nv_lshl_add_u32 v40, v82, 4, v40\ns_add_u32 s64, s61, 0x1d528\nv_lshl_add_u32 v41, v81, 4, s64\nv_add_u32 v85, 2, v81\nv_lshrrev_b32 v84, 2, v85\n"
    "v_lshl_add_u32 v41, v84, 4, v41\ns_add_u32 s65, s61, 0x1d550\nv_lshl_add_u32 v42, v81, 4, s65\nv_bfe_u32 v86, v0, 2, 4\nv_lshl_add_u32 v42, v86, 4, v42\n"
    "s_mul_i32 s68, s52, 0x480\ns_add_u32 s67, s49, s68\ns_add_u32 s66, s67, 0x250\nv_lshl_add_u32 v43, v81, 4, s66\nv_add_u32 v88, 1, v81\nv_lshrrev_b32 v87, 5, v88\n"
    "v_lshl_add_u32 v43, v87, 6, v43\ns_add_u32 s69, s67, 0x260\nv_lshl_add_u32 v44, v81, 4, s69\nv_lshrrev_b32 v89, 5, v85\nv_lshl_add_u32 v44, v89, 6, v44\n"
    "s_add_u32 s70, s67, 0x1f90\nv_lshl_add_u32 v90, v81, 4, s70\nv_lshrrev_b32 v91, 6, v88\nv_lshl_add_u32 v90, v91, 7, v90\ns_add_u32 s73, s67, 0x1f80\n"
    "s_add_u32 s72, s73, 0x400\ns_add_u32 s71, s72, 0xfffffb80\nv_mov_b32 v92, s71\nv_add_u32 v93, s52, v88\nv_cmp_gt_u32 vcc, 0x41, v93\ns_nop 1\n"
    "v_cndmask_b32 v45, v92, v90, vcc\ns_add_u32 s74, s73, 0x900\nv_lshl_add_u32 v29, v81, 4, s74\ns_add_u32 s76, s67, 0x440\ns_add_u32 s75, s76, 0xfffff700\n"
    "v_mov_b32 v94, s75\nv_cmp_gt_u32 vcc, 64, v88\ns_nop 1\nv_cndmask_b32 v30, v94, v43, vcc\nv_cmp_gt_u32 vcc, 64, v85\ns_nop 1\nv_cndmask_b32 v31, v94, v44, vcc\n"
    "s_lshl3_add_u32 s77, s25, 0x27fd0\nv_lshl_add_u32 v11, v52, 2, s77\nv_mov_b32 v34, 125952\nv_mov_b32 v32, 163216\ns_barrier\ns_cmp_eq_u32 s6, 0\ns_cbranch_scc0 .LL46\n"
    "ds_read_b64 v[32:33], v32\nds_read_u16 v35, v34 offset:1536\nds_read_u16 v34, v34\ns_waitcnt lgkmcnt(0)\ns_barrier\n.LL46:\n"
    "global_load_dwordx4 v[240:243], v21, s[32:33] sc1\nglobal_load_dwordx4 v[208:211], v20, s[32:33] offset:-4096 sc1\n"
    "global_load_dwordx4 v[212:215], v22, s[32:33] offset:-4096 sc1\nglobal_load_dwordx4 v[216:219], v23, s[32:33] offset:-4096 sc1\n"
    "global_load_dwordx4 v[220:223], v24, s[32:33] offset:-4096 sc1\nglobal_load_dwordx4 v[224:227], v25, s[32:33] offset:-4096 sc1\n"
    "global_load_dwordx4 v[228:231], v26, s[32:33] offset:-4096 sc1\nglobal_load_dwordx4 v[232:235], v27, s[32:33] offset:-4096 sc1\n"
    "global_load_dwordx4 v[236:239], v28, s[32:33] offset:-4096 sc1\ns_setprio 0\ns_cmp_eq_u32 s6, 0\ns_cbranch_scc0 .LL29\nv_cvt_f32_f16 v34, v34\nv_cvt_f32_f16 v35, v35\n"
    "v_readfirstlane_b32 s88, v32\nv_readfirstlane_b32 s90, v33\nv_readfirstlane_b32 s92, v34\nv_readfirstlane_b32 s94, v35\n.LL29:\nds_read_b64 v[64:65], v19 offset:896\n"
    "ds_read_b64 v[66:67], v19 offset:904\nds_read_b64 v[68:69], v19 offset:864\nds_read_b64 v[70:71], v19 offset:872\nds_read_b64 v[72:73], v19 offset:832\n"
    "ds_read_b64 v[74:75], v19 offset:840\nds_read_b64 v[76:77], v19 offset:800\nds_read_b64 v[78:79], v19 offset:808\nds_read_b64 v[80:81], v19 offset:704\n"
    "ds_read_b64 v[82:83], v19 offset:712\nds_read_b64 v[84:85], v19 offset:672\nds_read_b64 v[86:87], v19 offset:680\nds_read_b64 v[88:89], v19 offset:640\n"
    "ds_read_b64 v[90:91], v19 offset:648\nds_read_b64 v[92:93], v19 offset:608\nds_read_b64 v[94:95], v19 offset:616\nds_read_b64 v[96:97], v10 offset:512\n"
    "ds_read_b64 v[98:99], v10 offset:520\nds_read_b64 v[100:101], v10 offset:480\nds_read_b64 v[102:103], v10 offset:488\nds_read_b64 v[104:105], v10 offset:448\n"
    "ds_read_b64 v[106:107], v10 offset:456\nds_read_b64 v[108:109], v10 offset:416\nds_read_b64 v[110:111], v10 offset:424\nds_read_b64 v[112:113], v10 offset:320\n"
    "ds_read_b64 v[114:115], v10 offset:328\nds_read_b64 v[116:117], v10 offset:288\nds_read_b64 v[118:119], v10 offset:296\nds_read_b64 v[120:121], v10 offset:256\n"
    "ds_read_b64 v[122:123], v10 offset:264\nds_read_b64 v[124:125], v10 offset:224\nds_read_b64 v[126:127], v10 offset:232\ns_waitcnt lgkmcnt(0)\ns_mov_b32 s84, 0x3b000000\n"
    "s_mov_b32 s86, 0x3727c5ac\ns_mov_b32 s80, 0x10000\ns_waitcnt vmcnt(8)\n.Lln32:\nv_max3_u32 v60, v240, v241, v242\nv_max_u32 v60, v60, v243\nv_cmp_eq_u32 s[82:83], -1, v60\n"
    "v_lshlrev_b32 v32, 16, v240\nv_and_b32 v33, 0xffff0000, v240\nv_lshlrev_b32 v34, 16, v241\nv_and_b32 v35, 0xffff0000, v241\nv_lshlrev_b32 v36, 16, v242\n"
    "v_and_b32 v37, 0xffff0000, v242\nv_lshlrev_b32 v38, 16, v243\nv_and_b32 v39, 0xffff0000, v243\ns_waitcnt vmcnt(4)\nv_pk_add_f32 v[48:49], v[208:209], v[216:217]\n"
    "v_pk_add_f32 v[50:51], v[210:211], v[218:219]\nv_pk_add_f32 v[52:53], v[212:213], v[220:221]\nv_pk_add_f32 v[54:55], v[214:215], v[222:223]\ns_waitcnt vmcnt(1)\n"
    "v_pk_add_f32 v[56:57], v[224:225], v[232:233]\nv_pk_add_f32 v[58:59], v[226:227], v[234:235]\nv_pk_add_f32 v[48:49], v[48:49], v[56:57]\n"
    "v_pk_add_f32 v[50:51], v[50:51], v[58:59]\nv_pk_mul_f32 v[48:49], v[48:49], s[84:85] op_sel_hi:[1,0]\nv_pk_mul_f32 v[50:51], v[50:51], s[84:85] op_sel_hi:[1,0]\n"
    "s_waitcnt vmcnt(0)\nv_pk_add_f32 v[56:57], v[228:229], v[236:237]\nv_pk_add_f32 v[58:59], v[230:231], v[238:239]\nv_pk_add_f32 v[52:53], v[52:53], v[56:57]\n"
    "v_pk_add_f32 v[54:55], v[54:55], v[58:59]\nv_pk_mul_f32 v[56:57], v[48:49], v[48:49]\nv_pk_mul_f32 v[58:59], v[50:51], v[50:51]\n"
    "v_pk_fma_f32 v[52:53], v[52:53], s[84:85], v[56:57] op_sel_hi:[1,0,1] neg_lo:[0,0,1] neg_hi:[0,0,1]\n"
    "v_pk_fma_f32 v[54:55], v[54:55], s[84:85], v[58:59] op_sel_hi:[1,0,1] neg_lo:[0,0,1] neg_hi:[0,0,1]\nv_cvt_pk_bf16_f32 v48, 0, v48\nv_cvt_pk_bf16_f32 v49, 0, v49\n"
    "v_pk_add_f32 v[56:57], v[52:53], v[54:55]\nv_cvt_pk_bf16_f32 v50, 0, v50\nv_cvt_pk_bf16_f32 v51, 0, v51\nv_cmp_u_f32 vcc, v56, v57\nv_max_f32 v52, 0, v52\n"
    "v_max_f32 v53, 0, v53\nv_max_f32 v54, 0, v54\nv_max_f32 v55, 0, v55\ns_or_b64 vcc, vcc, s[82:83]\ns_cbranch_vccnz .Lls34\n.Llo33:\nv_cvt_pk_bf16_f32 v52, 0, v52\n"
    "v_cvt_pk_bf16_f32 v53, 0, v53\nv_cvt_pk_bf16_f32 v54, 0, v54\nv_cvt_pk_bf16_f32 v55, 0, v55\nv_pk_add_f32 v[52:53], v[52:53], s[86:87] op_sel_hi:[1,0]\n"
    "v_pk_add_f32 v[54:55], v[54:55], s[86:87] op_sel_hi:[1,0]\nv_pk_add_f32 v[244:245], v[32:33], v[48:49] op_sel:[0,0] op_sel_hi:[1,0] neg_lo:[0,1] neg_hi:[0,1]\n"
    "v_pk_add_f32 v[246:247], v[34:35], v[48:49] op_sel:[0,1] op_sel_hi:[1,1] neg_lo:[0,1] neg_hi:[0,1]\nv_cvt_pk_bf16_f32 v52, 0, v52\nv_cvt_pk_bf16_f32 v53, 0, v53\n"
    "v_cvt_pk_bf16_f32 v54, 0, v54\nv_cvt_pk_bf16_f32 v55, 0, v55\nv_sqrt_f32 v52, v52\n"
    "v_pk_add_f32 v[248:249], v[36:37], v[50:51] op_sel:[0,0] op_sel_hi:[1,0] neg_lo:[0,1] neg_hi:[0,1]\nv_sqrt_f32 v53, v53\n"
    "v_pk_add_f32 v[250:251], v[38:39], v[50:51] op_sel:[0,1] op_sel_hi:[1,1] neg_lo:[0,1] neg_hi:[0,1]\nv_sqrt_f32 v54, v54\nv_cvt_pk_bf16_f32 v244, 0, v244\n"
    "v_sqrt_f32 v55, v55\nv_cvt_pk_bf16_f32 v245, 0, v245\nv_cvt_pk_bf16_f32 v52, 0, v52\nv_cvt_pk_bf16_f32 v53, 0, v53\nv_cvt_pk_bf16_f32 v54, 0, v54\n"
    "v_cvt_pk_bf16_f32 v55, 0, v55\nv_rcp_f32 v52, v52\nv_cvt_pk_bf16_f32 v246, 0, v246\nv_rcp_f32 v53, v53\nv_cvt_pk_bf16_f32 v247, 0, v247\nv_rcp_f32 v54, v54\n"
    "v_cvt_pk_bf16_f32 v248, 0, v248\nv_rcp_f32 v55, v55\nv_cvt_pk_bf16_f32 v249, 0, v249\nv_cvt_pk_bf16_f32 v250, 0, v250\nv_cvt_pk_bf16_f32 v251, 0, v251\n"
    "v_pk_mul_f32 v[244:245], v[244:245], v[52:53] op_sel:[0,0] op_sel_hi:[1,0]\nv_pk_mul_f32 v[246:247], v[246:247], v[52:53] op_sel:[0,1] op_sel_hi:[1,1]\n"
    "v_pk_mul_f32 v[248:249], v[248:249], v[54:55] op_sel:[0,0] op_sel_hi:[1,0]\nv_pk_mul_f32 v[250:251], v[250:251], v[54:55] op_sel:[0,1] op_sel_hi:[1,1]\n"
    "v_cvt_pk_bf16_f32 v244, 0, v244\nv_cvt_pk_bf16_f32 v246, 0, v246\nv_cvt_pk_bf16_f32 v248, 0, v248\nv_cvt_pk_bf16_f32 v250, 0, v250\nv_cvt_pk_f16_f32 v56, v244, v246\n"
    "v_cvt_pk_f16_f32 v57, v248, v250\nv_cvt_pk_bf16_f32 v245, 0, v245\nv_cvt_pk_bf16_f32 v247, 0, v247\nds_write_b64 v14, v[56:57]\nv_cvt_pk_bf16_f32 v249, 0, v249\n"
    "v_cvt_pk_bf16_f32 v251, 0, v251\nv_cvt_pk_f16_f32 v58, v245, v247\nv_cvt_pk_f16_f32 v59, v249, v251\nds_write_b64 v14, v[58:59] offset:6480\ns_waitcnt lgkmcnt(0)\n"
    "s_barrier\ns_bitcmp1_b32 s3, 0\ns_cbranch_scc1 .LL48\ns_setprio 0\nds_read_b64 v[196:197], v41\nds_read_b128 v[198:201], v40\nds_read_b64 v[202:203], v42\n"
    "ds_read_b64 v[48:49], v41 offset:2560\nds_read_b128 v[50:53], v40 offset:2560\nds_read_b64 v[54:55], v42 offset:2560\ns_waitcnt lgkmcnt(3)\n"
    "v_alignbit_b32 v204, v197, v196, 16\nv_alignbit_b32 v205, v198, v197, 16\nv_alignbit_b32 v206, v199, v198, 16\nv_alignbit_b32 v207, v200, v199, 16\n"
    "v_alignbit_b32 v208, v201, v200, 16\nv_alignbit_b32 v209, v202, v201, 16\nv_alignbit_b32 v210, v203, v202, 16\nv_pk_add_f16 v212, v196, v204\n"
    "v_pk_add_f16 v213, v197, v205\nv_pk_add_f16 v214, v198, v206\nv_pk_add_f16 v215, v199, v207\nv_pk_add_f16 v216, v200, v208\nv_pk_add_f16 v217, v201, v209\n"
    "v_pk_add_f16 v218, v202, v210\nv_pk_add_f16 v211, v214, v215\nv_pk_add_f16 v219, v216, v217\nv_pk_add_f16 v232, v205, v214\nv_pk_add_f16 v233, v206, v215\n"
    "v_pk_add_f16 v234, v207, v216\nv_pk_add_f16 v235, v208, v217\nds_write_b64 v43, v[232:233] offset:4040\nv_pk_add_f16 v228, v213, v214\nv_pk_add_f16 v230, v215, v216\n"
    "ds_write_b64 v44, v[234:235] offset:4032\nv_pk_add_f16 v229, v211, v200\nv_pk_add_f16 v231, v219, v202\nv_pk_add_f16 v228, v228, v199\nv_pk_add_f16 v230, v230, v201\n"
    "v_pk_add_f16 v236, v213, v211\nv_pk_add_f16 v237, v211, v216\nds_write_b128 v43, v[228:231]\nv_pk_add_f16 v238, v215, v219\nv_pk_add_f16 v239, v219, v218\n"
    "v_pk_add_f16 v220, v212, v200\nv_pk_add_f16 v221, v213, v201\nv_pk_add_f16 v222, v214, v202\nv_pk_add_f16 v223, v215, v203\nv_pk_add_f16 v224, v236, v204\n"
    "v_pk_add_f16 v225, v237, v205\nv_pk_add_f16 v226, v238, v206\nv_pk_add_f16 v227, v239, v207\nds_write_b128 v29, v[224:227]\nv_pk_add_f16 v220, v220, v236\n"
    "v_pk_add_f16 v221, v221, v237\nv_pk_add_f16 v222, v222, v238\nv_pk_add_f16 v223, v223, v239\nds_write_b128 v45, v[220:223]\ns_mov_b64 exec, 1\n"
    "ds_write_b32 v12, v13 offset:80\ns_mov_b64 exec, -1\ns_waitcnt lgkmcnt(6)\nv_alignbit_b32 v204, v50, v49, 16\nv_alignbit_b32 v205, v51, v50, 16\n"
    "v_alignbit_b32 v206, v52, v51, 16\nv_alignbit_b32 v207, v53, v52, 16\nv_alignbit_b32 v208, v54, v53, 16\nv_pk_add_f16 v212, v49, v204\nv_pk_add_f16 v213, v50, v205\n"
    "v_pk_add_f16 v214, v51, v206\nv_pk_add_f16 v215, v52, v207\nv_pk_add_f16 v216, v53, v208\nv_pk_add_f16 v60, v204, v213\nv_pk_add_f16 v61, v205, v214\n"
    "v_pk_add_f16 v62, v206, v215\nv_pk_add_f16 v63, v207, v216\nds_write_b64 v30, v[60:61] offset:6344\nv_pk_add_f16 v56, v212, v51\nv_pk_add_f16 v57, v213, v52\n"
    "v_pk_add_f16 v58, v214, v53\nv_pk_add_f16 v59, v215, v54\nds_write_b64 v31, v[62:63] offset:6336\nv_pk_add_f16 v56, v56, v213\nv_pk_add_f16 v57, v57, v214\n"
    "v_pk_add_f16 v58, v58, v215\nv_pk_add_f16 v59, v59, v216\nds_write_b128 v30, v[56:59] offset:2304\ns_mov_b32 s80, 0x100000\n.Lsl51:\nds_read_b32 v28, v11\n"
    "s_waitcnt lgkmcnt(0)\nv_cmp_gt_i32 vcc, v13, v28\ns_cbranch_vccz .Lsr50\ns_sleep 0\ns_sub_u32 s80, s80, 1\ns_cbranch_scc0 .Lsl51\n.Lsr50:\ns_setprio 0\n"
    "ds_read_b64_tr_b16 v[48:49], v2 offset:1984\nds_read_b64_tr_b16 v[50:51], v2 offset:2048\nds_read_b64_tr_b16 v[52:53], v2 offset:1728\n"
    "ds_read_b64_tr_b16 v[54:55], v2 offset:1792\nds_read_b64_tr_b16 v[56:57], v2 offset:1408\nds_read_b64_tr_b16 v[58:59], v2 offset:1472\n"
    "ds_read_b64_tr_b16 v[60:61], v2 offset:1152\nds_read_b64_tr_b16 v[62:63], v2 offset:1216\nds_read_b32 v22, v5\nds_read_b32 v23, v5 offset:6480\nds_read_b64 v[46:47], v6\n"
    "ds_read_b64 v[20:21], v6 offset:6480\ns_waitcnt lgkmcnt(10)\nv_mfma_f32_32x32x16_f16 v[196:211], v[48:51], v[64:67], 0\nds_read_b64_tr_b16 v[48:49], v2 offset:832\n"
    "ds_read_b64_tr_b16 v[50:51], v2 offset:896\nv_mul_f32 v244, s96, v244\nv_mul_f32 v245, s97, v245\ns_waitcnt lgkmcnt(10)\n"
    "v_mfma_f32_32x32x16_f16 v[196:211], v[52:55], v[68:71], v[196:211]\nds_read_b64_tr_b16 v[52:53], v2 offset:576\nds_read_b64_tr_b16 v[54:55], v2 offset:640\n"
    "v_mul_f32 v246, s96, v246\nv_mul_f32 v247, s97, v247\ns_waitcnt lgkmcnt(10)\nv_mfma_f32_32x32x16_f16 v[196:211], v[56:59], v[72:75], v[196:211]\n"
    "ds_read_b64_tr_b16 v[56:57], v2 offset:6016\nds_read_b64_tr_b16 v[58:59], v2 offset:6080\nv_mul_f32 v248, s96, v248\nv_mul_f32 v249, s97, v249\ns_waitcnt lgkmcnt(10)\n"
    "v_mfma_f32_32x32x16_f16 v[196:211], v[60:63], v[76:79], v[196:211]\nds_read_b64_tr_b16 v[60:61], v2 offset:5760\nds_read_b64_tr_b16 v[62:63], v2 offset:5824\n"
    "v_mul_f32 v250, s96, v250\nv_mul_f32 v251, s97, v251\ns_waitcnt lgkmcnt(6)\nv_mfma_f32_32x32x16_f16 v[196:211], v[48:51], v[96:99], v[196:211]\n"
    "ds_read_b64_tr_b16 v[48:49], v2 offset:5440\nds_read_b64_tr_b16 v[50:51], v2 offset:5504\nv_cvt_pk_bf16_f32 v244, 0, v244\nv_cvt_pk_bf16_f32 v245, 0, v245\n"
    "s_waitcnt lgkmcnt(6)\nv_mfma_f32_32x32x16_f16 v[196:211], v[52:55], v[100:103], v[196:211]\nds_read_b64_tr_b16 v[52:53], v2 offset:5184\n"
    "ds_read_b64_tr_b16 v[54:55], v2 offset:5248\nv_cvt_pk_bf16_f32 v246, 0, v246\nv_cvt_pk_bf16_f32 v247, 0, v247\ns_waitcnt lgkmcnt(6)\n"
    "v_mfma_f32_32x32x16_f16 v[196:211], v[56:59], v[80:83], v[196:211]\nds_read_b64_tr_b16 v[56:57], v2 offset:4864\nds_read_b64_tr_b16 v[58:59], v2 offset:4928\n"
    "v_cvt_pk_bf16_f32 v248, 0, v248\nv_cvt_pk_bf16_f32 v249, 0, v249\ns_waitcnt lgkmcnt(6)\nv_mfma_f32_32x32x16_f16 v[196:211], v[60:63], v[84:87], v[196:211]\n"
    "ds_read_b64_tr_b16 v[60:61], v2 offset:4608\nds_read_b64_tr_b16 v[62:63], v2 offset:4672\ns_mov_b64 exec, 1\nds_write_b32 v12, v13 offset:64\ns_mov_b64 exec, -1\n"
    "s_waitcnt lgkmcnt(7)\nv_mfma_f32_32x32x16_f16 v[196:211], v[48:51], v[88:91], v[196:211]\nv_cvt_pk_bf16_f32 v250, 0, v250\nv_cvt_pk_bf16_f32 v251, 0, v251\n"
    "s_waitcnt lgkmcnt(5)\nv_mfma_f32_32x32x16_f16 v[196:211], v[52:55], v[92:95], v[196:211]\nv_cvt_f32_f16_sdwa v24, v22 dst_sel:DWORD dst_unused:UNUSED_PAD src0_sel:WORD_1\n"
    "v_cvt_f32_f16_sdwa v26, v46 dst_sel:DWORD dst_unused:UNUSED_PAD src0_sel:WORD_1\ns_waitcnt lgkmcnt(3)\n"
    "v_mfma_f32_32x32x16_f16 v[196:211], v[56:59], v[112:115], v[196:211]\nv_cvt_f32_f16 v25, v46\nv_cvt_f32_f16 v27, v47\ns_waitcnt lgkmcnt(1)\n"
    "v_mfma_f32_32x32x16_f16 v[196:211], v[60:63], v[116:119], v[196:211]\nv_cvt_f32_f16_sdwa v28, v23 dst_sel:DWORD dst_unused:UNUSED_PAD src0_sel:WORD_1\n"
    "v_cvt_f32_f16_sdwa v30, v20 dst_sel:DWORD dst_unused:UNUSED_PAD src0_sel:WORD_1\nv_cvt_f32_f16 v29, v20\nv_cvt_f32_f16 v31, v21\nv_mul_f32_e64 v24, -s92, v24\n"
    "v_mul_f32_e64 v25, -s92, v25\nv_mul_f32_e64 v26, -s92, v26\nv_mul_f32_e64 v27, -s92, v27\nv_mul_f32_e64 v28, -s94, v28\nv_mul_f32_e64 v29, -s94, v29\n"
    "v_mul_f32_e64 v30, -s94, v30\nv_mul_f32_e64 v31, -s94, v31\nds_write_b128 v7, v[196:199] offset:2048\nds_write_b128 v7, v[200:203] offset:3072\n"
    "ds_write_b128 v7, v[204:207] offset:4096\nds_write_b128 v7, v[208:211] offset:5120\ns_setprio 0\ns_branch .LL49\n.LL48:\ns_setprio 0\nds_read_b64 v[196:197], v41\n"
    "ds_read_b128 v[198:201], v40\nds_read_b64 v[202:203], v42\ns_waitcnt lgkmcnt(0)\nv_alignbit_b32 v204, v197, v196, 16\nv_alignbit_b32 v205, v198, v197, 16\n"
    "v_alignbit_b32 v206, v199, v198, 16\nv_alignbit_b32 v207, v200, v199, 16\nv_alignbit_b32 v208, v201, v200, 16\nv_alignbit_b32 v209, v202, v201, 16\n"
    "v_alignbit_b32 v210, v203, v202, 16\nv_pk_add_f16 v212, v196, v204\nv_pk_add_f16 v213, v197, v205\nv_pk_add_f16 v214, v198, v206\nv_pk_add_f16 v215, v199, v207\n"
    "v_pk_add_f16 v216, v200, v208\nv_pk_add_f16 v217, v201, v209\nv_pk_add_f16 v218, v202, v210\nv_pk_add_f16 v211, v214, v215\nv_pk_add_f16 v219, v216, v217\n"
    "v_pk_add_f16 v232, v205, v214\nv_pk_add_f16 v233, v206, v215\nv_pk_add_f16 v234, v207, v216\nv_pk_add_f16 v235, v208, v217\nds_write_b64 v43, v[232:233] offset:4040\n"
    "v_pk_add_f16 v228, v213, v214\nv_pk_add_f16 v230, v215, v216\nds_write_b64 v44, v[234:235] offset:4032\nv_pk_add_f16 v229, v211, v200\nv_pk_add_f16 v231, v219, v202\n"
    "v_pk_add_f16 v228, v228, v199\nv_pk_add_f16 v230, v230, v201\nv_pk_add_f16 v236, v213, v211\nv_pk_add_f16 v237, v211, v216\nds_write_b128 v43, v[228:231]\n"
    "v_pk_add_f16 v238, v215, v219\nv_pk_add_f16 v239, v219, v218\nv_pk_add_f16 v220, v212, v200\nv_pk_add_f16 v221, v213, v201\nv_pk_add_f16 v222, v214, v202\n"
    "v_pk_add_f16 v223, v215, v203\nv_pk_add_f16 v224, v236, v204\nv_pk_add_f16 v225, v237, v205\nv_pk_add_f16 v226, v238, v206\nv_pk_add_f16 v227, v239, v207\n"
    "ds_write_b128 v29, v[224:227]\nv_pk_add_f16 v220, v220, v236\nv_pk_add_f16 v221, v221, v237\nv_pk_add_f16 v222, v222, v238\nv_pk_add_f16 v223, v223, v239\n"
    "ds_write_b128 v45, v[220:223]\ns_mov_b64 exec, 1\nds_write_b32 v12, v13 offset:80\ns_mov_b64 exec, -1\ns_mov_b32 s80, 0x100000\n.Lsl53:\nds_read_b32 v28, v11\n"
    "s_waitcnt lgkmcnt(0)\nv_cmp_gt_i32 vcc, v13, v28\ns_cbranch_vccz .Lsr52\ns_sleep 0\ns_sub_u32 s80, s80, 1\ns_cbranch_scc0 .Lsl53\n.Lsr52:\ns_setprio 0\n"
    "ds_read_b64_tr_b16 v[48:49], v3 offset:512\nds_read_b64_tr_b16 v[50:51], v3 offset:640\nds_read_b64_tr_b16 v[52:53], v3 offset:0\n"
    "ds_read_b64_tr_b16 v[54:55], v3 offset:128\nds_read_b64_tr_b16 v[56:57], v4 offset:512\nds_read_b64_tr_b16 v[58:59], v4 offset:640\n"
    "ds_read_b64_tr_b16 v[60:61], v4 offset:0\nds_read_b64_tr_b16 v[62:63], v4 offset:128\nds_read_b32 v22, v5\nds_read_b32 v23, v5 offset:6480\nds_read_b64 v[46:47], v6\n"
    "ds_read_b64 v[20:21], v6 offset:6480\ns_waitcnt lgkmcnt(10)\nv_mfma_f32_32x32x16_f16 v[228:243], v[48:51], v[96:99], 0\nds_read_b64_tr_b16 v[48:49], v3 offset:2816\n"
    "ds_read_b64_tr_b16 v[50:51], v3 offset:2944\nv_mul_f32 v244, s96, v244\nv_mul_f32 v245, s97, v245\ns_waitcnt lgkmcnt(10)\n"
    "v_mfma_f32_32x32x16_f16 v[228:243], v[52:55], v[100:103], v[228:243]\nds_read_b64_tr_b16 v[52:53], v3 offset:2304\nds_read_b64_tr_b16 v[54:55], v3 offset:2432\n"
    "v_mul_f32 v246, s96, v246\nv_mul_f32 v247, s97, v247\ns_waitcnt lgkmcnt(10)\nv_mfma_f32_32x32x16_f16 v[212:227], v[56:59], v[104:107], 0\n"
    "ds_read_b64_tr_b16 v[56:57], v4 offset:2816\nds_read_b64_tr_b16 v[58:59], v4 offset:2944\nv_mul_f32 v248, s96, v248\nv_mul_f32 v249, s97, v249\ns_waitcnt lgkmcnt(10)\n"
    "v_mfma_f32_32x32x16_f16 v[212:227], v[60:63], v[108:111], v[212:227]\nds_read_b64_tr_b16 v[60:61], v4 offset:2304\nds_read_b64_tr_b16 v[62:63], v4 offset:2432\n"
    "v_mul_f32 v250, s96, v250\nv_mul_f32 v251, s97, v251\ns_waitcnt lgkmcnt(6)\nv_mfma_f32_32x32x16_f16 v[228:243], v[48:51], v[112:115], v[228:243]\n"
    "ds_read_b64_tr_b16 v[48:49], v2 offset:832\nds_read_b64_tr_b16 v[50:51], v2 offset:896\nv_cvt_pk_bf16_f32 v244, 0, v244\nv_cvt_pk_bf16_f32 v245, 0, v245\n"
    "s_waitcnt lgkmcnt(6)\nv_mfma_f32_32x32x16_f16 v[228:243], v[52:55], v[116:119], v[228:243]\nds_read_b64_tr_b16 v[52:53], v2 offset:576\n"
    "ds_read_b64_tr_b16 v[54:55], v2 offset:640\nv_cvt_pk_bf16_f32 v246, 0, v246\nv_cvt_pk_bf16_f32 v247, 0, v247\ns_waitcnt lgkmcnt(6)\n"
    "v_mfma_f32_32x32x16_f16 v[212:227], v[56:59], v[120:123], v[212:227]\nds_read_b64_tr_b16 v[56:57], v2 offset:256\nds_read_b64_tr_b16 v[58:59], v2 offset:320\n"
    "v_cvt_pk_bf16_f32 v248, 0, v248\nv_cvt_pk_bf16_f32 v249, 0, v249\ns_waitcnt lgkmcnt(6)\nv_mfma_f32_32x32x16_f16 v[212:227], v[60:63], v[124:127], v[212:227]\n"
    "ds_read_b64_tr_b16 v[60:61], v2 offset:0\nds_read_b64_tr_b16 v[62:63], v2 offset:64\nv_cvt_pk_bf16_f32 v250, 0, v250\nv_cvt_pk_bf16_f32 v251, 0, v251\n"
    "s_waitcnt lgkmcnt(6)\nv_mfma_f32_32x32x16_f16 v[196:211], v[48:51], v[64:67], 0\nds_read_b64_tr_b16 v[48:49], v2 offset:4864\nds_read_b64_tr_b16 v[50:51], v2 offset:4928\n"
    "v_cvt_f32_f16_sdwa v24, v22 dst_sel:DWORD dst_unused:UNUSED_PAD src0_sel:WORD_1\nv_cvt_f32_f16_sdwa v26, v46 dst_sel:DWORD dst_unused:UNUSED_PAD src0_sel:WORD_1\n"
    "s_waitcnt lgkmcnt(6)\nv_mfma_f32_32x32x16_f16 v[196:211], v[52:55], v[68:71], v[196:211]\nds_read_b64_tr_b16 v[52:53], v2 offset:4608\n"
    "ds_read_b64_tr_b16 v[54:55], v2 offset:4672\nv_cvt_f32_f16 v25, v46\nv_cvt_f32_f16 v27, v47\ns_waitcnt lgkmcnt(6)\n"
    "v_mfma_f32_32x32x16_f16 v[196:211], v[56:59], v[72:75], v[196:211]\nds_read_b64_tr_b16 v[56:57], v2 offset:4288\nds_read_b64_tr_b16 v[58:59], v2 offset:4352\n"
    "v_cvt_f32_f16_sdwa v28, v23 dst_sel:DWORD dst_unused:UNUSED_PAD src0_sel:WORD_1\nv_cvt_f32_f16_sdwa v30, v20 dst_sel:DWORD dst_unused:UNUSED_PAD src0_sel:WORD_1\n"
    "s_waitcnt lgkmcnt(6)\nv_mfma_f32_32x32x16_f16 v[196:211], v[60:63], v[76:79], v[196:211]\nds_read_b64_tr_b16 v[60:61], v2 offset:4032\n"
    "ds_read_b64_tr_b16 v[62:63], v2 offset:4096\ns_mov_b64 exec, 1\nds_write_b32 v12, v13 offset:64\ns_mov_b64 exec, -1\ns_waitcnt lgkmcnt(7)\n"
    "v_mfma_f32_32x32x16_f16 v[196:211], v[48:51], v[80:83], v[196:211]\nv_add_f32 v236, v236, v220\nv_add_f32 v237, v237, v221\nv_add_f32 v238, v238, v222\n"
    "v_add_f32 v239, v239, v223\ns_waitcnt lgkmcnt(5)\nv_mfma_f32_32x32x16_f16 v[196:211], v[52:55], v[84:87], v[196:211]\nv_add_f32 v240, v240, v224\n"
    "v_add_f32 v241, v241, v225\nv_add_f32 v242, v242, v226\nv_add_f32 v243, v243, v227\ns_waitcnt lgkmcnt(3)\n"
    "v_mfma_f32_32x32x16_f16 v[196:211], v[56:59], v[88:91], v[196:211]\nv_xor_b32 v45, 32, v8\ns_waitcnt lgkmcnt(1)\n"
    "v_mfma_f32_32x32x16_f16 v[196:211], v[60:63], v[92:95], v[196:211]\nv_cvt_f32_f16 v29, v20\nv_cvt_f32_f16 v31, v21\nds_write_b128 v8, v[228:231] offset:0\n"
    "ds_write_b128 v45, v[232:235] offset:0\nds_write_b128 v8, v[236:239] offset:2048\nds_write_b128 v45, v[240:243] offset:2048\nv_mul_f32_e64 v24, -s92, v24\n"
    "v_mul_f32_e64 v25, -s92, v25\nv_mul_f32_e64 v26, -s92, v26\nv_mul_f32_e64 v27, -s92, v27\nv_mul_f32_e64 v28, -s94, v28\nv_mul_f32_e64 v29, -s94, v29\n"
    "v_mul_f32_e64 v30, -s94, v30\nv_mul_f32_e64 v31, -s94, v31\nds_write_b128 v7, v[196:199]\nds_write_b128 v7, v[200:203] offset:1024\ns_setprio 0\n.LL49:\n"
    "s_waitcnt lgkmcnt(0)\ns_barrier\nds_read_b128 v[48:51], v15\nds_read_b128 v[52:55], v15 offset:12672\nds_read_b128 v[56:59], v16\nds_read_b128 v[196:199], v9\n"
    "ds_read_b128 v[60:63], v16 offset:10240\nds_read_b128 v[200:203], v9 offset:10240\ns_waitcnt lgkmcnt(4)\nv_pk_add_f32 v[24:25], v[24:25], v[48:49]\n"
    "v_pk_add_f32 v[26:27], v[26:27], v[50:51]\nv_pk_add_f32 v[28:29], v[28:29], v[52:53]\nv_pk_add_f32 v[30:31], v[30:31], v[54:55]\ns_waitcnt lgkmcnt(2)\n"
    "v_pk_add_f32 v[56:57], v[56:57], v[196:197]\nv_pk_add_f32 v[58:59], v[58:59], v[198:199]\ns_waitcnt lgkmcnt(0)\nv_pk_add_f32 v[60:61], v[60:61], v[200:201]\n"
    "v_pk_add_f32 v[62:63], v[62:63], v[202:203]\nv_pk_add_f32 v[24:25], v[24:25], v[56:57]\nv_pk_add_f32 v[26:27], v[26:27], v[58:59]\n"
    "v_pk_add_f32 v[28:29], v[28:29], v[60:61]\nv_pk_add_f32 v[30:31], v[30:31], v[62:63]\nv_pk_mul_f32 v[24:25], v[24:25], s[88:89] op_sel_hi:[1,0]\n"
    "v_pk_mul_f32 v[26:27], v[26:27], s[88:89] op_sel_hi:[1,0]\nv_pk_mul_f32 v[28:29], v[28:29], s[90:91] op_sel_hi:[1,0]\n"
    "v_pk_mul_f32 v[30:31], v[30:31], s[90:91] op_sel_hi:[1,0]\nv_cvt_pk_bf16_f32 v48, 0, v24\nv_cvt_pk_bf16_f32 v49, 0, v28\nv_cvt_pk_bf16_f32 v50, 0, v25\n"
    "v_cvt_pk_bf16_f32 v51, 0, v29\nv_cvt_pk_bf16_f32 v52, 0, v26\nv_cvt_pk_bf16_f32 v53, 0, v30\nv_cvt_pk_bf16_f32 v54, 0, v27\nv_cvt_pk_bf16_f32 v55, 0, v31\n"
    "v_pk_add_f32 v[244:245], v[244:245], v[48:49]\nv_pk_add_f32 v[246:247], v[246:247], v[50:51]\nv_pk_add_f32 v[248:249], v[248:249], v[52:53]\n"
    "v_pk_add_f32 v[250:251], v[250:251], v[54:55]\nv_cvt_pk_bf16_f32 v244, 0, v244\nv_cvt_pk_bf16_f32 v245, 0, v245\nv_cvt_pk_bf16_f32 v246, 0, v246\n"
    "v_cvt_pk_bf16_f32 v247, 0, v247\nv_cvt_pk_bf16_f32 v248, 0, v248\nv_cvt_pk_bf16_f32 v249, 0, v249\nv_cvt_pk_bf16_f32 v250, 0, v250\nv_cvt_pk_bf16_f32 v251, 0, v251\n"
    "v_pk_add_f32 v[32:33], v[32:33], v[244:245]\nv_pk_add_f32 v[34:35], v[34:35], v[246:247]\nv_pk_add_f32 v[36:37], v[36:37], v[248:249]\n"
    "v_pk_add_f32 v[38:39], v[38:39], v[250:251]\ns_cmp_eq_u32 s6, 0\ns_cbranch_scc0 .LL55\ns_mov_b32 s81, 0xfffefffe\ns_mov_b32 vcc_lo, 0x55555555\n"
    "s_mov_b32 vcc_hi, 0x55555555\nv_cvt_pk_bf16_f32 v54, v33, v35\nv_cvt_pk_bf16_f32 v55, v37, v39\nv_cvt_pk_bf16_f32 v52, v32, v34\nv_cvt_pk_bf16_f32 v53, v36, v38\n"
    "v_pk_min_u16 v54, v54, s81\nv_pk_min_u16 v55, v55, s81\nv_pk_min_u16 v52, v52, s81\nv_pk_min_u16 v53, v53, s81\ns_nop 0\n"
    "v_cndmask_b32_dpp v48, v54, v52, vcc quad_perm:[1,0,3,2] row_mask:0xf bank_mask:0xf\nv_cndmask_b32_dpp v49, v55, v53, vcc quad_perm:[1,0,3,2] row_mask:0xf bank_mask:0xf\n"
    "s_not_b64 vcc, vcc\nv_cndmask_b32_dpp v50, v52, v54, vcc quad_perm:[1,0,3,2] row_mask:0xf bank_mask:0xf\n"
    "v_cndmask_b32_dpp v51, v53, v55, vcc quad_perm:[1,0,3,2] row_mask:0xf bank_mask:0xf\nglobal_store_dwordx4 v17, v[48:51], s[32:33] sc1\ns_branch .LH213\n.LL55:\n"
    "v_cvt_pk_bf16_f32 v48, v32, v33\nv_cvt_pk_bf16_f32 v49, v34, v35\nv_cvt_pk_bf16_f32 v50, v36, v37\nv_cvt_pk_bf16_f32 v51, v38, v39\nv_min_u32 v48, -2, v48\n"
    "v_min_u32 v49, -2, v49\nv_min_u32 v50, -2, v50\nv_min_u32 v51, -2, v51\nglobal_store_dwordx4 v17, v[48:51], s[32:33] sc1\nv_and_b32 v4, 7, v0\ns_lshl_b32 s15, s41, 13\n"
    "s_add_u32 s14, s15, 0x1200000\nv_lshl_add_u32 v2, v4, 10, s14\nv_lshrrev_b32 v5, 3, v0\nv_lshl_add_u32 v2, v5, 2, v2\ns_lshl_b32 s17, s36, 16\ns_lshl_b32 s18, s35, 7\n"
    "s_add_u32 s16, s17, s18\nv_lshl_add_u32 v3, v5, 10, s16\nv_lshl_add_u32 v3, v4, 4, v3\ns_mov_b32 s80, 0x10000\ns_sleep 21\n.LL56:\n"
    "global_load_dword v52, v2, s[32:33] offset:0 sc1\nglobal_load_dword v53, v2, s[32:33] offset:256 sc1\nglobal_load_dword v54, v2, s[32:33] offset:512 sc1\n"
    "global_load_dword v55, v2, s[32:33] offset:768 sc1\ns_waitcnt vmcnt(0)\nv_max3_u32 v4, v52, v53, v54\nv_max_u32 v4, v4, v55\nv_cmp_eq_u32 vcc, -1, v4\n"
    "s_cbranch_vccz .LL57\ns_sub_u32 s80, s80, 1\ns_cbranch_scc0 .LL56\n.LL57:\nglobal_store_dwordx4 v3, v[52:55], s[30:31] sc1\ns_endpgm\n.LH213:\ns_mov_b32 s6, 1\n"
    "s_mov_b32 s7, 8388608\nv_and_b32 v196, 63, v0\nv_lshlrev_b32 v26, 4, v196\nv_bfe_u32 v197, v0, 1, 5\nv_lshl_add_u32 v27, v197, 6, 32\nv_and_b32 v198, 1, v0\n"
    "v_lshl_add_u32 v27, v198, 4, v27\ns_mov_b32 s14, 0x1020\nv_lshl_add_u32 v9, v197, 6, s14\nv_lshl_add_u32 v9, v198, 4, v9\nv_bfe_u32 v199, v0, 4, 2\ns_lshl_b32 s16, s3, 6\n"
    "s_add_u32 s15, s16, 0x27c80\nv_lshl_add_u32 v39, v199, 4, s15\nv_bfe_u32 v201, v0, 2, 2\nv_lshl_add_u32 v200, v199, 3, v201\ns_lshr_b32 s20, s3, 1\ns_add_u32 s19, s20, 1\n"
    "s_lshl_b32 s18, s19, 15\ns_and_b32 s22, s3, 3\ns_lshl_b32 s21, s22, 13\ns_add_u32 s17, s18, s21\nv_lshl_add_u32 v32, v200, 6, s17\nv_and_b32 v202, 3, v0\n"
    "v_lshl_add_u32 v32, v202, 3, v32\nv_xor_b32 v204, s3, v199\nv_and_b32 v203, 1, v204\nv_lshl_add_u32 v32, v203, 5, v32\ns_add_u32 s44, s3, 1\ns_and_b32 s25, s44, 3\n"
    "s_lshl_b32 s24, s25, 13\ns_add_u32 s23, s18, s24\nv_lshl_add_u32 v33, v200, 6, s23\nv_lshl_add_u32 v33, v202, 3, v33\nv_lshl_add_u32 v33, v203, 5, v33\n"
    "s_add_u32 s48, s3, 2\ns_and_b32 s47, s48, 3\ns_lshl_b32 s46, s47, 13\ns_add_u32 s45, s18, s46\nv_lshl_add_u32 v34, v200, 6, s45\nv_lshl_add_u32 v34, v202, 3, v34\n"
    "v_lshl_add_u32 v34, v203, 5, v34\ns_add_u32 s52, s3, 3\ns_and_b32 s51, s52, 3\ns_lshl_b32 s50, s51, 13\ns_add_u32 s49, s18, s50\nv_lshl_add_u32 v35, v200, 6, s49\n"
    "v_lshl_add_u32 v35, v202, 3, v35\nv_lshl_add_u32 v35, v203, 5, v35\nds_read_b128 v[248:251], v39\ns_lshl2_add_u32 s15, s37, s3\ns_lshl_b32 s14, s15, 15\n"
    "s_add_u32 s62, s14, 0x600000\ns_mul_i32 s16, s3, 0x3000\ns_add_u32 s64, s16, 0x18000\ns_lshl_b32 s21, s37, 2\ns_max_u32 s20, s21, 1\ns_add_u32 s19, s20, -1\n"
    "s_lshl_b32 s18, s19, 15\ns_add_u32 s17, s18, 0x600000\ns_lshl_b32 s22, s3, 13\ns_add_u32 s60, s17, s22\ns_lshl_b32 s24, s3, 12\ns_add_u32 s23, s24, 0x24000\n"
    "s_add_u32 s65, s23, 0xfffffc00\ns_and_b32 s66, s42, 7\ns_lshl_b32 s25, s66, 12\ns_add_u32 s44, s62, s25\ns_add_u32 s69, s42, 1\ns_and_b32 s68, s69, 7\n"
    "s_lshl_b32 s67, s68, 12\ns_add_u32 s45, s62, s67\ns_add_u32 s72, s42, 2\ns_and_b32 s71, s72, 7\ns_lshl_b32 s70, s71, 12\ns_add_u32 s46, s62, s70\ns_add_u32 s75, s42, 3\n"
    "s_and_b32 s74, s75, 7\ns_lshl_b32 s73, s74, 12\ns_add_u32 s47, s62, s73\ns_add_u32 s78, s42, 4\ns_and_b32 s77, s78, 7\ns_lshl_b32 s76, s77, 12\ns_add_u32 s48, s62, s76\n"
    "s_add_u32 s16, s42, 5\ns_and_b32 s15, s16, 7\ns_lshl_b32 s14, s15, 12\ns_add_u32 s49, s62, s14\ns_add_u32 s19, s42, 6\ns_and_b32 s18, s19, 7\ns_lshl_b32 s17, s18, 12\n"
    "s_add_u32 s50, s62, s17\ns_add_u32 s22, s42, 7\ns_and_b32 s21, s22, 7\ns_lshl_b32 s20, s21, 12\ns_add_u32 s51, s62, s20\ns_add_u32 s60, s60, s32\ns_addc_u32 s61, s33, 0\n"
    "ds_read_b64_tr_b16 v[64:65], v32 offset:0\nds_read_b64_tr_b16 v[66:67], v32 offset:256\nds_read_b64_tr_b16 v[68:69], v32 offset:2048\n"
    "ds_read_b64_tr_b16 v[70:71], v32 offset:2304\nds_read_b64_tr_b16 v[72:73], v32 offset:4096\nds_read_b64_tr_b16 v[74:75], v32 offset:4352\n"
    "ds_read_b64_tr_b16 v[76:77], v32 offset:6144\nds_read_b64_tr_b16 v[78:79], v32 offset:6400\nds_read_b64_tr_b16 v[80:81], v33 offset:0\n"
    "ds_read_b64_tr_b16 v[82:83], v33 offset:256\nds_read_b64_tr_b16 v[84:85], v33 offset:2048\nds_read_b64_tr_b16 v[86:87], v33 offset:2304\n"
    "ds_read_b64_tr_b16 v[88:89], v33 offset:4096\nds_read_b64_tr_b16 v[90:91], v33 offset:4352\nds_read_b64_tr_b16 v[92:93], v33 offset:6144\n"
    "ds_read_b64_tr_b16 v[94:95], v33 offset:6400\nds_read_b64_tr_b16 v[96:97], v34 offset:0\nds_read_b64_tr_b16 v[98:99], v34 offset:256\n"
    "ds_read_b64_tr_b16 v[100:101], v34 offset:2048\nds_read_b64_tr_b16 v[102:103], v34 offset:2304\nds_read_b64_tr_b16 v[104:105], v34 offset:4096\n"
    "ds_read_b64_tr_b16 v[106:107], v34 offset:4352\nds_read_b64_tr_b16 v[108:109], v34 offset:6144\nds_read_b64_tr_b16 v[110:111], v34 offset:6400\n"
    "ds_read_b64_tr_b16 v[112:113], v35 offset:0\nds_read_b64_tr_b16 v[114:115], v35 offset:256\nds_read_b64_tr_b16 v[116:117], v35 offset:2048\n"
    "ds_read_b64_tr_b16 v[118:119], v35 offset:2304\nds_read_b64_tr_b16 v[120:121], v35 offset:4096\nds_read_b64_tr_b16 v[122:123], v35 offset:4352\n"
    "ds_read_b64_tr_b16 v[124:125], v35 offset:6144\nds_read_b64_tr_b16 v[126:127], v35 offset:6400\ns_waitcnt lgkmcnt(0)\ns_sleep 15\ns_setprio 3\ns_barrier\n"
    "s_add_u32 m0, s64, 0\ns_add_u32 s52, s32, s44\ns_addc_u32 s53, s33, 0\nglobal_load_lds_dwordx4 v26, s[52:53] offset:0 sc1\n"
    "global_load_lds_dwordx4 v26, s[52:53] offset:1024 sc1\nglobal_load_lds_dwordx4 v26, s[52:53] offset:2048 sc1\nglobal_load_lds_dwordx4 v26, s[52:53] offset:3072 sc1\n"
    "s_add_u32 m0, s64, 4096\ns_add_u32 s52, s32, s45\ns_addc_u32 s53, s33, 0\nglobal_load_lds_dwordx4 v26, s[52:53] offset:0 sc1\n"
    "global_load_lds_dwordx4 v26, s[52:53] offset:1024 sc1\nglobal_load_lds_dwordx4 v26, s[52:53] offset:2048 sc1\nglobal_load_lds_dwordx4 v26, s[52:53] offset:3072 sc1\n"
    "v_mov_b64 v[10:11], 0\nv_mov_b64 v[12:13], 0\nv_mov_b64 v[14:15], 0\nv_mov_b64 v[16:17], 0\nv_mov_b64 v[18:19], 0\nv_mov_b64 v[20:21], 0\nv_mov_b64 v[22:23], 0\n"
    "v_mov_b64 v[24:25], 0\nv_add_u32 v1, 8388608, v1\nv_bfe_u32 v197, v0, 4, 2\nv_bfe_u32 v198, v0, 2, 2\nv_lshl_add_u32 v196, v197, 3, v198\ns_and_b32 s16, s3, 3\n"
    "s_lshl_b32 s15, s16, 12\ns_add_u32 s14, s15, 0x24000\nv_lshl_add_u32 v28, v196, 5, s14\nv_and_b32 v199, 3, v0\nv_lshl_add_u32 v28, v199, 3, v28\ns_add_u32 s20, s3, 1\n"
    "s_and_b32 s19, s20, 3\ns_lshl_b32 s18, s19, 12\ns_add_u32 s17, s18, 0x24000\nv_lshl_add_u32 v29, v196, 5, s17\nv_lshl_add_u32 v29, v199, 3, v29\ns_add_u32 s24, s3, 2\n"
    "s_and_b32 s23, s24, 3\ns_lshl_b32 s22, s23, 12\ns_add_u32 s21, s22, 0x24000\nv_lshl_add_u32 v30, v196, 5, s21\nv_lshl_add_u32 v30, v199, 3, v30\ns_add_u32 s68, s3, 3\n"
    "s_and_b32 s67, s68, 3\ns_lshl_b32 s66, s67, 12\ns_add_u32 s25, s66, 0x24000\nv_lshl_add_u32 v31, v196, 5, s25\nv_lshl_add_u32 v31, v199, 3, v31\nv_and_b32 v200, 63, v0\n"
    "s_lshl_b32 s72, s35, 15\ns_and_b32 s74, s36, 3\ns_lshl_b32 s73, s74, 13\ns_add_u32 s71, s72, s73\ns_lshl_b32 s75, s3, 11\ns_add_u32 s70, s71, s75\n"
    "s_add_u32 s69, s70, 0xd00000\nv_lshl_add_u32 v36, v200, 4, s69\nv_and_b32 v196, 63, v0\ns_max_u32 s20, s37, 1\ns_add_u32 s19, s20, -1\ns_lshr_b32 s18, s19, 2\n"
    "s_and_b32 s21, s35, 1\ns_lshl1_add_u32 s17, s18, s21\ns_lshl_b32 s16, s17, 15\ns_and_b32 s23, s19, 3\ns_lshl_b32 s22, s23, 13\ns_add_u32 s15, s16, s22\n"
    "s_add_u32 s14, s15, 0xd01800\nv_lshl_add_u32 v37, v196, 4, s14\nv_and_b32 v198, 7, v0\nv_bfe_u32 v199, v0, 4, 2\nv_lshl_add_u32 v197, v198, 2, v199\n"
    "s_lshl_b32 s25, s3, 12\ns_add_u32 s24, s25, 0x24c00\nv_lshl_add_u32 v38, v197, 3, s24\nv_and_b32 v200, 8, v0\nv_lshl_add_u32 v38, v200, 5, v38\n"
    "s_add_u32 s66, s25, 0x24000\nv_lshl_add_u32 v43, v197, 3, s66\nv_lshl_add_u32 v43, v200, 5, v43\nv_and_b32 v201, 15, v0\ns_add_u32 s67, s66, 0x80\n"
    "v_lshl_add_u32 v44, v201, 3, s67\ns_lshl_b32 s69, s3, 5\ns_add_u32 s68, s69, 0x18040\nv_lshl_add_u32 v40, v199, 3, s68\ns_mov_b32 s70, 0xd0\n"
    "v_mad_u32_u24 v40, v201, s70, v40\nv_and_b32 v202, 31, v0\ns_mov_b32 s71, 0x27f80\nv_lshl_add_u32 v41, v202, 2, s71\nv_mov_b32 v42, 2\ns_lshl2_add_u32 s74, s40, s3\n"
    "s_lshl_b32 s73, s74, 15\ns_add_u32 s72, s73, 0xe05000\nv_lshl_add_u32 v4, v196, 4, s72\ns_add_u32 s75, s73, 0xe07000\nv_lshl_add_u32 v5, v196, 4, s75\n"
    "s_mov_b32 s86, 0x3b000000\ns_add_u32 m0, s64, 8192\ns_add_u32 s52, s32, s46\ns_addc_u32 s53, s33, 0\nglobal_load_lds_dwordx4 v26, s[52:53] offset:0 sc1\n"
    "global_load_lds_dwordx4 v26, s[52:53] offset:1024 sc1\nglobal_load_lds_dwordx4 v26, s[52:53] offset:2048 sc1\nglobal_load_lds_dwordx4 v26, s[52:53] offset:3072 sc1\n"
    "s_setprio 0\ns_waitcnt vmcnt(8)\ns_waitcnt lgkmcnt(0)\ns_barrier\ns_add_u32 m0, s65, 1024\ns_nop 0\nglobal_load_lds_dwordx4 v27, s[60:61] offset:0 sc1\n"
    "s_add_u32 m0, s65, 0\ns_nop 0\nglobal_load_lds_dwordx4 v27, s[60:61] offset:2048 sc1\ns_add_u32 m0, s65, 3072\ns_nop 0\nglobal_load_lds_dwordx4 v9, s[60:61] offset:0 sc1\n"
    "s_add_u32 m0, s65, 2048\ns_nop 0\nglobal_load_lds_dwordx4 v9, s[60:61] offset:2048 sc1\ns_waitcnt vmcnt(8)\ns_waitcnt lgkmcnt(0)\ns_barrier\ns_add_u32 m0, s64, 0\n"
    "s_add_u32 s52, s32, s47\ns_addc_u32 s53, s33, 0\nglobal_load_lds_dwordx4 v26, s[52:53] offset:0 sc1\nglobal_load_lds_dwordx4 v26, s[52:53] offset:1024 sc1\n"
    "global_load_lds_dwordx4 v26, s[52:53] offset:2048 sc1\nglobal_load_lds_dwordx4 v26, s[52:53] offset:3072 sc1\ns_waitcnt vmcnt(8)\ns_waitcnt lgkmcnt(0)\ns_barrier\n"
    "s_add_u32 m0, s64, 4096\ns_add_u32 s52, s32, s48\ns_addc_u32 s53, s33, 0\nglobal_load_lds_dwordx4 v26, s[52:53] offset:0 sc1\n"
    "global_load_lds_dwordx4 v26, s[52:53] offset:1024 sc1\nglobal_load_lds_dwordx4 v26, s[52:53] offset:2048 sc1\nglobal_load_lds_dwordx4 v26, s[52:53] offset:3072 sc1\n"
    "s_waitcnt vmcnt(8)\nds_read_b64_tr_b16 v[196:197], v28 offset:0\nds_read_b64_tr_b16 v[198:199], v28 offset:128\nds_read_b64_tr_b16 v[200:201], v28 offset:1024\n"
    "ds_read_b64_tr_b16 v[202:203], v28 offset:1152\nds_read_b64_tr_b16 v[204:205], v28 offset:2048\nds_read_b64_tr_b16 v[206:207], v28 offset:2176\n"
    "ds_read_b64_tr_b16 v[208:209], v28 offset:3072\nds_read_b64_tr_b16 v[210:211], v28 offset:3200\ns_waitcnt vmcnt(4)\ns_waitcnt lgkmcnt(0)\ns_barrier\n"
    "s_add_u32 m0, s64, 8192\ns_add_u32 s52, s32, s49\ns_addc_u32 s53, s33, 0\nglobal_load_lds_dwordx4 v26, s[52:53] offset:0 sc1\n"
    "global_load_lds_dwordx4 v26, s[52:53] offset:1024 sc1\nglobal_load_lds_dwordx4 v26, s[52:53] offset:2048 sc1\nglobal_load_lds_dwordx4 v26, s[52:53] offset:3072 sc1\n"
    "v_pk_maximum3_f16 v62, v196, v197, v198\nv_pk_maximum3_f16 v63, v199, v200, v201\nv_pk_maximum3_f16 v62, v62, v202, v203\nv_pk_maximum3_f16 v63, v63, v204, v205\n"
    "v_pk_maximum3_f16 v62, v62, v206, v207\nv_pk_maximum3_f16 v63, v63, v208, v209\nv_pk_maximum3_f16 v62, v62, v210, v211\nv_pk_maximum3_f16 v62, v62, v63, v63\n"
    "v_cmp_u_f16_sdwa vcc, v62, v62 src0_sel:WORD_0 src1_sel:WORD_1\ns_cbranch_vccnz .Lxs58\n.Lxr59:\ns_waitcnt vmcnt(4)\ns_waitcnt lgkmcnt(0)\ns_barrier\ns_add_u32 m0, s64, 0\n"
    "s_add_u32 s52, s32, s50\ns_addc_u32 s53, s33, 0\nglobal_load_lds_dwordx4 v26, s[52:53] offset:0 sc1\nglobal_load_lds_dwordx4 v26, s[52:53] offset:1024 sc1\n"
    "global_load_lds_dwordx4 v26, s[52:53] offset:2048 sc1\nglobal_load_lds_dwordx4 v26, s[52:53] offset:3072 sc1\ns_setprio 2\n"
    "v_mfma_f32_16x16x32_bf16 v[46:49], v[64:67], v[196:199], 0\nv_mfma_f32_16x16x32_bf16 v[46:49], v[68:71], v[200:203], v[46:49]\n"
    "v_mfma_f32_16x16x32_bf16 v[46:49], v[72:75], v[204:207], v[46:49]\nv_mfma_f32_16x16x32_bf16 v[46:49], v[76:79], v[208:211], v[46:49]\ns_setprio 0\n"
    "ds_read_b64_tr_b16 v[196:197], v29 offset:0\nds_read_b64_tr_b16 v[198:199], v29 offset:128\nds_read_b64_tr_b16 v[200:201], v29 offset:1024\n"
    "ds_read_b64_tr_b16 v[202:203], v29 offset:1152\nds_read_b64_tr_b16 v[204:205], v29 offset:2048\nds_read_b64_tr_b16 v[206:207], v29 offset:2176\n"
    "ds_read_b64_tr_b16 v[208:209], v29 offset:3072\nds_read_b64_tr_b16 v[210:211], v29 offset:3200\ns_waitcnt vmcnt(4)\ns_waitcnt lgkmcnt(0)\ns_barrier\n"
    "s_add_u32 m0, s64, 4096\ns_add_u32 s52, s32, s51\ns_addc_u32 s53, s33, 0\nglobal_load_lds_dwordx4 v26, s[52:53] offset:0 sc1\n"
    "global_load_lds_dwordx4 v26, s[52:53] offset:1024 sc1\nglobal_load_lds_dwordx4 v26, s[52:53] offset:2048 sc1\nglobal_load_lds_dwordx4 v26, s[52:53] offset:3072 sc1\n"
    "global_load_dwordx4 v[212:215], v36, s[32:33] offset:0 sc1\nglobal_load_dwordx4 v[216:219], v36, s[32:33] offset:1024 sc1\n"
    "global_load_dwordx4 v[220:223], v37, s[32:33] offset:0 sc1\nglobal_load_dwordx4 v[224:227], v37, s[32:33] offset:1024 sc1\ns_setprio 2\n"
    "v_mfma_f32_16x16x32_bf16 v[46:49], v[80:83], v[196:199], v[46:49]\nv_mfma_f32_16x16x32_bf16 v[46:49], v[84:87], v[200:203], v[46:49]\n"
    "v_mfma_f32_16x16x32_bf16 v[46:49], v[88:91], v[204:207], v[46:49]\nv_mfma_f32_16x16x32_bf16 v[46:49], v[92:95], v[208:211], v[46:49]\ns_setprio 0\n"
    "ds_read_b64_tr_b16 v[196:197], v30 offset:0\nds_read_b64_tr_b16 v[198:199], v30 offset:128\nds_read_b64_tr_b16 v[200:201], v30 offset:1024\n"
    "ds_read_b64_tr_b16 v[202:203], v30 offset:1152\nds_read_b64_tr_b16 v[204:205], v30 offset:2048\nds_read_b64_tr_b16 v[206:207], v30 offset:2176\n"
    "ds_read_b64_tr_b16 v[208:209], v30 offset:3072\nds_read_b64_tr_b16 v[210:211], v30 offset:3200\ns_waitcnt vmcnt(8)\ns_waitcnt lgkmcnt(0)\ns_barrier\ns_setprio 2\n"
    "v_mfma_f32_16x16x32_bf16 v[46:49], v[96:99], v[196:199], v[46:49]\nv_mfma_f32_16x16x32_bf16 v[46:49], v[100:103], v[200:203], v[46:49]\n"
    "v_mfma_f32_16x16x32_bf16 v[46:49], v[104:107], v[204:207], v[46:49]\nv_mfma_f32_16x16x32_bf16 v[46:49], v[108:111], v[208:211], v[46:49]\ns_setprio 0\n"
    "ds_read_b64_tr_b16 v[196:197], v31 offset:0\nds_read_b64_tr_b16 v[198:199], v31 offset:128\nds_read_b64_tr_b16 v[200:201], v31 offset:1024\n"
    "ds_read_b64_tr_b16 v[202:203], v31 offset:1152\nds_read_b64_tr_b16 v[204:205], v31 offset:2048\nds_read_b64_tr_b16 v[206:207], v31 offset:2176\n"
    "ds_read_b64_tr_b16 v[208:209], v31 offset:3072\nds_read_b64_tr_b16 v[210:211], v31 offset:3200\ns_waitcnt vmcnt(4)\ns_waitcnt lgkmcnt(0)\ns_barrier\ns_setprio 2\n"
    "v_mfma_f32_16x16x32_bf16 v[46:49], v[112:115], v[196:199], v[46:49]\nv_mfma_f32_16x16x32_bf16 v[46:49], v[116:119], v[200:203], v[46:49]\n"
    "v_mfma_f32_16x16x32_bf16 v[46:49], v[120:123], v[204:207], v[46:49]\nv_mfma_f32_16x16x32_bf16 v[46:49], v[124:127], v[208:211], v[46:49]\ns_setprio 0\ns_waitcnt vmcnt(0)\n"
    "s_setprio 2\ns_mov_b32 s80, 0x10000\n.Lpl63:\nv_add_f32 v228, v212, v216\nv_add_f32 v229, v213, v217\nv_add_f32 v230, v214, v218\nv_add_f32 v231, v215, v219\n"
    "v_permlane16_swap_b32 v228, v229\ns_nop 0\nv_permlane16_swap_b32 v230, v231\nv_add_f32 v228, v228, v229\nv_add_f32 v230, v230, v231\ns_nop 1\n"
    "v_permlane32_swap_b32 v228, v230\nv_add_f32 v228, v228, v230\nv_cmp_u_f32 vcc, v228, v228\ns_cbranch_vccnz .Lps61\n.Lpr62:\nv_mov_b32 v229, v228\ns_nop 1\n"
    "v_mov_b32_dpp v229, v228 row_ror:8 row_mask:0xf bank_mask:0x3\nv_mov_b32_dpp v228, v228 row_ror:8 row_mask:0xf bank_mask:0xc\nv_mul_f32 v228, s86, v228\n"
    "v_mul_f32 v230, v228, v228\nv_fma_f32 v229, v229, s86, -v230\nv_max_f32 v229, 0, v229\nv_cvt_pk_bf16_f32 v229, 0, v229\nv_add_f32 v229, 0x3727c5ac, v229\n"
    "v_cvt_pk_bf16_f32 v229, 0, v229\nv_sqrt_f32 v229, v229\ns_nop 0\nv_cvt_pk_bf16_f32 v229, 0, v229\nv_rcp_f32 v229, v229\nv_cvt_pk_bf16_f32 v228, -v228, -v228\n"
    "s_mov_b32 exec_lo, 0xff00ff00\ns_mov_b32 exec_hi, 0xff00ff00\nv_mov_b32 v228, 0\ns_mov_b64 exec, -1\nds_write_b64 v38, v[228:229]\ns_waitcnt lgkmcnt(0)\ns_barrier\n"
    "s_setprio 0\nds_write_b32 v41, v42\nglobal_load_dwordx4 v[64:67], v4, s[32:33] offset:-4096 sc1\nglobal_load_dwordx4 v[68:71], v4, s[32:33] offset:-3072 sc1\n"
    "global_load_dwordx4 v[72:75], v4, s[32:33] offset:-2048 sc1\nglobal_load_dwordx4 v[76:79], v4, s[32:33] offset:-1024 sc1\n"
    "global_load_dwordx4 v[80:83], v4, s[32:33] offset:0 sc1\nglobal_load_dwordx4 v[84:87], v4, s[32:33] offset:1024 sc1\n"
    "global_load_dwordx4 v[88:91], v4, s[32:33] offset:2048 sc1\nglobal_load_dwordx4 v[92:95], v4, s[32:33] offset:3072 sc1\ns_mov_b32 s80, 0x10000\n.Lpl68:\n"
    "v_add_f32 v232, v220, v224\nv_add_f32 v233, v221, v225\nv_add_f32 v234, v222, v226\nv_add_f32 v235, v223, v227\nv_permlane16_swap_b32 v232, v233\ns_nop 0\n"
    "v_permlane16_swap_b32 v234, v235\nv_add_f32 v232, v232, v233\nv_add_f32 v234, v234, v235\ns_nop 1\nv_permlane32_swap_b32 v232, v234\nv_add_f32 v232, v232, v234\n"
    "v_cmp_u_f32 vcc, v232, v232\ns_cbranch_vccnz .Lps66\n.Lpr67:\nv_mov_b32 v233, v232\ns_nop 1\nv_mov_b32_dpp v233, v232 row_ror:8 row_mask:0xf bank_mask:0x3\n"
    "v_mov_b32_dpp v232, v232 row_ror:8 row_mask:0xf bank_mask:0xc\nv_mul_f32 v232, s86, v232\nv_mul_f32 v234, v232, v232\nv_fma_f32 v233, v233, s86, -v234\n"
    "v_max_f32 v233, 0, v233\nv_cvt_pk_bf16_f32 v233, 0, v233\nv_add_f32 v233, 0x3727c5ac, v233\nv_cvt_pk_bf16_f32 v233, 0, v233\nv_sqrt_f32 v233, v233\ns_nop 0\n"
    "v_cvt_pk_bf16_f32 v233, 0, v233\nv_rcp_f32 v233, v233\nv_cvt_pk_bf16_f32 v232, 0, v232\nds_write_b64 v43, v[232:233]\nds_read_b64 v[232:233], v44\ns_waitcnt lgkmcnt(0)\n"
    "v_fma_f32 v46, -v232, v248, v46\nv_fma_f32 v47, -v232, v249, v47\nv_fma_f32 v48, -v232, v250, v48\nv_fma_f32 v49, -v232, v251, v49\nv_mul_f32 v46, v46, v233\n"
    "v_mul_f32 v47, v47, v233\nv_mul_f32 v48, v48, v233\nv_mul_f32 v49, v49, v233\nv_cvt_pk_bf16_f32 v46, v46, v47\nv_cvt_pk_bf16_f32 v47, v48, v49\nds_write_b64 v40, v[46:47]\n"
    "v_mov_b32 v62, 0\ns_branch .LHL12\n.LG1:\ns_waitcnt lgkmcnt(0)\ns_mov_b32 s86, 16777216\ns_load_dword s34, s[32:33], s86\ns_mov_b32 s6, 0\ns_mov_b32 s7, 0\n"
    "s_and_b32 s35, s2, 7\ns_lshr_b32 s36, s2, 3\ns_bfe_u32 s18, s2, 0x20001\ns_bfe_u32 s19, s2, 0x20003\ns_lshl2_add_u32 s37, s18, s19\ns_and_b32 s20, s2, 1\n"
    "s_lshr_b32 s21, s2, 5\ns_lshl3_add_u32 s38, s20, s21\ns_bfe_u32 s22, s2, 0x30003\ns_lshl3_add_u32 s39, s35, s22\ns_lshr_b32 s40, s2, 6\ns_lshl_b32 s23, s35, 5\n"
    "s_add_u32 s41, s23, s36\ns_lshl1_add_u32 s24, s19, s21\ns_and_b32 s42, s24, 7\ns_lshl_b32 s86, s41, 2\ns_load_dword s96, s[26:27], s86\ns_load_dword s95, s[28:29], s86\n"
    "v_mov_b32 v64, 0\nv_mov_b32 v65, 0xf149f2ca\nv_bfe_u32 v68, v0, 4, 2\nv_lshlrev_b32 v67, 2, v68\ns_add_u32 s19, s37, s3\ns_cmp_eq_u32 s19, 0\ns_cselect_b32 s18, 8, 0\n"
    "v_and_b32 v71, 7, v0\nv_add_u32 v70, 1, v71\nv_max_u32 v69, s18, v70\nv_sub_u32 v66, v67, v69\nv_add_u32 v73, 8, v71\nv_sub_u32 v72, v73, v69\nv_cmp_le_u32 vcc, v66, v72\n"
    "s_nop 1\nv_cndmask_b32 v2, v65, v64, vcc\nv_add_u32 v75, 1, v67\nv_sub_u32 v74, v75, v69\nv_cmp_le_u32 vcc, v74, v72\ns_nop 1\nv_cndmask_b32 v3, v65, v64, vcc\n"
    "v_add_u32 v77, 2, v67\nv_sub_u32 v76, v77, v69\nv_cmp_le_u32 vcc, v76, v72\ns_nop 1\nv_cndmask_b32 v4, v65, v64, vcc\nv_add_u32 v79, 3, v67\nv_sub_u32 v78, v79, v69\n"
    "v_cmp_le_u32 vcc, v78, v72\ns_nop 1\nv_cndmask_b32 v5, v65, v64, vcc\ns_mul_i32 s21, s3, 0xd00\ns_add_u32 s20, s21, 0x18040\nv_lshl_add_u32 v6, v68, 4, s20\n"
    "v_and_b32 v81, 15, v0\nv_max_u32 v80, 1, v81\ns_mov_b32 s22, 0x1a0\nv_mad_u32_u24 v6, v80, s22, v6\ns_add_u32 s23, s21, 0x18d00\nv_lshl_add_u32 v7, v68, 4, s23\n"
    "v_mad_u32_u24 v7, v71, s22, v7\nv_and_b32 v82, 3, v0\ns_add_u32 s24, s21, 0x18080\nv_lshl_add_u32 v8, v82, 3, s24\nv_bfe_u32 v85, v0, 2, 2\nv_add_u32 v84, v67, v85\n"
    "v_max_u32 v83, 1, v84\nv_mad_u32_u24 v8, v83, s22, v8\nv_bfe_u32 v86, v0, 3, 1\ns_lshr_b32 s47, s3, 1\ns_lshl2_add_u32 s46, s37, s47\ns_lshl_b32 s45, s46, 15\n"
    "s_lshl_b32 s48, s38, 11\ns_add_u32 s44, s45, s48\ns_and_b32 s50, s3, 1\ns_lshl_b32 s49, s50, 8\ns_add_u32 s25, s44, s49\nv_lshl_add_u32 v9, v86, 10, s25\n"
    "v_bfe_u32 v87, v0, 5, 1\nv_lshl_add_u32 v9, v87, 9, v9\nv_bfe_u32 v88, v0, 4, 1\nv_lshl_add_u32 v9, v88, 4, v9\nv_lshl_add_u32 v9, v71, 5, v9\nv_mov_b64 v[10:11], 0\n"
    "v_mov_b64 v[12:13], 0\nv_mov_b64 v[14:15], 0\nv_mov_b64 v[16:17], 0\nv_mov_b64 v[18:19], 0\nv_mov_b64 v[20:21], 0\nv_mov_b64 v[22:23], 0\nv_mov_b64 v[24:25], 0\n"
    "v_bfe_u32 v65, v0, 4, 1\nv_bfe_u32 v66, v0, 5, 1\nv_xor_b32 v64, v65, v66\nv_bfe_u32 v69, v0, 2, 2\nv_lshl_add_u32 v68, v66, 3, v69\nv_lshlrev_b32 v67, 6, v68\n"
    "v_lshl_add_u32 v26, v64, 5, v67\nv_and_b32 v70, 3, v0\nv_lshl_add_u32 v26, v70, 3, v26\ns_mul_i32 s19, s3, 0x1a00\ns_add_u32 s18, s19, 0x18d00\n"
    "v_lshl_add_u32 v27, v66, 3, s18\nv_and_b32 v71, 31, v0\ns_mov_b32 s20, 0xd0\nv_mad_u32_u24 v27, v71, s20, v27\ns_mov_b32 s21, 0x27c00\nv_lshl_add_u32 v28, v71, 2, s21\n"
    "s_mul_i32 s23, s3, 0x3000\ns_add_u32 s22, s23, 0x18000\nv_lshl_add_u32 v29, v71, 7, s22\nv_bfe_u32 v73, v0, 1, 3\nv_xor_b32 v72, v66, v73\nv_lshl_add_u32 v29, v72, 4, v29\n"
    "s_and_b32 s44, s41, 3\ns_lshr_b32 s47, s41, 2\ns_lshl_b32 s46, s47, 7\ns_add_u32 s45, s46, 0x1002000\ns_lshl3_add_u32 s25, s44, s45\ns_lshl2_add_u32 s24, s3, s25\n"
    "v_mov_b32 v30, s24\nv_and_b32 v74, 0x7f, v0\ns_mov_b32 s48, 0x23800\nv_lshl_add_u32 v31, v74, 3, s48\ns_mov_b32 s49, 0x23000\nv_lshl_add_u32 v32, v74, 4, s49\n"
    "v_and_b32 v75, 63, v0\ns_lshr_b32 s52, s3, 1\ns_lshl_b32 s51, s52, 8\ns_add_u32 s50, s51, 0x20000\nv_lshl_add_u32 v33, v75, 2, s50\ns_lshl_b32 s54, s41, 11\n"
    "s_add_u32 s53, s54, 0xe00000\nv_lshl_add_u32 v38, v0, 4, s53\ns_add_u32 s55, s23, 0x1a000\nv_add_u32 v39, s55, v26\ns_mul_i32 s57, s3, 0x2200\ns_add_u32 s56, s57, 0x1f400\n"
    "v_lshl_add_u32 v188, v75, 3, s56\ns_lshl_b32 s59, s39, 9\ns_add_u32 s58, s59, 0x1008000\nv_lshl_add_u32 v187, v70, 7, s58\nv_mov_b32 v191, 0x10000\n"
    "s_lshl_b32 s62, s41, 13\ns_lshl_b32 s63, s3, 11\ns_add_u32 s61, s62, s63\ns_add_u32 s60, s61, 0x200000\nv_lshl_add_u32 v192, v75, 4, s60\ns_lshl_b32 s66, s2, 8\n"
    "s_lshl_b32 s67, s3, 6\ns_add_u32 s65, s66, s67\ns_add_u32 s64, s65, 0x400000\nv_lshl_add_u32 v193, v70, 4, s64\ns_and_b32 s70, s2, 3\ns_lshr_b32 s73, s2, 2\n"
    "s_lshl_b32 s72, s73, 7\ns_add_u32 s71, s72, 0x1004000\ns_lshl4_add_u32 s69, s70, s71\ns_lshl2_add_u32 s68, s3, s69\nv_mov_b32 v194, s68\ns_add_u32 s74, s61, 0x600000\n"
    "v_lshl_add_u32 v200, v75, 4, s74\ns_lshl_b32 s77, s41, 10\ns_lshl_b32 s78, s3, 8\ns_add_u32 s76, s77, s78\ns_add_u32 s75, s76, 0xd00000\nv_lshl_add_u32 v201, v75, 4, s75\n"
    "v_and_b32 v76, 15, v0\ns_add_u32 s79, s63, 0x1004000\nv_lshl_add_u32 v202, v76, 7, s79\nv_bfe_u32 v77, v0, 4, 2\nv_lshl_add_u32 v202, v77, 4, v202\nv_mov_b32 v196, -1\n"
    "v_mov_b32 v197, -1\nv_mov_b32 v198, -1\nv_mov_b32 v199, -1\ns_lshl_b32 s19, s39, 15\ns_add_u32 s18, s19, 0x1000\nv_lshl_add_u32 v182, v0, 4, s18\n"
    "s_add_u32 s20, s18, 0x2000\nv_lshl_add_u32 v183, v0, 4, s20\ns_add_u32 s21, s18, 0x4000\nv_lshl_add_u32 v184, v0, 4, s21\ns_add_u32 s22, s18, 0x6000\n"
    "v_lshl_add_u32 v185, v0, 4, s22\ns_mov_b32 s23, 0x1f600\nv_lshl_add_u32 v186, v0, 4, s23\nv_bfe_u32 v64, v0, 4, 2\ns_mul_i32 s25, s3, 0x1760\ns_add_u32 s24, s25, 0x18000\n"
    "v_lshl_add_u32 v189, v64, 4, s24\nv_and_b32 v65, 15, v0\ns_mov_b32 s44, 0x220\nv_mad_u32_u24 v189, v65, s44, v189\ns_lshr_b32 s47, s3, 1\ns_mul_i32 s46, s47, 0x4400\n"
    "s_add_u32 s45, s46, 0x1f400\nv_lshl_add_u32 v190, v64, 4, s45\nv_mad_u32_u24 v190, v65, s44, v190\ns_cmp_eq_u32 s3, 3\ns_cbranch_scc0 .LL74\nv_mov_b32 v64, 0\n"
    "v_lshlrev_b32 v65, 2, v0\nv_add_u32 v65, 162048, v65\ns_mov_b32 exec_hi, 0\nds_write_b32 v65, v64\ns_mov_b64 exec, 15\nv_mov_b32 v64, 0x3f803f80\ns_mov_b32 exec_lo, -1\n"
    "s_mov_b32 exec_hi, 15\nds_write_b32 v65, v64 offset:880\ns_mov_b64 exec, -1\n.LL74:\ns_and_b32 s54, s42, 7\ns_lshl_b32 s44, s54, 12\ns_add_u32 s56, s42, 1\n"
    "s_and_b32 s55, s56, 7\ns_lshl_b32 s45, s55, 12\ns_add_u32 s58, s42, 2\ns_and_b32 s57, s58, 7\ns_lshl_b32 s46, s57, 12\ns_add_u32 s60, s42, 3\ns_and_b32 s59, s60, 7\n"
    "s_lshl_b32 s47, s59, 12\ns_add_u32 s62, s42, 4\ns_and_b32 s61, s62, 7\ns_lshl_b32 s48, s61, 12\ns_add_u32 s64, s42, 5\ns_and_b32 s63, s64, 7\ns_lshl_b32 s49, s63, 12\n"
    "s_add_u32 s66, s42, 6\ns_and_b32 s65, s66, 7\ns_lshl_b32 s50, s65, 12\ns_add_u32 s68, s42, 7\ns_and_b32 s67, s68, 7\ns_lshl_b32 s51, s67, 12\ns_mov_b32 s81, 0x3b000000\n"
    "s_mov_b32 s84, -1\ns_mov_b32 s85, 0\nv_mov_b32 v143, 0x3f803f80\nv_add_u32 v116, s44, v26\nv_add_u32 v118, 0x10000, v116\nv_mov_b32 v112, 0\nv_mov_b32 v113, 0\n"
    "v_mov_b32 v114, 0\nv_mov_b32 v115, 0\nv_mov_b32 v121, 0\nv_mov_b32 v122, 0\nv_mov_b32 v123, 0\nv_mov_b32 v125, 0\nv_mov_b32 v126, 0\nv_mov_b32 v127, 0\nv_mov_b32 v129, 0\n"
    "v_mov_b32 v130, 0\nv_mov_b32 v131, 0\nv_mov_b32 v133, 0\nv_mov_b32 v134, 0\nv_mov_b32 v135, 0\nv_xor_b32 v176, 32, v29\nv_xor_b32 v177, 64, v29\nv_xor_b32 v178, 0x60, v29\n"
    "s_waitcnt lgkmcnt(0)\ns_lshl_b32 s97, s96, 16\ns_and_b32 s96, s96, 0xffff0000\ns_cmp_lt_u32 s3, 2\ns_cselect_b32 s96, s97, s96\ns_mov_b32 s97, 0x3fb8aa3b\n"
    "v_mov_b32 v66, s96\nv_mul_f32 v67, s97, v66\nv_fma_f32 v68, s97, v66, -v67\nv_exp_f32 v67, v67\nv_fmac_f32 v68, 0x32a57060, v66\nv_mul_f32 v68, 0x3f317218, v68\n"
    "v_fmac_f32 v67, v67, v68\nv_cvt_pk_bf16_f32 v67, 0, v67\nv_mul_f32 v66, s97, v67\nv_mul_f32 v68, 0x3e22f983, v67\nv_readfirstlane_b32 s92, v67\n"
    "v_readfirstlane_b32 s93, v66\nv_readfirstlane_b32 s94, v68\ns_and_b32 s97, s95, 0xffff0000\ns_lshl_b32 s96, s95, 16\ns_add_u32 s34, s34, 1\nv_mov_b32 v180, s34\n"
    "s_cmp_lt_u32 s3, 2\ns_cbranch_scc0 .LL73\ns_waitcnt vmcnt(0)\nds_read_b64_tr_b16 v[244:245], v39\nds_read_b64_tr_b16 v[246:247], v39 offset:256\ns_waitcnt lgkmcnt(0)\n"
    ".LL73:\ns_barrier\nds_read_b128 v[144:147], v29 offset:0\nds_read_b128 v[148:151], v176 offset:0\nds_read_b128 v[152:155], v177 offset:0\n"
    "ds_read_b128 v[156:159], v178 offset:0\nds_read_b64_tr_b16 v[44:45], v116 offset:0\nds_read_b64_tr_b16 v[46:47], v116 offset:256\n"
    "ds_read_b64_tr_b16 v[48:49], v116 offset:1024\nds_read_b64_tr_b16 v[50:51], v116 offset:1280\nds_read_b64_tr_b16 v[52:53], v116 offset:2048\n"
    "ds_read_b64_tr_b16 v[54:55], v116 offset:2304\nds_read_b64_tr_b16 v[56:57], v116 offset:3072\nds_read_b64_tr_b16 v[58:59], v116 offset:3328\n"
    "ds_read_b64_tr_b16 v[60:61], v116 offset:32768\nds_read_b64_tr_b16 v[62:63], v116 offset:33024\ns_waitcnt lgkmcnt(13)\nv_dot2c_f32_bf16 v112, v143, v144\n"
    "v_dot2c_f32_bf16 v113, v144, v144\nv_dot2c_f32_bf16 v114, v143, v145\nv_dot2c_f32_bf16 v115, v145, v145\nv_dot2c_f32_bf16 v112, v143, v146\n"
    "v_dot2c_f32_bf16 v113, v146, v146\nv_dot2c_f32_bf16 v114, v143, v147\nv_dot2c_f32_bf16 v115, v147, v147\ns_waitcnt lgkmcnt(12)\nv_dot2c_f32_bf16 v112, v143, v148\n"
    "v_dot2c_f32_bf16 v113, v148, v148\nv_dot2c_f32_bf16 v114, v143, v149\nv_dot2c_f32_bf16 v115, v149, v149\nv_dot2c_f32_bf16 v112, v143, v150\n"
    "v_dot2c_f32_bf16 v113, v150, v150\nv_dot2c_f32_bf16 v114, v143, v151\nv_dot2c_f32_bf16 v115, v151, v151\ns_waitcnt lgkmcnt(11)\nv_dot2c_f32_bf16 v112, v143, v152\n"
    "v_dot2c_f32_bf16 v113, v152, v152\nv_dot2c_f32_bf16 v114, v143, v153\nv_dot2c_f32_bf16 v115, v153, v153\nv_dot2c_f32_bf16 v112, v143, v154\n"
    "v_dot2c_f32_bf16 v113, v154, v154\nv_dot2c_f32_bf16 v114, v143, v155\nv_dot2c_f32_bf16 v115, v155, v155\ns_waitcnt lgkmcnt(10)\nv_dot2c_f32_bf16 v112, v143, v156\n"
    "v_dot2c_f32_bf16 v113, v156, v156\nv_dot2c_f32_bf16 v114, v143, v157\nv_dot2c_f32_bf16 v115, v157, v157\nv_dot2c_f32_bf16 v112, v143, v158\n"
    "v_dot2c_f32_bf16 v113, v158, v158\nv_dot2c_f32_bf16 v114, v143, v159\nv_dot2c_f32_bf16 v115, v159, v159\ns_waitcnt lgkmcnt(8)\n"
    "v_mfma_f32_32x32x16_bf16 v[64:79], v[44:47], v[144:147], 0\nds_read_b64_tr_b16 v[44:45], v116 offset:33792\nds_read_b64_tr_b16 v[46:47], v116 offset:34048\n"
    "v_add_u32 v117, s45, v26\nv_add_u32 v119, 0x10000, v117\ns_waitcnt lgkmcnt(8)\nv_mfma_f32_32x32x16_bf16 v[64:79], v[48:51], v[148:151], v[64:79]\n"
    "ds_read_b64_tr_b16 v[48:49], v116 offset:34816\nds_read_b64_tr_b16 v[50:51], v116 offset:35072\ns_cmp_lt_u32 s3, 2\ns_cbranch_scc0 .LL71\n"
    "global_store_dwordx4 v38, v[244:247], s[32:33] sc1\n.LL71:\ns_waitcnt lgkmcnt(8)\nv_mfma_f32_32x32x16_bf16 v[64:79], v[52:55], v[152:155], v[64:79]\n"
    "ds_read_b64_tr_b16 v[52:53], v116 offset:35840\nds_read_b64_tr_b16 v[54:55], v116 offset:36096\ns_waitcnt lgkmcnt(8)\n"
    "v_mfma_f32_32x32x16_bf16 v[64:79], v[56:59], v[156:159], v[64:79]\nds_read_b64_tr_b16 v[56:57], v118 offset:0\nds_read_b64_tr_b16 v[58:59], v118 offset:256\n"
    "s_waitcnt lgkmcnt(8)\nv_mfma_f32_32x32x16_bf16 v[80:95], v[60:63], v[144:147], 0\nds_read_b64_tr_b16 v[60:61], v118 offset:1024\n"
    "ds_read_b64_tr_b16 v[62:63], v118 offset:1280\ns_waitcnt lgkmcnt(8)\nv_mfma_f32_32x32x16_bf16 v[80:95], v[44:47], v[148:151], v[80:95]\n"
    "ds_read_b64_tr_b16 v[44:45], v118 offset:2048\nds_read_b64_tr_b16 v[46:47], v118 offset:2304\ns_waitcnt lgkmcnt(8)\n"
    "v_mfma_f32_32x32x16_bf16 v[80:95], v[48:51], v[152:155], v[80:95]\nds_read_b64_tr_b16 v[48:49], v118 offset:3072\nds_read_b64_tr_b16 v[50:51], v118 offset:3328\ns_barrier\n"
    "s_waitcnt lgkmcnt(8)\nv_mfma_f32_32x32x16_bf16 v[80:95], v[52:55], v[156:159], v[80:95]\nds_read_b128 v[160:163], v29 offset:4096\n"
    "ds_read_b64_tr_b16 v[52:53], v117 offset:0\nds_read_b64_tr_b16 v[54:55], v117 offset:256\ns_waitcnt lgkmcnt(9)\n"
    "v_mfma_f32_32x32x16_bf16 v[96:111], v[56:59], v[144:147], 0\nds_read_b128 v[164:167], v176 offset:4096\nds_read_b64_tr_b16 v[56:57], v117 offset:1024\n"
    "ds_read_b64_tr_b16 v[58:59], v117 offset:1280\ns_waitcnt lgkmcnt(10)\nv_mfma_f32_32x32x16_bf16 v[96:111], v[60:63], v[148:151], v[96:111]\n"
    "ds_read_b128 v[168:171], v177 offset:4096\nds_read_b64_tr_b16 v[60:61], v117 offset:2048\nds_read_b64_tr_b16 v[62:63], v117 offset:2304\ns_waitcnt lgkmcnt(11)\n"
    "v_mfma_f32_32x32x16_bf16 v[96:111], v[44:47], v[152:155], v[96:111]\nds_read_b128 v[172:175], v178 offset:4096\nds_read_b64_tr_b16 v[44:45], v117 offset:3072\n"
    "ds_read_b64_tr_b16 v[46:47], v117 offset:3328\ns_waitcnt lgkmcnt(12)\nv_mfma_f32_32x32x16_bf16 v[96:111], v[48:51], v[156:159], v[96:111]\n"
    "ds_read_b64_tr_b16 v[48:49], v117 offset:32768\nds_read_b64_tr_b16 v[50:51], v117 offset:33024\ns_waitcnt lgkmcnt(13)\nv_dot2c_f32_bf16 v112, v143, v160\n"
    "v_dot2c_f32_bf16 v113, v160, v160\nv_dot2c_f32_bf16 v114, v143, v161\nv_dot2c_f32_bf16 v115, v161, v161\nv_dot2c_f32_bf16 v112, v143, v162\n"
    "v_dot2c_f32_bf16 v113, v162, v162\nv_dot2c_f32_bf16 v114, v143, v163\nv_dot2c_f32_bf16 v115, v163, v163\ns_waitcnt lgkmcnt(10)\nv_dot2c_f32_bf16 v112, v143, v164\n"
    "v_dot2c_f32_bf16 v113, v164, v164\nv_dot2c_f32_bf16 v114, v143, v165\nv_dot2c_f32_bf16 v115, v165, v165\nv_dot2c_f32_bf16 v112, v143, v166\n"
    "v_dot2c_f32_bf16 v113, v166, v166\nv_dot2c_f32_bf16 v114, v143, v167\nv_dot2c_f32_bf16 v115, v167, v167\ns_waitcnt lgkmcnt(7)\nv_dot2c_f32_bf16 v112, v143, v168\n"
    "v_dot2c_f32_bf16 v113, v168, v168\nv_dot2c_f32_bf16 v114, v143, v169\nv_dot2c_f32_bf16 v115, v169, v169\nv_dot2c_f32_bf16 v112, v143, v170\n"
    "v_dot2c_f32_bf16 v113, v170, v170\nv_dot2c_f32_bf16 v114, v143, v171\nv_dot2c_f32_bf16 v115, v171, v171\ns_waitcnt lgkmcnt(4)\nv_dot2c_f32_bf16 v112, v143, v172\n"
    "v_dot2c_f32_bf16 v113, v172, v172\nv_dot2c_f32_bf16 v114, v143, v173\nv_dot2c_f32_bf16 v115, v173, v173\nv_dot2c_f32_bf16 v112, v143, v174\n"
    "v_dot2c_f32_bf16 v113, v174, v174\nv_dot2c_f32_bf16 v114, v143, v175\nv_dot2c_f32_bf16 v115, v175, v175\nv_mfma_f32_32x32x16_bf16 v[64:79], v[52:55], v[160:163], v[64:79]\n"
    "ds_read_b64_tr_b16 v[52:53], v117 offset:33792\nds_read_b64_tr_b16 v[54:55], v117 offset:34048\nv_add_u32 v116, s46, v26\nv_add_u32 v118, 0x10000, v116\n"
    "v_mfma_f32_32x32x16_bf16 v[64:79], v[56:59], v[164:167], v[64:79]\nds_read_b64_tr_b16 v[56:57], v117 offset:34816\nds_read_b64_tr_b16 v[58:59], v117 offset:35072\n"
    "v_mfma_f32_32x32x16_bf16 v[64:79], v[60:63], v[168:171], v[64:79]\nds_read_b64_tr_b16 v[60:61], v117 offset:35840\nds_read_b64_tr_b16 v[62:63], v117 offset:36096\n"
    "s_waitcnt lgkmcnt(8)\nv_mfma_f32_32x32x16_bf16 v[64:79], v[44:47], v[172:175], v[64:79]\nds_read_b64_tr_b16 v[44:45], v119 offset:0\n"
    "ds_read_b64_tr_b16 v[46:47], v119 offset:256\ns_waitcnt lgkmcnt(8)\nv_mfma_f32_32x32x16_bf16 v[80:95], v[48:51], v[160:163], v[80:95]\n"
    "ds_read_b64_tr_b16 v[48:49], v119 offset:1024\nds_read_b64_tr_b16 v[50:51], v119 offset:1280\ns_waitcnt lgkmcnt(8)\n"
    "v_mfma_f32_32x32x16_bf16 v[80:95], v[52:55], v[164:167], v[80:95]\nds_read_b64_tr_b16 v[52:53], v119 offset:2048\nds_read_b64_tr_b16 v[54:55], v119 offset:2304\n"
    "s_waitcnt lgkmcnt(8)\nv_mfma_f32_32x32x16_bf16 v[80:95], v[56:59], v[168:171], v[80:95]\nds_read_b64_tr_b16 v[56:57], v119 offset:3072\n"
    "ds_read_b64_tr_b16 v[58:59], v119 offset:3328\ns_barrier\ns_waitcnt lgkmcnt(8)\nv_mfma_f32_32x32x16_bf16 v[80:95], v[60:63], v[172:175], v[80:95]\n"
    "ds_read_b128 v[144:147], v29 offset:8192\nds_read_b64_tr_b16 v[60:61], v116 offset:0\nds_read_b64_tr_b16 v[62:63], v116 offset:256\ns_waitcnt lgkmcnt(9)\n"
    "v_mfma_f32_32x32x16_bf16 v[96:111], v[44:47], v[160:163], v[96:111]\nds_read_b128 v[148:151], v176 offset:8192\nds_read_b64_tr_b16 v[44:45], v116 offset:1024\n"
    "ds_read_b64_tr_b16 v[46:47], v116 offset:1280\ns_waitcnt lgkmcnt(10)\nv_mfma_f32_32x32x16_bf16 v[96:111], v[48:51], v[164:167], v[96:111]\n"
    "ds_read_b128 v[152:155], v177 offset:8192\nds_read_b64_tr_b16 v[48:49], v116 offset:2048\nds_read_b64_tr_b16 v[50:51], v116 offset:2304\ns_waitcnt lgkmcnt(11)\n"
    "v_mfma_f32_32x32x16_bf16 v[96:111], v[52:55], v[168:171], v[96:111]\nds_read_b128 v[156:159], v178 offset:8192\nds_read_b64_tr_b16 v[52:53], v116 offset:3072\n"
    "ds_read_b64_tr_b16 v[54:55], v116 offset:3328\ns_waitcnt lgkmcnt(12)\nv_mfma_f32_32x32x16_bf16 v[96:111], v[56:59], v[172:175], v[96:111]\n"
    "ds_read_b64_tr_b16 v[56:57], v116 offset:32768\nds_read_b64_tr_b16 v[58:59], v116 offset:33024\ns_waitcnt lgkmcnt(13)\nv_dot2c_f32_bf16 v112, v143, v144\n"
    "v_dot2c_f32_bf16 v113, v144, v144\nv_dot2c_f32_bf16 v114, v143, v145\nv_dot2c_f32_bf16 v115, v145, v145\nv_dot2c_f32_bf16 v112, v143, v146\n"
    "v_dot2c_f32_bf16 v113, v146, v146\nv_dot2c_f32_bf16 v114, v143, v147\nv_dot2c_f32_bf16 v115, v147, v147\ns_waitcnt lgkmcnt(10)\nv_dot2c_f32_bf16 v112, v143, v148\n"
    "v_dot2c_f32_bf16 v113, v148, v148\nv_dot2c_f32_bf16 v114, v143, v149\nv_dot2c_f32_bf16 v115, v149, v149\nv_dot2c_f32_bf16 v112, v143, v150\n"
    "v_dot2c_f32_bf16 v113, v150, v150\nv_dot2c_f32_bf16 v114, v143, v151\nv_dot2c_f32_bf16 v115, v151, v151\ns_waitcnt lgkmcnt(7)\nv_dot2c_f32_bf16 v112, v143, v152\n"
    "v_dot2c_f32_bf16 v113, v152, v152\nv_dot2c_f32_bf16 v114, v143, v153\nv_dot2c_f32_bf16 v115, v153, v153\nv_dot2c_f32_bf16 v112, v143, v154\n"
    "v_dot2c_f32_bf16 v113, v154, v154\nv_dot2c_f32_bf16 v114, v143, v155\nv_dot2c_f32_bf16 v115, v155, v155\ns_waitcnt lgkmcnt(4)\nv_dot2c_f32_bf16 v112, v143, v156\n"
    "v_dot2c_f32_bf16 v113, v156, v156\nv_dot2c_f32_bf16 v114, v143, v157\nv_dot2c_f32_bf16 v115, v157, v157\nv_dot2c_f32_bf16 v112, v143, v158\n"
    "v_dot2c_f32_bf16 v113, v158, v158\nv_dot2c_f32_bf16 v114, v143, v159\nv_dot2c_f32_bf16 v115, v159, v159\nv_mfma_f32_32x32x16_bf16 v[64:79], v[60:63], v[144:147], v[64:79]\n"
    "ds_read_b64_tr_b16 v[60:61], v116 offset:33792\nds_read_b64_tr_b16 v[62:63], v116 offset:34048\nv_add_u32 v117, s47, v26\nv_add_u32 v119, 0x10000, v117\n"
    "v_mfma_f32_32x32x16_bf16 v[64:79], v[44:47], v[148:151], v[64:79]\nds_read_b64_tr_b16 v[44:45], v116 offset:34816\nds_read_b64_tr_b16 v[46:47], v116 offset:35072\n"
    "v_mfma_f32_32x32x16_bf16 v[64:79], v[48:51], v[152:155], v[64:79]\nds_read_b64_tr_b16 v[48:49], v116 offset:35840\nds_read_b64_tr_b16 v[50:51], v116 offset:36096\n"
    "s_waitcnt lgkmcnt(8)\nv_mfma_f32_32x32x16_bf16 v[64:79], v[52:55], v[156:159], v[64:79]\nds_read_b64_tr_b16 v[52:53], v118 offset:0\n"
    "ds_read_b64_tr_b16 v[54:55], v118 offset:256\ns_waitcnt lgkmcnt(8)\nv_mfma_f32_32x32x16_bf16 v[80:95], v[56:59], v[144:147], v[80:95]\n"
    "ds_read_b64_tr_b16 v[56:57], v118 offset:1024\nds_read_b64_tr_b16 v[58:59], v118 offset:1280\ns_waitcnt lgkmcnt(8)\n"
    "v_mfma_f32_32x32x16_bf16 v[80:95], v[60:63], v[148:151], v[80:95]\nds_read_b64_tr_b16 v[60:61], v118 offset:2048\nds_read_b64_tr_b16 v[62:63], v118 offset:2304\n"
    "s_waitcnt lgkmcnt(8)\nv_mfma_f32_32x32x16_bf16 v[80:95], v[44:47], v[152:155], v[80:95]\nds_read_b64_tr_b16 v[44:45], v118 offset:3072\n"
    "ds_read_b64_tr_b16 v[46:47], v118 offset:3328\ns_barrier\ns_waitcnt lgkmcnt(8)\nv_mfma_f32_32x32x16_bf16 v[80:95], v[48:51], v[156:159], v[80:95]\n"
    "ds_read_b128 v[160:163], v29 offset:0\nds_read_b64_tr_b16 v[48:49], v117 offset:0\nds_read_b64_tr_b16 v[50:51], v117 offset:256\ns_waitcnt lgkmcnt(9)\n"
    "v_mfma_f32_32x32x16_bf16 v[96:111], v[52:55], v[144:147], v[96:111]\nds_read_b128 v[164:167], v176 offset:0\nds_read_b64_tr_b16 v[52:53], v117 offset:1024\n"
    "ds_read_b64_tr_b16 v[54:55], v117 offset:1280\ns_waitcnt lgkmcnt(10)\nv_mfma_f32_32x32x16_bf16 v[96:111], v[56:59], v[148:151], v[96:111]\n"
    "ds_read_b128 v[168:171], v177 offset:0\nds_read_b64_tr_b16 v[56:57], v117 offset:2048\nds_read_b64_tr_b16 v[58:59], v117 offset:2304\ns_waitcnt lgkmcnt(11)\n"
    "v_mfma_f32_32x32x16_bf16 v[96:111], v[60:63], v[152:155], v[96:111]\nds_read_b128 v[172:175], v178 offset:0\nds_read_b64_tr_b16 v[60:61], v117 offset:3072\n"
    "ds_read_b64_tr_b16 v[62:63], v117 offset:3328\ns_waitcnt lgkmcnt(12)\nv_mfma_f32_32x32x16_bf16 v[96:111], v[44:47], v[156:159], v[96:111]\n"
    "ds_read_b64_tr_b16 v[44:45], v117 offset:32768\nds_read_b64_tr_b16 v[46:47], v117 offset:33024\ns_waitcnt lgkmcnt(13)\nv_dot2c_f32_bf16 v112, v143, v160\n"
    "v_dot2c_f32_bf16 v113, v160, v160\nv_dot2c_f32_bf16 v114, v143, v161\nv_dot2c_f32_bf16 v115, v161, v161\nv_dot2c_f32_bf16 v112, v143, v162\n"
    "v_dot2c_f32_bf16 v113, v162, v162\nv_dot2c_f32_bf16 v114, v143, v163\nv_dot2c_f32_bf16 v115, v163, v163\ns_waitcnt lgkmcnt(10)\nv_dot2c_f32_bf16 v112, v143, v164\n"
    "v_dot2c_f32_bf16 v113, v164, v164\nv_dot2c_f32_bf16 v114, v143, v165\nv_dot2c_f32_bf16 v115, v165, v165\nv_dot2c_f32_bf16 v112, v143, v166\n"
    "v_dot2c_f32_bf16 v113, v166, v166\nv_dot2c_f32_bf16 v114, v143, v167\nv_dot2c_f32_bf16 v115, v167, v167\ns_waitcnt lgkmcnt(7)\nv_dot2c_f32_bf16 v112, v143, v168\n"
    "v_dot2c_f32_bf16 v113, v168, v168\nv_dot2c_f32_bf16 v114, v143, v169\nv_dot2c_f32_bf16 v115, v169, v169\nv_dot2c_f32_bf16 v112, v143, v170\n"
    "v_dot2c_f32_bf16 v113, v170, v170\nv_dot2c_f32_bf16 v114, v143, v171\nv_dot2c_f32_bf16 v115, v171, v171\ns_waitcnt lgkmcnt(4)\nv_dot2c_f32_bf16 v112, v143, v172\n"
    "v_dot2c_f32_bf16 v113, v172, v172\nv_dot2c_f32_bf16 v114, v143, v173\nv_dot2c_f32_bf16 v115, v173, v173\nv_dot2c_f32_bf16 v112, v143, v174\n"
    "v_dot2c_f32_bf16 v113, v174, v174\nv_dot2c_f32_bf16 v114, v143, v175\nv_dot2c_f32_bf16 v115, v175, v175\nv_mfma_f32_32x32x16_bf16 v[64:79], v[48:51], v[160:163], v[64:79]\n"
    "ds_read_b64_tr_b16 v[48:49], v117 offset:33792\nds_read_b64_tr_b16 v[50:51], v117 offset:34048\nv_add_u32 v116, s48, v26\nv_add_u32 v118, 0x10000, v116\n"
    "v_mfma_f32_32x32x16_bf16 v[64:79], v[52:55], v[164:167], v[64:79]\nds_read_b64_tr_b16 v[52:53], v117 offset:34816\nds_read_b64_tr_b16 v[54:55], v117 offset:35072\n"
    "s_cmp_lt_u32 s3, 2\ns_cbranch_scc0 .LL72\ns_waitcnt vmcnt(0)\ns_mov_b64 exec, 1\nglobal_store_dword v30, v180, s[32:33] sc1\ns_mov_b64 exec, -1\n.LL72:\n"
    "v_mfma_f32_32x32x16_bf16 v[64:79], v[56:59], v[168:171], v[64:79]\nds_read_b64_tr_b16 v[56:57], v117 offset:35840\nds_read_b64_tr_b16 v[58:59], v117 offset:36096\n"
    "s_waitcnt lgkmcnt(8)\nv_mfma_f32_32x32x16_bf16 v[64:79], v[60:63], v[172:175], v[64:79]\nds_read_b64_tr_b16 v[60:61], v119 offset:0\n"
    "ds_read_b64_tr_b16 v[62:63], v119 offset:256\ns_waitcnt lgkmcnt(8)\nv_mfma_f32_32x32x16_bf16 v[80:95], v[44:47], v[160:163], v[80:95]\n"
    "ds_read_b64_tr_b16 v[44:45], v119 offset:1024\nds_read_b64_tr_b16 v[46:47], v119 offset:1280\ns_waitcnt lgkmcnt(8)\n"
    "v_mfma_f32_32x32x16_bf16 v[80:95], v[48:51], v[164:167], v[80:95]\nds_read_b64_tr_b16 v[48:49], v119 offset:2048\nds_read_b64_tr_b16 v[50:51], v119 offset:2304\n"
    "s_waitcnt lgkmcnt(8)\nv_mfma_f32_32x32x16_bf16 v[80:95], v[52:55], v[168:171], v[80:95]\nds_read_b64_tr_b16 v[52:53], v119 offset:3072\n"
    "ds_read_b64_tr_b16 v[54:55], v119 offset:3328\ns_barrier\ns_waitcnt lgkmcnt(8)\nv_mfma_f32_32x32x16_bf16 v[80:95], v[56:59], v[172:175], v[80:95]\n"
    "ds_read_b128 v[144:147], v29 offset:4096\nds_read_b64_tr_b16 v[56:57], v116 offset:0\nds_read_b64_tr_b16 v[58:59], v116 offset:256\ns_waitcnt lgkmcnt(9)\n"
    "v_mfma_f32_32x32x16_bf16 v[96:111], v[60:63], v[160:163], v[96:111]\nds_read_b128 v[148:151], v176 offset:4096\nds_read_b64_tr_b16 v[60:61], v116 offset:1024\n"
    "ds_read_b64_tr_b16 v[62:63], v116 offset:1280\ns_waitcnt lgkmcnt(10)\nv_mfma_f32_32x32x16_bf16 v[96:111], v[44:47], v[164:167], v[96:111]\n"
    "ds_read_b128 v[152:155], v177 offset:4096\nds_read_b64_tr_b16 v[44:45], v116 offset:2048\nds_read_b64_tr_b16 v[46:47], v116 offset:2304\ns_waitcnt lgkmcnt(11)\n"
    "v_mfma_f32_32x32x16_bf16 v[96:111], v[48:51], v[168:171], v[96:111]\nds_read_b128 v[156:159], v178 offset:4096\nds_read_b64_tr_b16 v[48:49], v116 offset:3072\n"
    "ds_read_b64_tr_b16 v[50:51], v116 offset:3328\ns_waitcnt lgkmcnt(12)\nv_mfma_f32_32x32x16_bf16 v[96:111], v[52:55], v[172:175], v[96:111]\n"
    "ds_read_b64_tr_b16 v[52:53], v116 offset:32768\nds_read_b64_tr_b16 v[54:55], v116 offset:33024\ns_waitcnt lgkmcnt(13)\nv_dot2c_f32_bf16 v112, v143, v144\n"
    "v_dot2c_f32_bf16 v113, v144, v144\nv_dot2c_f32_bf16 v114, v143, v145\nv_dot2c_f32_bf16 v115, v145, v145\nv_dot2c_f32_bf16 v112, v143, v146\n"
    "v_dot2c_f32_bf16 v113, v146, v146\nv_dot2c_f32_bf16 v114, v143, v147\nv_dot2c_f32_bf16 v115, v147, v147\ns_waitcnt lgkmcnt(10)\nv_dot2c_f32_bf16 v112, v143, v148\n"
    "v_dot2c_f32_bf16 v113, v148, v148\nv_dot2c_f32_bf16 v114, v143, v149\nv_dot2c_f32_bf16 v115, v149, v149\nv_dot2c_f32_bf16 v112, v143, v150\n"
    "v_dot2c_f32_bf16 v113, v150, v150\nv_dot2c_f32_bf16 v114, v143, v151\nv_dot2c_f32_bf16 v115, v151, v151\ns_waitcnt lgkmcnt(7)\nv_dot2c_f32_bf16 v112, v143, v152\n"
    "v_dot2c_f32_bf16 v113, v152, v152\nv_dot2c_f32_bf16 v114, v143, v153\nv_dot2c_f32_bf16 v115, v153, v153\nv_dot2c_f32_bf16 v112, v143, v154\n"
    "v_dot2c_f32_bf16 v113, v154, v154\nv_dot2c_f32_bf16 v114, v143, v155\nv_dot2c_f32_bf16 v115, v155, v155\ns_waitcnt lgkmcnt(4)\nv_dot2c_f32_bf16 v112, v143, v156\n"
    "v_dot2c_f32_bf16 v113, v156, v156\nv_dot2c_f32_bf16 v114, v143, v157\nv_dot2c_f32_bf16 v115, v157, v157\nv_dot2c_f32_bf16 v112, v143, v158\n"
    "v_dot2c_f32_bf16 v113, v158, v158\nv_dot2c_f32_bf16 v114, v143, v159\nv_dot2c_f32_bf16 v115, v159, v159\nv_mfma_f32_32x32x16_bf16 v[64:79], v[56:59], v[144:147], v[64:79]\n"
    "ds_read_b64_tr_b16 v[56:57], v116 offset:33792\nds_read_b64_tr_b16 v[58:59], v116 offset:34048\nv_add_u32 v117, s49, v26\nv_add_u32 v119, 0x10000, v117\n"
    "v_mfma_f32_32x32x16_bf16 v[64:79], v[60:63], v[148:151], v[64:79]\nds_read_b64_tr_b16 v[60:61], v116 offset:34816\nds_read_b64_tr_b16 v[62:63], v116 offset:35072\n"
    "v_mfma_f32_32x32x16_bf16 v[64:79], v[44:47], v[152:155], v[64:79]\nds_read_b64_tr_b16 v[44:45], v116 offset:35840\nds_read_b64_tr_b16 v[46:47], v116 offset:36096\n"
    "s_waitcnt lgkmcnt(8)\nv_mfma_f32_32x32x16_bf16 v[64:79], v[48:51], v[156:159], v[64:79]\nds_read_b64_tr_b16 v[48:49], v118 offset:0\n"
    "ds_read_b64_tr_b16 v[50:51], v118 offset:256\ns_waitcnt lgkmcnt(8)\nv_mfma_f32_32x32x16_bf16 v[80:95], v[52:55], v[144:147], v[80:95]\n"
    "ds_read_b64_tr_b16 v[52:53], v118 offset:1024\nds_read_b64_tr_b16 v[54:55], v118 offset:1280\ns_waitcnt lgkmcnt(8)\n"
    "v_mfma_f32_32x32x16_bf16 v[80:95], v[56:59], v[148:151], v[80:95]\nds_read_b64_tr_b16 v[56:57], v118 offset:2048\nds_read_b64_tr_b16 v[58:59], v118 offset:2304\n"
    "s_waitcnt lgkmcnt(8)\nv_mfma_f32_32x32x16_bf16 v[80:95], v[60:63], v[152:155], v[80:95]\nds_read_b64_tr_b16 v[60:61], v118 offset:3072\n"
    "ds_read_b64_tr_b16 v[62:63], v118 offset:3328\ns_barrier\ns_waitcnt lgkmcnt(8)\nv_mfma_f32_32x32x16_bf16 v[80:95], v[44:47], v[156:159], v[80:95]\n"
    "ds_read_b128 v[160:163], v29 offset:8192\nds_read_b64_tr_b16 v[44:45], v117 offset:0\nds_read_b64_tr_b16 v[46:47], v117 offset:256\ns_waitcnt lgkmcnt(9)\n"
    "v_mfma_f32_32x32x16_bf16 v[96:111], v[48:51], v[144:147], v[96:111]\nds_read_b128 v[164:167], v176 offset:8192\nds_read_b64_tr_b16 v[48:49], v117 offset:1024\n"
    "ds_read_b64_tr_b16 v[50:51], v117 offset:1280\ns_waitcnt lgkmcnt(10)\nv_mfma_f32_32x32x16_bf16 v[96:111], v[52:55], v[148:151], v[96:111]\n"
    "ds_read_b128 v[168:171], v177 offset:8192\nds_read_b64_tr_b16 v[52:53], v117 offset:2048\nds_read_b64_tr_b16 v[54:55], v117 offset:2304\ns_waitcnt lgkmcnt(11)\n"
    "v_mfma_f32_32x32x16_bf16 v[96:111], v[56:59], v[152:155], v[96:111]\nds_read_b128 v[172:175], v178 offset:8192\nds_read_b64_tr_b16 v[56:57], v117 offset:3072\n"
    "ds_read_b64_tr_b16 v[58:59], v117 offset:3328\ns_waitcnt lgkmcnt(12)\nv_mfma_f32_32x32x16_bf16 v[96:111], v[60:63], v[156:159], v[96:111]\n"
    "ds_read_b64_tr_b16 v[60:61], v117 offset:32768\nds_read_b64_tr_b16 v[62:63], v117 offset:33024\ns_waitcnt lgkmcnt(13)\nv_dot2c_f32_bf16 v112, v143, v160\n"
    "v_dot2c_f32_bf16 v113, v160, v160\nv_dot2c_f32_bf16 v114, v143, v161\nv_dot2c_f32_bf16 v115, v161, v161\nv_dot2c_f32_bf16 v112, v143, v162\n"
    "v_dot2c_f32_bf16 v113, v162, v162\nv_dot2c_f32_bf16 v114, v143, v163\nv_dot2c_f32_bf16 v115, v163, v163\ns_waitcnt lgkmcnt(10)\nv_dot2c_f32_bf16 v112, v143, v164\n"
    "v_dot2c_f32_bf16 v113, v164, v164\nv_dot2c_f32_bf16 v114, v143, v165\nv_dot2c_f32_bf16 v115, v165, v165\nv_dot2c_f32_bf16 v112, v143, v166\n"
    "v_dot2c_f32_bf16 v113, v166, v166\nv_dot2c_f32_bf16 v114, v143, v167\nv_dot2c_f32_bf16 v115, v167, v167\ns_waitcnt lgkmcnt(7)\nv_dot2c_f32_bf16 v112, v143, v168\n"
    "v_dot2c_f32_bf16 v113, v168, v168\nv_dot2c_f32_bf16 v114, v143, v169\nv_dot2c_f32_bf16 v115, v169, v169\nv_dot2c_f32_bf16 v112, v143, v170\n"
    "v_dot2c_f32_bf16 v113, v170, v170\nv_dot2c_f32_bf16 v114, v143, v171\nv_dot2c_f32_bf16 v115, v171, v171\ns_waitcnt lgkmcnt(4)\nv_dot2c_f32_bf16 v112, v143, v172\n"
    "v_dot2c_f32_bf16 v113, v172, v172\nv_dot2c_f32_bf16 v114, v143, v173\nv_dot2c_f32_bf16 v115, v173, v173\nv_dot2c_f32_bf16 v112, v143, v174\n"
    "v_dot2c_f32_bf16 v113, v174, v174\nv_dot2c_f32_bf16 v114, v143, v175\nv_dot2c_f32_bf16 v115, v175, v175\nv_mfma_f32_32x32x16_bf16 v[64:79], v[44:47], v[160:163], v[64:79]\n"
    "ds_read_b64_tr_b16 v[44:45], v117 offset:33792\nds_read_b64_tr_b16 v[46:47], v117 offset:34048\nv_add_u32 v116, s50, v26\nv_add_u32 v118, 0x10000, v116\n"
    "v_mfma_f32_32x32x16_bf16 v[64:79], v[48:51], v[164:167], v[64:79]\nds_read_b64_tr_b16 v[48:49], v117 offset:34816\nds_read_b64_tr_b16 v[50:51], v117 offset:35072\n"
    "v_mfma_f32_32x32x16_bf16 v[64:79], v[52:55], v[168:171], v[64:79]\nds_read_b64_tr_b16 v[52:53], v117 offset:35840\nds_read_b64_tr_b16 v[54:55], v117 offset:36096\n"
    "s_waitcnt lgkmcnt(8)\nv_mfma_f32_32x32x16_bf16 v[64:79], v[56:59], v[172:175], v[64:79]\nds_read_b64_tr_b16 v[56:57], v119 offset:0\n"
    "ds_read_b64_tr_b16 v[58:59], v119 offset:256\ns_waitcnt lgkmcnt(8)\nv_mfma_f32_32x32x16_bf16 v[80:95], v[60:63], v[160:163], v[80:95]\n"
    "ds_read_b64_tr_b16 v[60:61], v119 offset:1024\nds_read_b64_tr_b16 v[62:63], v119 offset:1280\ns_waitcnt lgkmcnt(8)\n"
    "v_mfma_f32_32x32x16_bf16 v[80:95], v[44:47], v[164:167], v[80:95]\nds_read_b64_tr_b16 v[44:45], v119 offset:2048\nds_read_b64_tr_b16 v[46:47], v119 offset:2304\n"
    "s_waitcnt lgkmcnt(8)\nv_mfma_f32_32x32x16_bf16 v[80:95], v[48:51], v[168:171], v[80:95]\nds_read_b64_tr_b16 v[48:49], v119 offset:3072\n"
    "ds_read_b64_tr_b16 v[50:51], v119 offset:3328\ns_barrier\ns_waitcnt lgkmcnt(8)\nv_mfma_f32_32x32x16_bf16 v[80:95], v[52:55], v[172:175], v[80:95]\n"
    "ds_read_b128 v[144:147], v29 offset:0\nds_read_b64_tr_b16 v[52:53], v116 offset:0\nds_read_b64_tr_b16 v[54:55], v116 offset:256\ns_waitcnt lgkmcnt(9)\n"
    "v_mfma_f32_32x32x16_bf16 v[96:111], v[56:59], v[160:163], v[96:111]\nds_read_b128 v[148:151], v176 offset:0\nds_read_b64_tr_b16 v[56:57], v116 offset:1024\n"
    "ds_read_b64_tr_b16 v[58:59], v116 offset:1280\ns_waitcnt lgkmcnt(10)\nv_mfma_f32_32x32x16_bf16 v[96:111], v[60:63], v[164:167], v[96:111]\n"
    "ds_read_b128 v[152:155], v177 offset:0\nds_read_b64_tr_b16 v[60:61], v116 offset:2048\nds_read_b64_tr_b16 v[62:63], v116 offset:2304\ns_waitcnt lgkmcnt(11)\n"
    "v_mfma_f32_32x32x16_bf16 v[96:111], v[44:47], v[168:171], v[96:111]\nds_read_b128 v[156:159], v178 offset:0\nds_read_b64_tr_b16 v[44:45], v116 offset:3072\n"
    "ds_read_b64_tr_b16 v[46:47], v116 offset:3328\ns_waitcnt lgkmcnt(12)\nv_mfma_f32_32x32x16_bf16 v[96:111], v[48:51], v[172:175], v[96:111]\n"
    "ds_read_b64_tr_b16 v[48:49], v116 offset:32768\nds_read_b64_tr_b16 v[50:51], v116 offset:33024\ns_waitcnt lgkmcnt(13)\nv_dot2c_f32_bf16 v112, v143, v144\n"
    "v_dot2c_f32_bf16 v113, v144, v144\nv_dot2c_f32_bf16 v114, v143, v145\nv_dot2c_f32_bf16 v115, v145, v145\nv_dot2c_f32_bf16 v112, v143, v146\n"
    "v_dot2c_f32_bf16 v113, v146, v146\nv_dot2c_f32_bf16 v114, v143, v147\nv_dot2c_f32_bf16 v115, v147, v147\ns_waitcnt lgkmcnt(10)\nv_dot2c_f32_bf16 v112, v143, v148\n"
    "v_dot2c_f32_bf16 v113, v148, v148\nv_dot2c_f32_bf16 v114, v143, v149\nv_dot2c_f32_bf16 v115, v149, v149\nv_dot2c_f32_bf16 v112, v143, v150\n"
    "v_dot2c_f32_bf16 v113, v150, v150\nv_dot2c_f32_bf16 v114, v143, v151\nv_dot2c_f32_bf16 v115, v151, v151\ns_waitcnt lgkmcnt(7)\nv_dot2c_f32_bf16 v112, v143, v152\n"
    "v_dot2c_f32_bf16 v113, v152, v152\nv_dot2c_f32_bf16 v114, v143, v153\nv_dot2c_f32_bf16 v115, v153, v153\nv_dot2c_f32_bf16 v112, v143, v154\n"
    "v_dot2c_f32_bf16 v113, v154, v154\nv_dot2c_f32_bf16 v114, v143, v155\nv_dot2c_f32_bf16 v115, v155, v155\ns_waitcnt lgkmcnt(4)\nv_dot2c_f32_bf16 v112, v143, v156\n"
    "v_dot2c_f32_bf16 v113, v156, v156\nv_dot2c_f32_bf16 v114, v143, v157\nv_dot2c_f32_bf16 v115, v157, v157\nv_dot2c_f32_bf16 v112, v143, v158\n"
    "v_dot2c_f32_bf16 v113, v158, v158\nv_dot2c_f32_bf16 v114, v143, v159\nv_dot2c_f32_bf16 v115, v159, v159\nv_mfma_f32_32x32x16_bf16 v[64:79], v[52:55], v[144:147], v[64:79]\n"
    "ds_read_b64_tr_b16 v[52:53], v116 offset:33792\nds_read_b64_tr_b16 v[54:55], v116 offset:34048\nv_add_u32 v117, s51, v26\nv_add_u32 v119, 0x10000, v117\n"
    "v_mfma_f32_32x32x16_bf16 v[64:79], v[56:59], v[148:151], v[64:79]\nds_read_b64_tr_b16 v[56:57], v116 offset:34816\nds_read_b64_tr_b16 v[58:59], v116 offset:35072\n"
    "v_mfma_f32_32x32x16_bf16 v[64:79], v[60:63], v[152:155], v[64:79]\nds_read_b64_tr_b16 v[60:61], v116 offset:35840\nds_read_b64_tr_b16 v[62:63], v116 offset:36096\n"
    "s_waitcnt lgkmcnt(8)\nv_mfma_f32_32x32x16_bf16 v[64:79], v[44:47], v[156:159], v[64:79]\nds_read_b64_tr_b16 v[44:45], v118 offset:0\n"
    "ds_read_b64_tr_b16 v[46:47], v118 offset:256\ns_waitcnt lgkmcnt(8)\nv_mfma_f32_32x32x16_bf16 v[80:95], v[48:51], v[144:147], v[80:95]\n"
    "ds_read_b64_tr_b16 v[48:49], v118 offset:1024\nds_read_b64_tr_b16 v[50:51], v118 offset:1280\ns_waitcnt lgkmcnt(8)\n"
    "v_mfma_f32_32x32x16_bf16 v[80:95], v[52:55], v[148:151], v[80:95]\nds_read_b64_tr_b16 v[52:53], v118 offset:2048\nds_read_b64_tr_b16 v[54:55], v118 offset:2304\n"
    "s_waitcnt lgkmcnt(8)\nv_mfma_f32_32x32x16_bf16 v[80:95], v[56:59], v[152:155], v[80:95]\nds_read_b64_tr_b16 v[56:57], v118 offset:3072\n"
    "ds_read_b64_tr_b16 v[58:59], v118 offset:3328\ns_waitcnt lgkmcnt(8)\nv_mfma_f32_32x32x16_bf16 v[80:95], v[60:63], v[156:159], v[80:95]\ns_waitcnt lgkmcnt(6)\n"
    "v_mfma_f32_32x32x16_bf16 v[96:111], v[44:47], v[144:147], v[96:111]\ns_waitcnt lgkmcnt(4)\nv_mfma_f32_32x32x16_bf16 v[96:111], v[48:51], v[148:151], v[96:111]\n"
    "s_waitcnt lgkmcnt(2)\nv_mfma_f32_32x32x16_bf16 v[96:111], v[52:55], v[152:155], v[96:111]\ns_waitcnt lgkmcnt(0)\n"
    "v_mfma_f32_32x32x16_bf16 v[96:111], v[56:59], v[156:159], v[96:111]\ns_barrier\nds_read_b128 v[160:163], v29 offset:4096\nds_read_b128 v[164:167], v176 offset:4096\n"
    "ds_read_b128 v[168:171], v177 offset:4096\nds_read_b128 v[172:175], v178 offset:4096\nds_read_b64_tr_b16 v[60:61], v117 offset:0\n"
    "ds_read_b64_tr_b16 v[62:63], v117 offset:256\nds_read_b64_tr_b16 v[44:45], v117 offset:1024\nds_read_b64_tr_b16 v[46:47], v117 offset:1280\n"
    "ds_read_b64_tr_b16 v[48:49], v117 offset:2048\nds_read_b64_tr_b16 v[50:51], v117 offset:2304\nds_read_b64_tr_b16 v[52:53], v117 offset:3072\n"
    "ds_read_b64_tr_b16 v[54:55], v117 offset:3328\nds_read_b64_tr_b16 v[56:57], v117 offset:32768\nds_read_b64_tr_b16 v[58:59], v117 offset:33024\ns_waitcnt lgkmcnt(13)\n"
    "v_dot2c_f32_bf16 v112, v143, v160\nv_dot2c_f32_bf16 v113, v160, v160\nv_dot2c_f32_bf16 v114, v143, v161\nv_dot2c_f32_bf16 v115, v161, v161\n"
    "v_dot2c_f32_bf16 v112, v143, v162\nv_dot2c_f32_bf16 v113, v162, v162\nv_dot2c_f32_bf16 v114, v143, v163\nv_dot2c_f32_bf16 v115, v163, v163\ns_waitcnt lgkmcnt(12)\n"
    "v_dot2c_f32_bf16 v112, v143, v164\nv_dot2c_f32_bf16 v113, v164, v164\nv_dot2c_f32_bf16 v114, v143, v165\nv_dot2c_f32_bf16 v115, v165, v165\n"
    "v_dot2c_f32_bf16 v112, v143, v166\nv_dot2c_f32_bf16 v113, v166, v166\nv_dot2c_f32_bf16 v114, v143, v167\nv_dot2c_f32_bf16 v115, v167, v167\ns_waitcnt lgkmcnt(11)\n"
    "v_dot2c_f32_bf16 v112, v143, v168\nv_dot2c_f32_bf16 v113, v168, v168\nv_dot2c_f32_bf16 v114, v143, v169\nv_dot2c_f32_bf16 v115, v169, v169\n"
    "v_dot2c_f32_bf16 v112, v143, v170\nv_dot2c_f32_bf16 v113, v170, v170\nv_dot2c_f32_bf16 v114, v143, v171\nv_dot2c_f32_bf16 v115, v171, v171\ns_waitcnt lgkmcnt(10)\n"
    "v_dot2c_f32_bf16 v112, v143, v172\nv_dot2c_f32_bf16 v113, v172, v172\nv_dot2c_f32_bf16 v114, v143, v173\nv_dot2c_f32_bf16 v115, v173, v173\n"
    "v_dot2c_f32_bf16 v112, v143, v174\nv_dot2c_f32_bf16 v113, v174, v174\nv_dot2c_f32_bf16 v114, v143, v175\nv_dot2c_f32_bf16 v115, v175, v175\ns_waitcnt lgkmcnt(8)\n"
    "v_mfma_f32_32x32x16_bf16 v[64:79], v[60:63], v[160:163], v[64:79]\nds_read_b64_tr_b16 v[60:61], v117 offset:33792\nds_read_b64_tr_b16 v[62:63], v117 offset:34048\n"
    "v_add_f32 v112, v112, v114\nv_add_f32 v113, v113, v115\ns_waitcnt lgkmcnt(8)\nv_mfma_f32_32x32x16_bf16 v[64:79], v[44:47], v[164:167], v[64:79]\n"
    "ds_read_b64_tr_b16 v[44:45], v117 offset:34816\nds_read_b64_tr_b16 v[46:47], v117 offset:35072\nv_permlane32_swap_b32 v112, v113\nv_add_f32 v138, v112, v113\n"
    "v_add_f32 v137, v112, v113\ns_waitcnt lgkmcnt(8)\nv_mfma_f32_32x32x16_bf16 v[64:79], v[48:51], v[168:171], v[64:79]\nds_read_b64_tr_b16 v[48:49], v117 offset:35840\n"
    "ds_read_b64_tr_b16 v[50:51], v117 offset:36096\nv_permlane32_swap_b32 v138, v137\nv_mul_f32 v138, s81, v138\nv_mul_f32 v112, v138, v138\ns_waitcnt lgkmcnt(8)\n"
    "v_mfma_f32_32x32x16_bf16 v[64:79], v[52:55], v[172:175], v[64:79]\nds_read_b64_tr_b16 v[52:53], v119 offset:0\nds_read_b64_tr_b16 v[54:55], v119 offset:256\n"
    "v_fma_f32 v137, v137, s81, -v112\nv_max_f32 v137, 0, v137\nv_cvt_pk_bf16_f32 v137, 0, v137\ns_waitcnt lgkmcnt(8)\n"
    "v_mfma_f32_32x32x16_bf16 v[80:95], v[56:59], v[160:163], v[80:95]\nds_read_b64_tr_b16 v[56:57], v119 offset:1024\nds_read_b64_tr_b16 v[58:59], v119 offset:1280\n"
    "v_add_f32 v137, 0x3727c5ac, v137\nv_cvt_pk_bf16_f32 v137, 0, v137\ns_waitcnt lgkmcnt(8)\nv_mfma_f32_32x32x16_bf16 v[80:95], v[60:63], v[164:167], v[80:95]\n"
    "ds_read_b64_tr_b16 v[60:61], v119 offset:2048\nds_read_b64_tr_b16 v[62:63], v119 offset:2304\nv_sqrt_f32 v137, v137\nv_cvt_pk_bf16_f32 v138, -v138, -v138\ns_barrier\n"
    "ds_read_b32 v124, v28 offset:0\nds_read_b32 v128, v28 offset:128\nds_read_b32 v132, v28 offset:256\ns_waitcnt lgkmcnt(11)\n"
    "v_mfma_f32_32x32x16_bf16 v[80:95], v[44:47], v[168:171], v[80:95]\nds_read_b64_tr_b16 v[44:45], v119 offset:3072\nds_read_b64_tr_b16 v[46:47], v119 offset:3328\n"
    "v_cndmask_b32 v120, 0, v138, s[84:85]\nv_cvt_pk_bf16_f32 v137, 0, v137\nv_rcp_f32 v137, v137\ns_waitcnt lgkmcnt(11)\n"
    "v_mfma_f32_32x32x16_bf16 v[80:95], v[48:51], v[172:175], v[80:95]\ns_waitcnt lgkmcnt(0)\nv_cvt_pk_bf16_f32 v140, 0, v124\nv_cvt_pk_bf16_f32 v141, 0, v128\n"
    "v_cvt_pk_bf16_f32 v142, 0, v132\nv_mfma_f32_32x32x16_bf16 v[96:111], v[52:55], v[160:163], v[96:111]\nv_sub_f32 v124, v124, v140\nv_sub_f32 v128, v128, v141\n"
    "v_sub_f32 v132, v132, v142\nv_cvt_pk_bf16_f32 v124, v140, v124\nv_cvt_pk_bf16_f32 v128, v141, v128\nv_cvt_pk_bf16_f32 v132, v142, v132\n"
    "v_mfma_f32_32x32x16_bf16 v[64:79], v[124:127], v[120:123], v[64:79]\nv_mfma_f32_32x32x16_bf16 v[80:95], v[128:131], v[120:123], v[80:95]\n"
    "v_mfma_f32_32x32x16_bf16 v[96:111], v[56:59], v[164:167], v[96:111]\ns_nop 9\nv_mul_f32 v64, v64, v137\nv_mul_f32 v65, v65, v137\nv_mul_f32 v66, v66, v137\n"
    "v_mul_f32 v67, v67, v137\nv_cvt_pk_bf16_f32 v64, v64, v65\nv_cvt_pk_bf16_f32 v65, v66, v67\nds_write_b64 v27, v[64:65] offset:0\n"
    "v_mfma_f32_32x32x16_bf16 v[96:111], v[60:63], v[168:171], v[96:111]\nv_mul_f32 v68, v68, v137\nv_mul_f32 v69, v69, v137\nv_mul_f32 v70, v70, v137\n"
    "v_mul_f32 v71, v71, v137\nv_cvt_pk_bf16_f32 v68, v68, v69\nv_cvt_pk_bf16_f32 v69, v70, v71\nds_write_b64 v27, v[68:69] offset:16\n"
    "v_mfma_f32_32x32x16_bf16 v[96:111], v[44:47], v[172:175], v[96:111]\nv_mul_f32 v72, v72, v137\nv_mul_f32 v73, v73, v137\nv_mul_f32 v74, v74, v137\n"
    "v_mul_f32 v75, v75, v137\nv_cvt_pk_bf16_f32 v72, v72, v73\nv_cvt_pk_bf16_f32 v73, v74, v75\nds_write_b64 v27, v[72:73] offset:32\n"
    "v_mfma_f32_32x32x16_bf16 v[96:111], v[132:135], v[120:123], v[96:111]\nv_mul_f32 v76, v76, v137\nv_mul_f32 v77, v77, v137\nv_mul_f32 v78, v78, v137\n"
    "v_mul_f32 v79, v79, v137\nv_cvt_pk_bf16_f32 v76, v76, v77\nv_cvt_pk_bf16_f32 v77, v78, v79\nds_write_b64 v27, v[76:77] offset:48\n"
    "v_pk_mul_f32 v[80:81], v[80:81], v[136:137] op_sel:[0,1]\nv_pk_mul_f32 v[82:83], v[82:83], v[136:137] op_sel:[0,1]\nv_cvt_pk_bf16_f32 v80, v80, v81\n"
    "v_cvt_pk_bf16_f32 v81, v82, v83\nds_write_b64 v27, v[80:81] offset:64\nv_pk_mul_f32 v[84:85], v[84:85], v[136:137] op_sel:[0,1]\n"
    "v_pk_mul_f32 v[86:87], v[86:87], v[136:137] op_sel:[0,1]\nv_cvt_pk_bf16_f32 v84, v84, v85\nv_cvt_pk_bf16_f32 v85, v86, v87\nds_write_b64 v27, v[84:85] offset:80\n"
    "v_pk_mul_f32 v[88:89], v[88:89], v[136:137] op_sel:[0,1]\nv_pk_mul_f32 v[90:91], v[90:91], v[136:137] op_sel:[0,1]\nv_cvt_pk_bf16_f32 v88, v88, v89\n"
    "v_cvt_pk_bf16_f32 v89, v90, v91\nds_write_b64 v27, v[88:89] offset:96\nv_pk_mul_f32 v[92:93], v[92:93], v[136:137] op_sel:[0,1]\n"
    "v_pk_mul_f32 v[94:95], v[94:95], v[136:137] op_sel:[0,1]\nv_cvt_pk_bf16_f32 v92, v92, v93\nv_cvt_pk_bf16_f32 v93, v94, v95\nds_write_b64 v27, v[92:93] offset:112\n"
    "v_pk_mul_f32 v[96:97], v[96:97], v[136:137] op_sel:[0,1]\nv_pk_mul_f32 v[98:99], v[98:99], v[136:137] op_sel:[0,1]\nv_cvt_pk_bf16_f32 v96, v96, v97\n"
    "v_cvt_pk_bf16_f32 v97, v98, v99\nds_write_b64 v27, v[96:97] offset:128\nv_pk_mul_f32 v[100:101], v[100:101], v[136:137] op_sel:[0,1]\n"
    "v_pk_mul_f32 v[102:103], v[102:103], v[136:137] op_sel:[0,1]\nv_cvt_pk_bf16_f32 v100, v100, v101\nv_cvt_pk_bf16_f32 v101, v102, v103\n"
    "ds_write_b64 v27, v[100:101] offset:144\nv_pk_mul_f32 v[104:105], v[104:105], v[136:137] op_sel:[0,1]\nv_pk_mul_f32 v[106:107], v[106:107], v[136:137] op_sel:[0,1]\n"
    "v_cvt_pk_bf16_f32 v104, v104, v105\nv_cvt_pk_bf16_f32 v105, v106, v107\nds_write_b64 v27, v[104:105] offset:160\n"
    "v_pk_mul_f32 v[108:109], v[108:109], v[136:137] op_sel:[0,1]\nv_pk_mul_f32 v[110:111], v[110:111], v[136:137] op_sel:[0,1]\nv_cvt_pk_bf16_f32 v108, v108, v109\n"
    "v_cvt_pk_bf16_f32 v109, v110, v111\nds_write_b64 v27, v[108:109] offset:176\n.LGL75:\ns_waitcnt lgkmcnt(0)\ns_barrier\nds_read_b128 v[64:67], v6\n"
    "ds_read_b128 v[68:71], v6 offset:208\ns_mov_b32 exec_lo, 0x00ff00ff\ns_mov_b32 exec_hi, 0xff00ff00\nds_read_b128 v[10:13], v7\nds_read_b128 v[14:17], v7 offset:208\n"
    "s_mov_b64 exec, -1\nds_read_b64_tr_b16 v[72:73], v8 offset:0\nds_read_b64_tr_b16 v[74:75], v8 offset:32\nds_read_b64_tr_b16 v[76:77], v8 offset:208\n"
    "ds_read_b64_tr_b16 v[78:79], v8 offset:240\ns_mov_b32 s81, 0x3eb8aa3b\ns_waitcnt lgkmcnt(5)\nv_mfma_f32_16x16x32_bf16 v[80:83], v[64:67], v[10:13], v[2:5]\n"
    "s_waitcnt lgkmcnt(4)\nv_mfma_f32_16x16x32_bf16 v[84:87], v[68:71], v[14:17], v[2:5]\ns_nop 5\nv_cvt_pk_bf16_f32 v80, 0, v80\nv_cvt_pk_bf16_f32 v81, 0, v81\n"
    "v_cvt_pk_bf16_f32 v82, 0, v82\nv_cvt_pk_bf16_f32 v83, 0, v83\nv_cvt_pk_bf16_f32 v84, 0, v84\nv_cvt_pk_bf16_f32 v85, 0, v85\nv_cvt_pk_bf16_f32 v86, 0, v86\n"
    "v_cvt_pk_bf16_f32 v87, 0, v87\nv_max_f32 v88, v80, v81\nv_max_f32 v89, v84, v85\nv_max3_f32 v88, v88, v82, v83\nv_max3_f32 v89, v89, v86, v87\ns_nop 1\n"
    "v_permlane16_swap_b32 v88, v89\nv_max_f32 v90, v88, v89\nv_max_f32 v89, v88, v89\ns_nop 1\nv_permlane32_swap_b32 v90, v89\nv_max_f32 v90, v90, v89\n"
    "v_mul_f32 v88, 0xbeb8aa3b, v90\nv_mul_f32 v89, 0xbeb8aa3b, v90\ns_nop 1\nv_permlane16_swap_b32 v88, v89\nv_fma_f32 v80, v80, s81, v88\nv_fma_f32 v81, v81, s81, v88\n"
    "v_fma_f32 v82, v82, s81, v88\nv_fma_f32 v83, v83, s81, v88\nv_fma_f32 v84, v84, s81, v89\nv_fma_f32 v85, v85, s81, v89\nv_fma_f32 v86, v86, s81, v89\n"
    "v_fma_f32 v87, v87, s81, v89\nv_exp_f32 v80, v80\nv_exp_f32 v81, v81\nv_exp_f32 v82, v82\nv_exp_f32 v83, v83\nv_exp_f32 v84, v84\nv_exp_f32 v85, v85\nv_exp_f32 v86, v86\n"
    "v_exp_f32 v87, v87\nv_add_f32 v88, v80, v81\nv_add_f32 v91, v82, v83\nv_add_f32 v89, v84, v85\nv_add_f32 v92, v86, v87\nv_add_f32 v88, v88, v91\nv_add_f32 v89, v89, v92\n"
    "s_nop 1\nv_permlane16_swap_b32 v88, v89\nv_add_f32 v90, v88, v89\nv_add_f32 v89, v88, v89\ns_nop 1\nv_permlane32_swap_b32 v90, v89\nv_add_f32 v90, v90, v89\n"
    "v_rcp_f32 v88, v90\nv_rcp_f32 v89, v90\ns_nop 1\nv_permlane16_swap_b32 v88, v89\nv_mul_f32 v80, v80, v88\nv_mul_f32 v81, v81, v88\nv_mul_f32 v82, v82, v88\n"
    "v_mul_f32 v83, v83, v88\nv_mul_f32 v84, v84, v89\nv_mul_f32 v85, v85, v89\nv_mul_f32 v86, v86, v89\nv_mul_f32 v87, v87, v89\ns_mov_b32 exec_lo, 0x00ff00ff\n"
    "s_mov_b32 exec_hi, 0x00ff00ff\nv_cvt_pk_bf16_f32 v18, v80, v81\nv_cvt_pk_bf16_f32 v19, v82, v83\nv_cvt_pk_bf16_f32 v22, v84, v85\nv_cvt_pk_bf16_f32 v23, v86, v87\n"
    "s_not_b64 exec, exec\nv_cvt_pk_bf16_f32 v20, v80, v81\nv_cvt_pk_bf16_f32 v21, v82, v83\nv_cvt_pk_bf16_f32 v24, v84, v85\nv_cvt_pk_bf16_f32 v25, v86, v87\n"
    "s_mov_b64 exec, -1\ns_waitcnt lgkmcnt(0)\nv_mfma_f32_16x16x32_bf16 v[64:67], v[72:75], v[18:21], 0\nv_mfma_f32_16x16x32_bf16 v[68:71], v[76:79], v[22:25], 0\ns_nop 6\n"
    "v_cvt_pk_bf16_f32 v80, v64, v65\nv_cvt_pk_bf16_f32 v81, v66, v67\nv_cvt_pk_bf16_f32 v82, v68, v69\nv_cvt_pk_bf16_f32 v83, v70, v71\nv_min_u32 v80, -2, v80\n"
    "v_min_u32 v82, -2, v82\nv_min_u32 v81, -2, v81\nv_min_u32 v83, -2, v83\nv_permlane16_swap_b32 v80, v82\ns_nop 0\nv_permlane16_swap_b32 v81, v83\n"
    "global_store_dwordx4 v9, v[80:83], s[32:33] sc1\ns_cmp_eq_u32 s6, 0\ns_cbranch_scc1 .LL77\ns_lshl_b32 s15, s39, 15\ns_add_u32 s14, s15, 0x801000\n"
    "v_lshl_add_u32 v182, v0, 4, s14\ns_add_u32 s16, s15, 0x803000\nv_lshl_add_u32 v183, v0, 4, s16\ns_add_u32 s17, s15, 0x805000\nv_lshl_add_u32 v184, v0, 4, s17\n"
    "s_add_u32 s18, s15, 0x807000\nv_lshl_add_u32 v185, v0, 4, s18\ns_mov_b32 s19, 0x1f600\nv_lshl_add_u32 v186, v0, 4, s19\nv_and_b32 v100, 63, v0\ns_lshl_b32 s22, s41, 13\n"
    "s_lshl_b32 s23, s3, 11\ns_add_u32 s21, s22, s23\ns_add_u32 s20, s21, 0x1200000\nv_lshl_add_u32 v200, v100, 4, s20\nv_mov_b32 v196, -1\nv_mov_b32 v197, -1\n"
    "v_mov_b32 v198, -1\nv_mov_b32 v199, -1\ns_barrier\ns_mov_b32 s14, 0x1d550\nv_lshl_add_u32 v14, v0, 3, s14\nv_lshrrev_b32 v48, 3, v0\nv_lshl_add_u32 v14, v48, 4, v14\n"
    "s_mov_b32 s15, 0x20300\nv_lshl_add_u32 v15, v48, 7, s15\nv_xor_b32 v50, v0, v48\nv_and_b32 v49, 7, v50\nv_lshl_add_u32 v15, v49, 4, v15\ns_mov_b32 s16, 0x17800\n"
    "v_lshl_add_u32 v16, v0, 4, s16\ns_lshl_b32 s18, s41, 7\ns_add_u32 s17, s18, 0x600000\nv_lshl_add_u32 v51, v48, 15, s17\nv_and_b32 v52, 1, v0\n"
    "v_lshl_add_u32 v51, v52, 6, v51\nv_bfe_u32 v53, v0, 1, 2\nv_lshl_add_u32 v51, v53, 4, v51\nv_lshrrev_b32 v55, 4, v0\ns_lshl_b32 s21, s35, 18\ns_lshl_b32 s22, s36, 8\n"
    "s_add_u32 s20, s21, s22\ns_add_u32 s19, s20, 0x1200000\nv_lshl_add_u32 v54, v55, 13, s19\nv_and_b32 v56, 15, v0\nv_lshl_add_u32 v54, v56, 4, v54\ns_cmp_eq_u32 s6, 0\n"
    "s_cselect_b64 vcc, -1, 0\nv_cndmask_b32 v17, v54, v51, vcc\nv_bfe_u32 v59, v0, 5, 1\nv_and_b32 v61, 31, v0\nv_sub_u32 v60, 0x3ff, v61\nv_lshl_add_u32 v58, v59, 3, v60\n"
    "v_lshrrev_b32 v57, 2, v58\ns_lshr_b32 s25, s3, 1\ns_mul_i32 s24, s25, 0x2100\ns_add_u32 s23, s24, 0x17800\nv_lshl_add_u32 v19, v57, 3, s23\nv_and_b32 v62, 3, v58\n"
    "s_mov_b32 s44, 0x840\nv_mad_u32_u24 v19, v62, s44, v19\ns_lshl2_add_u32 s45, s3, 0x27f80\nv_mov_b32 v12, s45\ns_add_u32 s46, s6, 12\nv_mov_b32 v13, s46\n"
    "v_and_b32 v63, 0xff, v0\nv_mov_b32 v65, 0x1d000\nv_mov_b32 v67, 0x1fdb0\nv_mov_b32 v69, 0x20b10\nv_mov_b32 v70, 0x21870\nv_cmp_gt_u32 vcc, 0xa1, v63\ns_nop 1\n"
    "v_cndmask_b32 v68, v70, v69, vcc\nv_cmp_gt_u32 vcc, 0x7b, v63\ns_nop 1\nv_cndmask_b32 v66, v68, v67, vcc\nv_cmp_gt_u32 vcc, 0x55, v63\ns_nop 1\n"
    "v_cndmask_b32 v64, v66, v65, vcc\nv_lshl_add_u32 v18, v63, 4, v64\nv_mov_b32 v72, 0x1950\nv_mov_b32 v73, 0x3180\nv_cmp_gt_u32 vcc, 0x55, v63\ns_nop 1\n"
    "v_cndmask_b32 v71, v73, v72, vcc\nv_add_u32 v1, v18, v71\ns_mul_i32 s49, s25, 0x1950\ns_and_b32 s51, s3, 1\ns_mul_i32 s50, s51, 0xa00\ns_add_u32 s48, s49, s50\n"
    "s_add_u32 s47, s48, 0x1d000\nv_lshl_add_u32 v2, v59, 4, s47\ns_mov_b32 s52, 0x50\nv_mad_u32_u24 v2, v61, s52, v2\ns_mul_i32 s55, s25, 0x3180\ns_lshl_b32 s56, s51, 12\n"
    "s_add_u32 s54, s55, s56\ns_add_u32 s53, s54, 0x20300\nv_lshl_add_u32 v3, v61, 7, s53\nv_and_b32 v75, 7, v0\nv_xor_b32 v74, v59, v75\nv_lshl_add_u32 v3, v74, 4, v3\n"
    "s_lshl3_add_u32 s57, s25, 0x27fc0\nv_lshl_add_u32 v7, v52, 2, s57\ns_add_u32 s14, s7, 0x401000\nv_lshl_add_u32 v20, v0, 4, s14\ns_lshl_b32 s17, s41, 13\n"
    "s_add_u32 s16, s7, s17\ns_add_u32 s15, s16, 0x200000\nv_lshl_add_u32 v21, v0, 4, s15\nv_add_u32 v22, 8192, v20\nv_add_u32 v23, 16384, v20\nv_add_u32 v24, 24576, v20\n"
    "v_add_u32 v25, 32768, v20\nv_add_u32 v26, 40960, v20\nv_add_u32 v27, 49152, v20\nv_add_u32 v28, 57344, v20\nv_and_b32 v56, 63, v0\ns_lshl_b32 s16, s41, 13\n"
    "s_lshl_b32 s17, s3, 11\ns_add_u32 s15, s16, s17\ns_add_u32 s14, s15, 0x800000\nv_lshl_add_u32 v4, v56, 4, s14\ns_add_u32 s18, s15, 0xa00000\n"
    "v_lshl_add_u32 v5, v56, 4, s18\nv_and_b32 v57, 3, v0\ns_lshl_b32 s21, s2, 8\ns_lshl_b32 s22, s3, 6\ns_add_u32 s20, s21, s22\ns_add_u32 s19, s20, 0xc00000\n"
    "v_lshl_add_u32 v6, v57, 4, s19\nv_and_b32 v58, 0x7f, v0\ns_lshr_b32 s25, s3, 1\ns_mul_i32 s24, s25, 0x2100\ns_add_u32 s23, s24, 0x18000\nv_lshl_add_u32 v47, v58, 4, s23\n"
    "v_and_b32 v59, 15, v0\ns_mov_b32 s44, 0x27600\nv_lshl_add_u32 v29, v59, 4, s44\nv_bfe_u32 v60, v0, 3, 1\ns_lshl_b32 s48, s40, 14\ns_add_u32 s47, s7, s48\n"
    "s_lshl_b32 s49, s39, 7\ns_add_u32 s46, s47, s49\ns_add_u32 s45, s46, 0x400000\nv_lshl_add_u32 v30, v60, 13, s45\nv_and_b32 v61, 7, v0\nv_lshl_add_u32 v30, v61, 4, v30\n"
    "v_mov_b32 v8, -1\nv_mov_b32 v9, -1\nv_mov_b32 v10, -1\nv_mov_b32 v11, -1\ns_sleep 10\nglobal_load_dwordx4 v[208:211], v182, s[32:33] offset:-4096 sc1\n"
    "global_load_dwordx4 v[212:215], v182, s[32:33] offset:0 sc1\nglobal_load_dwordx4 v[216:219], v183, s[32:33] offset:-4096 sc1\n"
    "global_load_dwordx4 v[220:223], v183, s[32:33] offset:0 sc1\nglobal_load_dwordx4 v[224:227], v184, s[32:33] offset:-4096 sc1\n"
    "global_load_dwordx4 v[228:231], v184, s[32:33] offset:0 sc1\nglobal_load_dwordx4 v[232:235], v185, s[32:33] offset:-4096 sc1\n"
    "global_load_dwordx4 v[236:239], v185, s[32:33] offset:0 sc1\nglobal_store_dwordx4 v200, v[196:199], s[32:33] offset:0 sc1\n"
    "global_store_dwordx4 v200, v[196:199], s[32:33] offset:1024 sc1\ns_branch .LL78\n.LL77:\nds_read_b32 v242, v33\nds_read_b32 v243, v33 offset:512\n"
    "ds_read_b64 v[240:241], v31\nds_read_b64 v[244:245], v31 offset:1024\nds_read_b128 v[246:249], v32\nds_read_b64 v[250:251], v32 offset:16\n"
    "global_load_dwordx4 v[22:25], v187, s[32:33] sc1\ns_bitcmp1_b32 s3, 0\ns_cselect_b32 s82, 0x43000000, 0\ns_mov_b32 s86, 0x41000000\ns_waitcnt lgkmcnt(0)\ns_barrier\n"
    "v_lshlrev_b32 v112, 16, v242\nv_and_b32 v113, 0xffff0000, v242\nv_lshlrev_b32 v114, 16, v243\nv_and_b32 v115, 0xffff0000, v243\nv_mul_f32 v112, s93, v112\n"
    "v_mul_f32 v113, s93, v113\nv_mul_f32 v114, s94, v114\nv_mul_f32 v115, s94, v115\nv_mul_f32 v132, s82, v112\nv_mul_f32 v133, s82, v113\nv_mul_f32 v134, s86, v112\n"
    "v_mul_f32 v135, s86, v113\nv_mul_f32 v117, s82, v114\nv_mul_f32 v119, s82, v115\nv_mul_f32 v121, s86, v114\nv_mul_f32 v123, s86, v115\nv_exp_f32 v132, v132\n"
    "v_exp_f32 v133, v133\nv_exp_f32 v134, v134\nv_exp_f32 v135, v135\nv_fract_f32 v117, v117\nv_fract_f32 v119, v119\nv_fract_f32 v121, v121\nv_fract_f32 v123, v123\n"
    "v_cos_f32 v116, v117\nv_cos_f32 v118, v119\nv_cos_f32 v120, v121\nv_cos_f32 v122, v123\nv_sin_f32 v117, v117\nv_sin_f32 v119, v119\nv_sin_f32 v121, v121\n"
    "v_sin_f32 v123, v123\nv_mul_f32 v116, v116, v132\nv_mul_f32 v118, v118, v133\nv_mul_f32 v120, v120, v134\nv_mul_f32 v122, v122, v135\nv_mul_f32 v117, v117, v132\n"
    "v_mul_f32 v119, v119, v133\nv_mul_f32 v121, v121, v134\nv_mul_f32 v123, v123, v135\ns_lshr_b32 s83, s3, 1\ns_mul_i32 s83, s83, 0x6000600\ns_mov_b32 s84, 0xffff\n"
    "v_add_u32 v240, s83, v240\nv_add_u32 v241, s83, v241\nv_add_u32 v244, s83, v244\nv_add_u32 v245, s83, v245\nv_add_u32 v246, s83, v246\nv_add_u32 v247, s83, v247\n"
    "v_add_u32 v248, s83, v248\nv_add_u32 v249, s83, v249\nv_add_u32 v250, s83, v250\nv_add_u32 v251, s83, v251\nv_cvt_pk_f16_f32 v144, v116, v117\n"
    "v_cvt_pk_f16_f32 v145, v118, v119\nv_pk_mul_f32 v[124:125], v[116:117], v[120:121] op_sel_hi:[0,1]\nv_pk_mul_f32 v[126:127], v[118:119], v[122:123] op_sel_hi:[0,1]\n"
    "v_pk_fma_f32 v[116:117], v[116:117], v[120:121], v[124:125] op_sel:[1,1,0] op_sel_hi:[1,0,1] neg_lo:[0,1,0]\n"
    "v_pk_fma_f32 v[118:119], v[118:119], v[122:123], v[126:127] op_sel:[1,1,0] op_sel_hi:[1,0,1] neg_lo:[0,1,0]\nv_cvt_pk_f16_f32 v146, v116, v117\n"
    "v_cvt_pk_f16_f32 v147, v118, v119\nv_pk_mul_f32 v[124:125], v[116:117], v[120:121] op_sel_hi:[0,1]\nv_pk_mul_f32 v[126:127], v[118:119], v[122:123] op_sel_hi:[0,1]\n"
    "v_pk_fma_f32 v[116:117], v[116:117], v[120:121], v[124:125] op_sel:[1,1,0] op_sel_hi:[1,0,1] neg_lo:[0,1,0]\n"
    "v_pk_fma_f32 v[118:119], v[118:119], v[122:123], v[126:127] op_sel:[1,1,0] op_sel_hi:[1,0,1] neg_lo:[0,1,0]\nv_cvt_pk_f16_f32 v148, v116, v117\n"
    "v_cvt_pk_f16_f32 v149, v118, v119\nv_pk_mul_f32 v[124:125], v[116:117], v[120:121] op_sel_hi:[0,1]\nv_pk_mul_f32 v[126:127], v[118:119], v[122:123] op_sel_hi:[0,1]\n"
    "v_pk_fma_f32 v[116:117], v[116:117], v[120:121], v[124:125] op_sel:[1,1,0] op_sel_hi:[1,0,1] neg_lo:[0,1,0]\n"
    "v_pk_fma_f32 v[118:119], v[118:119], v[122:123], v[126:127] op_sel:[1,1,0] op_sel_hi:[1,0,1] neg_lo:[0,1,0]\nv_cvt_pk_f16_f32 v150, v116, v117\n"
    "v_cvt_pk_f16_f32 v151, v118, v119\nv_pk_mul_f32 v[124:125], v[116:117], v[120:121] op_sel_hi:[0,1]\nv_pk_mul_f32 v[126:127], v[118:119], v[122:123] op_sel_hi:[0,1]\n"
    "v_pk_fma_f32 v[116:117], v[116:117], v[120:121], v[124:125] op_sel:[1,1,0] op_sel_hi:[1,0,1] neg_lo:[0,1,0]\n"
    "v_pk_fma_f32 v[118:119], v[118:119], v[122:123], v[126:127] op_sel:[1,1,0] op_sel_hi:[1,0,1] neg_lo:[0,1,0]\nv_cvt_pk_f16_f32 v152, v116, v117\n"
    "v_cvt_pk_f16_f32 v153, v118, v119\nv_pk_mul_f32 v[124:125], v[116:117], v[120:121] op_sel_hi:[0,1]\nv_pk_mul_f32 v[126:127], v[118:119], v[122:123] op_sel_hi:[0,1]\n"
    "v_pk_fma_f32 v[116:117], v[116:117], v[120:121], v[124:125] op_sel:[1,1,0] op_sel_hi:[1,0,1] neg_lo:[0,1,0]\n"
    "v_pk_fma_f32 v[118:119], v[118:119], v[122:123], v[126:127] op_sel:[1,1,0] op_sel_hi:[1,0,1] neg_lo:[0,1,0]\nv_cvt_pk_f16_f32 v154, v116, v117\n"
    "v_cvt_pk_f16_f32 v155, v118, v119\nv_pk_mul_f32 v[124:125], v[116:117], v[120:121] op_sel_hi:[0,1]\nv_pk_mul_f32 v[126:127], v[118:119], v[122:123] op_sel_hi:[0,1]\n"
    "v_pk_fma_f32 v[116:117], v[116:117], v[120:121], v[124:125] op_sel:[1,1,0] op_sel_hi:[1,0,1] neg_lo:[0,1,0]\n"
    "v_pk_fma_f32 v[118:119], v[118:119], v[122:123], v[126:127] op_sel:[1,1,0] op_sel_hi:[1,0,1] neg_lo:[0,1,0]\nv_cvt_pk_f16_f32 v156, v116, v117\n"
    "v_cvt_pk_f16_f32 v157, v118, v119\nv_pk_mul_f32 v[124:125], v[116:117], v[120:121] op_sel_hi:[0,1]\nv_pk_mul_f32 v[126:127], v[118:119], v[122:123] op_sel_hi:[0,1]\n"
    "v_pk_fma_f32 v[116:117], v[116:117], v[120:121], v[124:125] op_sel:[1,1,0] op_sel_hi:[1,0,1] neg_lo:[0,1,0]\n"
    "v_pk_fma_f32 v[118:119], v[118:119], v[122:123], v[126:127] op_sel:[1,1,0] op_sel_hi:[1,0,1] neg_lo:[0,1,0]\nv_cvt_pk_f16_f32 v158, v116, v117\n"
    "v_cvt_pk_f16_f32 v159, v118, v119\nv_pk_mul_f32 v[124:125], v[116:117], v[120:121] op_sel_hi:[0,1]\nv_pk_mul_f32 v[126:127], v[118:119], v[122:123] op_sel_hi:[0,1]\n"
    "v_pk_fma_f32 v[116:117], v[116:117], v[120:121], v[124:125] op_sel:[1,1,0] op_sel_hi:[1,0,1] neg_lo:[0,1,0]\n"
    "v_pk_fma_f32 v[118:119], v[118:119], v[122:123], v[126:127] op_sel:[1,1,0] op_sel_hi:[1,0,1] neg_lo:[0,1,0]\nv_cvt_pk_f16_f32 v160, v116, v117\n"
    "v_cvt_pk_f16_f32 v161, v118, v119\nv_pk_mul_f32 v[124:125], v[116:117], v[120:121] op_sel_hi:[0,1]\nv_pk_mul_f32 v[126:127], v[118:119], v[122:123] op_sel_hi:[0,1]\n"
    "v_pk_fma_f32 v[116:117], v[116:117], v[120:121], v[124:125] op_sel:[1,1,0] op_sel_hi:[1,0,1] neg_lo:[0,1,0]\n"
    "v_pk_fma_f32 v[118:119], v[118:119], v[122:123], v[126:127] op_sel:[1,1,0] op_sel_hi:[1,0,1] neg_lo:[0,1,0]\nv_cvt_pk_f16_f32 v162, v116, v117\n"
    "v_cvt_pk_f16_f32 v163, v118, v119\nv_pk_mul_f32 v[124:125], v[116:117], v[120:121] op_sel_hi:[0,1]\nv_pk_mul_f32 v[126:127], v[118:119], v[122:123] op_sel_hi:[0,1]\n"
    "v_pk_fma_f32 v[116:117], v[116:117], v[120:121], v[124:125] op_sel:[1,1,0] op_sel_hi:[1,0,1] neg_lo:[0,1,0]\n"
    "v_pk_fma_f32 v[118:119], v[118:119], v[122:123], v[126:127] op_sel:[1,1,0] op_sel_hi:[1,0,1] neg_lo:[0,1,0]\nv_cvt_pk_f16_f32 v164, v116, v117\n"
    "v_cvt_pk_f16_f32 v165, v118, v119\nv_pk_mul_f32 v[124:125], v[116:117], v[120:121] op_sel_hi:[0,1]\nv_pk_mul_f32 v[126:127], v[118:119], v[122:123] op_sel_hi:[0,1]\n"
    "v_pk_fma_f32 v[116:117], v[116:117], v[120:121], v[124:125] op_sel:[1,1,0] op_sel_hi:[1,0,1] neg_lo:[0,1,0]\n"
    "v_pk_fma_f32 v[118:119], v[118:119], v[122:123], v[126:127] op_sel:[1,1,0] op_sel_hi:[1,0,1] neg_lo:[0,1,0]\nv_cvt_pk_f16_f32 v166, v116, v117\n"
    "v_cvt_pk_f16_f32 v167, v118, v119\nv_pk_mul_f32 v[124:125], v[116:117], v[120:121] op_sel_hi:[0,1]\nv_pk_mul_f32 v[126:127], v[118:119], v[122:123] op_sel_hi:[0,1]\n"
    "v_pk_fma_f32 v[116:117], v[116:117], v[120:121], v[124:125] op_sel:[1,1,0] op_sel_hi:[1,0,1] neg_lo:[0,1,0]\n"
    "v_pk_fma_f32 v[118:119], v[118:119], v[122:123], v[126:127] op_sel:[1,1,0] op_sel_hi:[1,0,1] neg_lo:[0,1,0]\nv_cvt_pk_f16_f32 v168, v116, v117\n"
    "v_cvt_pk_f16_f32 v169, v118, v119\nv_pk_mul_f32 v[124:125], v[116:117], v[120:121] op_sel_hi:[0,1]\nv_pk_mul_f32 v[126:127], v[118:119], v[122:123] op_sel_hi:[0,1]\n"
    "v_pk_fma_f32 v[116:117], v[116:117], v[120:121], v[124:125] op_sel:[1,1,0] op_sel_hi:[1,0,1] neg_lo:[0,1,0]\n"
    "v_pk_fma_f32 v[118:119], v[118:119], v[122:123], v[126:127] op_sel:[1,1,0] op_sel_hi:[1,0,1] neg_lo:[0,1,0]\nv_cvt_pk_f16_f32 v170, v116, v117\n"
    "v_cvt_pk_f16_f32 v171, v118, v119\nv_pk_mul_f32 v[124:125], v[116:117], v[120:121] op_sel_hi:[0,1]\nv_pk_mul_f32 v[126:127], v[118:119], v[122:123] op_sel_hi:[0,1]\n"
    "v_pk_fma_f32 v[116:117], v[116:117], v[120:121], v[124:125] op_sel:[1,1,0] op_sel_hi:[1,0,1] neg_lo:[0,1,0]\n"
    "v_pk_fma_f32 v[118:119], v[118:119], v[122:123], v[126:127] op_sel:[1,1,0] op_sel_hi:[1,0,1] neg_lo:[0,1,0]\nv_cvt_pk_f16_f32 v172, v116, v117\n"
    "v_cvt_pk_f16_f32 v173, v118, v119\nv_pk_mul_f32 v[124:125], v[116:117], v[120:121] op_sel_hi:[0,1]\nv_pk_mul_f32 v[126:127], v[118:119], v[122:123] op_sel_hi:[0,1]\n"
    "v_pk_fma_f32 v[116:117], v[116:117], v[120:121], v[124:125] op_sel:[1,1,0] op_sel_hi:[1,0,1] neg_lo:[0,1,0]\n"
    "v_pk_fma_f32 v[118:119], v[118:119], v[122:123], v[126:127] op_sel:[1,1,0] op_sel_hi:[1,0,1] neg_lo:[0,1,0]\nv_cvt_pk_f16_f32 v174, v116, v117\n"
    "v_cvt_pk_f16_f32 v175, v118, v119\ns_waitcnt vmcnt(0)\ns_mov_b32 s80, 0x10000\n.Ltp79:\nv_xor_b32 v22, s34, v22\nv_xor_b32 v23, s34, v23\nv_xor_b32 v24, s34, v24\n"
    "v_xor_b32 v25, s34, v25\nv_or3_b32 v22, v22, v23, v24\nv_or_b32 v22, v22, v25\nv_cmp_ne_u32 vcc, 0, v22\ns_cbranch_vccz .Ltq80\ns_sub_u32 s80, s80, 1\n"
    "s_cbranch_scc1 .Ltq80\ns_sleep 2\nglobal_load_dwordx4 v[22:25], v187, s[32:33] sc1\ns_waitcnt vmcnt(0)\ns_branch .Ltp79\n.Ltq80:\ns_sleep 4\n"
    "global_load_dwordx4 v[208:211], v182, s[32:33] offset:-4096 sc1\nglobal_load_dwordx4 v[212:215], v182, s[32:33] offset:0 sc1\n"
    "global_load_dwordx4 v[216:219], v183, s[32:33] offset:-4096 sc1\nglobal_load_dwordx4 v[220:223], v183, s[32:33] offset:0 sc1\n"
    "global_load_dwordx4 v[224:227], v184, s[32:33] offset:-4096 sc1\nglobal_load_dwordx4 v[228:231], v184, s[32:33] offset:0 sc1\n"
    "global_load_dwordx4 v[232:235], v185, s[32:33] offset:-4096 sc1\nglobal_load_dwordx4 v[236:239], v185, s[32:33] offset:0 sc1\n"
    "global_store_dwordx4 v200, v[196:199], s[32:33] offset:0 sc1\nglobal_store_dwordx4 v200, v[196:199], s[32:33] offset:1024 sc1\ns_mov_b32 s14, 0x1d550\n"
    "v_lshl_add_u32 v14, v0, 3, s14\nv_lshrrev_b32 v48, 3, v0\nv_lshl_add_u32 v14, v48, 4, v14\ns_mov_b32 s15, 0x20300\nv_lshl_add_u32 v15, v48, 7, s15\nv_xor_b32 v50, v0, v48\n"
    "v_and_b32 v49, 7, v50\nv_lshl_add_u32 v15, v49, 4, v15\ns_mov_b32 s16, 0x17800\nv_lshl_add_u32 v16, v0, 4, s16\ns_lshl_b32 s18, s41, 7\ns_add_u32 s17, s18, 0x600000\n"
    "v_lshl_add_u32 v51, v48, 15, s17\nv_and_b32 v52, 1, v0\nv_lshl_add_u32 v51, v52, 6, v51\nv_bfe_u32 v53, v0, 1, 2\nv_lshl_add_u32 v51, v53, 4, v51\n"
    "v_lshrrev_b32 v55, 4, v0\ns_lshl_b32 s21, s35, 18\ns_lshl_b32 s22, s36, 8\ns_add_u32 s20, s21, s22\ns_add_u32 s19, s20, 0x1200000\nv_lshl_add_u32 v54, v55, 13, s19\n"
    "v_and_b32 v56, 15, v0\nv_lshl_add_u32 v54, v56, 4, v54\ns_cmp_eq_u32 s6, 0\ns_cselect_b64 vcc, -1, 0\nv_cndmask_b32 v17, v54, v51, vcc\nv_bfe_u32 v59, v0, 5, 1\n"
    "v_and_b32 v61, 31, v0\nv_sub_u32 v60, 0x3ff, v61\nv_lshl_add_u32 v58, v59, 3, v60\nv_lshrrev_b32 v57, 2, v58\ns_lshr_b32 s25, s3, 1\ns_mul_i32 s24, s25, 0x2100\n"
    "s_add_u32 s23, s24, 0x17800\nv_lshl_add_u32 v19, v57, 3, s23\nv_and_b32 v62, 3, v58\ns_mov_b32 s44, 0x840\nv_mad_u32_u24 v19, v62, s44, v19\n"
    "s_lshl2_add_u32 s45, s3, 0x27f80\nv_mov_b32 v12, s45\ns_add_u32 s46, s6, 12\nv_mov_b32 v13, s46\nv_and_b32 v63, 0xff, v0\nv_mov_b32 v65, 0x1d000\nv_mov_b32 v67, 0x1fdb0\n"
    "v_mov_b32 v69, 0x20b10\nv_mov_b32 v70, 0x21870\nv_cmp_gt_u32 vcc, 0xa1, v63\ns_nop 1\nv_cndmask_b32 v68, v70, v69, vcc\nv_cmp_gt_u32 vcc, 0x7b, v63\ns_nop 1\n"
    "v_cndmask_b32 v66, v68, v67, vcc\nv_cmp_gt_u32 vcc, 0x55, v63\ns_nop 1\nv_cndmask_b32 v64, v66, v65, vcc\nv_lshl_add_u32 v18, v63, 4, v64\nv_mov_b32 v72, 0x1950\n"
    "v_mov_b32 v73, 0x3180\nv_cmp_gt_u32 vcc, 0x55, v63\ns_nop 1\nv_cndmask_b32 v71, v73, v72, vcc\nv_add_u32 v1, v18, v71\ns_mul_i32 s49, s25, 0x1950\ns_and_b32 s51, s3, 1\n"
    "s_mul_i32 s50, s51, 0xa00\ns_add_u32 s48, s49, s50\ns_add_u32 s47, s48, 0x1d000\nv_lshl_add_u32 v2, v59, 4, s47\ns_mov_b32 s52, 0x50\nv_mad_u32_u24 v2, v61, s52, v2\n"
    "s_mul_i32 s55, s25, 0x3180\ns_lshl_b32 s56, s51, 12\ns_add_u32 s54, s55, s56\ns_add_u32 s53, s54, 0x20300\nv_lshl_add_u32 v3, v61, 7, s53\nv_and_b32 v75, 7, v0\n"
    "v_xor_b32 v74, v59, v75\nv_lshl_add_u32 v3, v74, 4, v3\ns_lshl3_add_u32 s57, s25, 0x27fc0\nv_lshl_add_u32 v7, v52, 2, s57\ns_add_u32 s14, s7, 0x401000\n"
    "v_lshl_add_u32 v20, v0, 4, s14\ns_lshl_b32 s17, s41, 13\ns_add_u32 s16, s7, s17\ns_add_u32 s15, s16, 0x200000\nv_lshl_add_u32 v21, v0, 4, s15\nv_add_u32 v22, 8192, v20\n"
    "v_add_u32 v23, 16384, v20\nv_add_u32 v24, 24576, v20\nv_add_u32 v25, 32768, v20\nv_add_u32 v26, 40960, v20\nv_add_u32 v27, 49152, v20\nv_add_u32 v28, 57344, v20\n"
    "v_and_b32 v56, 63, v0\ns_lshl_b32 s16, s41, 13\ns_lshl_b32 s17, s3, 11\ns_add_u32 s15, s16, s17\ns_add_u32 s14, s15, 0x800000\nv_lshl_add_u32 v4, v56, 4, s14\n"
    "s_add_u32 s18, s15, 0xa00000\nv_lshl_add_u32 v5, v56, 4, s18\nv_and_b32 v57, 3, v0\ns_lshl_b32 s21, s2, 8\ns_lshl_b32 s22, s3, 6\ns_add_u32 s20, s21, s22\n"
    "s_add_u32 s19, s20, 0xc00000\nv_lshl_add_u32 v6, v57, 4, s19\nv_and_b32 v58, 0x7f, v0\ns_lshr_b32 s25, s3, 1\ns_mul_i32 s24, s25, 0x2100\ns_add_u32 s23, s24, 0x18000\n"
    "v_lshl_add_u32 v47, v58, 4, s23\nv_and_b32 v59, 15, v0\ns_mov_b32 s44, 0x27600\nv_lshl_add_u32 v29, v59, 4, s44\nv_bfe_u32 v60, v0, 3, 1\ns_lshl_b32 s48, s40, 14\n"
    "s_add_u32 s47, s7, s48\ns_lshl_b32 s49, s39, 7\ns_add_u32 s46, s47, s49\ns_add_u32 s45, s46, 0x400000\nv_lshl_add_u32 v30, v60, 13, s45\nv_and_b32 v61, 7, v0\n"
    "v_lshl_add_u32 v30, v61, 4, v30\nv_mov_b32 v8, -1\nv_mov_b32 v9, -1\nv_mov_b32 v10, -1\nv_mov_b32 v11, -1\n.LL78:\ns_mov_b32 s80, 0x10000\n.LL81:\ns_waitcnt vmcnt(9)\n"
    "ds_write_b128 v186, v[208:211] offset:0\nv_max3_u32 v31, v208, v209, 0\nv_max3_u32 v31, v210, v211, v31\ns_waitcnt vmcnt(8)\nds_write_b128 v186, v[212:215] offset:4096\n"
    "v_max3_u32 v31, v212, v213, v31\nv_max3_u32 v31, v214, v215, v31\nv_cmp_eq_u32 vcc, -1, v31\ns_cbranch_vccnz .LL83\n.LL82:\ns_waitcnt lgkmcnt(0)\ns_barrier\n.LL92:\n"
    "s_waitcnt vmcnt(7)\nds_write_b128 v186, v[216:219] offset:8192\nv_max3_u32 v31, v216, v217, 0\nv_max3_u32 v31, v218, v219, v31\ns_waitcnt vmcnt(6)\n"
    "ds_write_b128 v186, v[220:223] offset:12288\nv_max3_u32 v31, v220, v221, v31\nv_max3_u32 v31, v222, v223, v31\nv_cmp_eq_u32 vcc, -1, v31\ns_cbranch_vccnz .LL94\n.LL93:\n"
    "s_waitcnt lgkmcnt(0)\ns_barrier\n.LL101:\ns_waitcnt vmcnt(5)\nds_write_b128 v186, v[224:227] offset:16384\nv_max3_u32 v31, v224, v225, 0\nv_max3_u32 v31, v226, v227, v31\n"
    "s_waitcnt vmcnt(4)\nds_write_b128 v186, v[228:231] offset:20480\nv_max3_u32 v31, v228, v229, v31\nv_max3_u32 v31, v230, v231, v31\nv_cmp_eq_u32 vcc, -1, v31\n"
    "s_cbranch_vccnz .LL103\n.LL102:\ns_waitcnt lgkmcnt(0)\ns_barrier\n.LL108:\ns_waitcnt vmcnt(3)\nds_write_b128 v186, v[232:235] offset:24576\nv_max3_u32 v31, v232, v233, 0\n"
    "v_max3_u32 v31, v234, v235, v31\ns_waitcnt vmcnt(2)\nds_write_b128 v186, v[236:239] offset:28672\nv_max3_u32 v31, v236, v237, v31\nv_max3_u32 v31, v238, v239, v31\n"
    "v_cmp_eq_u32 vcc, -1, v31\ns_cbranch_vccnz .LL110\n.LL109:\ns_waitcnt lgkmcnt(0)\ns_barrier\ns_cmp_eq_u32 s6, 0\ns_cbranch_scc1 .LL113\nds_write_b128 v47, v[40:43]\n"
    "v_alignbit_b32 v48, v41, v40, 16\nv_alignbit_b32 v49, v42, v41, 16\nv_alignbit_b32 v50, v43, v42, 16\nv_alignbit_b32 v51, v44, v43, 16\n"
    "ds_write_b128 v47, v[48:51] offset:2112\nv_mov_b32 v52, v41\nv_mov_b32 v53, v42\nv_mov_b32 v54, v43\nv_mov_b32 v55, v44\nds_write_b128 v47, v[52:55] offset:4224\n"
    "v_alignbit_b32 v56, v42, v41, 16\nv_alignbit_b32 v57, v43, v42, 16\nv_alignbit_b32 v58, v44, v43, 16\nv_alignbit_b32 v59, v45, v44, 16\n"
    "ds_write_b128 v47, v[56:59] offset:6336\ns_waitcnt vmcnt(0)\ns_branch .LL114\n.LL113:\ns_mov_b64 exec, 0xffff\nglobal_store_dwordx4 v201, v[196:199], s[32:33] sc1\n"
    "s_mov_b64 exec, -1\nglobal_load_dwordx4 v[112:115], v202, s[32:33] sc1\nv_and_or_b32 v124, v240, s84, v191\nv_alignbit_b32 v125, 1, v240, 16\n"
    "v_and_or_b32 v126, v241, s84, v191\nv_alignbit_b32 v127, 1, v241, 16\nv_and_or_b32 v128, v244, s84, v191\nv_alignbit_b32 v129, 1, v244, 16\n"
    "v_and_or_b32 v130, v245, s84, v191\nv_alignbit_b32 v131, 1, v245, 16\nv_and_or_b32 v132, v246, s84, v191\nv_alignbit_b32 v133, 1, v246, 16\n"
    "v_and_or_b32 v134, v247, s84, v191\nv_alignbit_b32 v135, 1, v247, 16\nv_and_or_b32 v136, v248, s84, v191\nv_alignbit_b32 v137, 1, v248, 16\n"
    "v_and_or_b32 v138, v249, s84, v191\nv_alignbit_b32 v139, 1, v249, 16\nv_and_or_b32 v140, v250, s84, v191\nv_alignbit_b32 v141, 1, v250, 16\n"
    "v_and_or_b32 v142, v251, s84, v191\nv_alignbit_b32 v143, 1, v251, 16\ns_waitcnt vmcnt(0)\ns_mov_b32 s80, 0x10000\n.Ltp134:\nv_xor_b32 v112, s34, v112\n"
    "v_xor_b32 v113, s34, v113\nv_xor_b32 v114, s34, v114\nv_xor_b32 v115, s34, v115\nv_or3_b32 v112, v112, v113, v114\nv_or_b32 v112, v112, v115\nv_cmp_ne_u32 vcc, 0, v112\n"
    "s_cbranch_vccz .Ltq135\ns_sub_u32 s80, s80, 1\ns_cbranch_scc1 .Ltq135\ns_sleep 2\nglobal_load_dwordx4 v[112:115], v202, s[32:33] sc1\ns_waitcnt vmcnt(0)\ns_branch .Ltp134\n"
    ".Ltq135:\n.LL114:\ns_waitcnt lgkmcnt(0)\ns_barrier\ns_cmp_eq_u32 s3, 3\ns_cbranch_scc0 .LL132\nds_read_b128 v[208:211], v29 offset:0\n"
    "ds_read_b128 v[212:215], v29 offset:256\nds_read_b128 v[216:219], v29 offset:512\nds_read_b128 v[220:223], v29 offset:768\ns_waitcnt lgkmcnt(2)\n"
    "v_pk_add_f32 v[208:209], v[208:209], v[212:213]\nv_pk_add_f32 v[210:211], v[210:211], v[214:215]\ns_waitcnt lgkmcnt(0)\nv_pk_add_f32 v[216:217], v[216:217], v[220:221]\n"
    "v_pk_add_f32 v[218:219], v[218:219], v[222:223]\nv_pk_add_f32 v[208:209], v[208:209], v[216:217]\nv_pk_add_f32 v[210:211], v[210:211], v[218:219]\n"
    "v_min_u32 v208, -2, v208\nv_min_u32 v209, -2, v209\nv_min_u32 v210, -2, v210\nv_min_u32 v211, -2, v211\ns_mov_b64 exec, 0xffff\n"
    "global_store_dwordx4 v30, v[208:211], s[32:33] sc1\ns_mov_b64 exec, -1\n.LL132:\ns_cmp_eq_u32 s6, 0\ns_cbranch_scc0 .LL133\nds_write_b64 v188, v[144:145] offset:0\n"
    "ds_write_b64 v188, v[146:147] offset:544\nds_write_b64 v188, v[148:149] offset:1088\nds_write_b64 v188, v[150:151] offset:1632\nds_write_b64 v188, v[152:153] offset:2176\n"
    "ds_write_b64 v188, v[154:155] offset:2720\nds_write_b64 v188, v[156:157] offset:3264\nds_write_b64 v188, v[158:159] offset:3808\nds_write_b64 v188, v[160:161] offset:4352\n"
    "ds_write_b64 v188, v[162:163] offset:4896\nds_write_b64 v188, v[164:165] offset:5440\nds_write_b64 v188, v[166:167] offset:5984\nds_write_b64 v188, v[168:169] offset:6528\n"
    "ds_write_b64 v188, v[170:171] offset:7072\nds_write_b64 v188, v[172:173] offset:7616\nds_write_b64 v188, v[174:175] offset:8160\ns_waitcnt lgkmcnt(0)\ns_barrier\n"
    "ds_read_b128 v[48:51], v189 offset:0\nds_read_b128 v[52:55], v190 offset:0\nds_read_b128 v[56:59], v190 offset:8704\nds_read_b128 v[60:63], v189 offset:64\n"
    "ds_read_b128 v[64:67], v190 offset:64\nds_read_b128 v[68:71], v190 offset:8768\nds_read_b128 v[72:75], v189 offset:128\nds_read_b128 v[76:79], v190 offset:128\n"
    "ds_read_b128 v[80:83], v190 offset:8832\nds_read_b128 v[84:87], v189 offset:192\nds_read_b128 v[88:91], v190 offset:192\nds_read_b128 v[92:95], v190 offset:8896\n"
    "ds_read_b128 v[96:99], v189 offset:256\nds_read_b128 v[100:103], v190 offset:256\nds_read_b128 v[104:107], v190 offset:8960\ns_waitcnt lgkmcnt(12)\n"
    "v_mfma_f32_16x16x32_f16 v[108:111], v[48:51], v[52:55], 0\nv_mfma_f32_16x16x32_f16 v[112:115], v[48:51], v[56:59], 0\nds_read_b128 v[48:51], v189 offset:320\n"
    "ds_read_b128 v[52:55], v190 offset:320\nds_read_b128 v[56:59], v190 offset:9024\ns_waitcnt lgkmcnt(12)\nv_mfma_f32_16x16x32_f16 v[108:111], v[60:63], v[64:67], v[108:111]\n"
    "v_mfma_f32_16x16x32_f16 v[112:115], v[60:63], v[68:71], v[112:115]\nds_read_b128 v[60:63], v189 offset:384\nds_read_b128 v[64:67], v190 offset:384\n"
    "ds_read_b128 v[68:71], v190 offset:9088\ns_waitcnt lgkmcnt(12)\nv_mfma_f32_16x16x32_f16 v[108:111], v[72:75], v[76:79], v[108:111]\n"
    "v_mfma_f32_16x16x32_f16 v[112:115], v[72:75], v[80:83], v[112:115]\nds_read_b128 v[72:75], v189 offset:448\nds_read_b128 v[76:79], v190 offset:448\n"
    "ds_read_b128 v[80:83], v190 offset:9152\ns_waitcnt lgkmcnt(12)\nv_mfma_f32_16x16x32_f16 v[108:111], v[84:87], v[88:91], v[108:111]\n"
    "v_mfma_f32_16x16x32_f16 v[112:115], v[84:87], v[92:95], v[112:115]\ns_waitcnt lgkmcnt(9)\nv_mfma_f32_16x16x32_f16 v[108:111], v[96:99], v[100:103], v[108:111]\n"
    "v_mfma_f32_16x16x32_f16 v[112:115], v[96:99], v[104:107], v[112:115]\ns_waitcnt lgkmcnt(6)\nv_mfma_f32_16x16x32_f16 v[108:111], v[48:51], v[52:55], v[108:111]\n"
    "v_mfma_f32_16x16x32_f16 v[112:115], v[48:51], v[56:59], v[112:115]\ns_waitcnt lgkmcnt(3)\nv_mfma_f32_16x16x32_f16 v[108:111], v[60:63], v[64:67], v[108:111]\n"
    "v_mfma_f32_16x16x32_f16 v[112:115], v[60:63], v[68:71], v[112:115]\ns_waitcnt lgkmcnt(0)\nv_mfma_f32_16x16x32_f16 v[108:111], v[72:75], v[76:79], v[108:111]\n"
    "v_mfma_f32_16x16x32_f16 v[112:115], v[72:75], v[80:83], v[112:115]\ns_nop 6\nv_cvt_pk_f16_f32 v48, v108, v109\nds_write_b16 v124, v48 offset:59392\n"
    "ds_write_b16_d16_hi v125, v48 offset:59392\nv_cvt_pk_f16_f32 v49, v110, v111\nds_write_b16 v126, v49 offset:59392\nds_write_b16_d16_hi v127, v49 offset:59392\n"
    "v_cvt_pk_f16_f32 v50, v112, v113\nds_write_b16 v128, v50 offset:59392\nds_write_b16_d16_hi v129, v50 offset:59392\nv_cvt_pk_f16_f32 v51, v114, v115\n"
    "ds_write_b16 v130, v51 offset:59392\nds_write_b16_d16_hi v131, v51 offset:59392\n.LL133:\ns_waitcnt lgkmcnt(0)\ns_barrier\ns_or_b32 s81, s2, s3\ns_or_b32 s81, s81, s6\n"
    "s_cbranch_scc1 .LL130\nv_mov_b32 v60, 16777216\nv_mov_b32 v61, s34\ns_mov_b64 exec, 1\nglobal_store_dword v60, v61, s[32:33] sc1\ns_mov_b64 exec, -1\n.LL130:\n"
    "global_load_dwordx4 v[240:243], v21, s[32:33] sc1\nglobal_load_dwordx4 v[208:211], v20, s[32:33] offset:-4096 sc1\n"
    "global_load_dwordx4 v[212:215], v22, s[32:33] offset:-4096 sc1\nglobal_load_dwordx4 v[216:219], v23, s[32:33] offset:-4096 sc1\n"
    "global_load_dwordx4 v[220:223], v24, s[32:33] offset:-4096 sc1\nglobal_load_dwordx4 v[224:227], v25, s[32:33] offset:-4096 sc1\n"
    "global_load_dwordx4 v[228:231], v26, s[32:33] offset:-4096 sc1\nglobal_load_dwordx4 v[232:235], v27, s[32:33] offset:-4096 sc1\n"
    "global_load_dwordx4 v[236:239], v28, s[32:33] offset:-4096 sc1\ns_cmp_eq_u32 s6, 0\ns_cbranch_scc0 .LL115\nv_mov_b32 v60, 163216\nds_read_b64 v[60:61], v60\n"
    "ds_read_u16 v64, v132 offset:59392\nds_read_u16 v65, v133 offset:59392\nds_read_u16 v66, v134 offset:59392\nds_read_u16 v67, v135 offset:59392\n"
    "ds_read_u16 v68, v136 offset:59392\nds_read_u16 v69, v137 offset:59392\nds_read_u16 v70, v138 offset:59392\nds_read_u16 v71, v139 offset:59392\n"
    "ds_read_u16 v72, v140 offset:59392\nds_read_u16 v73, v141 offset:59392\nds_read_u16 v74, v142 offset:59392\nds_read_u16 v75, v143 offset:59392\ns_waitcnt lgkmcnt(0)\n"
    "v_lshl_or_b32 v40, v65, 16, v64\nv_lshl_or_b32 v41, v67, 16, v66\nv_lshl_or_b32 v42, v69, 16, v68\nv_lshl_or_b32 v43, v71, 16, v70\nv_lshl_or_b32 v44, v73, 16, v72\n"
    "v_lshl_or_b32 v45, v75, 16, v74\nv_readfirstlane_b32 s88, v60\nv_readfirstlane_b32 s90, v61\nds_write_b128 v47, v[40:43]\nv_alignbit_b32 v48, v41, v40, 16\n"
    "v_alignbit_b32 v49, v42, v41, 16\nv_alignbit_b32 v50, v43, v42, 16\nv_alignbit_b32 v51, v44, v43, 16\nds_write_b128 v47, v[48:51] offset:2112\nv_mov_b32 v52, v41\n"
    "v_mov_b32 v53, v42\nv_mov_b32 v54, v43\nv_mov_b32 v55, v44\nds_write_b128 v47, v[52:55] offset:4224\nv_alignbit_b32 v56, v42, v41, 16\nv_alignbit_b32 v57, v43, v42, 16\n"
    "v_alignbit_b32 v58, v44, v43, 16\nv_alignbit_b32 v59, v45, v44, 16\nds_write_b128 v47, v[56:59] offset:6336\ns_waitcnt lgkmcnt(0)\ns_barrier\n.LL115:\n"
    "v_mov_b64 v[60:61], 0\nv_mov_b64 v[62:63], 0\ns_cmp_eq_u32 s3, 3\ns_cbranch_scc1 .LL117\ns_cmp_eq_u32 s3, 2\ns_cselect_b32 exec_hi, 3, -1\nds_write_b128 v18, v[60:63]\n"
    "ds_write_b128 v1, v[60:63]\ns_mov_b64 exec, -1\n.LL117:\nds_read_b64 v[64:65], v19 offset:2016\nds_read_b64 v[66:67], v19 offset:2024\n"
    "ds_read_b64 v[68:69], v19 offset:1984\nds_read_b64 v[70:71], v19 offset:1992\nds_read_b64 v[72:73], v19 offset:1952\nds_read_b64 v[74:75], v19 offset:1960\n"
    "ds_read_b64 v[76:77], v19 offset:1920\nds_read_b64 v[78:79], v19 offset:1928\nds_read_b64 v[80:81], v19 offset:1888\nds_read_b64 v[82:83], v19 offset:1896\n"
    "ds_read_b64 v[84:85], v19 offset:1856\nds_read_b64 v[86:87], v19 offset:1864\nds_read_b64 v[88:89], v19 offset:1824\nds_read_b64 v[90:91], v19 offset:1832\n"
    "ds_read_b64 v[92:93], v19 offset:1792\nds_read_b64 v[94:95], v19 offset:1800\nds_read_b64 v[96:97], v19 offset:1760\nds_read_b64 v[98:99], v19 offset:1768\n"
    "ds_read_b64 v[100:101], v19 offset:1728\nds_read_b64 v[102:103], v19 offset:1736\nds_read_b64 v[104:105], v19 offset:1696\nds_read_b64 v[106:107], v19 offset:1704\n"
    "ds_read_b64 v[108:109], v19 offset:1664\nds_read_b64 v[110:111], v19 offset:1672\nds_read_b64 v[112:113], v19 offset:1632\nds_read_b64 v[114:115], v19 offset:1640\n"
    "ds_read_b64 v[116:117], v19 offset:1600\nds_read_b64 v[118:119], v19 offset:1608\nds_read_b64 v[120:121], v19 offset:1568\nds_read_b64 v[122:123], v19 offset:1576\n"
    "ds_read_b64 v[124:125], v19 offset:1536\nds_read_b64 v[126:127], v19 offset:1544\nds_read_b64 v[128:129], v19 offset:1504\nds_read_b64 v[130:131], v19 offset:1512\n"
    "ds_read_b64 v[132:133], v19 offset:1472\nds_read_b64 v[134:135], v19 offset:1480\nds_read_b64 v[136:137], v19 offset:1440\nds_read_b64 v[138:139], v19 offset:1448\n"
    "ds_read_b64 v[140:141], v19 offset:1408\nds_read_b64 v[142:143], v19 offset:1416\nds_read_b64 v[144:145], v19 offset:1376\nds_read_b64 v[146:147], v19 offset:1384\n"
    "ds_read_b64 v[148:149], v19 offset:1344\nds_read_b64 v[150:151], v19 offset:1352\nds_read_b64 v[152:153], v19 offset:1312\nds_read_b64 v[154:155], v19 offset:1320\n"
    "ds_read_b64 v[156:157], v19 offset:1280\nds_read_b64 v[158:159], v19 offset:1288\nds_read_b64 v[160:161], v19 offset:1248\nds_read_b64 v[162:163], v19 offset:1256\n"
    "ds_read_b64 v[164:165], v19 offset:1216\nds_read_b64 v[166:167], v19 offset:1224\nds_read_b64 v[168:169], v19 offset:1184\nds_read_b64 v[170:171], v19 offset:1192\n"
    "ds_read_b64 v[172:173], v19 offset:1152\nds_read_b64 v[174:175], v19 offset:1160\nds_read_b64 v[176:177], v19 offset:1120\nds_read_b64 v[178:179], v19 offset:1128\n"
    "ds_read_b64 v[180:181], v19 offset:1088\nds_read_b64 v[182:183], v19 offset:1096\nds_read_b64 v[184:185], v19 offset:1056\nds_read_b64 v[186:187], v19 offset:1064\n"
    "ds_read_b64 v[188:189], v19 offset:1024\nds_read_b64 v[190:191], v19 offset:1032\nds_read_b64 v[192:193], v19 offset:992\nds_read_b64 v[194:195], v19 offset:1000\n"
    "ds_read_b64 v[196:197], v19 offset:960\nds_read_b64 v[198:199], v19 offset:968\nds_read_b64 v[200:201], v19 offset:928\nds_read_b64 v[202:203], v19 offset:936\n"
    "s_waitcnt lgkmcnt(0)\ns_mov_b32 s84, 0x3b000000\ns_mov_b32 s86, 0x3727c5ac\ns_mov_b32 s80, 0x10000\ns_waitcnt vmcnt(8)\n.Lln118:\nv_max3_u32 v60, v240, v241, v242\n"
    "v_max_u32 v60, v60, v243\nv_cmp_eq_u32 s[82:83], -1, v60\nv_lshlrev_b32 v32, 16, v240\nv_and_b32 v33, 0xffff0000, v240\nv_lshlrev_b32 v34, 16, v241\n"
    "v_and_b32 v35, 0xffff0000, v241\nv_lshlrev_b32 v36, 16, v242\nv_and_b32 v37, 0xffff0000, v242\nv_lshlrev_b32 v38, 16, v243\nv_and_b32 v39, 0xffff0000, v243\n"
    "s_waitcnt vmcnt(4)\nv_pk_add_f32 v[48:49], v[208:209], v[216:217]\nv_pk_add_f32 v[50:51], v[210:211], v[218:219]\nv_pk_add_f32 v[52:53], v[212:213], v[220:221]\n"
    "v_pk_add_f32 v[54:55], v[214:215], v[222:223]\ns_waitcnt vmcnt(1)\nv_pk_add_f32 v[56:57], v[224:225], v[232:233]\nv_pk_add_f32 v[58:59], v[226:227], v[234:235]\n"
    "v_pk_add_f32 v[48:49], v[48:49], v[56:57]\nv_pk_add_f32 v[50:51], v[50:51], v[58:59]\nv_pk_mul_f32 v[48:49], v[48:49], s[84:85] op_sel_hi:[1,0]\n"
    "v_pk_mul_f32 v[50:51], v[50:51], s[84:85] op_sel_hi:[1,0]\ns_waitcnt vmcnt(0)\nv_pk_add_f32 v[56:57], v[228:229], v[236:237]\n"
    "v_pk_add_f32 v[58:59], v[230:231], v[238:239]\nv_pk_add_f32 v[52:53], v[52:53], v[56:57]\nv_pk_add_f32 v[54:55], v[54:55], v[58:59]\n"
    "v_pk_mul_f32 v[56:57], v[48:49], v[48:49]\nv_pk_mul_f32 v[58:59], v[50:51], v[50:51]\n"
    "v_pk_fma_f32 v[52:53], v[52:53], s[84:85], v[56:57] op_sel_hi:[1,0,1] neg_lo:[0,0,1] neg_hi:[0,0,1]\n"
    "v_pk_fma_f32 v[54:55], v[54:55], s[84:85], v[58:59] op_sel_hi:[1,0,1] neg_lo:[0,0,1] neg_hi:[0,0,1]\nv_cvt_pk_bf16_f32 v48, 0, v48\nv_cvt_pk_bf16_f32 v49, 0, v49\n"
    "v_pk_add_f32 v[56:57], v[52:53], v[54:55]\nv_cvt_pk_bf16_f32 v50, 0, v50\nv_cvt_pk_bf16_f32 v51, 0, v51\nv_cmp_u_f32 vcc, v56, v57\nv_max_f32 v52, 0, v52\n"
    "v_max_f32 v53, 0, v53\nv_max_f32 v54, 0, v54\nv_max_f32 v55, 0, v55\ns_or_b64 vcc, vcc, s[82:83]\ns_cbranch_vccnz .Lls120\n.Llo119:\nv_cvt_pk_bf16_f32 v52, 0, v52\n"
    "v_cvt_pk_bf16_f32 v53, 0, v53\nv_cvt_pk_bf16_f32 v54, 0, v54\nv_cvt_pk_bf16_f32 v55, 0, v55\nv_pk_add_f32 v[52:53], v[52:53], s[86:87] op_sel_hi:[1,0]\n"
    "v_pk_add_f32 v[54:55], v[54:55], s[86:87] op_sel_hi:[1,0]\nv_pk_add_f32 v[244:245], v[32:33], v[48:49] op_sel:[0,0] op_sel_hi:[1,0] neg_lo:[0,1] neg_hi:[0,1]\n"
    "v_pk_add_f32 v[246:247], v[34:35], v[48:49] op_sel:[0,1] op_sel_hi:[1,1] neg_lo:[0,1] neg_hi:[0,1]\nv_cvt_pk_bf16_f32 v52, 0, v52\nv_cvt_pk_bf16_f32 v53, 0, v53\n"
    "v_cvt_pk_bf16_f32 v54, 0, v54\nv_cvt_pk_bf16_f32 v55, 0, v55\nv_sqrt_f32 v52, v52\n"
    "v_pk_add_f32 v[248:249], v[36:37], v[50:51] op_sel:[0,0] op_sel_hi:[1,0] neg_lo:[0,1] neg_hi:[0,1]\nv_sqrt_f32 v53, v53\n"
    "v_pk_add_f32 v[250:251], v[38:39], v[50:51] op_sel:[0,1] op_sel_hi:[1,1] neg_lo:[0,1] neg_hi:[0,1]\nv_sqrt_f32 v54, v54\nv_cvt_pk_bf16_f32 v244, 0, v244\n"
    "v_sqrt_f32 v55, v55\nv_cvt_pk_bf16_f32 v245, 0, v245\nv_cvt_pk_bf16_f32 v52, 0, v52\nv_cvt_pk_bf16_f32 v53, 0, v53\nv_cvt_pk_bf16_f32 v54, 0, v54\n"
    "v_cvt_pk_bf16_f32 v55, 0, v55\nv_rcp_f32 v52, v52\nv_cvt_pk_bf16_f32 v246, 0, v246\nv_rcp_f32 v53, v53\nv_cvt_pk_bf16_f32 v247, 0, v247\nv_rcp_f32 v54, v54\n"
    "v_cvt_pk_bf16_f32 v248, 0, v248\nv_rcp_f32 v55, v55\nv_cvt_pk_bf16_f32 v249, 0, v249\nv_cvt_pk_bf16_f32 v250, 0, v250\nv_cvt_pk_bf16_f32 v251, 0, v251\n"
    "v_pk_mul_f32 v[244:245], v[244:245], v[52:53] op_sel:[0,0] op_sel_hi:[1,0]\nv_pk_mul_f32 v[246:247], v[246:247], v[52:53] op_sel:[0,1] op_sel_hi:[1,1]\n"
    "v_pk_mul_f32 v[248:249], v[248:249], v[54:55] op_sel:[0,0] op_sel_hi:[1,0]\nv_pk_mul_f32 v[250:251], v[250:251], v[54:55] op_sel:[0,1] op_sel_hi:[1,1]\n"
    "v_cvt_pk_bf16_f32 v244, 0, v244\nv_cvt_pk_bf16_f32 v246, 0, v246\nv_cvt_pk_bf16_f32 v248, 0, v248\nv_cvt_pk_bf16_f32 v250, 0, v250\nv_cvt_pk_f16_f32 v56, v244, v246\n"
    "v_cvt_pk_f16_f32 v57, v248, v250\nv_cvt_pk_bf16_f32 v245, 0, v245\nv_cvt_pk_bf16_f32 v247, 0, v247\nds_write_b64 v14, v[56:57]\nv_cvt_pk_bf16_f32 v249, 0, v249\n"
    "v_cvt_pk_bf16_f32 v251, 0, v251\nv_cvt_pk_f16_f32 v58, v245, v247\nv_cvt_pk_f16_f32 v59, v249, v251\nds_write_b64 v14, v[58:59] offset:6480\ns_waitcnt lgkmcnt(0)\n"
    "s_barrier\nds_read_b128 v[220:223], v2 offset:1392\nds_read_b128 v[224:227], v2 offset:1360\nds_read_b128 v[228:231], v2 offset:1312\n"
    "ds_read_b128 v[232:235], v2 offset:1280\ns_cmp_eq_u32 s6, 0\ns_cbranch_scc0 .LL131\nglobal_store_dwordx4 v4, v[8:11], s[32:33] offset:0 sc1\n"
    "global_store_dwordx4 v4, v[8:11], s[32:33] offset:1024 sc1\nglobal_store_dwordx4 v5, v[8:11], s[32:33] offset:0 sc1\n"
    "global_store_dwordx4 v5, v[8:11], s[32:33] offset:1024 sc1\ns_mov_b64 exec, 15\nglobal_store_dwordx4 v6, v[8:11], s[32:33] sc1\ns_mov_b64 exec, -1\n.LL131:\n"
    "s_waitcnt lgkmcnt(3)\nv_mfma_f32_32x32x16_f16 v[204:219], v[64:67], v[220:223], 0\nds_read_b128 v[220:223], v2 offset:1232\ns_waitcnt lgkmcnt(3)\n"
    "v_mfma_f32_32x32x16_f16 v[204:219], v[68:71], v[224:227], v[204:219]\nds_read_b128 v[224:227], v2 offset:1200\ns_waitcnt lgkmcnt(3)\n"
    "v_mfma_f32_32x32x16_f16 v[204:219], v[72:75], v[228:231], v[204:219]\nds_read_b128 v[68:71], v2 offset:1152\ns_waitcnt lgkmcnt(3)\n"
    "v_mfma_f32_32x32x16_f16 v[204:219], v[76:79], v[232:235], v[204:219]\nds_read_b128 v[228:231], v2 offset:1120\ns_waitcnt lgkmcnt(3)\n"
    "v_mfma_f32_32x32x16_f16 v[204:219], v[80:83], v[220:223], v[204:219]\nds_read_b128 v[72:75], v2 offset:1072\ns_waitcnt lgkmcnt(3)\n"
    "v_mfma_f32_32x32x16_f16 v[204:219], v[84:87], v[224:227], v[204:219]\nds_read_b128 v[232:235], v2 offset:1040\ns_waitcnt lgkmcnt(3)\n"
    "v_mfma_f32_32x32x16_f16 v[204:219], v[88:91], v[68:71], v[204:219]\nds_read_b128 v[76:79], v2 offset:992\ns_waitcnt lgkmcnt(3)\n"
    "v_mfma_f32_32x32x16_f16 v[204:219], v[92:95], v[228:231], v[204:219]\nds_read_b128 v[220:223], v2 offset:960\ns_waitcnt lgkmcnt(3)\n"
    "v_mfma_f32_32x32x16_f16 v[204:219], v[96:99], v[72:75], v[204:219]\nds_read_b128 v[80:83], v2 offset:912\ns_waitcnt lgkmcnt(3)\n"
    "v_mfma_f32_32x32x16_f16 v[204:219], v[100:103], v[232:235], v[204:219]\nds_read_b128 v[224:227], v2 offset:880\ns_waitcnt lgkmcnt(3)\n"
    "v_mfma_f32_32x32x16_f16 v[204:219], v[104:107], v[76:79], v[204:219]\nds_read_b128 v[84:87], v2 offset:832\ns_waitcnt lgkmcnt(3)\n"
    "v_mfma_f32_32x32x16_f16 v[204:219], v[108:111], v[220:223], v[204:219]\nds_read_b128 v[68:71], v2 offset:800\ns_waitcnt lgkmcnt(3)\n"
    "v_mfma_f32_32x32x16_f16 v[204:219], v[112:115], v[80:83], v[204:219]\nds_read_b128 v[88:91], v2 offset:752\ns_waitcnt lgkmcnt(3)\n"
    "v_mfma_f32_32x32x16_f16 v[204:219], v[116:119], v[224:227], v[204:219]\nds_read_b128 v[228:231], v2 offset:720\ns_waitcnt lgkmcnt(3)\n"
    "v_mfma_f32_32x32x16_f16 v[204:219], v[120:123], v[84:87], v[204:219]\nds_read_b128 v[92:95], v2 offset:672\ns_waitcnt lgkmcnt(3)\n"
    "v_mfma_f32_32x32x16_f16 v[204:219], v[124:127], v[68:71], v[204:219]\nds_read_b128 v[72:75], v2 offset:640\ns_waitcnt lgkmcnt(3)\n"
    "v_mfma_f32_32x32x16_f16 v[204:219], v[128:131], v[88:91], v[204:219]\nds_read_b128 v[96:99], v2 offset:592\ns_waitcnt lgkmcnt(3)\n"
    "v_mfma_f32_32x32x16_f16 v[204:219], v[132:135], v[228:231], v[204:219]\nds_read_b128 v[232:235], v2 offset:560\ns_waitcnt lgkmcnt(3)\n"
    "v_mfma_f32_32x32x16_f16 v[204:219], v[136:139], v[92:95], v[204:219]\nds_read_b128 v[100:103], v2 offset:512\ns_waitcnt lgkmcnt(3)\n"
    "v_mfma_f32_32x32x16_f16 v[204:219], v[140:143], v[72:75], v[204:219]\nds_read_b128 v[76:79], v2 offset:480\ns_waitcnt lgkmcnt(3)\n"
    "v_mfma_f32_32x32x16_f16 v[204:219], v[144:147], v[96:99], v[204:219]\nds_read_b128 v[104:107], v2 offset:432\ns_waitcnt lgkmcnt(3)\n"
    "v_mfma_f32_32x32x16_f16 v[204:219], v[148:151], v[232:235], v[204:219]\nds_read_b128 v[220:223], v2 offset:400\ns_waitcnt lgkmcnt(3)\n"
    "v_mfma_f32_32x32x16_f16 v[204:219], v[152:155], v[100:103], v[204:219]\nds_read_b128 v[108:111], v2 offset:352\nv_mul_f32 v244, s96, v244\nv_mul_f32 v245, s97, v245\n"
    "v_mul_f32 v246, s96, v246\ns_waitcnt lgkmcnt(3)\nv_mfma_f32_32x32x16_f16 v[204:219], v[156:159], v[76:79], v[204:219]\nds_read_b128 v[80:83], v2 offset:320\n"
    "v_mul_f32 v247, s97, v247\nv_mul_f32 v248, s96, v248\nv_mul_f32 v249, s97, v249\ns_waitcnt lgkmcnt(3)\n"
    "v_mfma_f32_32x32x16_f16 v[204:219], v[160:163], v[104:107], v[204:219]\nds_read_b128 v[112:115], v2 offset:272\nv_mul_f32 v250, s96, v250\nv_mul_f32 v251, s97, v251\n"
    "v_cvt_pk_bf16_f32 v244, 0, v244\ns_waitcnt lgkmcnt(3)\nv_mfma_f32_32x32x16_f16 v[204:219], v[164:167], v[220:223], v[204:219]\nds_read_b128 v[224:227], v2 offset:240\n"
    "v_cvt_pk_bf16_f32 v245, 0, v245\nv_cvt_pk_bf16_f32 v246, 0, v246\nv_cvt_pk_bf16_f32 v247, 0, v247\ns_waitcnt lgkmcnt(3)\n"
    "v_mfma_f32_32x32x16_f16 v[204:219], v[168:171], v[108:111], v[204:219]\nds_read_b128 v[116:119], v2 offset:192\nv_cvt_pk_bf16_f32 v248, 0, v248\n"
    "v_cvt_pk_bf16_f32 v249, 0, v249\nv_cvt_pk_bf16_f32 v250, 0, v250\ns_waitcnt lgkmcnt(3)\nv_mfma_f32_32x32x16_f16 v[204:219], v[172:175], v[80:83], v[204:219]\n"
    "ds_read_b128 v[84:87], v2 offset:160\nv_cvt_pk_bf16_f32 v251, 0, v251\nv_xor_b32 v238, 32, v3\nv_xor_b32 v239, 64, v3\ns_waitcnt lgkmcnt(3)\n"
    "v_mfma_f32_32x32x16_f16 v[204:219], v[176:179], v[112:115], v[204:219]\nds_read_b128 v[120:123], v2 offset:112\nv_xor_b32 v240, 0x60, v3\nv_mov_b32 v24, 0\n"
    "v_mov_b32 v25, 0\ns_waitcnt lgkmcnt(3)\nv_mfma_f32_32x32x16_f16 v[204:219], v[180:183], v[224:227], v[204:219]\nds_read_b128 v[68:71], v2 offset:80\nv_mov_b32 v26, 0\n"
    "v_mov_b32 v27, 0\nv_mov_b32 v28, 0\ns_waitcnt lgkmcnt(3)\nv_mfma_f32_32x32x16_f16 v[204:219], v[184:187], v[116:119], v[204:219]\nds_read_b128 v[124:127], v2 offset:32\n"
    "v_mov_b32 v29, 0\nv_mov_b32 v30, 0\nv_mov_b32 v31, 0\ns_waitcnt lgkmcnt(3)\nv_mfma_f32_32x32x16_f16 v[204:219], v[188:191], v[84:87], v[204:219]\ns_waitcnt lgkmcnt(2)\n"
    "v_mfma_f32_32x32x16_f16 v[204:219], v[192:195], v[120:123], v[204:219]\nds_read_b32 v241, v7\ns_waitcnt lgkmcnt(2)\n"
    "v_mfma_f32_32x32x16_f16 v[204:219], v[196:199], v[68:71], v[204:219]\ns_waitcnt lgkmcnt(1)\nv_mfma_f32_32x32x16_f16 v[204:219], v[200:203], v[124:127], v[204:219]\n"
    "s_waitcnt lgkmcnt(0)\nv_cmp_gt_i32 vcc, v13, v241\ns_cbranch_vccnz .Lfs138\n.Lfr139:\ns_nop 8\nds_write_b128 v3, v[204:207]\nds_write_b128 v238, v[208:211]\n"
    "ds_write_b128 v239, v[212:215]\nds_write_b128 v240, v[216:219]\ns_waitcnt vmcnt(0)\ns_waitcnt lgkmcnt(0)\ns_barrier\nds_read_b128 v[48:51], v15\n"
    "ds_read_b128 v[52:55], v15 offset:12672\ns_cmp_lt_u32 s3, 2\ns_cbranch_scc1 .LL141\nds_read_b128 v[24:27], v16\nds_read_b128 v[28:31], v16 offset:10240\n.LL141:\n"
    "s_waitcnt lgkmcnt(0)\nv_pk_add_f32 v[24:25], v[24:25], v[48:49]\nv_pk_add_f32 v[26:27], v[26:27], v[50:51]\nv_pk_add_f32 v[28:29], v[28:29], v[52:53]\n"
    "v_pk_add_f32 v[30:31], v[30:31], v[54:55]\nv_pk_mul_f32 v[24:25], v[24:25], s[88:89] op_sel_hi:[1,0]\nv_pk_mul_f32 v[26:27], v[26:27], s[88:89] op_sel_hi:[1,0]\n"
    "v_pk_mul_f32 v[28:29], v[28:29], s[90:91] op_sel_hi:[1,0]\nv_pk_mul_f32 v[30:31], v[30:31], s[90:91] op_sel_hi:[1,0]\nv_cvt_pk_bf16_f32 v48, 0, v24\n"
    "v_cvt_pk_bf16_f32 v49, 0, v28\nv_cvt_pk_bf16_f32 v50, 0, v25\nv_cvt_pk_bf16_f32 v51, 0, v29\nv_cvt_pk_bf16_f32 v52, 0, v26\nv_cvt_pk_bf16_f32 v53, 0, v30\n"
    "v_cvt_pk_bf16_f32 v54, 0, v27\nv_cvt_pk_bf16_f32 v55, 0, v31\nv_pk_add_f32 v[244:245], v[244:245], v[48:49]\nv_pk_add_f32 v[246:247], v[246:247], v[50:51]\n"
    "v_pk_add_f32 v[248:249], v[248:249], v[52:53]\nv_pk_add_f32 v[250:251], v[250:251], v[54:55]\nv_cvt_pk_bf16_f32 v244, 0, v244\nv_cvt_pk_bf16_f32 v245, 0, v245\n"
    "v_cvt_pk_bf16_f32 v246, 0, v246\nv_cvt_pk_bf16_f32 v247, 0, v247\nv_cvt_pk_bf16_f32 v248, 0, v248\nv_cvt_pk_bf16_f32 v249, 0, v249\nv_cvt_pk_bf16_f32 v250, 0, v250\n"
    "v_cvt_pk_bf16_f32 v251, 0, v251\nv_pk_add_f32 v[32:33], v[32:33], v[244:245]\nv_pk_add_f32 v[34:35], v[34:35], v[246:247]\nv_pk_add_f32 v[36:37], v[36:37], v[248:249]\n"
    "v_pk_add_f32 v[38:39], v[38:39], v[250:251]\ns_cmp_eq_u32 s6, 0\ns_cbranch_scc0 .LL142\ns_mov_b32 s81, 0xfffefffe\ns_mov_b32 vcc_lo, 0x55555555\n"
    "s_mov_b32 vcc_hi, 0x55555555\nv_cvt_pk_bf16_f32 v54, v33, v35\nv_cvt_pk_bf16_f32 v55, v37, v39\nv_cvt_pk_bf16_f32 v52, v32, v34\nv_cvt_pk_bf16_f32 v53, v36, v38\n"
    "v_pk_min_u16 v54, v54, s81\nv_pk_min_u16 v55, v55, s81\nv_pk_min_u16 v52, v52, s81\nv_pk_min_u16 v53, v53, s81\ns_nop 0\n"
    "v_cndmask_b32_dpp v48, v54, v52, vcc quad_perm:[1,0,3,2] row_mask:0xf bank_mask:0xf\nv_cndmask_b32_dpp v49, v55, v53, vcc quad_perm:[1,0,3,2] row_mask:0xf bank_mask:0xf\n"
    "s_not_b64 vcc, vcc\nv_cndmask_b32_dpp v50, v52, v54, vcc quad_perm:[1,0,3,2] row_mask:0xf bank_mask:0xf\n"
    "v_cndmask_b32_dpp v51, v53, v55, vcc quad_perm:[1,0,3,2] row_mask:0xf bank_mask:0xf\nglobal_store_dwordx4 v17, v[48:51], s[32:33] sc1\ns_branch .LG276\n.LL142:\n"
    "v_cvt_pk_bf16_f32 v48, v32, v33\nv_cvt_pk_bf16_f32 v49, v34, v35\nv_cvt_pk_bf16_f32 v50, v36, v37\nv_cvt_pk_bf16_f32 v51, v38, v39\nv_min_u32 v48, -2, v48\n"
    "v_min_u32 v49, -2, v49\nv_min_u32 v50, -2, v50\nv_min_u32 v51, -2, v51\nglobal_store_dwordx4 v17, v[48:51], s[32:33] sc1\nv_and_b32 v4, 7, v0\ns_lshl_b32 s15, s41, 13\n"
    "s_add_u32 s14, s15, 0x1200000\nv_lshl_add_u32 v2, v4, 10, s14\nv_lshrrev_b32 v5, 3, v0\nv_lshl_add_u32 v2, v5, 2, v2\ns_lshl_b32 s17, s36, 16\ns_lshl_b32 s18, s35, 7\n"
    "s_add_u32 s16, s17, s18\nv_lshl_add_u32 v3, v5, 10, s16\nv_lshl_add_u32 v3, v4, 4, v3\ns_mov_b32 s80, 0x10000\ns_sleep 21\n.LL143:\n"
    "global_load_dword v52, v2, s[32:33] offset:0 sc1\nglobal_load_dword v53, v2, s[32:33] offset:256 sc1\nglobal_load_dword v54, v2, s[32:33] offset:512 sc1\n"
    "global_load_dword v55, v2, s[32:33] offset:768 sc1\ns_waitcnt vmcnt(0)\nv_max3_u32 v4, v52, v53, v54\nv_max_u32 v4, v4, v55\nv_cmp_eq_u32 vcc, -1, v4\n"
    "s_cbranch_vccz .LL144\ns_sub_u32 s80, s80, 1\ns_cbranch_scc0 .LL143\n.LL144:\nglobal_store_dwordx4 v3, v[52:55], s[30:31] sc1\ns_endpgm\n.LG276:\ns_mov_b32 s6, 1\n"
    "s_mov_b32 s7, 8388608\nv_bfe_u32 v65, v0, 4, 1\nv_bfe_u32 v66, v0, 5, 1\nv_xor_b32 v64, v65, v66\nv_bfe_u32 v69, v0, 2, 2\nv_lshl_add_u32 v68, v66, 3, v69\n"
    "v_lshlrev_b32 v67, 6, v68\nv_lshl_add_u32 v26, v64, 5, v67\nv_and_b32 v70, 3, v0\nv_lshl_add_u32 v26, v70, 3, v26\ns_mul_i32 s15, s3, 0x1a00\ns_add_u32 s14, s15, 0x18d00\n"
    "v_lshl_add_u32 v27, v66, 3, s14\nv_and_b32 v71, 31, v0\ns_mov_b32 s16, 0xd0\nv_mad_u32_u24 v27, v71, s16, v27\ns_mov_b32 s17, 0x27c00\nv_lshl_add_u32 v28, v71, 2, s17\n"
    "s_mul_i32 s19, s3, 0x3000\ns_add_u32 s18, s19, 0x18000\nv_add_u32 v29, s18, v67\nv_lshl_add_u32 v29, v65, 5, v29\nv_lshl_add_u32 v29, v70, 3, v29\nv_and_b32 v72, 63, v0\n"
    "s_lshl_b32 s23, s35, 15\ns_and_b32 s25, s36, 3\ns_lshl_b32 s24, s25, 13\ns_add_u32 s22, s23, s24\ns_lshl_b32 s44, s3, 11\ns_add_u32 s21, s22, s44\n"
    "s_add_u32 s20, s21, 0xd00000\nv_lshl_add_u32 v34, v72, 2, s20\nv_lshlrev_b32 v35, 4, v72\ns_lshl_b32 s46, s3, 12\ns_add_u32 s45, s46, 0x24c00\n"
    "v_lshl_add_u32 v36, v72, 3, s45\ns_and_b32 s54, s42, 7\ns_lshl_b32 s44, s54, 12\ns_add_u32 s56, s42, 1\ns_and_b32 s55, s56, 7\ns_lshl_b32 s45, s55, 12\n"
    "s_add_u32 s58, s42, 2\ns_and_b32 s57, s58, 7\ns_lshl_b32 s46, s57, 12\ns_add_u32 s60, s42, 3\ns_and_b32 s59, s60, 7\ns_lshl_b32 s47, s59, 12\ns_add_u32 s62, s42, 4\n"
    "s_and_b32 s61, s62, 7\ns_lshl_b32 s48, s61, 12\ns_add_u32 s64, s42, 5\ns_and_b32 s63, s64, 7\ns_lshl_b32 s49, s63, 12\ns_add_u32 s66, s42, 6\ns_and_b32 s65, s66, 7\n"
    "s_lshl_b32 s50, s65, 12\ns_add_u32 s68, s42, 7\ns_and_b32 s67, s68, 7\ns_lshl_b32 s51, s67, 12\ns_mov_b32 s81, 0x3b000000\ns_mov_b32 s84, -1\ns_mov_b32 s85, 0\n"
    "v_mov_b32 v143, 0x3f803f80\nv_add_u32 v116, s44, v26\nv_add_u32 v118, 0x10000, v116\nv_mov_b32 v112, 0\nv_mov_b32 v113, 0\nv_mov_b32 v114, 0\nv_mov_b32 v115, 0\n"
    "v_mov_b32 v121, 0\nv_mov_b32 v122, 0\nv_mov_b32 v123, 0\nv_mov_b32 v125, 0\nv_mov_b32 v126, 0\nv_mov_b32 v127, 0\nv_mov_b32 v129, 0\nv_mov_b32 v130, 0\nv_mov_b32 v131, 0\n"
    "v_mov_b32 v133, 0\nv_mov_b32 v134, 0\nv_mov_b32 v135, 0\nds_read_b32 v124, v28 offset:0\nds_read_b32 v128, v28 offset:128\nds_read_b32 v132, v28 offset:256\n"
    "s_waitcnt lgkmcnt(0)\nv_cvt_pk_bf16_f32 v140, 0, v124\nv_cvt_pk_bf16_f32 v141, 0, v128\nv_cvt_pk_bf16_f32 v142, 0, v132\nv_sub_f32 v124, v124, v140\n"
    "v_sub_f32 v128, v128, v141\nv_sub_f32 v132, v132, v142\nv_cvt_pk_bf16_f32 v124, v140, v124\nv_cvt_pk_bf16_f32 v128, v141, v128\nv_cvt_pk_bf16_f32 v132, v142, v132\n"
    "s_lshr_b32 s86, s44, 4\nv_add_u32 v34, s86, v34\ns_lshl2_add_u32 s15, s37, s3\ns_lshl_b32 s14, s15, 15\ns_add_u32 s82, s14, 0x600000\ns_mul_i32 s16, s3, 0x3000\n"
    "s_add_u32 s87, s16, 0x18000\ns_add_u32 s82, s82, s32\ns_addc_u32 s83, s33, 0\nds_read_b64_tr_b16 v[192:193], v116 offset:0\nds_read_b64_tr_b16 v[194:195], v116 offset:256\n"
    "ds_read_b64_tr_b16 v[196:197], v116 offset:1024\nds_read_b64_tr_b16 v[198:199], v116 offset:1280\nds_read_b64_tr_b16 v[200:201], v116 offset:2048\n"
    "ds_read_b64_tr_b16 v[202:203], v116 offset:2304\nds_read_b64_tr_b16 v[204:205], v116 offset:3072\nds_read_b64_tr_b16 v[206:207], v116 offset:3328\n"
    "ds_read_b64_tr_b16 v[208:209], v116 offset:32768\nds_read_b64_tr_b16 v[210:211], v116 offset:33024\nds_read_b64_tr_b16 v[212:213], v116 offset:33792\n"
    "ds_read_b64_tr_b16 v[214:215], v116 offset:34048\ns_barrier\nv_mov_b32 v64, 0\nv_mov_b32 v65, 0xf149f2ca\nv_bfe_u32 v68, v0, 4, 2\nv_lshlrev_b32 v67, 2, v68\n"
    "s_add_u32 s15, s37, s3\ns_cmp_eq_u32 s15, 0\ns_cselect_b32 s14, 8, 0\nv_and_b32 v71, 7, v0\nv_add_u32 v70, 1, v71\nv_max_u32 v69, s14, v70\nv_sub_u32 v66, v67, v69\n"
    "v_add_u32 v73, 8, v71\nv_sub_u32 v72, v73, v69\nv_cmp_le_u32 vcc, v66, v72\ns_nop 1\nv_cndmask_b32 v2, v65, v64, vcc\nv_add_u32 v75, 1, v67\nv_sub_u32 v74, v75, v69\n"
    "v_cmp_le_u32 vcc, v74, v72\ns_nop 1\nv_cndmask_b32 v3, v65, v64, vcc\nv_add_u32 v77, 2, v67\nv_sub_u32 v76, v77, v69\nv_cmp_le_u32 vcc, v76, v72\ns_nop 1\n"
    "v_cndmask_b32 v4, v65, v64, vcc\nv_add_u32 v79, 3, v67\nv_sub_u32 v78, v79, v69\nv_cmp_le_u32 vcc, v78, v72\ns_nop 1\nv_cndmask_b32 v5, v65, v64, vcc\n"
    "s_mul_i32 s17, s3, 0xd00\ns_add_u32 s16, s17, 0x18040\nv_lshl_add_u32 v6, v68, 4, s16\nv_and_b32 v81, 15, v0\nv_max_u32 v80, 1, v81\ns_mov_b32 s18, 0x1a0\n"
    "v_mad_u32_u24 v6, v80, s18, v6\ns_add_u32 s19, s17, 0x18d00\nv_lshl_add_u32 v7, v68, 4, s19\nv_mad_u32_u24 v7, v71, s18, v7\nv_and_b32 v82, 3, v0\n"
    "s_add_u32 s20, s17, 0x18080\nv_lshl_add_u32 v8, v82, 3, s20\nv_bfe_u32 v85, v0, 2, 2\nv_add_u32 v84, v67, v85\nv_max_u32 v83, 1, v84\nv_mad_u32_u24 v8, v83, s18, v8\n"
    "v_bfe_u32 v86, v0, 3, 1\ns_lshr_b32 s54, s3, 1\ns_lshl2_add_u32 s25, s37, s54\ns_lshl_b32 s24, s25, 15\ns_lshl_b32 s55, s38, 11\ns_add_u32 s23, s24, s55\n"
    "s_and_b32 s57, s3, 1\ns_lshl_b32 s56, s57, 8\ns_add_u32 s22, s23, s56\ns_add_u32 s21, s22, 0x800000\nv_lshl_add_u32 v9, v86, 10, s21\nv_bfe_u32 v87, v0, 5, 1\n"
    "v_lshl_add_u32 v9, v87, 9, v9\nv_bfe_u32 v88, v0, 4, 1\nv_lshl_add_u32 v9, v88, 4, v9\nv_lshl_add_u32 v9, v71, 5, v9\nv_mov_b64 v[10:11], 0\nv_mov_b64 v[12:13], 0\n"
    "v_mov_b64 v[14:15], 0\nv_mov_b64 v[16:17], 0\nv_mov_b64 v[18:19], 0\nv_mov_b64 v[20:21], 0\nv_mov_b64 v[22:23], 0\nv_mov_b64 v[24:25], 0\ns_barrier\n"
    "ds_read_b64_tr_b16 v[144:145], v29 offset:0\nds_read_b64_tr_b16 v[146:147], v29 offset:256\nds_read_b64_tr_b16 v[148:149], v29 offset:1024\n"
    "ds_read_b64_tr_b16 v[150:151], v29 offset:1280\nds_read_b64_tr_b16 v[152:153], v29 offset:2048\nds_read_b64_tr_b16 v[154:155], v29 offset:2304\n"
    "ds_read_b64_tr_b16 v[156:157], v29 offset:3072\nds_read_b64_tr_b16 v[158:159], v29 offset:3328\ns_waitcnt lgkmcnt(0)\nv_pk_maximum3_f16 v138, v144, v145, v146\n"
    "v_pk_maximum3_f16 v139, v147, v148, v149\nv_pk_maximum3_f16 v138, v138, v150, v151\nv_pk_maximum3_f16 v139, v139, v152, v153\nv_pk_maximum3_f16 v138, v138, v154, v155\n"
    "v_pk_maximum3_f16 v139, v139, v156, v157\nv_pk_maximum3_f16 v138, v138, v158, v159\nv_pk_maximum3_f16 v138, v138, v139, v139\n"
    "v_cmp_u_f16_sdwa vcc, v138, v138 src0_sel:WORD_0 src1_sel:WORD_1\ns_cbranch_vccnz .Lxs145\n.Lxr146:\nv_dot2c_f32_bf16 v112, v143, v144\nv_dot2c_f32_bf16 v113, v144, v144\n"
    "v_dot2c_f32_bf16 v114, v143, v145\nv_dot2c_f32_bf16 v115, v145, v145\nv_dot2c_f32_bf16 v112, v143, v146\nv_dot2c_f32_bf16 v113, v146, v146\n"
    "v_dot2c_f32_bf16 v114, v143, v147\nv_dot2c_f32_bf16 v115, v147, v147\nv_dot2c_f32_bf16 v112, v143, v148\nv_dot2c_f32_bf16 v113, v148, v148\n"
    "v_dot2c_f32_bf16 v114, v143, v149\nv_dot2c_f32_bf16 v115, v149, v149\nv_dot2c_f32_bf16 v112, v143, v150\nv_dot2c_f32_bf16 v113, v150, v150\n"
    "v_dot2c_f32_bf16 v114, v143, v151\nv_dot2c_f32_bf16 v115, v151, v151\nv_dot2c_f32_bf16 v112, v143, v152\nv_dot2c_f32_bf16 v113, v152, v152\n"
    "v_dot2c_f32_bf16 v114, v143, v153\nv_dot2c_f32_bf16 v115, v153, v153\nv_dot2c_f32_bf16 v112, v143, v154\nv_dot2c_f32_bf16 v113, v154, v154\n"
    "v_dot2c_f32_bf16 v114, v143, v155\nv_dot2c_f32_bf16 v115, v155, v155\nv_dot2c_f32_bf16 v112, v143, v156\nv_dot2c_f32_bf16 v113, v156, v156\n"
    "v_dot2c_f32_bf16 v114, v143, v157\nv_dot2c_f32_bf16 v115, v157, v157\nv_dot2c_f32_bf16 v112, v143, v158\nv_dot2c_f32_bf16 v113, v158, v158\n"
    "v_dot2c_f32_bf16 v114, v143, v159\nv_dot2c_f32_bf16 v115, v159, v159\nv_mfma_f32_32x32x16_bf16 v[64:79], v[192:195], v[144:147], 0\n"
    "ds_read_b64_tr_b16 v[192:193], v116 offset:34816\nds_read_b64_tr_b16 v[194:195], v116 offset:35072\nv_add_u32 v117, s45, v26\nv_add_u32 v119, 0x10000, v117\n"
    "v_add_f32 v112, v112, v114\nv_add_f32 v113, v113, v115\nv_mfma_f32_32x32x16_bf16 v[64:79], v[196:199], v[148:151], v[64:79]\n"
    "ds_read_b64_tr_b16 v[196:197], v116 offset:35840\nds_read_b64_tr_b16 v[198:199], v116 offset:36096\ns_nop 0\nv_permlane32_swap_b32 v112, v113\nv_add_f32 v112, v112, v113\n"
    "v_mfma_f32_32x32x16_bf16 v[64:79], v[200:203], v[152:155], v[64:79]\nds_read_b64_tr_b16 v[200:201], v118 offset:0\nds_read_b64_tr_b16 v[202:203], v118 offset:256\n"
    "v_min_u32 v112, -2, v112\nglobal_store_dword v34, v112, s[32:33] sc1\ns_barrier\nv_mfma_f32_32x32x16_bf16 v[64:79], v[204:207], v[156:159], v[64:79]\n"
    "ds_read_b64_tr_b16 v[204:205], v118 offset:1024\nds_read_b64_tr_b16 v[206:207], v118 offset:1280\nds_read_b64_tr_b16 v[160:161], v29 offset:4096\n"
    "ds_read_b64_tr_b16 v[162:163], v29 offset:4352\nv_mfma_f32_32x32x16_bf16 v[80:95], v[208:211], v[144:147], 0\nds_read_b64_tr_b16 v[208:209], v118 offset:2048\n"
    "ds_read_b64_tr_b16 v[210:211], v118 offset:2304\nds_read_b64_tr_b16 v[164:165], v29 offset:5120\nds_read_b64_tr_b16 v[166:167], v29 offset:5376\n"
    "v_mfma_f32_32x32x16_bf16 v[80:95], v[212:215], v[148:151], v[80:95]\nds_read_b64_tr_b16 v[212:213], v118 offset:3072\nds_read_b64_tr_b16 v[214:215], v118 offset:3328\n"
    "ds_read_b64_tr_b16 v[168:169], v29 offset:6144\nds_read_b64_tr_b16 v[170:171], v29 offset:6400\ns_waitcnt lgkmcnt(14)\n"
    "v_mfma_f32_32x32x16_bf16 v[80:95], v[192:195], v[152:155], v[80:95]\nds_read_b64_tr_b16 v[192:193], v117 offset:0\nds_read_b64_tr_b16 v[194:195], v117 offset:256\n"
    "ds_read_b64_tr_b16 v[172:173], v29 offset:7168\nds_read_b64_tr_b16 v[174:175], v29 offset:7424\nv_mfma_f32_32x32x16_bf16 v[80:95], v[196:199], v[156:159], v[80:95]\n"
    "ds_read_b64_tr_b16 v[196:197], v117 offset:1024\nds_read_b64_tr_b16 v[198:199], v117 offset:1280\ns_waitcnt lgkmcnt(10)\nv_pk_maximum3_f16 v138, v160, v161, v162\n"
    "v_pk_maximum3_f16 v139, v163, v164, v165\nv_mfma_f32_32x32x16_bf16 v[96:111], v[200:203], v[144:147], 0\nds_read_b64_tr_b16 v[200:201], v117 offset:2048\n"
    "ds_read_b64_tr_b16 v[202:203], v117 offset:2304\nv_pk_maximum3_f16 v138, v138, v166, v167\ns_waitcnt lgkmcnt(4)\nv_pk_maximum3_f16 v139, v139, v168, v169\n"
    "v_mfma_f32_32x32x16_bf16 v[96:111], v[204:207], v[148:151], v[96:111]\nds_read_b64_tr_b16 v[204:205], v117 offset:3072\nds_read_b64_tr_b16 v[206:207], v117 offset:3328\n"
    "v_pk_maximum3_f16 v138, v138, v170, v171\nv_pk_maximum3_f16 v139, v139, v172, v173\nv_mfma_f32_32x32x16_bf16 v[96:111], v[208:211], v[152:155], v[96:111]\n"
    "ds_read_b64_tr_b16 v[208:209], v117 offset:32768\nds_read_b64_tr_b16 v[210:211], v117 offset:33024\nv_pk_maximum3_f16 v138, v138, v174, v175\n"
    "v_pk_maximum3_f16 v138, v138, v139, v139\nv_mfma_f32_32x32x16_bf16 v[96:111], v[212:215], v[156:159], v[96:111]\nds_read_b64_tr_b16 v[212:213], v117 offset:33792\n"
    "ds_read_b64_tr_b16 v[214:215], v117 offset:34048\nv_cmp_u_f16_sdwa vcc, v138, v138 src0_sel:WORD_0 src1_sel:WORD_1\ns_cbranch_vccnz .Lxs148\n.Lxr149:\n"
    "s_waitcnt lgkmcnt(8)\nv_mfma_f32_32x32x16_bf16 v[64:79], v[192:195], v[160:163], v[64:79]\nds_read_b64_tr_b16 v[192:193], v117 offset:34816\n"
    "ds_read_b64_tr_b16 v[194:195], v117 offset:35072\nv_add_u32 v116, s46, v26\nv_add_u32 v118, 0x10000, v116\n"
    "v_mfma_f32_32x32x16_bf16 v[64:79], v[196:199], v[164:167], v[64:79]\nds_read_b64_tr_b16 v[196:197], v117 offset:35840\nds_read_b64_tr_b16 v[198:199], v117 offset:36096\n"
    "s_waitcnt lgkmcnt(8)\nv_mfma_f32_32x32x16_bf16 v[64:79], v[200:203], v[168:171], v[64:79]\nds_read_b64_tr_b16 v[200:201], v119 offset:0\n"
    "ds_read_b64_tr_b16 v[202:203], v119 offset:256\ns_barrier\nv_mfma_f32_32x32x16_bf16 v[64:79], v[204:207], v[172:175], v[64:79]\n"
    "ds_read_b64_tr_b16 v[204:205], v119 offset:1024\nds_read_b64_tr_b16 v[206:207], v119 offset:1280\nds_read_b64_tr_b16 v[144:145], v29 offset:8192\n"
    "ds_read_b64_tr_b16 v[146:147], v29 offset:8448\ns_waitcnt lgkmcnt(10)\nv_mfma_f32_32x32x16_bf16 v[80:95], v[208:211], v[160:163], v[80:95]\n"
    "ds_read_b64_tr_b16 v[208:209], v119 offset:2048\nds_read_b64_tr_b16 v[210:211], v119 offset:2304\nds_read_b64_tr_b16 v[148:149], v29 offset:9216\n"
    "ds_read_b64_tr_b16 v[150:151], v29 offset:9472\nv_mfma_f32_32x32x16_bf16 v[80:95], v[212:215], v[164:167], v[80:95]\nds_read_b64_tr_b16 v[212:213], v119 offset:3072\n"
    "ds_read_b64_tr_b16 v[214:215], v119 offset:3328\nds_read_b64_tr_b16 v[152:153], v29 offset:10240\nds_read_b64_tr_b16 v[154:155], v29 offset:10496\ns_waitcnt lgkmcnt(14)\n"
    "v_mfma_f32_32x32x16_bf16 v[80:95], v[192:195], v[168:171], v[80:95]\nds_read_b64_tr_b16 v[192:193], v116 offset:0\nds_read_b64_tr_b16 v[194:195], v116 offset:256\n"
    "ds_read_b64_tr_b16 v[156:157], v29 offset:11264\nds_read_b64_tr_b16 v[158:159], v29 offset:11520\nv_mfma_f32_32x32x16_bf16 v[80:95], v[196:199], v[172:175], v[80:95]\n"
    "ds_read_b64_tr_b16 v[196:197], v116 offset:1024\nds_read_b64_tr_b16 v[198:199], v116 offset:1280\ns_waitcnt lgkmcnt(10)\nv_pk_maximum3_f16 v138, v144, v145, v146\n"
    "v_pk_maximum3_f16 v139, v147, v148, v149\nv_mfma_f32_32x32x16_bf16 v[96:111], v[200:203], v[160:163], v[96:111]\nds_read_b64_tr_b16 v[200:201], v116 offset:2048\n"
    "ds_read_b64_tr_b16 v[202:203], v116 offset:2304\nv_pk_maximum3_f16 v138, v138, v150, v151\ns_waitcnt lgkmcnt(4)\nv_pk_maximum3_f16 v139, v139, v152, v153\n"
    "v_mfma_f32_32x32x16_bf16 v[96:111], v[204:207], v[164:167], v[96:111]\nds_read_b64_tr_b16 v[204:205], v116 offset:3072\nds_read_b64_tr_b16 v[206:207], v116 offset:3328\n"
    "v_pk_maximum3_f16 v138, v138, v154, v155\nv_pk_maximum3_f16 v139, v139, v156, v157\nv_mfma_f32_32x32x16_bf16 v[96:111], v[208:211], v[168:171], v[96:111]\n"
    "ds_read_b64_tr_b16 v[208:209], v116 offset:32768\nds_read_b64_tr_b16 v[210:211], v116 offset:33024\nv_pk_maximum3_f16 v138, v138, v158, v159\n"
    "v_pk_maximum3_f16 v138, v138, v139, v139\nv_mfma_f32_32x32x16_bf16 v[96:111], v[212:215], v[172:175], v[96:111]\nds_read_b64_tr_b16 v[212:213], v116 offset:33792\n"
    "ds_read_b64_tr_b16 v[214:215], v116 offset:34048\nv_cmp_u_f16_sdwa vcc, v138, v138 src0_sel:WORD_0 src1_sel:WORD_1\ns_cbranch_vccnz .Lxs151\n.Lxr152:\n"
    "s_waitcnt lgkmcnt(8)\nv_mfma_f32_32x32x16_bf16 v[64:79], v[192:195], v[144:147], v[64:79]\nds_read_b64_tr_b16 v[192:193], v116 offset:34816\n"
    "ds_read_b64_tr_b16 v[194:195], v116 offset:35072\nv_add_u32 v117, s47, v26\nv_add_u32 v119, 0x10000, v117\n"
    "v_mfma_f32_32x32x16_bf16 v[64:79], v[196:199], v[148:151], v[64:79]\nds_read_b64_tr_b16 v[196:197], v116 offset:35840\nds_read_b64_tr_b16 v[198:199], v116 offset:36096\n"
    "s_waitcnt lgkmcnt(8)\nv_mfma_f32_32x32x16_bf16 v[64:79], v[200:203], v[152:155], v[64:79]\nds_read_b64_tr_b16 v[200:201], v118 offset:0\n"
    "ds_read_b64_tr_b16 v[202:203], v118 offset:256\ns_barrier\nv_mfma_f32_32x32x16_bf16 v[64:79], v[204:207], v[156:159], v[64:79]\n"
    "ds_read_b64_tr_b16 v[204:205], v118 offset:1024\nds_read_b64_tr_b16 v[206:207], v118 offset:1280\nds_read_b64_tr_b16 v[160:161], v29 offset:0\n"
    "ds_read_b64_tr_b16 v[162:163], v29 offset:256\ns_waitcnt lgkmcnt(10)\nv_mfma_f32_32x32x16_bf16 v[80:95], v[208:211], v[144:147], v[80:95]\n"
    "ds_read_b64_tr_b16 v[208:209], v118 offset:2048\nds_read_b64_tr_b16 v[210:211], v118 offset:2304\nds_read_b64_tr_b16 v[164:165], v29 offset:1024\n"
    "ds_read_b64_tr_b16 v[166:167], v29 offset:1280\nv_mfma_f32_32x32x16_bf16 v[80:95], v[212:215], v[148:151], v[80:95]\nds_read_b64_tr_b16 v[212:213], v118 offset:3072\n"
    "ds_read_b64_tr_b16 v[214:215], v118 offset:3328\nds_read_b64_tr_b16 v[168:169], v29 offset:2048\nds_read_b64_tr_b16 v[170:171], v29 offset:2304\ns_waitcnt lgkmcnt(14)\n"
    "v_mfma_f32_32x32x16_bf16 v[80:95], v[192:195], v[152:155], v[80:95]\nds_read_b64_tr_b16 v[192:193], v117 offset:0\nds_read_b64_tr_b16 v[194:195], v117 offset:256\n"
    "ds_read_b64_tr_b16 v[172:173], v29 offset:3072\nds_read_b64_tr_b16 v[174:175], v29 offset:3328\nv_mfma_f32_32x32x16_bf16 v[80:95], v[196:199], v[156:159], v[80:95]\n"
    "ds_read_b64_tr_b16 v[196:197], v117 offset:1024\nds_read_b64_tr_b16 v[198:199], v117 offset:1280\ns_waitcnt lgkmcnt(10)\nv_pk_maximum3_f16 v138, v160, v161, v162\n"
    "v_pk_maximum3_f16 v139, v163, v164, v165\nv_mfma_f32_32x32x16_bf16 v[96:111], v[200:203], v[144:147], v[96:111]\nds_read_b64_tr_b16 v[200:201], v117 offset:2048\n"
    "ds_read_b64_tr_b16 v[202:203], v117 offset:2304\nv_pk_maximum3_f16 v138, v138, v166, v167\ns_waitcnt lgkmcnt(4)\nv_pk_maximum3_f16 v139, v139, v168, v169\n"
    "v_mfma_f32_32x32x16_bf16 v[96:111], v[204:207], v[148:151], v[96:111]\nds_read_b64_tr_b16 v[204:205], v117 offset:3072\nds_read_b64_tr_b16 v[206:207], v117 offset:3328\n"
    "v_pk_maximum3_f16 v138, v138, v170, v171\nv_pk_maximum3_f16 v139, v139, v172, v173\nv_mfma_f32_32x32x16_bf16 v[96:111], v[208:211], v[152:155], v[96:111]\n"
    "ds_read_b64_tr_b16 v[208:209], v117 offset:32768\nds_read_b64_tr_b16 v[210:211], v117 offset:33024\nv_pk_maximum3_f16 v138, v138, v174, v175\n"
    "v_pk_maximum3_f16 v138, v138, v139, v139\nv_mfma_f32_32x32x16_bf16 v[96:111], v[212:215], v[156:159], v[96:111]\nds_read_b64_tr_b16 v[212:213], v117 offset:33792\n"
    "ds_read_b64_tr_b16 v[214:215], v117 offset:34048\nv_cmp_u_f16_sdwa vcc, v138, v138 src0_sel:WORD_0 src1_sel:WORD_1\ns_cbranch_vccnz .Lxs154\n.Lxr155:\n"
    "s_waitcnt lgkmcnt(8)\nv_mfma_f32_32x32x16_bf16 v[64:79], v[192:195], v[160:163], v[64:79]\nds_read_b64_tr_b16 v[192:193], v117 offset:34816\n"
    "ds_read_b64_tr_b16 v[194:195], v117 offset:35072\nv_add_u32 v116, s48, v26\nv_add_u32 v118, 0x10000, v116\n"
    "v_mfma_f32_32x32x16_bf16 v[64:79], v[196:199], v[164:167], v[64:79]\nds_read_b64_tr_b16 v[196:197], v117 offset:35840\nds_read_b64_tr_b16 v[198:199], v117 offset:36096\n"
    "s_waitcnt lgkmcnt(8)\nv_mfma_f32_32x32x16_bf16 v[64:79], v[200:203], v[168:171], v[64:79]\nds_read_b64_tr_b16 v[200:201], v119 offset:0\n"
    "ds_read_b64_tr_b16 v[202:203], v119 offset:256\ns_barrier\nv_mfma_f32_32x32x16_bf16 v[64:79], v[204:207], v[172:175], v[64:79]\n"
    "ds_read_b64_tr_b16 v[204:205], v119 offset:1024\nds_read_b64_tr_b16 v[206:207], v119 offset:1280\nds_read_b64_tr_b16 v[144:145], v29 offset:4096\n"
    "ds_read_b64_tr_b16 v[146:147], v29 offset:4352\ns_waitcnt lgkmcnt(10)\nv_mfma_f32_32x32x16_bf16 v[80:95], v[208:211], v[160:163], v[80:95]\n"
    "ds_read_b64_tr_b16 v[208:209], v119 offset:2048\nds_read_b64_tr_b16 v[210:211], v119 offset:2304\nds_read_b64_tr_b16 v[148:149], v29 offset:5120\n"
    "ds_read_b64_tr_b16 v[150:151], v29 offset:5376\nv_mfma_f32_32x32x16_bf16 v[80:95], v[212:215], v[164:167], v[80:95]\nds_read_b64_tr_b16 v[212:213], v119 offset:3072\n"
    "ds_read_b64_tr_b16 v[214:215], v119 offset:3328\nds_read_b64_tr_b16 v[152:153], v29 offset:6144\nds_read_b64_tr_b16 v[154:155], v29 offset:6400\ns_waitcnt lgkmcnt(14)\n"
    "v_mfma_f32_32x32x16_bf16 v[80:95], v[192:195], v[168:171], v[80:95]\nds_read_b64_tr_b16 v[192:193], v116 offset:0\nds_read_b64_tr_b16 v[194:195], v116 offset:256\n"
    "ds_read_b64_tr_b16 v[156:157], v29 offset:7168\nds_read_b64_tr_b16 v[158:159], v29 offset:7424\nv_mfma_f32_32x32x16_bf16 v[80:95], v[196:199], v[172:175], v[80:95]\n"
    "ds_read_b64_tr_b16 v[196:197], v116 offset:1024\nds_read_b64_tr_b16 v[198:199], v116 offset:1280\ns_waitcnt lgkmcnt(10)\nv_pk_maximum3_f16 v138, v144, v145, v146\n"
    "v_pk_maximum3_f16 v139, v147, v148, v149\nv_mfma_f32_32x32x16_bf16 v[96:111], v[200:203], v[160:163], v[96:111]\nds_read_b64_tr_b16 v[200:201], v116 offset:2048\n"
    "ds_read_b64_tr_b16 v[202:203], v116 offset:2304\nv_pk_maximum3_f16 v138, v138, v150, v151\ns_waitcnt lgkmcnt(4)\nv_pk_maximum3_f16 v139, v139, v152, v153\n"
    "v_mfma_f32_32x32x16_bf16 v[96:111], v[204:207], v[164:167], v[96:111]\nds_read_b64_tr_b16 v[204:205], v116 offset:3072\nds_read_b64_tr_b16 v[206:207], v116 offset:3328\n"
    "v_pk_maximum3_f16 v138, v138, v154, v155\nv_pk_maximum3_f16 v139, v139, v156, v157\nv_mfma_f32_32x32x16_bf16 v[96:111], v[208:211], v[168:171], v[96:111]\n"
    "ds_read_b64_tr_b16 v[208:209], v116 offset:32768\nds_read_b64_tr_b16 v[210:211], v116 offset:33024\nv_pk_maximum3_f16 v138, v138, v158, v159\n"
    "v_pk_maximum3_f16 v138, v138, v139, v139\nv_mfma_f32_32x32x16_bf16 v[96:111], v[212:215], v[172:175], v[96:111]\nds_read_b64_tr_b16 v[212:213], v116 offset:33792\n"
    "ds_read_b64_tr_b16 v[214:215], v116 offset:34048\nv_cmp_u_f16_sdwa vcc, v138, v138 src0_sel:WORD_0 src1_sel:WORD_1\ns_cbranch_vccnz .Lxs157\n.Lxr158:\n"
    "s_waitcnt lgkmcnt(8)\nv_mfma_f32_32x32x16_bf16 v[64:79], v[192:195], v[144:147], v[64:79]\nds_read_b64_tr_b16 v[192:193], v116 offset:34816\n"
    "ds_read_b64_tr_b16 v[194:195], v116 offset:35072\nv_add_u32 v117, s49, v26\nv_add_u32 v119, 0x10000, v117\n"
    "v_mfma_f32_32x32x16_bf16 v[64:79], v[196:199], v[148:151], v[64:79]\nds_read_b64_tr_b16 v[196:197], v116 offset:35840\nds_read_b64_tr_b16 v[198:199], v116 offset:36096\n"
    "s_waitcnt lgkmcnt(8)\nv_mfma_f32_32x32x16_bf16 v[64:79], v[200:203], v[152:155], v[64:79]\nds_read_b64_tr_b16 v[200:201], v118 offset:0\n"
    "ds_read_b64_tr_b16 v[202:203], v118 offset:256\ns_barrier\nv_mfma_f32_32x32x16_bf16 v[64:79], v[204:207], v[156:159], v[64:79]\n"
    "ds_read_b64_tr_b16 v[204:205], v118 offset:1024\nds_read_b64_tr_b16 v[206:207], v118 offset:1280\nds_read_b64_tr_b16 v[160:161], v29 offset:8192\n"
    "ds_read_b64_tr_b16 v[162:163], v29 offset:8448\ns_waitcnt lgkmcnt(10)\nv_mfma_f32_32x32x16_bf16 v[80:95], v[208:211], v[144:147], v[80:95]\n"
    "ds_read_b64_tr_b16 v[208:209], v118 offset:2048\nds_read_b64_tr_b16 v[210:211], v118 offset:2304\nds_read_b64_tr_b16 v[164:165], v29 offset:9216\n"
    "ds_read_b64_tr_b16 v[166:167], v29 offset:9472\nv_mfma_f32_32x32x16_bf16 v[80:95], v[212:215], v[148:151], v[80:95]\nds_read_b64_tr_b16 v[212:213], v118 offset:3072\n"
    "ds_read_b64_tr_b16 v[214:215], v118 offset:3328\nds_read_b64_tr_b16 v[168:169], v29 offset:10240\nds_read_b64_tr_b16 v[170:171], v29 offset:10496\ns_waitcnt lgkmcnt(14)\n"
    "v_mfma_f32_32x32x16_bf16 v[80:95], v[192:195], v[152:155], v[80:95]\nds_read_b64_tr_b16 v[192:193], v117 offset:0\nds_read_b64_tr_b16 v[194:195], v117 offset:256\n"
    "ds_read_b64_tr_b16 v[172:173], v29 offset:11264\nds_read_b64_tr_b16 v[174:175], v29 offset:11520\nv_mfma_f32_32x32x16_bf16 v[80:95], v[196:199], v[156:159], v[80:95]\n"
    "ds_read_b64_tr_b16 v[196:197], v117 offset:1024\nds_read_b64_tr_b16 v[198:199], v117 offset:1280\ns_waitcnt lgkmcnt(10)\nv_pk_maximum3_f16 v138, v160, v161, v162\n"
    "v_pk_maximum3_f16 v139, v163, v164, v165\nv_mfma_f32_32x32x16_bf16 v[96:111], v[200:203], v[144:147], v[96:111]\nds_read_b64_tr_b16 v[200:201], v117 offset:2048\n"
    "ds_read_b64_tr_b16 v[202:203], v117 offset:2304\nv_pk_maximum3_f16 v138, v138, v166, v167\ns_waitcnt lgkmcnt(4)\nv_pk_maximum3_f16 v139, v139, v168, v169\n"
    "v_mfma_f32_32x32x16_bf16 v[96:111], v[204:207], v[148:151], v[96:111]\nds_read_b64_tr_b16 v[204:205], v117 offset:3072\nds_read_b64_tr_b16 v[206:207], v117 offset:3328\n"
    "v_pk_maximum3_f16 v138, v138, v170, v171\nv_pk_maximum3_f16 v139, v139, v172, v173\nv_mfma_f32_32x32x16_bf16 v[96:111], v[208:211], v[152:155], v[96:111]\n"
    "ds_read_b64_tr_b16 v[208:209], v117 offset:32768\nds_read_b64_tr_b16 v[210:211], v117 offset:33024\nv_pk_maximum3_f16 v138, v138, v174, v175\n"
    "v_pk_maximum3_f16 v138, v138, v139, v139\nv_mfma_f32_32x32x16_bf16 v[96:111], v[212:215], v[156:159], v[96:111]\nds_read_b64_tr_b16 v[212:213], v117 offset:33792\n"
    "ds_read_b64_tr_b16 v[214:215], v117 offset:34048\nv_cmp_u_f16_sdwa vcc, v138, v138 src0_sel:WORD_0 src1_sel:WORD_1\ns_cbranch_vccnz .Lxs160\n.Lxr161:\n"
    "s_waitcnt lgkmcnt(8)\nv_mfma_f32_32x32x16_bf16 v[64:79], v[192:195], v[160:163], v[64:79]\nds_read_b64_tr_b16 v[192:193], v117 offset:34816\n"
    "ds_read_b64_tr_b16 v[194:195], v117 offset:35072\nv_add_u32 v116, s50, v26\nv_add_u32 v118, 0x10000, v116\n"
    "v_mfma_f32_32x32x16_bf16 v[64:79], v[196:199], v[164:167], v[64:79]\nds_read_b64_tr_b16 v[196:197], v117 offset:35840\nds_read_b64_tr_b16 v[198:199], v117 offset:36096\n"
    "s_waitcnt lgkmcnt(8)\nv_mfma_f32_32x32x16_bf16 v[64:79], v[200:203], v[168:171], v[64:79]\nds_read_b64_tr_b16 v[200:201], v119 offset:0\n"
    "ds_read_b64_tr_b16 v[202:203], v119 offset:256\ns_barrier\nv_mfma_f32_32x32x16_bf16 v[64:79], v[204:207], v[172:175], v[64:79]\n"
    "ds_read_b64_tr_b16 v[204:205], v119 offset:1024\nds_read_b64_tr_b16 v[206:207], v119 offset:1280\nds_read_b64_tr_b16 v[144:145], v29 offset:0\n"
    "ds_read_b64_tr_b16 v[146:147], v29 offset:256\ns_waitcnt lgkmcnt(10)\nv_mfma_f32_32x32x16_bf16 v[80:95], v[208:211], v[160:163], v[80:95]\n"
    "ds_read_b64_tr_b16 v[208:209], v119 offset:2048\nds_read_b64_tr_b16 v[210:211], v119 offset:2304\nds_read_b64_tr_b16 v[148:149], v29 offset:1024\n"
    "ds_read_b64_tr_b16 v[150:151], v29 offset:1280\nv_mfma_f32_32x32x16_bf16 v[80:95], v[212:215], v[164:167], v[80:95]\nds_read_b64_tr_b16 v[212:213], v119 offset:3072\n"
    "ds_read_b64_tr_b16 v[214:215], v119 offset:3328\nds_read_b64_tr_b16 v[152:153], v29 offset:2048\nds_read_b64_tr_b16 v[154:155], v29 offset:2304\ns_waitcnt lgkmcnt(14)\n"
    "v_mfma_f32_32x32x16_bf16 v[80:95], v[192:195], v[168:171], v[80:95]\nds_read_b64_tr_b16 v[192:193], v116 offset:0\nds_read_b64_tr_b16 v[194:195], v116 offset:256\n"
    "ds_read_b64_tr_b16 v[156:157], v29 offset:3072\nds_read_b64_tr_b16 v[158:159], v29 offset:3328\nv_mfma_f32_32x32x16_bf16 v[80:95], v[196:199], v[172:175], v[80:95]\n"
    "ds_read_b64_tr_b16 v[196:197], v116 offset:1024\nds_read_b64_tr_b16 v[198:199], v116 offset:1280\ns_waitcnt lgkmcnt(10)\nv_pk_maximum3_f16 v138, v144, v145, v146\n"
    "v_pk_maximum3_f16 v139, v147, v148, v149\nv_mfma_f32_32x32x16_bf16 v[96:111], v[200:203], v[160:163], v[96:111]\nds_read_b64_tr_b16 v[200:201], v116 offset:2048\n"
    "ds_read_b64_tr_b16 v[202:203], v116 offset:2304\nv_pk_maximum3_f16 v138, v138, v150, v151\ns_waitcnt lgkmcnt(4)\nv_pk_maximum3_f16 v139, v139, v152, v153\n"
    "v_mfma_f32_32x32x16_bf16 v[96:111], v[204:207], v[164:167], v[96:111]\nds_read_b64_tr_b16 v[204:205], v116 offset:3072\nds_read_b64_tr_b16 v[206:207], v116 offset:3328\n"
    "v_pk_maximum3_f16 v138, v138, v154, v155\nv_pk_maximum3_f16 v139, v139, v156, v157\nv_mfma_f32_32x32x16_bf16 v[96:111], v[208:211], v[168:171], v[96:111]\n"
    "ds_read_b64_tr_b16 v[208:209], v116 offset:32768\nds_read_b64_tr_b16 v[210:211], v116 offset:33024\nv_pk_maximum3_f16 v138, v138, v158, v159\n"
    "v_pk_maximum3_f16 v138, v138, v139, v139\nv_mfma_f32_32x32x16_bf16 v[96:111], v[212:215], v[172:175], v[96:111]\nds_read_b64_tr_b16 v[212:213], v116 offset:33792\n"
    "ds_read_b64_tr_b16 v[214:215], v116 offset:34048\nv_cmp_u_f16_sdwa vcc, v138, v138 src0_sel:WORD_0 src1_sel:WORD_1\ns_cbranch_vccnz .Lxs163\n.Lxr164:\n"
    "s_waitcnt lgkmcnt(8)\nv_mfma_f32_32x32x16_bf16 v[64:79], v[192:195], v[144:147], v[64:79]\nds_read_b64_tr_b16 v[192:193], v116 offset:34816\n"
    "ds_read_b64_tr_b16 v[194:195], v116 offset:35072\nv_add_u32 v117, s51, v26\nv_add_u32 v119, 0x10000, v117\n"
    "v_mfma_f32_32x32x16_bf16 v[64:79], v[196:199], v[148:151], v[64:79]\nds_read_b64_tr_b16 v[196:197], v116 offset:35840\nds_read_b64_tr_b16 v[198:199], v116 offset:36096\n"
    "s_waitcnt lgkmcnt(8)\nv_mfma_f32_32x32x16_bf16 v[64:79], v[200:203], v[152:155], v[64:79]\nds_read_b64_tr_b16 v[200:201], v118 offset:0\n"
    "ds_read_b64_tr_b16 v[202:203], v118 offset:256\ns_barrier\nv_mfma_f32_32x32x16_bf16 v[64:79], v[204:207], v[156:159], v[64:79]\n"
    "ds_read_b64_tr_b16 v[204:205], v118 offset:1024\nds_read_b64_tr_b16 v[206:207], v118 offset:1280\nds_read_b64_tr_b16 v[160:161], v29 offset:4096\n"
    "ds_read_b64_tr_b16 v[162:163], v29 offset:4352\ns_waitcnt lgkmcnt(10)\nv_mfma_f32_32x32x16_bf16 v[80:95], v[208:211], v[144:147], v[80:95]\n"
    "ds_read_b64_tr_b16 v[208:209], v118 offset:2048\nds_read_b64_tr_b16 v[210:211], v118 offset:2304\nds_read_b64_tr_b16 v[164:165], v29 offset:5120\n"
    "ds_read_b64_tr_b16 v[166:167], v29 offset:5376\nv_mfma_f32_32x32x16_bf16 v[80:95], v[212:215], v[148:151], v[80:95]\nds_read_b64_tr_b16 v[212:213], v118 offset:3072\n"
    "ds_read_b64_tr_b16 v[214:215], v118 offset:3328\nds_read_b64_tr_b16 v[168:169], v29 offset:6144\nds_read_b64_tr_b16 v[170:171], v29 offset:6400\ns_waitcnt lgkmcnt(14)\n"
    "v_mfma_f32_32x32x16_bf16 v[80:95], v[192:195], v[152:155], v[80:95]\nds_read_b64_tr_b16 v[192:193], v117 offset:0\nds_read_b64_tr_b16 v[194:195], v117 offset:256\n"
    "ds_read_b64_tr_b16 v[172:173], v29 offset:7168\nds_read_b64_tr_b16 v[174:175], v29 offset:7424\nv_mfma_f32_32x32x16_bf16 v[80:95], v[196:199], v[156:159], v[80:95]\n"
    "ds_read_b64_tr_b16 v[196:197], v117 offset:1024\nds_read_b64_tr_b16 v[198:199], v117 offset:1280\ns_waitcnt lgkmcnt(10)\nv_pk_maximum3_f16 v138, v160, v161, v162\n"
    "v_pk_maximum3_f16 v139, v163, v164, v165\nv_mfma_f32_32x32x16_bf16 v[96:111], v[200:203], v[144:147], v[96:111]\nds_read_b64_tr_b16 v[200:201], v117 offset:2048\n"
    "ds_read_b64_tr_b16 v[202:203], v117 offset:2304\nv_pk_maximum3_f16 v138, v138, v166, v167\ns_waitcnt lgkmcnt(4)\nv_pk_maximum3_f16 v139, v139, v168, v169\n"
    "v_mfma_f32_32x32x16_bf16 v[96:111], v[204:207], v[148:151], v[96:111]\nds_read_b64_tr_b16 v[204:205], v117 offset:3072\nds_read_b64_tr_b16 v[206:207], v117 offset:3328\n"
    "v_pk_maximum3_f16 v138, v138, v170, v171\nv_pk_maximum3_f16 v139, v139, v172, v173\nv_mfma_f32_32x32x16_bf16 v[96:111], v[208:211], v[152:155], v[96:111]\n"
    "ds_read_b64_tr_b16 v[208:209], v117 offset:32768\nds_read_b64_tr_b16 v[210:211], v117 offset:33024\nv_pk_maximum3_f16 v138, v138, v174, v175\n"
    "v_pk_maximum3_f16 v138, v138, v139, v139\nv_mfma_f32_32x32x16_bf16 v[96:111], v[212:215], v[156:159], v[96:111]\nds_read_b64_tr_b16 v[212:213], v117 offset:33792\n"
    "ds_read_b64_tr_b16 v[214:215], v117 offset:34048\nv_cmp_u_f16_sdwa vcc, v138, v138 src0_sel:WORD_0 src1_sel:WORD_1\ns_cbranch_vccnz .Lxs166\n.Lxr167:\n"
    "s_waitcnt lgkmcnt(8)\nv_mfma_f32_32x32x16_bf16 v[64:79], v[192:195], v[160:163], v[64:79]\nds_read_b64_tr_b16 v[192:193], v117 offset:34816\n"
    "ds_read_b64_tr_b16 v[194:195], v117 offset:35072\nv_mfma_f32_32x32x16_bf16 v[64:79], v[196:199], v[164:167], v[64:79]\nds_read_b64_tr_b16 v[196:197], v117 offset:35840\n"
    "ds_read_b64_tr_b16 v[198:199], v117 offset:36096\ns_waitcnt lgkmcnt(8)\nv_mfma_f32_32x32x16_bf16 v[64:79], v[200:203], v[168:171], v[64:79]\n"
    "ds_read_b64_tr_b16 v[200:201], v119 offset:0\nds_read_b64_tr_b16 v[202:203], v119 offset:256\nv_mfma_f32_32x32x16_bf16 v[64:79], v[204:207], v[172:175], v[64:79]\n"
    "ds_read_b64_tr_b16 v[204:205], v119 offset:1024\nds_read_b64_tr_b16 v[206:207], v119 offset:1280\ns_waitcnt lgkmcnt(8)\n"
    "v_mfma_f32_32x32x16_bf16 v[80:95], v[208:211], v[160:163], v[80:95]\nds_read_b64_tr_b16 v[208:209], v119 offset:2048\nds_read_b64_tr_b16 v[210:211], v119 offset:2304\n"
    "s_barrier\nds_read_b32 v120, v36\nds_read_b32 v137, v36 offset:4\nv_mfma_f32_32x32x16_bf16 v[80:95], v[212:215], v[164:167], v[80:95]\n"
    "ds_read_b64_tr_b16 v[212:213], v119 offset:3072\nds_read_b64_tr_b16 v[214:215], v119 offset:3328\ns_waitcnt lgkmcnt(10)\n"
    "v_mfma_f32_32x32x16_bf16 v[80:95], v[192:195], v[168:171], v[80:95]\nv_mfma_f32_32x32x16_bf16 v[80:95], v[196:199], v[172:175], v[80:95]\ns_waitcnt lgkmcnt(2)\n"
    "v_mfma_f32_32x32x16_bf16 v[64:79], v[124:127], v[120:123], v[64:79]\nv_mfma_f32_32x32x16_bf16 v[80:95], v[128:131], v[120:123], v[80:95]\n"
    "v_mfma_f32_32x32x16_bf16 v[96:111], v[200:203], v[160:163], v[96:111]\nv_mfma_f32_32x32x16_bf16 v[96:111], v[204:207], v[164:167], v[96:111]\ns_nop 8\n"
    "v_mul_f32 v64, v64, v137\nv_mul_f32 v65, v65, v137\nv_mul_f32 v66, v66, v137\nv_mul_f32 v67, v67, v137\nv_cvt_pk_bf16_f32 v64, v64, v65\ns_waitcnt lgkmcnt(0)\n"
    "v_mfma_f32_32x32x16_bf16 v[96:111], v[208:211], v[168:171], v[96:111]\nv_cvt_pk_bf16_f32 v65, v66, v67\nds_write_b64 v27, v[64:65] offset:0\nv_mul_f32 v68, v68, v137\n"
    "v_mul_f32 v69, v69, v137\nv_mul_f32 v70, v70, v137\nv_mfma_f32_32x32x16_bf16 v[96:111], v[212:215], v[172:175], v[96:111]\nv_mul_f32 v71, v71, v137\n"
    "v_cvt_pk_bf16_f32 v68, v68, v69\nv_cvt_pk_bf16_f32 v69, v70, v71\nds_write_b64 v27, v[68:69] offset:16\nv_mul_f32 v72, v72, v137\n"
    "v_mfma_f32_32x32x16_bf16 v[96:111], v[132:135], v[120:123], v[96:111]\nv_mul_f32 v73, v73, v137\nv_mul_f32 v74, v74, v137\nv_mul_f32 v75, v75, v137\n"
    "v_cvt_pk_bf16_f32 v72, v72, v73\nv_cvt_pk_bf16_f32 v73, v74, v75\nds_write_b64 v27, v[72:73] offset:32\nv_pk_mul_f32 v[76:77], v[76:77], v[136:137] op_sel:[0,1]\n"
    "v_pk_mul_f32 v[78:79], v[78:79], v[136:137] op_sel:[0,1]\nv_cvt_pk_bf16_f32 v76, v76, v77\nv_cvt_pk_bf16_f32 v77, v78, v79\nds_write_b64 v27, v[76:77] offset:48\n"
    "v_pk_mul_f32 v[80:81], v[80:81], v[136:137] op_sel:[0,1]\nv_pk_mul_f32 v[82:83], v[82:83], v[136:137] op_sel:[0,1]\nv_cvt_pk_bf16_f32 v80, v80, v81\n"
    "v_cvt_pk_bf16_f32 v81, v82, v83\nds_write_b64 v27, v[80:81] offset:64\nv_pk_mul_f32 v[84:85], v[84:85], v[136:137] op_sel:[0,1]\n"
    "v_pk_mul_f32 v[86:87], v[86:87], v[136:137] op_sel:[0,1]\nv_cvt_pk_bf16_f32 v84, v84, v85\nv_cvt_pk_bf16_f32 v85, v86, v87\nds_write_b64 v27, v[84:85] offset:80\n"
    "v_pk_mul_f32 v[88:89], v[88:89], v[136:137] op_sel:[0,1]\nv_pk_mul_f32 v[90:91], v[90:91], v[136:137] op_sel:[0,1]\nv_cvt_pk_bf16_f32 v88, v88, v89\n"
    "v_cvt_pk_bf16_f32 v89, v90, v91\nds_write_b64 v27, v[88:89] offset:96\nv_pk_mul_f32 v[92:93], v[92:93], v[136:137] op_sel:[0,1]\n"
    "v_pk_mul_f32 v[94:95], v[94:95], v[136:137] op_sel:[0,1]\nv_cvt_pk_bf16_f32 v92, v92, v93\nv_cvt_pk_bf16_f32 v93, v94, v95\nds_write_b64 v27, v[92:93] offset:112\n"
    "v_pk_mul_f32 v[96:97], v[96:97], v[136:137] op_sel:[0,1]\nv_pk_mul_f32 v[98:99], v[98:99], v[136:137] op_sel:[0,1]\nv_cvt_pk_bf16_f32 v96, v96, v97\n"
    "v_cvt_pk_bf16_f32 v97, v98, v99\nds_write_b64 v27, v[96:97] offset:128\nv_pk_mul_f32 v[100:101], v[100:101], v[136:137] op_sel:[0,1]\n"
    "v_pk_mul_f32 v[102:103], v[102:103], v[136:137] op_sel:[0,1]\nv_cvt_pk_bf16_f32 v100, v100, v101\nv_cvt_pk_bf16_f32 v101, v102, v103\n"
    "ds_write_b64 v27, v[100:101] offset:144\nv_pk_mul_f32 v[104:105], v[104:105], v[136:137] op_sel:[0,1]\nv_pk_mul_f32 v[106:107], v[106:107], v[136:137] op_sel:[0,1]\n"
    "v_cvt_pk_bf16_f32 v104, v104, v105\nv_cvt_pk_bf16_f32 v105, v106, v107\nds_write_b64 v27, v[104:105] offset:160\n"
    "v_pk_mul_f32 v[108:109], v[108:109], v[136:137] op_sel:[0,1]\nv_pk_mul_f32 v[110:111], v[110:111], v[136:137] op_sel:[0,1]\nv_cvt_pk_bf16_f32 v108, v108, v109\n"
    "v_cvt_pk_bf16_f32 v109, v110, v111\nds_write_b64 v27, v[108:109] offset:176\ns_branch .LGL75\n.Lls34:\ns_sub_u32 s80, s80, 1\ns_cbranch_scc1 .Llo33\ns_mov_b64 s[82:83], 0\n"
    "v_max3_u32 v60, v208, v209, v210\nv_max_u32 v60, v60, v211\nv_cmp_eq_u32 vcc, -1, v60\ns_nop 0\ns_or_b64 s[82:83], s[82:83], vcc\ns_cbranch_vccz .Lag35\n"
    "s_mov_b64 exec, vcc\nglobal_load_dwordx4 v[208:211], v20, s[32:33] offset:-4096 sc1\ns_mov_b64 exec, -1\n.Lag35:\nv_max3_u32 v60, v212, v213, v214\n"
    "v_max_u32 v60, v60, v215\nv_cmp_eq_u32 vcc, -1, v60\ns_nop 0\ns_or_b64 s[82:83], s[82:83], vcc\ns_cbranch_vccz .Lag36\ns_mov_b64 exec, vcc\n"
    "global_load_dwordx4 v[212:215], v22, s[32:33] offset:-4096 sc1\ns_mov_b64 exec, -1\n.Lag36:\nv_max3_u32 v60, v216, v217, v218\nv_max_u32 v60, v60, v219\n"
    "v_cmp_eq_u32 vcc, -1, v60\ns_nop 0\ns_or_b64 s[82:83], s[82:83], vcc\ns_cbranch_vccz .Lag37\ns_mov_b64 exec, vcc\n"
    "global_load_dwordx4 v[216:219], v23, s[32:33] offset:-4096 sc1\ns_mov_b64 exec, -1\n.Lag37:\nv_max3_u32 v60, v220, v221, v222\nv_max_u32 v60, v60, v223\n"
    "v_cmp_eq_u32 vcc, -1, v60\ns_nop 0\ns_or_b64 s[82:83], s[82:83], vcc\ns_cbranch_vccz .Lag38\ns_mov_b64 exec, vcc\n"
    "global_load_dwordx4 v[220:223], v24, s[32:33] offset:-4096 sc1\ns_mov_b64 exec, -1\n.Lag38:\nv_max3_u32 v60, v224, v225, v226\nv_max_u32 v60, v60, v227\n"
    "v_cmp_eq_u32 vcc, -1, v60\ns_nop 0\ns_or_b64 s[82:83], s[82:83], vcc\ns_cbranch_vccz .Lag39\ns_mov_b64 exec, vcc\n"
    "global_load_dwordx4 v[224:227], v25, s[32:33] offset:-4096 sc1\ns_mov_b64 exec, -1\n.Lag39:\nv_max3_u32 v60, v228, v229, v230\nv_max_u32 v60, v60, v231\n"
    "v_cmp_eq_u32 vcc, -1, v60\ns_nop 0\ns_or_b64 s[82:83], s[82:83], vcc\ns_cbranch_vccz .Lag40\ns_mov_b64 exec, vcc\n"
    "global_load_dwordx4 v[228:231], v26, s[32:33] offset:-4096 sc1\ns_mov_b64 exec, -1\n.Lag40:\nv_max3_u32 v60, v232, v233, v234\nv_max_u32 v60, v60, v235\n"
    "v_cmp_eq_u32 vcc, -1, v60\ns_nop 0\ns_or_b64 s[82:83], s[82:83], vcc\ns_cbranch_vccz .Lag41\ns_mov_b64 exec, vcc\n"
    "global_load_dwordx4 v[232:235], v27, s[32:33] offset:-4096 sc1\ns_mov_b64 exec, -1\n.Lag41:\nv_max3_u32 v60, v236, v237, v238\nv_max_u32 v60, v60, v239\n"
    "v_cmp_eq_u32 vcc, -1, v60\ns_nop 0\ns_or_b64 s[82:83], s[82:83], vcc\ns_cbranch_vccz .Lag42\ns_mov_b64 exec, vcc\n"
    "global_load_dwordx4 v[236:239], v28, s[32:33] offset:-4096 sc1\ns_mov_b64 exec, -1\n.Lag42:\nv_max3_u32 v60, v240, v241, v242\nv_max_u32 v60, v60, v243\n"
    "v_cmp_eq_u32 vcc, -1, v60\ns_nop 0\ns_or_b64 s[82:83], s[82:83], vcc\ns_cbranch_vccz .Lag43\ns_mov_b64 exec, vcc\nglobal_load_dwordx4 v[240:243], v21, s[32:33] sc1\n"
    "s_mov_b64 exec, -1\n.Lag43:\ns_cmp_eq_u64 s[82:83], 0\ns_cbranch_scc1 .Llo33\ns_waitcnt vmcnt(0)\ns_branch .Lln32\n.Lxs58:\ns_mov_b32 s80, 0x10000\n.Lxl60:\n"
    "v_pk_max_u16 v62, v196, v197\nv_pk_max_u16 v62, v62, v198\nv_pk_max_u16 v62, v62, v199\nv_pk_max_u16 v62, v62, v200\nv_pk_max_u16 v62, v62, v201\n"
    "v_pk_max_u16 v62, v62, v202\nv_pk_max_u16 v62, v62, v203\nv_pk_max_u16 v62, v62, v204\nv_pk_max_u16 v62, v62, v205\nv_pk_max_u16 v62, v62, v206\n"
    "v_pk_max_u16 v62, v62, v207\nv_pk_max_u16 v62, v62, v208\nv_pk_max_u16 v62, v62, v209\nv_pk_max_u16 v62, v62, v210\nv_pk_max_u16 v62, v62, v211\n"
    "v_pk_max_u16 v62, v62, v62 op_sel:[0,1] op_sel_hi:[1,0]\nv_cmp_eq_u32 vcc, -1, v62\ns_cbranch_vccz .Lxr59\ns_sub_u32 s80, s80, 1\ns_cbranch_scc1 .Lxr59\ns_sleep 2\n"
    "s_add_u32 m0, s65, 1024\ns_nop 0\nglobal_load_lds_dwordx4 v27, s[60:61] offset:0 sc1\ns_add_u32 m0, s65, 0\ns_nop 0\nglobal_load_lds_dwordx4 v27, s[60:61] offset:2048 sc1\n"
    "s_add_u32 m0, s65, 3072\ns_nop 0\nglobal_load_lds_dwordx4 v9, s[60:61] offset:0 sc1\ns_add_u32 m0, s65, 2048\ns_nop 0\n"
    "global_load_lds_dwordx4 v9, s[60:61] offset:2048 sc1\ns_waitcnt vmcnt(0)\nds_read_b64_tr_b16 v[196:197], v28 offset:0\nds_read_b64_tr_b16 v[198:199], v28 offset:128\n"
    "ds_read_b64_tr_b16 v[200:201], v28 offset:1024\nds_read_b64_tr_b16 v[202:203], v28 offset:1152\nds_read_b64_tr_b16 v[204:205], v28 offset:2048\n"
    "ds_read_b64_tr_b16 v[206:207], v28 offset:2176\nds_read_b64_tr_b16 v[208:209], v28 offset:3072\nds_read_b64_tr_b16 v[210:211], v28 offset:3200\ns_waitcnt lgkmcnt(0)\n"
    "s_branch .Lxl60\n.Lps61:\ns_sub_u32 s80, s80, 1\ns_cbranch_scc1 .Lpr62\ns_mov_b64 s[82:83], 0\nv_max3_u32 v229, v212, v213, v214\nv_max_u32 v229, v229, v215\n"
    "v_cmp_eq_u32 vcc, -1, v229\ns_nop 0\ns_or_b64 s[82:83], s[82:83], vcc\ns_cbranch_vccz .Lag64\ns_mov_b64 exec, vcc\n"
    "global_load_dwordx4 v[212:215], v36, s[32:33] offset:0 sc1\ns_mov_b64 exec, -1\n.Lag64:\nv_max3_u32 v229, v216, v217, v218\nv_max_u32 v229, v229, v219\n"
    "v_cmp_eq_u32 vcc, -1, v229\ns_nop 0\ns_or_b64 s[82:83], s[82:83], vcc\ns_cbranch_vccz .Lag65\ns_mov_b64 exec, vcc\n"
    "global_load_dwordx4 v[216:219], v36, s[32:33] offset:1024 sc1\ns_mov_b64 exec, -1\n.Lag65:\ns_cmp_eq_u64 s[82:83], 0\ns_cbranch_scc1 .Lpr62\ns_waitcnt vmcnt(0)\n"
    "s_branch .Lpl63\n.Lps66:\ns_sub_u32 s80, s80, 1\ns_cbranch_scc1 .Lpr67\ns_mov_b64 s[82:83], 0\nv_max3_u32 v233, v220, v221, v222\nv_max_u32 v233, v233, v223\n"
    "v_cmp_eq_u32 vcc, -1, v233\ns_nop 0\ns_or_b64 s[82:83], s[82:83], vcc\ns_cbranch_vccz .Lag69\ns_mov_b64 exec, vcc\n"
    "global_load_dwordx4 v[220:223], v37, s[32:33] offset:0 sc1\ns_mov_b64 exec, -1\n.Lag69:\nv_max3_u32 v233, v224, v225, v226\nv_max_u32 v233, v233, v227\n"
    "v_cmp_eq_u32 vcc, -1, v233\ns_nop 0\ns_or_b64 s[82:83], s[82:83], vcc\ns_cbranch_vccz .Lag70\ns_mov_b64 exec, vcc\n"
    "global_load_dwordx4 v[224:227], v37, s[32:33] offset:1024 sc1\ns_mov_b64 exec, -1\n.Lag70:\ns_cmp_eq_u64 s[82:83], 0\ns_cbranch_scc1 .Lpr67\ns_waitcnt vmcnt(0)\n"
    "s_branch .Lpl68\n.LL83:\ns_sub_u32 s80, s80, 1\ns_cbranch_scc1 .LL82\ns_waitcnt vmcnt(0) lgkmcnt(0)\nv_max3_u32 v31, v208, v209, v210\nv_max_u32 v31, v31, v211\n"
    "v_cmp_eq_u32 vcc, -1, v31\ns_nop 0\ns_or_b64 s[82:83], s[82:83], vcc\ns_cbranch_vccz .Lag84\ns_mov_b64 exec, vcc\n"
    "global_load_dwordx4 v[208:211], v182, s[32:33] offset:-4096 sc1\ns_mov_b64 exec, -1\n.Lag84:\nv_max3_u32 v31, v212, v213, v214\nv_max_u32 v31, v31, v215\n"
    "v_cmp_eq_u32 vcc, -1, v31\ns_nop 0\ns_or_b64 s[82:83], s[82:83], vcc\ns_cbranch_vccz .Lag85\ns_mov_b64 exec, vcc\n"
    "global_load_dwordx4 v[212:215], v182, s[32:33] offset:0 sc1\ns_mov_b64 exec, -1\n.Lag85:\nv_max3_u32 v31, v216, v217, v218\nv_max_u32 v31, v31, v219\n"
    "v_cmp_eq_u32 vcc, -1, v31\ns_nop 0\ns_or_b64 s[82:83], s[82:83], vcc\ns_cbranch_vccz .Lag86\ns_mov_b64 exec, vcc\n"
    "global_load_dwordx4 v[216:219], v183, s[32:33] offset:-4096 sc1\ns_mov_b64 exec, -1\n.Lag86:\nv_max3_u32 v31, v220, v221, v222\nv_max_u32 v31, v31, v223\n"
    "v_cmp_eq_u32 vcc, -1, v31\ns_nop 0\ns_or_b64 s[82:83], s[82:83], vcc\ns_cbranch_vccz .Lag87\ns_mov_b64 exec, vcc\n"
    "global_load_dwordx4 v[220:223], v183, s[32:33] offset:0 sc1\ns_mov_b64 exec, -1\n.Lag87:\nv_max3_u32 v31, v224, v225, v226\nv_max_u32 v31, v31, v227\n"
    "v_cmp_eq_u32 vcc, -1, v31\ns_nop 0\ns_or_b64 s[82:83], s[82:83], vcc\ns_cbranch_vccz .Lag88\ns_mov_b64 exec, vcc\n"
    "global_load_dwordx4 v[224:227], v184, s[32:33] offset:-4096 sc1\ns_mov_b64 exec, -1\n.Lag88:\nv_max3_u32 v31, v228, v229, v230\nv_max_u32 v31, v31, v231\n"
    "v_cmp_eq_u32 vcc, -1, v31\ns_nop 0\ns_or_b64 s[82:83], s[82:83], vcc\ns_cbranch_vccz .Lag89\ns_mov_b64 exec, vcc\n"
    "global_load_dwordx4 v[228:231], v184, s[32:33] offset:0 sc1\ns_mov_b64 exec, -1\n.Lag89:\nv_max3_u32 v31, v232, v233, v234\nv_max_u32 v31, v31, v235\n"
    "v_cmp_eq_u32 vcc, -1, v31\ns_nop 0\ns_or_b64 s[82:83], s[82:83], vcc\ns_cbranch_vccz .Lag90\ns_mov_b64 exec, vcc\n"
    "global_load_dwordx4 v[232:235], v185, s[32:33] offset:-4096 sc1\ns_mov_b64 exec, -1\n.Lag90:\nv_max3_u32 v31, v236, v237, v238\nv_max_u32 v31, v31, v239\n"
    "v_cmp_eq_u32 vcc, -1, v31\ns_nop 0\ns_or_b64 s[82:83], s[82:83], vcc\ns_cbranch_vccz .Lag91\ns_mov_b64 exec, vcc\n"
    "global_load_dwordx4 v[236:239], v185, s[32:33] offset:0 sc1\ns_mov_b64 exec, -1\n.Lag91:\ns_waitcnt vmcnt(0)\ns_branch .LL81\n.LL94:\ns_sub_u32 s80, s80, 1\n"
    "s_cbranch_scc1 .LL93\ns_waitcnt vmcnt(0) lgkmcnt(0)\nv_max3_u32 v31, v216, v217, v218\nv_max_u32 v31, v31, v219\nv_cmp_eq_u32 vcc, -1, v31\ns_nop 0\n"
    "s_or_b64 s[82:83], s[82:83], vcc\ns_cbranch_vccz .Lag95\ns_mov_b64 exec, vcc\nglobal_load_dwordx4 v[216:219], v183, s[32:33] offset:-4096 sc1\ns_mov_b64 exec, -1\n.Lag95:\n"
    "v_max3_u32 v31, v220, v221, v222\nv_max_u32 v31, v31, v223\nv_cmp_eq_u32 vcc, -1, v31\ns_nop 0\ns_or_b64 s[82:83], s[82:83], vcc\ns_cbranch_vccz .Lag96\n"
    "s_mov_b64 exec, vcc\nglobal_load_dwordx4 v[220:223], v183, s[32:33] offset:0 sc1\ns_mov_b64 exec, -1\n.Lag96:\nv_max3_u32 v31, v224, v225, v226\nv_max_u32 v31, v31, v227\n"
    "v_cmp_eq_u32 vcc, -1, v31\ns_nop 0\ns_or_b64 s[82:83], s[82:83], vcc\ns_cbranch_vccz .Lag97\ns_mov_b64 exec, vcc\n"
    "global_load_dwordx4 v[224:227], v184, s[32:33] offset:-4096 sc1\ns_mov_b64 exec, -1\n.Lag97:\nv_max3_u32 v31, v228, v229, v230\nv_max_u32 v31, v31, v231\n"
    "v_cmp_eq_u32 vcc, -1, v31\ns_nop 0\ns_or_b64 s[82:83], s[82:83], vcc\ns_cbranch_vccz .Lag98\ns_mov_b64 exec, vcc\n"
    "global_load_dwordx4 v[228:231], v184, s[32:33] offset:0 sc1\ns_mov_b64 exec, -1\n.Lag98:\nv_max3_u32 v31, v232, v233, v234\nv_max_u32 v31, v31, v235\n"
    "v_cmp_eq_u32 vcc, -1, v31\ns_nop 0\ns_or_b64 s[82:83], s[82:83], vcc\ns_cbranch_vccz .Lag99\ns_mov_b64 exec, vcc\n"
    "global_load_dwordx4 v[232:235], v185, s[32:33] offset:-4096 sc1\ns_mov_b64 exec, -1\n.Lag99:\nv_max3_u32 v31, v236, v237, v238\nv_max_u32 v31, v31, v239\n"
    "v_cmp_eq_u32 vcc, -1, v31\ns_nop 0\ns_or_b64 s[82:83], s[82:83], vcc\ns_cbranch_vccz .Lag100\ns_mov_b64 exec, vcc\n"
    "global_load_dwordx4 v[236:239], v185, s[32:33] offset:0 sc1\ns_mov_b64 exec, -1\n.Lag100:\ns_waitcnt vmcnt(0)\ns_branch .LL92\n.LL103:\ns_sub_u32 s80, s80, 1\n"
    "s_cbranch_scc1 .LL102\ns_waitcnt vmcnt(0) lgkmcnt(0)\nv_max3_u32 v31, v224, v225, v226\nv_max_u32 v31, v31, v227\nv_cmp_eq_u32 vcc, -1, v31\ns_nop 0\n"
    "s_or_b64 s[82:83], s[82:83], vcc\ns_cbranch_vccz .Lag104\ns_mov_b64 exec, vcc\nglobal_load_dwordx4 v[224:227], v184, s[32:33] offset:-4096 sc1\ns_mov_b64 exec, -1\n"
    ".Lag104:\nv_max3_u32 v31, v228, v229, v230\nv_max_u32 v31, v31, v231\nv_cmp_eq_u32 vcc, -1, v31\ns_nop 0\ns_or_b64 s[82:83], s[82:83], vcc\ns_cbranch_vccz .Lag105\n"
    "s_mov_b64 exec, vcc\nglobal_load_dwordx4 v[228:231], v184, s[32:33] offset:0 sc1\ns_mov_b64 exec, -1\n.Lag105:\nv_max3_u32 v31, v232, v233, v234\nv_max_u32 v31, v31, v235\n"
    "v_cmp_eq_u32 vcc, -1, v31\ns_nop 0\ns_or_b64 s[82:83], s[82:83], vcc\ns_cbranch_vccz .Lag106\ns_mov_b64 exec, vcc\n"
    "global_load_dwordx4 v[232:235], v185, s[32:33] offset:-4096 sc1\ns_mov_b64 exec, -1\n.Lag106:\nv_max3_u32 v31, v236, v237, v238\nv_max_u32 v31, v31, v239\n"
    "v_cmp_eq_u32 vcc, -1, v31\ns_nop 0\ns_or_b64 s[82:83], s[82:83], vcc\ns_cbranch_vccz .Lag107\ns_mov_b64 exec, vcc\n"
    "global_load_dwordx4 v[236:239], v185, s[32:33] offset:0 sc1\ns_mov_b64 exec, -1\n.Lag107:\ns_waitcnt vmcnt(0)\ns_branch .LL101\n.LL110:\ns_sub_u32 s80, s80, 1\n"
    "s_cbranch_scc1 .LL109\ns_waitcnt vmcnt(0) lgkmcnt(0)\nv_max3_u32 v31, v232, v233, v234\nv_max_u32 v31, v31, v235\nv_cmp_eq_u32 vcc, -1, v31\ns_nop 0\n"
    "s_or_b64 s[82:83], s[82:83], vcc\ns_cbranch_vccz .Lag111\ns_mov_b64 exec, vcc\nglobal_load_dwordx4 v[232:235], v185, s[32:33] offset:-4096 sc1\ns_mov_b64 exec, -1\n"
    ".Lag111:\nv_max3_u32 v31, v236, v237, v238\nv_max_u32 v31, v31, v239\nv_cmp_eq_u32 vcc, -1, v31\ns_nop 0\ns_or_b64 s[82:83], s[82:83], vcc\ns_cbranch_vccz .Lag112\n"
    "s_mov_b64 exec, vcc\nglobal_load_dwordx4 v[236:239], v185, s[32:33] offset:0 sc1\ns_mov_b64 exec, -1\n.Lag112:\ns_waitcnt vmcnt(0)\ns_branch .LL108\n.Lls120:\n"
    "s_sub_u32 s80, s80, 1\ns_cbranch_scc1 .Llo119\ns_mov_b64 s[82:83], 0\nv_max3_u32 v60, v208, v209, v210\nv_max_u32 v60, v60, v211\nv_cmp_eq_u32 vcc, -1, v60\ns_nop 0\n"
    "s_or_b64 s[82:83], s[82:83], vcc\ns_cbranch_vccz .Lag121\ns_mov_b64 exec, vcc\nglobal_load_dwordx4 v[208:211], v20, s[32:33] offset:-4096 sc1\ns_mov_b64 exec, -1\n"
    ".Lag121:\nv_max3_u32 v60, v212, v213, v214\nv_max_u32 v60, v60, v215\nv_cmp_eq_u32 vcc, -1, v60\ns_nop 0\ns_or_b64 s[82:83], s[82:83], vcc\ns_cbranch_vccz .Lag122\n"
    "s_mov_b64 exec, vcc\nglobal_load_dwordx4 v[212:215], v22, s[32:33] offset:-4096 sc1\ns_mov_b64 exec, -1\n.Lag122:\nv_max3_u32 v60, v216, v217, v218\n"
    "v_max_u32 v60, v60, v219\nv_cmp_eq_u32 vcc, -1, v60\ns_nop 0\ns_or_b64 s[82:83], s[82:83], vcc\ns_cbranch_vccz .Lag123\ns_mov_b64 exec, vcc\n"
    "global_load_dwordx4 v[216:219], v23, s[32:33] offset:-4096 sc1\ns_mov_b64 exec, -1\n.Lag123:\nv_max3_u32 v60, v220, v221, v222\nv_max_u32 v60, v60, v223\n"
    "v_cmp_eq_u32 vcc, -1, v60\ns_nop 0\ns_or_b64 s[82:83], s[82:83], vcc\ns_cbranch_vccz .Lag124\ns_mov_b64 exec, vcc\n"
    "global_load_dwordx4 v[220:223], v24, s[32:33] offset:-4096 sc1\ns_mov_b64 exec, -1\n.Lag124:\nv_max3_u32 v60, v224, v225, v226\nv_max_u32 v60, v60, v227\n"
    "v_cmp_eq_u32 vcc, -1, v60\ns_nop 0\ns_or_b64 s[82:83], s[82:83], vcc\ns_cbranch_vccz .Lag125\ns_mov_b64 exec, vcc\n"
    "global_load_dwordx4 v[224:227], v25, s[32:33] offset:-4096 sc1\ns_mov_b64 exec, -1\n.Lag125:\nv_max3_u32 v60, v228, v229, v230\nv_max_u32 v60, v60, v231\n"
    "v_cmp_eq_u32 vcc, -1, v60\ns_nop 0\ns_or_b64 s[82:83], s[82:83], vcc\ns_cbranch_vccz .Lag126\ns_mov_b64 exec, vcc\n"
    "global_load_dwordx4 v[228:231], v26, s[32:33] offset:-4096 sc1\ns_mov_b64 exec, -1\n.Lag126:\nv_max3_u32 v60, v232, v233, v234\nv_max_u32 v60, v60, v235\n"
    "v_cmp_eq_u32 vcc, -1, v60\ns_nop 0\ns_or_b64 s[82:83], s[82:83], vcc\ns_cbranch_vccz .Lag127\ns_mov_b64 exec, vcc\n"
    "global_load_dwordx4 v[232:235], v27, s[32:33] offset:-4096 sc1\ns_mov_b64 exec, -1\n.Lag127:\nv_max3_u32 v60, v236, v237, v238\nv_max_u32 v60, v60, v239\n"
    "v_cmp_eq_u32 vcc, -1, v60\ns_nop 0\ns_or_b64 s[82:83], s[82:83], vcc\ns_cbranch_vccz .Lag128\ns_mov_b64 exec, vcc\n"
    "global_load_dwordx4 v[236:239], v28, s[32:33] offset:-4096 sc1\ns_mov_b64 exec, -1\n.Lag128:\nv_max3_u32 v60, v240, v241, v242\nv_max_u32 v60, v60, v243\n"
    "v_cmp_eq_u32 vcc, -1, v60\ns_nop 0\ns_or_b64 s[82:83], s[82:83], vcc\ns_cbranch_vccz .Lag129\ns_mov_b64 exec, vcc\nglobal_load_dwordx4 v[240:243], v21, s[32:33] sc1\n"
    "s_mov_b64 exec, -1\n.Lag129:\ns_cmp_eq_u64 s[82:83], 0\ns_cbranch_scc1 .Llo119\ns_waitcnt vmcnt(0)\ns_branch .Lln118\n.Lfs138:\ns_mov_b32 s80, 0x100000\n.Lfl140:\n"
    "s_sleep 1\nds_read_b32 v241, v7\ns_waitcnt lgkmcnt(0)\nv_cmp_gt_i32 vcc, v13, v241\ns_cbranch_vccz .Lfr139\ns_sub_u32 s80, s80, 1\ns_cbranch_scc0 .Lfl140\n"
    "s_branch .Lfr139\n.Lxs145:\ns_mov_b32 s80, 0x10000\n.Lxl147:\nv_pk_max_u16 v138, v144, v145\nv_pk_max_u16 v138, v138, v146\nv_pk_max_u16 v138, v138, v147\n"
    "v_pk_max_u16 v138, v138, v148\nv_pk_max_u16 v138, v138, v149\nv_pk_max_u16 v138, v138, v150\nv_pk_max_u16 v138, v138, v151\nv_pk_max_u16 v138, v138, v152\n"
    "v_pk_max_u16 v138, v138, v153\nv_pk_max_u16 v138, v138, v154\nv_pk_max_u16 v138, v138, v155\nv_pk_max_u16 v138, v138, v156\nv_pk_max_u16 v138, v138, v157\n"
    "v_pk_max_u16 v138, v138, v158\nv_pk_max_u16 v138, v138, v159\nv_pk_max_u16 v138, v138, v138 op_sel:[0,1] op_sel_hi:[1,0]\nv_cmp_eq_u32 vcc, -1, v138\n"
    "s_cbranch_vccz .Lxr146\ns_sub_u32 s80, s80, 1\ns_cbranch_scc1 .Lxr146\ns_sleep 2\ns_add_u32 m0, s87, 0\ns_add_u32 s52, s82, s44\ns_addc_u32 s53, s83, 0\n"
    "global_load_lds_dwordx4 v35, s[52:53] offset:0 sc1\nglobal_load_lds_dwordx4 v35, s[52:53] offset:1024 sc1\nglobal_load_lds_dwordx4 v35, s[52:53] offset:2048 sc1\n"
    "global_load_lds_dwordx4 v35, s[52:53] offset:3072 sc1\ns_waitcnt vmcnt(0)\nds_read_b64_tr_b16 v[144:145], v29 offset:0\nds_read_b64_tr_b16 v[146:147], v29 offset:256\n"
    "ds_read_b64_tr_b16 v[148:149], v29 offset:1024\nds_read_b64_tr_b16 v[150:151], v29 offset:1280\nds_read_b64_tr_b16 v[152:153], v29 offset:2048\n"
    "ds_read_b64_tr_b16 v[154:155], v29 offset:2304\nds_read_b64_tr_b16 v[156:157], v29 offset:3072\nds_read_b64_tr_b16 v[158:159], v29 offset:3328\ns_waitcnt lgkmcnt(0)\n"
    "s_branch .Lxl147\n.Lxs148:\ns_mov_b32 s80, 0x10000\n.Lxl150:\nv_pk_max_u16 v138, v160, v161\nv_pk_max_u16 v138, v138, v162\nv_pk_max_u16 v138, v138, v163\n"
    "v_pk_max_u16 v138, v138, v164\nv_pk_max_u16 v138, v138, v165\nv_pk_max_u16 v138, v138, v166\nv_pk_max_u16 v138, v138, v167\nv_pk_max_u16 v138, v138, v168\n"
    "v_pk_max_u16 v138, v138, v169\nv_pk_max_u16 v138, v138, v170\nv_pk_max_u16 v138, v138, v171\nv_pk_max_u16 v138, v138, v172\nv_pk_max_u16 v138, v138, v173\n"
    "v_pk_max_u16 v138, v138, v174\nv_pk_max_u16 v138, v138, v175\nv_pk_max_u16 v138, v138, v138 op_sel:[0,1] op_sel_hi:[1,0]\nv_cmp_eq_u32 vcc, -1, v138\n"
    "s_cbranch_vccz .Lxr149\ns_sub_u32 s80, s80, 1\ns_cbranch_scc1 .Lxr149\ns_sleep 2\ns_add_u32 m0, s87, 4096\ns_add_u32 s52, s82, s45\ns_addc_u32 s53, s83, 0\n"
    "global_load_lds_dwordx4 v35, s[52:53] offset:0 sc1\nglobal_load_lds_dwordx4 v35, s[52:53] offset:1024 sc1\nglobal_load_lds_dwordx4 v35, s[52:53] offset:2048 sc1\n"
    "global_load_lds_dwordx4 v35, s[52:53] offset:3072 sc1\ns_waitcnt vmcnt(0)\nds_read_b64_tr_b16 v[160:161], v29 offset:4096\nds_read_b64_tr_b16 v[162:163], v29 offset:4352\n"
    "ds_read_b64_tr_b16 v[164:165], v29 offset:5120\nds_read_b64_tr_b16 v[166:167], v29 offset:5376\nds_read_b64_tr_b16 v[168:169], v29 offset:6144\n"
    "ds_read_b64_tr_b16 v[170:171], v29 offset:6400\nds_read_b64_tr_b16 v[172:173], v29 offset:7168\nds_read_b64_tr_b16 v[174:175], v29 offset:7424\ns_waitcnt lgkmcnt(0)\n"
    "s_branch .Lxl150\n.Lxs151:\ns_mov_b32 s80, 0x10000\n.Lxl153:\nv_pk_max_u16 v138, v144, v145\nv_pk_max_u16 v138, v138, v146\nv_pk_max_u16 v138, v138, v147\n"
    "v_pk_max_u16 v138, v138, v148\nv_pk_max_u16 v138, v138, v149\nv_pk_max_u16 v138, v138, v150\nv_pk_max_u16 v138, v138, v151\nv_pk_max_u16 v138, v138, v152\n"
    "v_pk_max_u16 v138, v138, v153\nv_pk_max_u16 v138, v138, v154\nv_pk_max_u16 v138, v138, v155\nv_pk_max_u16 v138, v138, v156\nv_pk_max_u16 v138, v138, v157\n"
    "v_pk_max_u16 v138, v138, v158\nv_pk_max_u16 v138, v138, v159\nv_pk_max_u16 v138, v138, v138 op_sel:[0,1] op_sel_hi:[1,0]\nv_cmp_eq_u32 vcc, -1, v138\n"
    "s_cbranch_vccz .Lxr152\ns_sub_u32 s80, s80, 1\ns_cbranch_scc1 .Lxr152\ns_sleep 2\ns_add_u32 m0, s87, 8192\ns_add_u32 s52, s82, s46\ns_addc_u32 s53, s83, 0\n"
    "global_load_lds_dwordx4 v35, s[52:53] offset:0 sc1\nglobal_load_lds_dwordx4 v35, s[52:53] offset:1024 sc1\nglobal_load_lds_dwordx4 v35, s[52:53] offset:2048 sc1\n"
    "global_load_lds_dwordx4 v35, s[52:53] offset:3072 sc1\ns_waitcnt vmcnt(0)\nds_read_b64_tr_b16 v[144:145], v29 offset:8192\nds_read_b64_tr_b16 v[146:147], v29 offset:8448\n"
    "ds_read_b64_tr_b16 v[148:149], v29 offset:9216\nds_read_b64_tr_b16 v[150:151], v29 offset:9472\nds_read_b64_tr_b16 v[152:153], v29 offset:10240\n"
    "ds_read_b64_tr_b16 v[154:155], v29 offset:10496\nds_read_b64_tr_b16 v[156:157], v29 offset:11264\nds_read_b64_tr_b16 v[158:159], v29 offset:11520\ns_waitcnt lgkmcnt(0)\n"
    "s_branch .Lxl153\n.Lxs154:\ns_mov_b32 s80, 0x10000\n.Lxl156:\nv_pk_max_u16 v138, v160, v161\nv_pk_max_u16 v138, v138, v162\nv_pk_max_u16 v138, v138, v163\n"
    "v_pk_max_u16 v138, v138, v164\nv_pk_max_u16 v138, v138, v165\nv_pk_max_u16 v138, v138, v166\nv_pk_max_u16 v138, v138, v167\nv_pk_max_u16 v138, v138, v168\n"
    "v_pk_max_u16 v138, v138, v169\nv_pk_max_u16 v138, v138, v170\nv_pk_max_u16 v138, v138, v171\nv_pk_max_u16 v138, v138, v172\nv_pk_max_u16 v138, v138, v173\n"
    "v_pk_max_u16 v138, v138, v174\nv_pk_max_u16 v138, v138, v175\nv_pk_max_u16 v138, v138, v138 op_sel:[0,1] op_sel_hi:[1,0]\nv_cmp_eq_u32 vcc, -1, v138\n"
    "s_cbranch_vccz .Lxr155\ns_sub_u32 s80, s80, 1\ns_cbranch_scc1 .Lxr155\ns_sleep 2\ns_add_u32 m0, s87, 0\ns_add_u32 s52, s82, s47\ns_addc_u32 s53, s83, 0\n"
    "global_load_lds_dwordx4 v35, s[52:53] offset:0 sc1\nglobal_load_lds_dwordx4 v35, s[52:53] offset:1024 sc1\nglobal_load_lds_dwordx4 v35, s[52:53] offset:2048 sc1\n"
    "global_load_lds_dwordx4 v35, s[52:53] offset:3072 sc1\ns_waitcnt vmcnt(0)\nds_read_b64_tr_b16 v[160:161], v29 offset:0\nds_read_b64_tr_b16 v[162:163], v29 offset:256\n"
    "ds_read_b64_tr_b16 v[164:165], v29 offset:1024\nds_read_b64_tr_b16 v[166:167], v29 offset:1280\nds_read_b64_tr_b16 v[168:169], v29 offset:2048\n"
    "ds_read_b64_tr_b16 v[170:171], v29 offset:2304\nds_read_b64_tr_b16 v[172:173], v29 offset:3072\nds_read_b64_tr_b16 v[174:175], v29 offset:3328\ns_waitcnt lgkmcnt(0)\n"
    "s_branch .Lxl156\n.Lxs157:\ns_mov_b32 s80, 0x10000\n.Lxl159:\nv_pk_max_u16 v138, v144, v145\nv_pk_max_u16 v138, v138, v146\nv_pk_max_u16 v138, v138, v147\n"
    "v_pk_max_u16 v138, v138, v148\nv_pk_max_u16 v138, v138, v149\nv_pk_max_u16 v138, v138, v150\nv_pk_max_u16 v138, v138, v151\nv_pk_max_u16 v138, v138, v152\n"
    "v_pk_max_u16 v138, v138, v153\nv_pk_max_u16 v138, v138, v154\nv_pk_max_u16 v138, v138, v155\nv_pk_max_u16 v138, v138, v156\nv_pk_max_u16 v138, v138, v157\n"
    "v_pk_max_u16 v138, v138, v158\nv_pk_max_u16 v138, v138, v159\nv_pk_max_u16 v138, v138, v138 op_sel:[0,1] op_sel_hi:[1,0]\nv_cmp_eq_u32 vcc, -1, v138\n"
    "s_cbranch_vccz .Lxr158\ns_sub_u32 s80, s80, 1\ns_cbranch_scc1 .Lxr158\ns_sleep 2\ns_add_u32 m0, s87, 4096\ns_add_u32 s52, s82, s48\ns_addc_u32 s53, s83, 0\n"
    "global_load_lds_dwordx4 v35, s[52:53] offset:0 sc1\nglobal_load_lds_dwordx4 v35, s[52:53] offset:1024 sc1\nglobal_load_lds_dwordx4 v35, s[52:53] offset:2048 sc1\n"
    "global_load_lds_dwordx4 v35, s[52:53] offset:3072 sc1\ns_waitcnt vmcnt(0)\nds_read_b64_tr_b16 v[144:145], v29 offset:4096\nds_read_b64_tr_b16 v[146:147], v29 offset:4352\n"
    "ds_read_b64_tr_b16 v[148:149], v29 offset:5120\nds_read_b64_tr_b16 v[150:151], v29 offset:5376\nds_read_b64_tr_b16 v[152:153], v29 offset:6144\n"
    "ds_read_b64_tr_b16 v[154:155], v29 offset:6400\nds_read_b64_tr_b16 v[156:157], v29 offset:7168\nds_read_b64_tr_b16 v[158:159], v29 offset:7424\ns_waitcnt lgkmcnt(0)\n"
    "s_branch .Lxl159\n.Lxs160:\ns_mov_b32 s80, 0x10000\n.Lxl162:\nv_pk_max_u16 v138, v160, v161\nv_pk_max_u16 v138, v138, v162\nv_pk_max_u16 v138, v138, v163\n"
    "v_pk_max_u16 v138, v138, v164\nv_pk_max_u16 v138, v138, v165\nv_pk_max_u16 v138, v138, v166\nv_pk_max_u16 v138, v138, v167\nv_pk_max_u16 v138, v138, v168\n"
    "v_pk_max_u16 v138, v138, v169\nv_pk_max_u16 v138, v138, v170\nv_pk_max_u16 v138, v138, v171\nv_pk_max_u16 v138, v138, v172\nv_pk_max_u16 v138, v138, v173\n"
    "v_pk_max_u16 v138, v138, v174\nv_pk_max_u16 v138, v138, v175\nv_pk_max_u16 v138, v138, v138 op_sel:[0,1] op_sel_hi:[1,0]\nv_cmp_eq_u32 vcc, -1, v138\n"
    "s_cbranch_vccz .Lxr161\ns_sub_u32 s80, s80, 1\ns_cbranch_scc1 .Lxr161\ns_sleep 2\ns_add_u32 m0, s87, 8192\ns_add_u32 s52, s82, s49\ns_addc_u32 s53, s83, 0\n"
    "global_load_lds_dwordx4 v35, s[52:53] offset:0 sc1\nglobal_load_lds_dwordx4 v35, s[52:53] offset:1024 sc1\nglobal_load_lds_dwordx4 v35, s[52:53] offset:2048 sc1\n"
    "global_load_lds_dwordx4 v35, s[52:53] offset:3072 sc1\ns_waitcnt vmcnt(0)\nds_read_b64_tr_b16 v[160:161], v29 offset:8192\nds_read_b64_tr_b16 v[162:163], v29 offset:8448\n"
    "ds_read_b64_tr_b16 v[164:165], v29 offset:9216\nds_read_b64_tr_b16 v[166:167], v29 offset:9472\nds_read_b64_tr_b16 v[168:169], v29 offset:10240\n"
    "ds_read_b64_tr_b16 v[170:171], v29 offset:10496\nds_read_b64_tr_b16 v[172:173], v29 offset:11264\nds_read_b64_tr_b16 v[174:175], v29 offset:11520\ns_waitcnt lgkmcnt(0)\n"
    "s_branch .Lxl162\n.Lxs163:\ns_mov_b32 s80, 0x10000\n.Lxl165:\nv_pk_max_u16 v138, v144, v145\nv_pk_max_u16 v138, v138, v146\nv_pk_max_u16 v138, v138, v147\n"
    "v_pk_max_u16 v138, v138, v148\nv_pk_max_u16 v138, v138, v149\nv_pk_max_u16 v138, v138, v150\nv_pk_max_u16 v138, v138, v151\nv_pk_max_u16 v138, v138, v152\n"
    "v_pk_max_u16 v138, v138, v153\nv_pk_max_u16 v138, v138, v154\nv_pk_max_u16 v138, v138, v155\nv_pk_max_u16 v138, v138, v156\nv_pk_max_u16 v138, v138, v157\n"
    "v_pk_max_u16 v138, v138, v158\nv_pk_max_u16 v138, v138, v159\nv_pk_max_u16 v138, v138, v138 op_sel:[0,1] op_sel_hi:[1,0]\nv_cmp_eq_u32 vcc, -1, v138\n"
    "s_cbranch_vccz .Lxr164\ns_sub_u32 s80, s80, 1\ns_cbranch_scc1 .Lxr164\ns_sleep 2\ns_add_u32 m0, s87, 0\ns_add_u32 s52, s82, s50\ns_addc_u32 s53, s83, 0\n"
    "global_load_lds_dwordx4 v35, s[52:53] offset:0 sc1\nglobal_load_lds_dwordx4 v35, s[52:53] offset:1024 sc1\nglobal_load_lds_dwordx4 v35, s[52:53] offset:2048 sc1\n"
    "global_load_lds_dwordx4 v35, s[52:53] offset:3072 sc1\ns_waitcnt vmcnt(0)\nds_read_b64_tr_b16 v[144:145], v29 offset:0\nds_read_b64_tr_b16 v[146:147], v29 offset:256\n"
    "ds_read_b64_tr_b16 v[148:149], v29 offset:1024\nds_read_b64_tr_b16 v[150:151], v29 offset:1280\nds_read_b64_tr_b16 v[152:153], v29 offset:2048\n"
    "ds_read_b64_tr_b16 v[154:155], v29 offset:2304\nds_read_b64_tr_b16 v[156:157], v29 offset:3072\nds_read_b64_tr_b16 v[158:159], v29 offset:3328\ns_waitcnt lgkmcnt(0)\n"
    "s_branch .Lxl165\n.Lxs166:\ns_mov_b32 s80, 0x10000\n.Lxl168:\nv_pk_max_u16 v138, v160, v161\nv_pk_max_u16 v138, v138, v162\nv_pk_max_u16 v138, v138, v163\n"
    "v_pk_max_u16 v138, v138, v164\nv_pk_max_u16 v138, v138, v165\nv_pk_max_u16 v138, v138, v166\nv_pk_max_u16 v138, v138, v167\nv_pk_max_u16 v138, v138, v168\n"
    "v_pk_max_u16 v138, v138, v169\nv_pk_max_u16 v138, v138, v170\nv_pk_max_u16 v138, v138, v171\nv_pk_max_u16 v138, v138, v172\nv_pk_max_u16 v138, v138, v173\n"
    "v_pk_max_u16 v138, v138, v174\nv_pk_max_u16 v138, v138, v175\nv_pk_max_u16 v138, v138, v138 op_sel:[0,1] op_sel_hi:[1,0]\nv_cmp_eq_u32 vcc, -1, v138\n"
    "s_cbranch_vccz .Lxr167\ns_sub_u32 s80, s80, 1\ns_cbranch_scc1 .Lxr167\ns_sleep 2\ns_add_u32 m0, s87, 4096\ns_add_u32 s52, s82, s51\ns_addc_u32 s53, s83, 0\n"
    "global_load_lds_dwordx4 v35, s[52:53] offset:0 sc1\nglobal_load_lds_dwordx4 v35, s[52:53] offset:1024 sc1\nglobal_load_lds_dwordx4 v35, s[52:53] offset:2048 sc1\n"
    "global_load_lds_dwordx4 v35, s[52:53] offset:3072 sc1\ns_waitcnt vmcnt(0)\nds_read_b64_tr_b16 v[160:161], v29 offset:4096\nds_read_b64_tr_b16 v[162:163], v29 offset:4352\n"
    "ds_read_b64_tr_b16 v[164:165], v29 offset:5120\nds_read_b64_tr_b16 v[166:167], v29 offset:5376\nds_read_b64_tr_b16 v[168:169], v29 offset:6144\n"
    "ds_read_b64_tr_b16 v[170:171], v29 offset:6400\nds_read_b64_tr_b16 v[172:173], v29 offset:7168\nds_read_b64_tr_b16 v[174:175], v29 offset:7424\ns_waitcnt lgkmcnt(0)\n"
    "s_branch .Lxl168\n.Lend:\n"
    :: "{s[0:1]}"(__builtin_amdgcn_kernarg_segment_ptr()), "{s2}"(blockIdx.x), "{v0}"(threadIdx.x),
       "i"((__attribute__((address_space(3))) char*)smem)
    : "v255", "s101", "vcc", "scc", "memory");
}

extern "C" void kernel_launch(void* const* d_in, const int* in_sizes, int n_in,
                              void* d_out, int out_size, void* d_ws, size_t ws_size,
                              hipStream_t stream) {
  Args a;
  a.x = (const u16*)d_in[0]; a.w = (const u16*)d_in[1]; a.wo = (const u16*)d_in[2];
  a.A_re = (const u16*)d_in[4]; a.A_im = (const u16*)d_in[5]; a.B_re = (const u16*)d_in[6]; a.B_im = (const u16*)d_in[7];
  a.C_re = (const u16*)d_in[8]; a.C_im = (const u16*)d_in[9]; a.log_dt = (const u16*)d_in[10]; a.Dskip = (const u16*)d_in[11];
  a.out = (u16*)d_out; a.ws = (char*)d_ws;
  k_all<<<256, 512, 0, stream>>>(a);
}
```

```cpp
#include <hip/hip_runtime.h>
#include <stdint.h>
typedef unsigned short u16;

struct Args { const u16 *x, *w, *wo, *A_re, *A_im, *B_re, *B_im, *C_re, *C_im, *log_dt, *Dskip; u16* out; char* ws; };

__global__ __launch_bounds__(512, 2) void k_all(const Args ar) {
  __shared__ __attribute__((aligned(128))) char smem[163840];
  asm volatile(
    ".pushsection .rodata\n.p2align 7\n.Ltabs:\n"
    ".short 1400,1400,1400,1400,1400,1400,1400,1400,1400,1400,1400,1400,1400,1400,1400,1400,1400,1400,1400,1400,1400,1400,1400,1400,1400,1400,1400,1400,1400,1400,1400,1400\n"
    ".short 1400,1400,1400,1400,1400,1400,1400,1400,1400,1400,1400,1400,1400,1400,1400,1400,1400,1400,1400,1400,1400,1400,1400,1400,1400,1400,1400,1400,1400,1400,1400,1400\n"
    ".short 1400,1400,1400,1400,1400,1400,1400,1400,1400,1400,1400,1400,1400,1400,1400,1400,1278,1274,1270,1266,1262,1258,1254,1250,1246,1242,1238,1234,1230,1226,1222,1218\n"
    ".short 1214,1210,1206,1202,1198,1194,1190,1186,1182,1178,1174,1170,1166,1162,1158,1154,1150,1146,1142,1138,1134,1130,1126,1122,1118,1114,1110,1106,1102,1098,1094,1090\n"
    ".short 1086,1082,1078,1074,1070,1066,1062,1058,1054,1050,1046,1042,1038,1034,1030,1026,1400,1400,1400,1400,1400,1400,1400,1400,1400,1400,1400,1400,1400,1400,1400,1400\n"
    ".short 1400,1400,1400,1400,1400,1400,1400,1400,1400,1400,1400,1400,1400,1400,1400,1400,1280,1276,1272,1268,1264,1260,1256,1252,1248,1244,1240,1236,1232,1228,1224,1220\n"
    ".short 1216,1212,1208,1204,1200,1196,1192,1188,1184,1180,1176,1172,1168,1164,1160,1156,1152,1148,1144,1140,1136,1132,1128,1124,1120,1116,1112,1108,1104,1100,1096,1092\n"
    ".short 1088,1084,1080,1076,1072,1068,1064,1060,1056,1052,1048,1044,1040,1036,1032,1028,1400,1400,1400,1400,1400,1400,1400,1400,1400,1400,1400,1400,1400,1400,1400,1400\n"
    ".short 1400,1400,1400,1400,1400,1400,1400,1400,1400,1400,1400,1400,1400,1400,1400,1400,1400,1024,1022,1018,1014,1010,1006,1002,998,994,990,986,982,978,974,970\n"
    ".short 966,962,958,954,950,946,942,938,934,930,926,922,918,914,910,906,902,898,894,890,886,882,878,874,870,866,862,858,854,850,846,842\n"
    ".short 838,834,830,826,822,818,814,810,806,802,798,794,790,786,782,778,1400,1400,1400,1400,1400,1400,1400,1400,1400,1400,1400,1400,1400,1400,1400,1400\n"
    ".short 1400,1400,1400,1400,1400,1400,1400,1400,1400,1400,1400,1400,1400,1400,1400,1400,1400,1024,1020,1016,1012,1008,1004,1000,996,992,988,984,980,976,972,968\n"
    ".short 964,960,956,952,948,944,940,936,932,928,924,920,916,912,908,904,900,896,892,888,884,880,876,872,868,864,860,856,852,848,844,840\n"
    ".short 836,832,828,824,820,816,812,808,804,800,796,792,788,784,780,776,1400,1400,1400,1400,1400,1400,1400,1400,1400,1400,1400,1400,1400,1400,1400,1400\n"
    ".short 1400,1400,1400,1400,1400,1400,1400,1400,1400,1400,1400,1400,1400,1400,1400,1400,1400,1400,774,774,774,772,772,772,772,772,770,770,770,768,768,768\n"
    ".short 768,766,764,764,764,762,760,760,760,758,756,756,756,754,752,752,752,750,748,748,748,746,744,744,744,742,740,740,740,738,736,736\n"
    ".short 736,734,732,732,732,730,728,728,728,726,724,724,724,722,720,720,720,718,716,716,716,714,712,712,712,710,708,708,708,706,704,704\n"
    ".short 704,702,700,700,700,698,696,696,696,694,692,692,692,690,688,688,688,686,684,684,684,682,680,680,680,678,676,676,676,674,672,672\n"
    ".short 672,670,668,668,668,666,664,664,664,662,660,660,660,658,656,656,656,654,652,652,652,650,648,648,648,646,644,644,644,642,640,640\n"
    ".short 640,638,636,636,636,634,632,632,632,630,628,628,628,626,624,624,624,622,620,620,620,618,616,616,616,614,612,612,612,610,608,608\n"
    ".short 608,606,604,604,604,602,600,600,600,598,596,596,596,594,592,592,592,590,588,588,588,586,584,584,584,582,580,580,580,578,576,576\n"
    ".short 576,574,572,572,572,570,568,568,568,566,564,564,564,562,560,560,560,558,556,556,556,554,552,552,552,550,548,548,548,546,544,544\n"
    ".short 544,542,540,540,540,538,536,536,536,534,532,532,532,530,528,528,528,526,524,524,524,522,520,520,520,518,516,516,516,514,512,512\n"
    ".short 510,508,506,504,502,500,498,496,494,492,490,488,486,484,482,480,478,476,474,472,470,468,466,464,462,460,458,456,454,452,450,448\n"
    ".short 446,444,442,440,438,436,434,432,430,428,426,424,422,420,418,416,414,412,410,408,406,404,402,400,398,396,394,392,390,388,386,384\n"
    ".short 382,380,378,376,374,372,370,368,366,364,362,360,358,356,354,352,350,348,346,344,342,340,338,336,334,332,330,328,326,324,322,320\n"
    ".short 318,316,314,312,310,308,306,304,302,300,298,296,294,292,290,288,286,284,282,280,278,276,274,272,270,268,266,264,262,260,258,256\n"
    ".short 254,252,250,248,246,244,242,240,238,236,234,232,230,228,226,224,222,220,218,216,214,212,210,208,206,204,202,200,198,196,194,192\n"
    ".short 190,188,186,184,182,180,178,176,174,172,170,168,166,164,162,160,158,156,154,152,150,148,146,144,142,140,138,136,134,132,130,128\n"
    ".short 126,124,122,120,118,116,114,112,110,108,106,104,102,100,98,96,94,92,90,88,86,84,82,80,78,76,74,72,70,68,66,64\n"
    ".short 62,60,58,56,54,52,50,48,46,44,42,40,38,36,34,32,30,28,26,24,22,20,18,16,14,12,10,8,6,4,2,0\n"
    ".short 1400,1400,1400,1400,1400,1400,1400,1400,1400,1400,1400,1400,1400,1400,1400,1400,1400,1400,1400,1400,1400,1400,1400,1400,1400,1400,1400,1400,1400,1400,1400,1400\n"
    ".short 0,2,4,6,16,18,20,22,32,34,36,38,48,50,52,54,64,66,68,70,80,82,84,86,96,98,100,102,112,114,116,118\n"
    ".short 128,130,132,134,144,146,148,150,160,162,164,166,176,178,180,182,192,194,196,198,208,210,212,214,224,226,228,230,240,242,244,246\n"
    ".short 8,10,12,14,24,26,28,30,40,42,44,46,56,58,60,62,72,74,76,78,88,90,92,94,104,106,108,110,120,122,124,126\n"
    ".short 136,138,140,142,152,154,156,158,168,170,172,174,184,186,188,190,200,202,204,206,216,218,220,222,232,234,236,238,248,250,252,254\n"
    ".short 514,516,518,1472,522,524,526,1474,530,532,534,1476,538,540,542,1478,546,548,550,1480,554,556,558,1482,562,564,566,1484,570,572,574,1486\n"
    ".short 578,580,582,1488,586,588,590,1490,594,596,598,1492,602,604,606,1494,610,612,614,1496,618,620,622,1498,626,628,630,1500,634,636,638,1502\n"
    ".short 1504,1504,1504,1504,1506,1506,1506,1506,1508,1508,1508,1508,1510,1510,1510,1510,1512,1512,1512,1512,1514,1514,1514,1514,1516,1516,1516,1516,1518,1518,1518,1518\n"
    ".short 1520,1520,1520,1520,1522,1522,1522,1522,1524,1524,1524,1524,1526,1526,1526,1526,1528,1528,1528,1528,1530,1530,1530,1530,1532,1532,1532,1532,1534,1534,1534,1534\n"
    ".short 512,768,896,1024,520,772,900,1026,528,776,904,1028,536,780,908,1030,544,784,912,1032,552,788,916,1034,560,792,920,1036,568,796,924,1038\n"
    ".short 576,800,928,1040,584,804,932,1042,592,808,936,1044,600,812,940,1046,608,816,944,1048,616,820,948,1050,624,824,952,1052,632,828,956,1054\n"
    ".short 1088,1152,1216,1280,1090,1154,1218,1442,1092,1156,1220,1444,1094,1158,1222,1446,1096,1160,1224,1448,1098,1162,1226,1450,1100,1164,1228,1452,1102,1166,1230,1454\n"
    ".short 1104,1168,1232,1456,1106,1170,1234,1458,1108,1172,1236,1460,1110,1174,1238,1462,1112,1176,1240,1464,1114,1178,1242,1466,1116,1180,1244,1468,1118,1182,1246,1470\n"
    ".short 770,898,1472,1472,774,902,1474,1474,778,906,1476,1476,782,910,1478,1478,786,914,1480,1480,790,918,1482,1482,794,922,1484,1484,798,926,1486,1486\n"
    ".short 802,930,1488,1488,806,934,1490,1490,810,938,1492,1492,814,942,1494,1494,818,946,1496,1496,822,950,1498,1498,826,954,1500,1500,830,958,1502,1502\n"
    ".short 1504,1504,1504,1504,1506,1506,1506,1506,1508,1508,1508,1508,1510,1510,1510,1510,1512,1512,1512,1512,1514,1514,1514,1514,1516,1516,1516,1516,1518,1518,1518,1518\n"
    ".short 1520,1520,1520,1520,1522,1522,1522,1522,1524,1524,1524,1524,1526,1526,1526,1526,1528,1528,1528,1528,1530,1530,1530,1530,1532,1532,1532,1532,1534,1534,1534,1534\n"
    ".short 256,258,260,262,272,274,276,278,288,290,292,294,304,306,308,310,320,322,324,326,336,338,340,342,352,354,356,358,368,370,372,374\n"
    ".short 384,386,388,390,400,402,404,406,416,418,420,422,432,434,436,438,448,450,452,454,464,466,468,470,480,482,484,486,496,498,500,502\n"
    ".short 264,266,268,270,280,282,284,286,296,298,300,302,312,314,316,318,328,330,332,334,344,346,348,350,360,362,364,366,376,378,380,382\n"
    ".short 392,394,396,398,408,410,412,414,424,426,428,430,440,442,444,446,456,458,460,462,472,474,476,478,488,490,492,494,504,506,508,510\n"
    ".short 642,644,646,1472,650,652,654,1474,658,660,662,1476,666,668,670,1478,674,676,678,1480,682,684,686,1482,690,692,694,1484,698,700,702,1486\n"
    ".short 706,708,710,1488,714,716,718,1490,722,724,726,1492,730,732,734,1494,738,740,742,1496,746,748,750,1498,754,756,758,1500,762,764,766,1502\n"
    ".short 1504,1504,1504,1504,1506,1506,1506,1506,1508,1508,1508,1508,1510,1510,1510,1510,1512,1512,1512,1512,1514,1514,1514,1514,1516,1516,1516,1516,1518,1518,1518,1518\n"
    ".short 1520,1520,1520,1520,1522,1522,1522,1522,1524,1524,1524,1524,1526,1526,1526,1526,1528,1528,1528,1528,1530,1530,1530,1530,1532,1532,1532,1532,1534,1534,1534,1534\n"
    ".short 640,832,960,1056,648,836,964,1058,656,840,968,1060,664,844,972,1062,672,848,976,1064,680,852,980,1066,688,856,984,1068,696,860,988,1070\n"
    ".short 704,864,992,1072,712,868,996,1074,720,872,1000,1076,728,876,1004,1078,736,880,1008,1080,744,884,1012,1082,752,888,1016,1084,760,892,1020,1086\n"
    ".short 1120,1184,1248,1440,1122,1186,1250,1442,1124,1188,1252,1444,1126,1190,1254,1446,1128,1192,1256,1448,1130,1194,1258,1450,1132,1196,1260,1452,1134,1198,1262,1454\n"
    ".short 1136,1200,1264,1456,1138,1202,1266,1458,1140,1204,1268,1460,1142,1206,1270,1462,1144,1208,1272,1464,1146,1210,1274,1466,1148,1212,1276,1468,1150,1214,1278,1470\n"
    ".short 834,962,1472,1472,838,966,1474,1474,842,970,1476,1476,846,974,1478,1478,850,978,1480,1480,854,982,1482,1482,858,986,1484,1484,862,990,1486,1486\n"
    ".short 866,994,1488,1488,870,998,1490,1490,874,1002,1492,1492,878,1006,1494,1494,882,1010,1496,1496,886,1014,1498,1498,890,1018,1500,1500,894,1022,1502,1502\n"
    ".short 1504,1504,1504,1504,1506,1506,1506,1506,1508,1508,1508,1508,1510,1510,1510,1510,1512,1512,1512,1512,1514,1514,1514,1514,1516,1516,1516,1516,1518,1518,1518,1518\n"
    ".short 1520,1520,1520,1520,1522,1522,1522,1522,1524,1524,1524,1524,1526,1526,1526,1526,1528,1528,1528,1528,1530,1530,1530,1530,1532,1532,1532,1532,1534,1534,1534,1534\n"
    ".popsection\ns_load_dwordx16 s[8:23], s[0:1], 0\ns_load_dwordx8 s[24:31], s[0:1], 64\ns_load_dwordx2 s[32:33], s[0:1], 96\nv_readfirstlane_b32 s3, v0\ns_nop 0\n"
    "s_cmpk_gt_u32 s3, 255\ns_cbranch_scc1 .LH2\ns_lshr_b32 s3, s3, 6\ns_cmp_lt_u32 s3, 2\ns_cbranch_scc0 .LG1\ns_lshr_b32 s77, s2, 3\ns_cmp_eq_u32 s3, 0\ns_cbranch_scc0 .LL4\n"
    "v_lshl_add_u32 v254, v0, 5, s77\nv_lshlrev_b32 v254, 7, v254\ns_getpc_b64 s[4:5]\ns_add_u32 s4, s4, .Ltabs@rel32@lo+4\ns_addc_u32 s5, s5, .Ltabs@rel32@hi+12\n"
    "s_getpc_b64 vcc\n.Lpc:\ns_sub_u32 vcc_lo, vcc_lo, s4\ns_add_u32 vcc_lo, vcc_lo, .Lend-.Lpc-4\nv_min_u32 v254, vcc_lo, v254\ns_mov_b64 exec, 0xffff\n"
    "global_load_dword v254, v254, s[4:5]\ns_mov_b64 exec, -1\n.LL4:\ns_and_b32 s79, s2, 7\ns_mul_i32 s78, s3, 12288\ns_add_u32 m0, s78, 106496\nv_lshrrev_b32 v252, 4, v0\n"
    "v_xor_b32 v252, v252, v0\nv_and_b32 v252, 2, v252\nv_and_or_b32 v252, v0, 1, v252\nv_lshlrev_b32 v252, 4, v252\nv_lshrrev_b32 v253, 2, v0\n"
    "v_lshl_or_b32 v252, v253, 10, v252\ns_bfe_u32 s78, s2, 0x40003\nv_lshl_or_b32 v252, s78, 15, v252\nv_lshl_or_b32 v252, s79, 7, v252\ns_lshr_b32 s78, s2, 7\n"
    "v_lshl_or_b32 v252, s78, 6, v252\ns_waitcnt lgkmcnt(0)\nglobal_load_lds_dwordx4 v252, s[12:13]\ns_branch .LG1\n.LH2:\ns_bfe_u32 s3, s3, 0x20006\nv_and_b32 v77, 63, v0\n"
    "s_bfe_u32 s86, s2, 0x20003\ns_bfe_u32 s63, s2, 0x20001\ns_lshl2_add_u32 s63, s63, s86\ns_lshr_b32 s87, s2, 5\ns_lshl1_add_u32 s58, s86, s87\ns_and_b32 s58, s58, 7\n"
    "s_mov_b32 s60, s58\ns_and_b32 s86, s2, 1\ns_lshl3_add_u32 s87, s86, s87\ns_lshl_b32 s87, s87, 6\ns_mul_i32 s86, s3, 49152\ns_add_u32 s67, s87, s86\n"
    "s_lshl2_add_u32 s86, s63, s3\ns_lshl_b32 s68, s86, 15\nv_lshrrev_b32 v80, 4, v77\nv_xor_b32 v81, v80, v77\nv_and_b32 v83, 2, v81\nv_and_or_b32 v83, v77, 1, v83\n"
    "v_lshlrev_b32 v83, 4, v83\nv_lshrrev_b32 v82, 2, v77\ns_movk_i32 s86, 0xc00\nv_mad_u32_u24 v76, v82, s86, v83\nv_and_b32 v81, 7, v81\nv_lshlrev_b32 v81, 4, v81\n"
    "v_lshrrev_b32 v83, 3, v77\nv_lshl_or_b32 v72, v83, 10, v81\nv_xor_b32 v73, 0x2040, v72\nv_add_u32 v74, 0x4000, v72\nv_add_u32 v75, 0x4000, v73\ns_mul_i32 s64, s3, 12288\n"
    "s_add_u32 s64, s64, 98304\ns_lshl_b32 s69, s3, 10\ns_waitcnt lgkmcnt(0)\ns_add_u32 s10, s10, s67\ns_addc_u32 s11, s11, 0\ns_add_u32 s70, s8, s68\ns_addc_u32 s71, s9, 0\n"
    "s_mul_i32 s86, s60, 196608\ns_add_u32 s54, s10, s86\ns_addc_u32 s55, s11, 0\ns_lshl_b32 s86, s60, 12\ns_add_u32 m0, s69, s86\ns_add_u32 s60, s60, 1\n"
    "global_load_lds_dwordx4 v76, s[54:55]\ns_add_u32 m0, m0, 31744\ns_and_b32 s60, s60, 7\nglobal_load_lds_dwordx4 v76, s[54:55] offset:1024\ns_add_u32 m0, m0, 31744\ns_nop 0\n"
    "global_load_lds_dwordx4 v76, s[54:55] offset:2048\ns_add_u32 m0, s64, 0\ns_lshl_b32 s86, s58, 7\ns_add_u32 s52, s70, s86\ns_addc_u32 s53, s71, 0\n"
    "global_load_lds_dwordx4 v72, s[52:53]\ns_add_u32 m0, s64, 1024\ns_add_u32 s58, s58, 1\nglobal_load_lds_dwordx4 v73, s[52:53]\ns_add_u32 m0, s64, 2048\n"
    "s_and_b32 s58, s58, 7\nglobal_load_lds_dwordx4 v74, s[52:53]\ns_add_u32 m0, s64, 3072\ns_nop 0\nglobal_load_lds_dwordx4 v75, s[52:53]\ns_setprio 3\ns_getpc_b64 s[4:5]\n"
    "s_add_u32 s4, s4, .Ltabs@rel32@lo+4\ns_addc_u32 s5, s5, .Ltabs@rel32@hi+12\ns_waitcnt lgkmcnt(0)\ns_mov_b32 s86, 16777216\ns_load_dword s34, s[32:33], s86\n"
    "s_mov_b32 s6, 0\ns_mov_b32 s7, 0\ns_and_b32 s35, s2, 7\ns_lshr_b32 s36, s2, 3\ns_bfe_u32 s44, s2, 0x20001\ns_bfe_u32 s45, s2, 0x20003\ns_lshl2_add_u32 s37, s44, s45\n"
    "s_and_b32 s46, s2, 1\ns_lshr_b32 s47, s2, 5\ns_lshl3_add_u32 s38, s46, s47\ns_bfe_u32 s48, s2, 0x30003\ns_lshl3_add_u32 s39, s35, s48\ns_lshr_b32 s40, s2, 6\n"
    "s_lshl_b32 s49, s35, 5\ns_add_u32 s41, s49, s36\ns_lshl1_add_u32 s50, s45, s47\ns_and_b32 s42, s50, 7\ns_lshl_b32 s86, s41, 2\ns_load_dword s96, s[26:27], s86\n"
    "s_load_dword s95, s[28:29], s86\nv_mov_b32 v96, 0\nv_mov_b32 v97, 0xf149f2ca\nv_bfe_u32 v100, v0, 4, 2\nv_lshlrev_b32 v99, 2, v100\nv_and_b32 v102, 7, v0\n"
    "v_add_u32 v101, 1, v102\nv_sub_u32 v98, v99, v101\nv_add_u32 v104, 8, v102\nv_sub_u32 v103, v104, v101\nv_cmp_le_u32 vcc, v98, v103\ns_nop 1\n"
    "v_cndmask_b32 v128, v97, v96, vcc\nv_add_u32 v106, 1, v99\nv_sub_u32 v105, v106, v101\nv_cmp_le_u32 vcc, v105, v103\ns_nop 1\nv_cndmask_b32 v129, v97, v96, vcc\n"
    "v_add_u32 v108, 2, v99\nv_sub_u32 v107, v108, v101\nv_cmp_le_u32 vcc, v107, v103\ns_nop 1\nv_cndmask_b32 v130, v97, v96, vcc\nv_add_u32 v110, 3, v99\n"
    "v_sub_u32 v109, v110, v101\nv_cmp_le_u32 vcc, v109, v103\ns_nop 1\nv_cndmask_b32 v131, v97, v96, vcc\ns_add_u32 s46, s3, 4\ns_mul_i32 s45, s46, 0xd00\n"
    "s_add_u32 s44, s45, 0x18040\nv_lshl_add_u32 v252, v100, 4, s44\nv_and_b32 v112, 15, v0\nv_max_u32 v111, 1, v112\ns_mov_b32 s47, 0x1a0\nv_mad_u32_u24 v252, v111, s47, v252\n"
    "s_add_u32 s48, s45, 0x18d00\nv_lshl_add_u32 v253, v100, 4, s48\nv_mad_u32_u24 v253, v102, s47, v253\nv_and_b32 v113, 3, v0\ns_add_u32 s49, s45, 0x18080\n"
    "v_lshl_add_u32 v254, v113, 3, s49\nv_bfe_u32 v116, v0, 2, 2\nv_add_u32 v115, v99, v116\nv_max_u32 v114, 1, v115\nv_mad_u32_u24 v254, v114, s47, v254\n"
    "v_bfe_u32 v117, v0, 3, 1\ns_lshr_b32 s59, s46, 1\ns_lshl2_add_u32 s57, s37, s59\ns_lshl_b32 s56, s57, 15\ns_lshl_b32 s65, s38, 11\ns_add_u32 s51, s56, s65\n"
    "s_and_b32 s72, s46, 1\ns_lshl_b32 s66, s72, 8\ns_add_u32 s50, s51, s66\nv_lshl_add_u32 v1, v117, 10, s50\nv_bfe_u32 v118, v0, 5, 1\nv_lshl_add_u32 v1, v118, 9, v1\n"
    "v_bfe_u32 v119, v0, 4, 1\nv_lshl_add_u32 v1, v119, 4, v1\nv_lshl_add_u32 v1, v102, 5, v1\nv_mov_b64 v[10:11], 0\nv_mov_b64 v[12:13], 0\nv_mov_b64 v[14:15], 0\n"
    "v_mov_b64 v[16:17], 0\nv_mov_b64 v[18:19], 0\nv_mov_b64 v[20:21], 0\nv_mov_b64 v[22:23], 0\nv_mov_b64 v[24:25], 0\nv_mov_b32 v96, 0x27f70\ns_lshr_b32 s45, s3, 1\n"
    "s_lshl_b32 s44, s45, 6\nv_and_b32 v99, 15, v0\ns_mov_b32 s46, 0x23c00\nv_lshl_add_u32 v98, v99, 10, s46\nv_bfe_u32 v101, v0, 4, 2\nv_xor_b32 v100, v101, v99\n"
    "v_lshl_add_u32 v98, v100, 4, v98\nv_xor_b32 v97, s44, v98\nv_cmp_gt_u32 vcc, 2, v99\ns_nop 1\nv_cndmask_b32 v26, v97, v96, vcc\nv_mov_b32 v102, 0\n"
    "v_cmp_gt_u32 vcc, 2, v99\ns_nop 1\nv_cndmask_b32 v27, -1, v102, vcc\nv_bfe_u32 v104, v0, 2, 2\nv_lshl_add_u32 v103, v101, 3, v104\ns_lshl_b32 s47, s45, 11\n"
    "v_lshl_add_u32 v29, v103, 6, s47\nv_and_b32 v105, 3, v0\nv_lshl_add_u32 v29, v105, 3, v29\nv_xor_b32 v107, s3, v101\nv_and_b32 v106, 1, v107\n"
    "v_lshl_add_u32 v29, v106, 5, v29\ns_add_u32 s49, s45, 1\ns_lshl_b32 s48, s49, 15\nv_add_u32 v28, s48, v29\ns_lshl_b32 s51, s3, 6\ns_add_u32 s50, s51, 0x27c80\n"
    "v_lshl_add_u32 v30, v101, 4, s50\ns_and_b32 s59, s3, 1\ns_lshl_b32 s57, s59, 6\ns_add_u32 s56, s57, 0x27c00\nv_lshl_add_u32 v31, v101, 4, s56\ns_lshl_b32 s66, s3, 7\n"
    "s_add_u32 s65, s66, 0x24000\nv_lshl_add_u32 v32, v99, 3, s65\ns_mov_b32 s72, 0x24000\nv_lshl_add_u32 v33, v99, 3, s72\ns_lshl_b32 s74, s3, 5\ns_add_u32 s73, s74, 0x18040\n"
    "v_lshl_add_u32 v34, v101, 3, s73\ns_mov_b32 s75, 0xd0\nv_mad_u32_u24 v34, v99, s75, v34\nv_and_b32 v108, 0xff, v0\nv_lshlrev_b32 v35, 4, v108\n"
    "s_lshl2_add_u32 s78, s40, s3\ns_lshl_b32 s77, s78, 9\ns_add_u32 s76, s77, 0x1002000\nv_lshl_add_u32 v36, v105, 7, s76\nv_and_b32 v109, 4, v0\n"
    "v_lshl_add_u32 v36, v109, 2, v36\nv_and_b32 v110, 63, v0\ns_lshl2_add_u32 s81, s37, s3\ns_lshl_b32 s80, s81, 15\ns_lshl_b32 s82, s38, 11\ns_add_u32 s79, s80, s82\n"
    "v_lshl_add_u32 v37, v110, 4, s79\ns_lshl_b32 s84, s78, 15\ns_add_u32 s83, s84, 0xe01000\nv_lshl_add_u32 v58, v110, 4, s83\ns_and_b32 s45, s38, 3\n"
    "s_lshl2_add_u32 s49, s37, s3\ns_lshl_b32 s48, s49, 9\ns_add_u32 s47, s48, 0x1008000\ns_lshr_b32 s51, s38, 2\ns_lshl_b32 s50, s51, 7\ns_add_u32 s46, s47, s50\n"
    "s_lshl2_add_u32 s44, s45, s46\nv_mov_b32 v60, s44\nv_mov_b32 v96, 0x27f70\ns_lshl_b32 s56, s3, 8\nv_and_b32 v99, 15, v0\ns_mov_b32 s57, 0x23c00\n"
    "v_lshl_add_u32 v98, v99, 10, s57\nv_bfe_u32 v101, v0, 4, 2\nv_xor_b32 v100, v101, v99\nv_lshl_add_u32 v98, v100, 4, v98\nv_xor_b32 v97, s56, v98\nv_cmp_gt_u32 vcc, 2, v99\n"
    "s_nop 1\nv_cndmask_b32 v40, v97, v96, vcc\nv_and_b32 v102, 63, v0\ns_lshl_b32 s59, s41, 9\nv_lshl_add_u32 v41, v102, 4, s59\ns_lshl_b32 s72, s41, 13\n"
    "s_lshl_b32 s73, s3, 11\ns_add_u32 s66, s72, s73\ns_add_u32 s65, s66, 0x200000\nv_lshl_add_u32 v42, v102, 4, s65\nv_and_b32 v103, 3, v0\ns_lshl_b32 s76, s2, 8\n"
    "s_lshl_b32 s77, s3, 6\ns_add_u32 s75, s76, s77\ns_add_u32 s74, s75, 0x400000\nv_lshl_add_u32 v43, v103, 4, s74\ns_and_b32 s80, s2, 3\ns_lshr_b32 s83, s2, 2\n"
    "s_lshl_b32 s82, s83, 7\ns_add_u32 s81, s82, 0x1004000\ns_lshl4_add_u32 s79, s80, s81\ns_lshl2_add_u32 s78, s3, s79\nv_mov_b32 v2, s78\ns_mul_i32 s85, s3, 0x1760\n"
    "s_add_u32 s84, s85, 0x18000\nv_lshl_add_u32 v3, v102, 3, s84\nv_and_b32 v96, 63, v0\ns_lshl2_add_u32 s46, s40, s3\ns_lshl_b32 s45, s46, 15\ns_add_u32 s44, s45, 0xe05000\n"
    "v_lshl_add_u32 v4, v96, 4, s44\ns_add_u32 s47, s45, 0xe07000\nv_lshl_add_u32 v5, v96, 4, s47\nv_and_b32 v97, 31, v0\ns_lshl_b32 s50, s39, 15\ns_lshl_b32 s51, s40, 8\n"
    "s_add_u32 s49, s50, s51\ns_lshl_b32 s56, s3, 6\ns_add_u32 s48, s49, s56\nv_lshl_add_u32 v6, v97, 10, s48\nv_bfe_u32 v98, v0, 5, 1\nv_lshl_add_u32 v6, v98, 4, v6\n"
    "s_add_u32 s57, s45, 0xe03000\nv_lshl_add_u32 v59, v96, 4, s57\ns_lshr_b32 s66, s3, 1\ns_lshl_b32 s65, s66, 8\ns_add_u32 s59, s65, 0x20000\nv_lshl_add_u32 v62, v96, 2, s59\n"
    "s_lshl2_add_u32 s72, s66, 0x27d90\nv_mov_b32 v7, s72\ns_mul_i32 s74, s66, 0x600\ns_add_u32 s73, s74, 0x1ed78\nv_mov_b32 v8, s73\ns_lshl_b32 s86, s63, 17\n"
    "s_sub_u32 s86, s86, 0x4000\ns_max_i32 s86, s86, 0\ns_lshl_b32 s73, s3, 2\ns_max_u32 s87, s73, 2\ns_add_u32 s73, s73, 4\ns_lshl_b32 s72, s87, 10\ns_add_u32 s86, s86, s72\n"
    "s_add_u32 s52, s8, s86\ns_addc_u32 s53, s9, 0\ns_add_u32 s72, s72, 146432\n.LL5:\ns_mov_b32 m0, s72\nv_xor_b32 v82, s87, v77\nv_lshlrev_b32 v82, 4, v82\n"
    "global_load_lds_dwordx4 v82, s[52:53]\ns_add_u32 s52, s52, 1024\ns_addc_u32 s53, s53, 0\ns_add_u32 s72, s72, 1024\ns_add_u32 s87, s87, 1\ns_cmp_lg_u32 s87, s73\n"
    "s_cbranch_scc1 .LL5\ns_mul_i32 s86, s60, 196608\ns_add_u32 s54, s10, s86\ns_addc_u32 s55, s11, 0\ns_lshl_b32 s86, s60, 12\ns_add_u32 m0, s69, s86\ns_add_u32 s60, s60, 1\n"
    "global_load_lds_dwordx4 v76, s[54:55]\ns_add_u32 m0, m0, 31744\ns_and_b32 s60, s60, 7\nglobal_load_lds_dwordx4 v76, s[54:55] offset:1024\ns_add_u32 m0, m0, 31744\ns_nop 0\n"
    "global_load_lds_dwordx4 v76, s[54:55] offset:2048\ns_add_u32 m0, s64, 4096\ns_lshl_b32 s86, s58, 7\ns_add_u32 s52, s70, s86\ns_addc_u32 s53, s71, 0\n"
    "global_load_lds_dwordx4 v72, s[52:53]\ns_add_u32 m0, s64, 5120\ns_add_u32 s58, s58, 1\nglobal_load_lds_dwordx4 v73, s[52:53]\ns_add_u32 m0, s64, 6144\n"
    "s_and_b32 s58, s58, 7\nglobal_load_lds_dwordx4 v74, s[52:53]\ns_add_u32 m0, s64, 7168\ns_nop 0\nglobal_load_lds_dwordx4 v75, s[52:53]\nv_mov_b32 v232, -1\n"
    "v_mov_b32 v233, -1\nv_mov_b32 v234, -1\nv_mov_b32 v235, -1\nv_bitop3_b32 v84, 64, v27, v26 bitop3:0x6a\ns_mov_b32 s61, s42\ns_mov_b32 s62, s42\ns_movk_i32 s86, 128\n"
    "v_bitop3_b32 v85, 64, v27, v40 bitop3:0x6a\nv_bitop3_b32 v86, s86, v27, v40 bitop3:0x6a\nv_bitop3_b32 v87, s86, v27, v85 bitop3:0x6a\ns_lshl_b32 s74, s3, 9\n"
    "s_and_b32 s75, s74, 512\ns_add_u32 s75, s75, 133120\ns_add_u32 s74, s74, 131072\ns_bitcmp1_b32 s3, 0\ns_cselect_b64 s[76:77], s[16:17], s[14:15]\n"
    "s_cselect_b64 s[72:73], s[20:21], s[18:19]\ns_cselect_b64 s[78:79], s[24:25], s[22:23]\ns_bitcmp1_b32 s3, 1\ns_cselect_b64 s[76:77], s[72:73], s[76:77]\n"
    "s_waitcnt lgkmcnt(0)\ns_lshl_b32 s97, s96, 16\ns_and_b32 s96, s96, 0xffff0000\ns_cmp_lt_u32 s3, 2\ns_cselect_b32 s96, s97, s96\ns_mov_b32 s97, 0x3fb8aa3b\n"
    "v_mov_b32 v108, s96\nv_mul_f32 v109, s97, v108\nv_fma_f32 v110, s97, v108, -v109\nv_exp_f32 v109, v109\nv_fmac_f32 v110, 0x32a57060, v108\n"
    "v_mul_f32 v110, 0x3f317218, v110\nv_fmac_f32 v109, v109, v110\nv_cvt_pk_bf16_f32 v109, 0, v109\nv_mul_f32 v108, s97, v109\nv_mul_f32 v110, 0xbe22f983, v109\n"
    "v_readfirstlane_b32 s92, v109\nv_readfirstlane_b32 s93, v108\nv_readfirstlane_b32 s94, v110\ns_and_b32 s97, s95, 0xffff0000\ns_lshl_b32 s96, s95, 16\n"
    "s_lshl_b32 s87, s61, 12\ns_lshl_b32 s86, s61, 7\nv_add_u32 v120, s87, v28\nv_add_u32 v122, s87, v29\ns_add_u32 s61, s61, 1\nv_xor_b32 v121, 0x800, v120\n"
    "s_and_b32 s61, s61, 7\nv_bitop3_b32 v88, s86, v27, v26 bitop3:0x6a\nv_bitop3_b32 v89, s86, v27, v84 bitop3:0x6a\ns_waitcnt vmcnt(7) lgkmcnt(0)\ns_barrier\ns_setprio 0\n"
    "ds_read_b128 v[132:135], v88\nds_read_b128 v[136:139], v89\nds_read_b64_tr_b16 v[96:97], v120\nds_read_b64_tr_b16 v[98:99], v120 offset:256\n"
    "ds_read_b64_tr_b16 v[100:101], v121\nds_read_b64_tr_b16 v[102:103], v121 offset:256\nds_read_b64_tr_b16 v[104:105], v122\nds_read_b64_tr_b16 v[106:107], v122 offset:256\n"
    "s_setprio 0\ns_mul_i32 s86, s60, 196608\ns_add_u32 s54, s10, s86\ns_addc_u32 s55, s11, 0\ns_lshl_b32 s86, s60, 12\ns_add_u32 m0, s69, s86\ns_add_u32 s60, s60, 1\n"
    "global_load_lds_dwordx4 v76, s[54:55]\ns_add_u32 m0, m0, 31744\ns_and_b32 s60, s60, 7\nglobal_load_lds_dwordx4 v76, s[54:55] offset:1024\ns_add_u32 m0, m0, 31744\ns_nop 0\n"
    "global_load_lds_dwordx4 v76, s[54:55] offset:2048\ns_add_u32 m0, s64, 8192\ns_lshl_b32 s86, s58, 7\ns_add_u32 s52, s70, s86\ns_addc_u32 s53, s71, 0\n"
    "global_load_lds_dwordx4 v72, s[52:53]\ns_add_u32 m0, s64, 9216\ns_add_u32 s58, s58, 1\nglobal_load_lds_dwordx4 v73, s[52:53]\ns_add_u32 m0, s64, 10240\n"
    "s_and_b32 s58, s58, 7\nglobal_load_lds_dwordx4 v74, s[52:53]\ns_add_u32 m0, s64, 11264\ns_nop 0\nglobal_load_lds_dwordx4 v75, s[52:53]\ns_setprio 0\n"
    "s_lshl_b32 s87, s61, 12\ns_lshl_b32 s86, s61, 7\nv_add_u32 v120, s87, v28\nv_add_u32 v122, s87, v29\ns_add_u32 s61, s61, 1\nv_xor_b32 v121, 0x800, v120\n"
    "s_and_b32 s61, s61, 7\nv_bitop3_b32 v88, s86, v27, v26 bitop3:0x6a\nv_bitop3_b32 v89, s86, v27, v84 bitop3:0x6a\ns_waitcnt vmcnt(7) lgkmcnt(0)\ns_barrier\ns_setprio 2\n"
    "v_mfma_f32_16x16x32_bf16 v[64:67], v[96:99], v[132:135], 0\nv_mfma_f32_16x16x32_bf16 v[68:71], v[104:107], v[132:135], 0\n"
    "v_mfma_f32_16x16x32_bf16 v[64:67], v[100:103], v[136:139], v[64:67]\ns_setprio 0\nds_read_b128 v[132:135], v88\nds_read_b128 v[136:139], v89\n"
    "ds_read_b64_tr_b16 v[96:97], v120\nds_read_b64_tr_b16 v[98:99], v120 offset:256\nds_read_b64_tr_b16 v[100:101], v121\nds_read_b64_tr_b16 v[102:103], v121 offset:256\n"
    "ds_read_b64_tr_b16 v[104:105], v122\nds_read_b64_tr_b16 v[106:107], v122 offset:256\ns_setprio 0\ns_mul_i32 s86, s60, 196608\ns_add_u32 s54, s10, s86\n"
    "s_addc_u32 s55, s11, 0\ns_lshl_b32 s86, s60, 12\ns_add_u32 m0, s69, s86\ns_add_u32 s60, s60, 1\nglobal_load_lds_dwordx4 v76, s[54:55]\ns_add_u32 m0, m0, 31744\n"
    "s_and_b32 s60, s60, 7\nglobal_load_lds_dwordx4 v76, s[54:55] offset:1024\ns_add_u32 m0, m0, 31744\ns_nop 0\nglobal_load_lds_dwordx4 v76, s[54:55] offset:2048\n"
    "s_add_u32 m0, s64, 0\ns_lshl_b32 s86, s58, 7\ns_add_u32 s52, s70, s86\ns_addc_u32 s53, s71, 0\nglobal_load_lds_dwordx4 v72, s[52:53]\ns_add_u32 m0, s64, 1024\n"
    "s_add_u32 s58, s58, 1\nglobal_load_lds_dwordx4 v73, s[52:53]\ns_add_u32 m0, s64, 2048\ns_and_b32 s58, s58, 7\nglobal_load_lds_dwordx4 v74, s[52:53]\n"
    "s_add_u32 m0, s64, 3072\ns_nop 0\nglobal_load_lds_dwordx4 v75, s[52:53]\ns_setprio 0\nds_read_b128 v[196:199], v40\nds_read_b128 v[200:203], v85\n"
    "ds_read_b128 v[204:207], v86\nds_read_b128 v[208:211], v87\ns_lshl_b32 s87, s61, 12\ns_lshl_b32 s86, s61, 7\nv_add_u32 v120, s87, v28\nv_add_u32 v122, s87, v29\n"
    "s_add_u32 s61, s61, 1\nv_xor_b32 v121, 0x800, v120\ns_and_b32 s61, s61, 7\nv_bitop3_b32 v88, s86, v27, v26 bitop3:0x6a\nv_bitop3_b32 v89, s86, v27, v84 bitop3:0x6a\n"
    "s_waitcnt vmcnt(7) lgkmcnt(0)\ns_barrier\ns_setprio 2\nv_mfma_f32_16x16x32_bf16 v[64:67], v[96:99], v[132:135], v[64:67]\n"
    "v_mfma_f32_16x16x32_bf16 v[68:71], v[104:107], v[132:135], v[68:71]\nv_mfma_f32_16x16x32_bf16 v[64:67], v[100:103], v[136:139], v[64:67]\n"
    "v_mfma_f32_16x16x32_bf16 v[92:95], v[196:199], v[196:199], 0\nv_mfma_f32_16x16x32_bf16 v[92:95], v[200:203], v[200:203], v[92:95]\n"
    "v_mfma_f32_16x16x32_bf16 v[92:95], v[204:207], v[204:207], v[92:95]\nv_mfma_f32_16x16x32_bf16 v[92:95], v[208:211], v[208:211], v[92:95]\ns_setprio 0\n"
    "ds_read_b128 v[132:135], v88\nds_read_b128 v[136:139], v89\nds_read_b64_tr_b16 v[96:97], v120\nds_read_b64_tr_b16 v[98:99], v120 offset:256\n"
    "ds_read_b64_tr_b16 v[100:101], v121\nds_read_b64_tr_b16 v[102:103], v121 offset:256\nds_read_b64_tr_b16 v[104:105], v122\nds_read_b64_tr_b16 v[106:107], v122 offset:256\n"
    "s_setprio 0\ns_mul_i32 s86, s60, 196608\ns_add_u32 s54, s10, s86\ns_addc_u32 s55, s11, 0\ns_lshl_b32 s86, s60, 12\ns_add_u32 m0, s69, s86\ns_add_u32 s60, s60, 1\n"
    "global_load_lds_dwordx4 v76, s[54:55]\ns_add_u32 m0, m0, 31744\ns_and_b32 s60, s60, 7\nglobal_load_lds_dwordx4 v76, s[54:55] offset:1024\ns_add_u32 m0, m0, 31744\ns_nop 0\n"
    "global_load_lds_dwordx4 v76, s[54:55] offset:2048\ns_add_u32 m0, s64, 4096\ns_lshl_b32 s86, s58, 7\ns_add_u32 s52, s70, s86\ns_addc_u32 s53, s71, 0\n"
    "global_load_lds_dwordx4 v72, s[52:53]\ns_add_u32 m0, s64, 5120\ns_add_u32 s58, s58, 1\nglobal_load_lds_dwordx4 v73, s[52:53]\ns_add_u32 m0, s64, 6144\n"
    "s_and_b32 s58, s58, 7\nglobal_load_lds_dwordx4 v74, s[52:53]\ns_add_u32 m0, s64, 7168\ns_nop 0\nglobal_load_lds_dwordx4 v75, s[52:53]\ns_setprio 0\n"
    "s_lshl_b32 s87, s61, 12\ns_lshl_b32 s86, s61, 7\nv_add_u32 v120, s87, v28\nv_add_u32 v122, s87, v29\ns_add_u32 s61, s61, 1\nv_xor_b32 v121, 0x800, v120\n"
    "s_and_b32 s61, s61, 7\nv_bitop3_b32 v88, s86, v27, v26 bitop3:0x6a\nv_bitop3_b32 v89, s86, v27, v84 bitop3:0x6a\ns_waitcnt vmcnt(7) lgkmcnt(0)\ns_barrier\ns_setprio 2\n"
    "v_mfma_f32_16x16x32_bf16 v[64:67], v[96:99], v[132:135], v[64:67]\nv_mfma_f32_16x16x32_bf16 v[68:71], v[104:107], v[132:135], v[68:71]\n"
    "v_mfma_f32_16x16x32_bf16 v[64:67], v[100:103], v[136:139], v[64:67]\ns_setprio 0\nds_read_b128 v[132:135], v88\nds_read_b128 v[136:139], v89\n"
    "ds_read_b64_tr_b16 v[96:97], v120\nds_read_b64_tr_b16 v[98:99], v120 offset:256\nds_read_b64_tr_b16 v[100:101], v121\nds_read_b64_tr_b16 v[102:103], v121 offset:256\n"
    "ds_read_b64_tr_b16 v[104:105], v122\nds_read_b64_tr_b16 v[106:107], v122 offset:256\ns_setprio 0\ns_mul_i32 s86, s60, 196608\ns_add_u32 s54, s10, s86\n"
    "s_addc_u32 s55, s11, 0\ns_lshl_b32 s86, s60, 12\ns_add_u32 m0, s69, s86\ns_add_u32 s60, s60, 1\nglobal_load_lds_dwordx4 v76, s[54:55]\ns_add_u32 m0, m0, 31744\n"
    "s_and_b32 s60, s60, 7\nglobal_load_lds_dwordx4 v76, s[54:55] offset:1024\ns_add_u32 m0, m0, 31744\ns_nop 0\nglobal_load_lds_dwordx4 v76, s[54:55] offset:2048\n"
    "s_add_u32 m0, s64, 8192\ns_lshl_b32 s86, s58, 7\ns_add_u32 s52, s70, s86\ns_addc_u32 s53, s71, 0\nglobal_load_lds_dwordx4 v72, s[52:53]\ns_add_u32 m0, s64, 9216\n"
    "s_add_u32 s58, s58, 1\nglobal_load_lds_dwordx4 v73, s[52:53]\ns_add_u32 m0, s64, 10240\ns_and_b32 s58, s58, 7\nglobal_load_lds_dwordx4 v74, s[52:53]\n"
    "s_add_u32 m0, s64, 11264\ns_nop 0\nglobal_load_lds_dwordx4 v75, s[52:53]\ns_setprio 0\ns_lshl_b32 s87, s61, 12\ns_lshl_b32 s86, s61, 7\nv_add_u32 v120, s87, v28\n"
    "v_add_u32 v122, s87, v29\ns_add_u32 s61, s61, 1\nv_xor_b32 v121, 0x800, v120\ns_and_b32 s61, s61, 7\nv_bitop3_b32 v88, s86, v27, v26 bitop3:0x6a\n"
    "v_bitop3_b32 v89, s86, v27, v84 bitop3:0x6a\ns_waitcnt vmcnt(7) lgkmcnt(0)\ns_barrier\ns_setprio 2\nv_mfma_f32_16x16x32_bf16 v[64:67], v[96:99], v[132:135], v[64:67]\n"
    "v_mfma_f32_16x16x32_bf16 v[68:71], v[104:107], v[132:135], v[68:71]\nv_mfma_f32_16x16x32_bf16 v[64:67], v[100:103], v[136:139], v[64:67]\ns_setprio 0\n"
    "ds_read_b128 v[132:135], v88\nds_read_b128 v[136:139], v89\nds_read_b64_tr_b16 v[96:97], v120\nds_read_b64_tr_b16 v[98:99], v120 offset:256\n"
    "ds_read_b64_tr_b16 v[100:101], v121\nds_read_b64_tr_b16 v[102:103], v121 offset:256\nds_read_b64_tr_b16 v[104:105], v122\nds_read_b64_tr_b16 v[106:107], v122 offset:256\n"
    "s_setprio 0\ns_mul_i32 s86, s60, 196608\ns_add_u32 s54, s10, s86\ns_addc_u32 s55, s11, 0\ns_lshl_b32 s86, s60, 12\ns_add_u32 m0, s69, s86\ns_add_u32 s60, s60, 1\n"
    "global_load_lds_dwordx4 v76, s[54:55]\ns_add_u32 m0, m0, 31744\ns_and_b32 s60, s60, 7\nglobal_load_lds_dwordx4 v76, s[54:55] offset:1024\ns_add_u32 m0, m0, 31744\ns_nop 0\n"
    "global_load_lds_dwordx4 v76, s[54:55] offset:2048\ns_add_u32 m0, s64, 0\ns_lshl_b32 s86, s58, 7\ns_add_u32 s52, s70, s86\ns_addc_u32 s53, s71, 0\n"
    "global_load_lds_dwordx4 v72, s[52:53]\ns_add_u32 m0, s64, 1024\ns_add_u32 s58, s58, 1\nglobal_load_lds_dwordx4 v73, s[52:53]\ns_add_u32 m0, s64, 2048\n"
    "s_and_b32 s58, s58, 7\nglobal_load_lds_dwordx4 v74, s[52:53]\ns_add_u32 m0, s64, 3072\ns_nop 0\nglobal_load_lds_dwordx4 v75, s[52:53]\ns_setprio 0\n"
    "s_lshl_b32 s87, s61, 12\ns_lshl_b32 s86, s61, 7\nv_add_u32 v120, s87, v28\nv_add_u32 v122, s87, v29\ns_add_u32 s61, s61, 1\nv_xor_b32 v121, 0x800, v120\n"
    "s_and_b32 s61, s61, 7\nv_bitop3_b32 v88, s86, v27, v26 bitop3:0x6a\nv_bitop3_b32 v89, s86, v27, v84 bitop3:0x6a\ns_waitcnt vmcnt(7) lgkmcnt(0)\ns_barrier\ns_setprio 2\n"
    "v_mfma_f32_16x16x32_bf16 v[64:67], v[96:99], v[132:135], v[64:67]\nv_mfma_f32_16x16x32_bf16 v[68:71], v[104:107], v[132:135], v[68:71]\n"
    "v_mfma_f32_16x16x32_bf16 v[64:67], v[100:103], v[136:139], v[64:67]\ns_setprio 0\nds_read_b128 v[132:135], v88\nds_read_b128 v[136:139], v89\n"
    "ds_read_b64_tr_b16 v[96:97], v120\nds_read_b64_tr_b16 v[98:99], v120 offset:256\nds_read_b64_tr_b16 v[100:101], v121\nds_read_b64_tr_b16 v[102:103], v121 offset:256\n"
    "ds_read_b64_tr_b16 v[104:105], v122\nds_read_b64_tr_b16 v[106:107], v122 offset:256\ns_setprio 0\ns_mul_i32 s86, s60, 196608\ns_add_u32 s54, s10, s86\n"
    "s_addc_u32 s55, s11, 0\ns_lshl_b32 s86, s60, 12\ns_add_u32 m0, s69, s86\ns_add_u32 s60, s60, 1\nglobal_load_lds_dwordx4 v76, s[54:55]\ns_add_u32 m0, m0, 31744\n"
    "s_and_b32 s60, s60, 7\nglobal_load_lds_dwordx4 v76, s[54:55] offset:1024\ns_add_u32 m0, m0, 31744\ns_nop 0\nglobal_load_lds_dwordx4 v76, s[54:55] offset:2048\n"
    "s_add_u32 m0, s64, 4096\ns_lshl_b32 s86, s58, 7\ns_add_u32 s52, s70, s86\ns_addc_u32 s53, s71, 0\nglobal_load_lds_dwordx4 v72, s[52:53]\ns_add_u32 m0, s64, 5120\n"
    "s_add_u32 s58, s58, 1\nglobal_load_lds_dwordx4 v73, s[52:53]\ns_add_u32 m0, s64, 6144\ns_and_b32 s58, s58, 7\nglobal_load_lds_dwordx4 v74, s[52:53]\n"
    "s_add_u32 m0, s64, 7168\ns_nop 0\nglobal_load_lds_dwordx4 v75, s[52:53]\ns_setprio 0\ns_lshl_b32 s87, s61, 12\ns_lshl_b32 s86, s61, 7\nv_add_u32 v120, s87, v28\n"
    "v_add_u32 v122, s87, v29\ns_add_u32 s61, s61, 1\nv_xor_b32 v121, 0x800, v120\ns_and_b32 s61, s61, 7\nv_bitop3_b32 v88, s86, v27, v26 bitop3:0x6a\n"
    "v_bitop3_b32 v89, s86, v27, v84 bitop3:0x6a\ns_waitcnt vmcnt(7) lgkmcnt(0)\ns_barrier\ns_setprio 2\nv_mfma_f32_16x16x32_bf16 v[64:67], v[96:99], v[132:135], v[64:67]\n"
    "v_mfma_f32_16x16x32_bf16 v[68:71], v[104:107], v[132:135], v[68:71]\nv_mfma_f32_16x16x32_bf16 v[64:67], v[100:103], v[136:139], v[64:67]\ns_setprio 0\n"
    "ds_read_b128 v[132:135], v88\nds_read_b128 v[136:139], v89\nds_read_b64_tr_b16 v[96:97], v120\nds_read_b64_tr_b16 v[98:99], v120 offset:256\n"
    "ds_read_b64_tr_b16 v[100:101], v121\nds_read_b64_tr_b16 v[102:103], v121 offset:256\nds_read_b64_tr_b16 v[104:105], v122\nds_read_b64_tr_b16 v[106:107], v122 offset:256\n"
    "s_setprio 0\nglobal_store_dwordx4 v37, v[232:235], s[32:33] offset:0 sc1\nglobal_store_dwordx4 v37, v[232:235], s[32:33] offset:1024 sc1\ns_mov_b32 m0, s75\n"
    "s_mov_b32 exec_hi, 0\nglobal_load_lds_dwordx4 v41, s[78:79]\ns_mov_b32 m0, s74\ns_nop 0\nglobal_load_lds_dwordx4 v41, s[76:77]\ns_mov_b64 exec, -1\n"
    "s_add_u32 m0, s69, 143360\ns_nop 0\nglobal_load_lds_dwordx4 v35, s[4:5]\nglobal_load_dwordx4 v[140:143], v36, s[32:33] sc1\n"
    "global_store_dwordx4 v42, v[232:235], s[32:33] offset:0 sc1\nglobal_store_dwordx4 v42, v[232:235], s[32:33] offset:1024 sc1\ns_mov_b64 exec, 15\n"
    "global_store_dwordx4 v43, v[232:235], s[32:33] sc1\ns_mov_b64 exec, -1\ns_setprio 0\ns_lshl_b32 s87, s61, 12\ns_lshl_b32 s86, s61, 7\nv_add_u32 v120, s87, v28\n"
    "v_add_u32 v122, s87, v29\ns_add_u32 s61, s61, 1\nv_xor_b32 v121, 0x800, v120\ns_and_b32 s61, s61, 7\nv_bitop3_b32 v88, s86, v27, v26 bitop3:0x6a\n"
    "v_bitop3_b32 v89, s86, v27, v84 bitop3:0x6a\ns_waitcnt vmcnt(9) lgkmcnt(0)\ns_barrier\ns_setprio 0\nv_mfma_f32_16x16x32_bf16 v[64:67], v[96:99], v[132:135], v[64:67]\n"
    "v_mfma_f32_16x16x32_bf16 v[68:71], v[104:107], v[132:135], v[68:71]\nv_mfma_f32_16x16x32_bf16 v[64:67], v[100:103], v[136:139], v[64:67]\ns_setprio 0\n"
    "ds_read_b128 v[132:135], v88\nds_read_b128 v[136:139], v89\nds_read_b64_tr_b16 v[96:97], v120\nds_read_b64_tr_b16 v[98:99], v120 offset:256\n"
    "ds_read_b64_tr_b16 v[100:101], v121\nds_read_b64_tr_b16 v[102:103], v121 offset:256\nds_read_b64_tr_b16 v[104:105], v122\nds_read_b64_tr_b16 v[106:107], v122 offset:256\n"
    "s_setprio 0\ns_setprio 0\ns_mov_b64 exec, 0xffff\nds_write_b32 v32, v93\ns_mov_b32 exec_lo, 0x100001\ns_mov_b32 exec_hi, 0x10000100\nds_write_b32 v32, v92 offset:4\n"
    "s_mov_b32 exec_lo, 0x200002\ns_mov_b32 exec_hi, 0x20000200\nds_write_b32 v32, v93 offset:4\ns_mov_b32 exec_lo, 0x400004\ns_mov_b32 exec_hi, 0x40000400\n"
    "ds_write_b32 v32, v94 offset:4\ns_mov_b32 exec_lo, 0x800008\ns_mov_b32 exec_hi, 0x80000800\nds_write_b32 v32, v95 offset:4\ns_mov_b64 exec, -1\ns_waitcnt lgkmcnt(5)\n"
    "s_setprio 0\nv_mfma_f32_16x16x32_bf16 v[64:67], v[96:99], v[132:135], v[64:67]\nv_mfma_f32_16x16x32_bf16 v[68:71], v[104:107], v[132:135], v[68:71]\n"
    "v_mfma_f32_16x16x32_bf16 v[64:67], v[100:103], v[136:139], v[64:67]\ns_add_u32 s34, s34, 1\ns_mov_b32 exec_lo, 0x20002\ns_mov_b32 exec_hi, 0x20002\ns_nop 3\n"
    "ds_add_f32 v31, v68 offset:0\nds_add_f32 v31, v69 offset:4\nds_add_f32 v31, v70 offset:8\nds_add_f32 v31, v71 offset:12\nds_write_b128 v30, v[64:67]\ns_mov_b64 exec, -1\n"
    "s_waitcnt lgkmcnt(0)\ns_barrier\ns_setprio 0\ns_setprio 1\nds_read_b64 v[108:109], v33 offset:0\nds_read_b64 v[110:111], v33 offset:128\n"
    "ds_read_b64 v[112:113], v33 offset:256\nds_read_b64 v[114:115], v33 offset:384\ns_mov_b32 s86, 0x3b000000\ns_waitcnt lgkmcnt(0)\n"
    "v_pk_add_f32 v[108:109], v[108:109], v[110:111]\nv_pk_add_f32 v[112:113], v[112:113], v[114:115]\nv_pk_add_f32 v[108:109], v[108:109], v[112:113]\n"
    "v_mul_f32 v108, s86, v108\nv_mul_f32 v110, v108, v108\nv_fma_f32 v109, v109, s86, -v110\nv_max_f32 v109, 0, v109\nv_cvt_pk_bf16_f32 v109, 0, v109\n"
    "v_add_f32 v109, 0x3727c5ac, v109\nv_cvt_pk_bf16_f32 v109, 0, v109\nv_sqrt_f32 v109, v109\ns_nop 0\nv_cvt_pk_bf16_f32 v109, 0, v109\nv_rcp_f32 v109, v109\n"
    "v_cvt_pk_bf16_f32 v90, 0, -v108\ns_nop 1\nv_fmac_f32_dpp v64, v64, v90 row_newbcast:1 row_mask:0xf bank_mask:0xf\n"
    "v_fmac_f32_dpp v65, v65, v90 row_newbcast:1 row_mask:0xf bank_mask:0xf\nv_fmac_f32_dpp v66, v66, v90 row_newbcast:1 row_mask:0xf bank_mask:0xf\n"
    "v_fmac_f32_dpp v67, v67, v90 row_newbcast:1 row_mask:0xf bank_mask:0xf\nv_mul_f32 v64, v64, v109\nv_mul_f32 v65, v65, v109\nv_mul_f32 v66, v66, v109\n"
    "v_mul_f32 v67, v67, v109\nv_cvt_pk_bf16_f32 v64, v64, v65\nv_cvt_pk_bf16_f32 v65, v66, v67\nds_write_b64 v34, v[64:65]\nv_mov_b32 v9, s34\ns_waitcnt vmcnt(7)\n"
    "s_mov_b64 exec, 1\nglobal_store_dword v60, v9, s[32:33] sc1\ns_mov_b64 exec, -1\ns_setprio 0\ns_waitcnt vmcnt(4)\nv_xor_b32 v140, s34, v140\nv_xor_b32 v141, s34, v141\n"
    "v_xor_b32 v142, s34, v142\nv_xor_b32 v143, s34, v143\nv_or3_b32 v140, v140, v141, v142\nv_or_b32 v140, v140, v143\nv_cmp_ne_u32 vcc, 0, v140\ns_nop 0\ns_cmp_eq_u64 vcc, 0\n"
    "s_cselect_b32 s43, 1, 0\ns_cbranch_scc1 .LL7\ns_waitcnt vmcnt(0)\ns_branch .LL8\n.LL7:\nglobal_load_dwordx4 v[132:135], v58, s[32:33] offset:-4096 sc1\n"
    "global_load_dwordx4 v[136:139], v58, s[32:33] offset:-3072 sc1\nglobal_load_dwordx4 v[140:143], v58, s[32:33] offset:-2048 sc1\n"
    "global_load_dwordx4 v[144:147], v58, s[32:33] offset:-1024 sc1\nglobal_load_dwordx4 v[148:151], v58, s[32:33] offset:0 sc1\n"
    "global_load_dwordx4 v[152:155], v58, s[32:33] offset:1024 sc1\nglobal_load_dwordx4 v[156:159], v58, s[32:33] offset:2048 sc1\n"
    "global_load_dwordx4 v[160:163], v58, s[32:33] offset:3072 sc1\n.LL8:\n.LHL12:\ns_setprio 1\ns_waitcnt lgkmcnt(0)\ns_barrier\nds_read_b128 v[200:203], v252\n"
    "ds_read_b128 v[204:207], v252 offset:208\ns_mov_b32 exec_lo, 0x00ff00ff\ns_mov_b32 exec_hi, 0xff00ff00\nds_read_b128 v[10:13], v253\n"
    "ds_read_b128 v[14:17], v253 offset:208\ns_mov_b64 exec, -1\nds_read_b64_tr_b16 v[208:209], v254 offset:0\nds_read_b64_tr_b16 v[210:211], v254 offset:32\n"
    "ds_read_b64_tr_b16 v[212:213], v254 offset:208\nds_read_b64_tr_b16 v[214:215], v254 offset:240\ns_cmp_eq_u32 s43, 0\ns_cbranch_scc1 .LL14\n"
    "global_load_dwordx4 v[112:115], v5, s[32:33] offset:0 sc1\nglobal_load_dwordx4 v[116:119], v5, s[32:33] offset:1024 sc1\n"
    "global_load_dwordx4 v[120:123], v5, s[32:33] offset:2048 sc1\nglobal_load_dwordx4 v[124:127], v5, s[32:33] offset:3072 sc1\n.LL14:\ns_mov_b32 s81, 0x3eb8aa3b\n"
    "s_waitcnt lgkmcnt(5)\nv_mfma_f32_16x16x32_bf16 v[216:219], v[200:203], v[10:13], v[128:131]\ns_waitcnt lgkmcnt(4)\n"
    "v_mfma_f32_16x16x32_bf16 v[220:223], v[204:207], v[14:17], v[128:131]\ns_nop 5\nv_max_f32 v224, v216, v217\ns_nop 0\nv_max_f32 v225, v220, v221\n"
    "v_max3_f32 v224, v224, v218, v219\nv_max3_f32 v225, v225, v222, v223\ns_nop 1\nv_permlane16_swap_b32 v224, v225\nv_max_f32 v226, v224, v225\nv_max_f32 v225, v224, v225\n"
    "s_nop 1\nv_permlane32_swap_b32 v226, v225\nv_max_f32 v226, v226, v225\nv_mul_f32 v224, 0xbeb8aa3b, v226\nv_mul_f32 v225, 0xbeb8aa3b, v226\ns_nop 1\n"
    "v_permlane16_swap_b32 v224, v225\nv_pk_fma_f32 v[216:217], v[216:217], s[80:81], v[224:225] op_sel:[0,1,0] op_sel_hi:[1,1,0]\n"
    "v_pk_fma_f32 v[218:219], v[218:219], s[80:81], v[224:225] op_sel:[0,1,0] op_sel_hi:[1,1,0]\n"
    "v_pk_fma_f32 v[220:221], v[220:221], s[80:81], v[224:225] op_sel:[0,1,1] op_sel_hi:[1,1,1]\n"
    "v_pk_fma_f32 v[222:223], v[222:223], s[80:81], v[224:225] op_sel:[0,1,1] op_sel_hi:[1,1,1]\nv_exp_f32 v216, v216\nv_exp_f32 v217, v217\nv_exp_f32 v218, v218\n"
    "v_exp_f32 v219, v219\nv_exp_f32 v220, v220\nv_exp_f32 v221, v221\nv_exp_f32 v222, v222\nv_exp_f32 v223, v223\ns_mov_b32 exec_lo, 0x00ff00ff\ns_mov_b32 exec_hi, 0x00ff00ff\n"
    "v_cvt_pk_bf16_f32 v18, v216, v217\nv_cvt_pk_bf16_f32 v19, v218, v219\nv_cvt_pk_bf16_f32 v22, v220, v221\nv_cvt_pk_bf16_f32 v23, v222, v223\ns_not_b64 exec, exec\n"
    "v_cvt_pk_bf16_f32 v20, v216, v217\nv_cvt_pk_bf16_f32 v21, v218, v219\nv_cvt_pk_bf16_f32 v24, v220, v221\nv_cvt_pk_bf16_f32 v25, v222, v223\ns_mov_b64 exec, -1\n"
    "v_pk_add_f32 v[216:217], v[216:217], v[218:219]\nv_pk_add_f32 v[220:221], v[220:221], v[222:223]\nv_add_f32 v224, v216, v217\nv_add_f32 v225, v220, v221\n"
    "s_waitcnt lgkmcnt(0)\nv_mfma_f32_16x16x32_bf16 v[200:203], v[208:211], v[18:21], 0\nv_mfma_f32_16x16x32_bf16 v[204:207], v[212:215], v[22:25], 0\n"
    "v_permlane16_swap_b32 v224, v225\nv_add_f32 v226, v224, v225\nv_add_f32 v225, v224, v225\ns_nop 1\nv_permlane32_swap_b32 v226, v225\nv_add_f32 v226, v226, v225\n"
    "v_rcp_f32 v224, v226\nv_rcp_f32 v225, v226\ns_nop 1\nv_permlane16_swap_b32 v224, v225\nv_pk_mul_f32 v[200:201], v[200:201], v[224:225] op_sel:[0,0] op_sel_hi:[1,0]\n"
    "v_pk_mul_f32 v[204:205], v[204:205], v[224:225] op_sel:[0,1] op_sel_hi:[1,1]\nv_pk_mul_f32 v[202:203], v[202:203], v[224:225] op_sel:[0,0] op_sel_hi:[1,0]\n"
    "v_pk_mul_f32 v[206:207], v[206:207], v[224:225] op_sel:[0,1] op_sel_hi:[1,1]\nv_cvt_pk_bf16_f32 v216, v200, v201\nv_cvt_pk_bf16_f32 v218, v204, v205\n"
    "v_cvt_pk_bf16_f32 v217, v202, v203\nv_cvt_pk_bf16_f32 v219, v206, v207\nv_permlane16_swap_b32 v216, v218\ns_nop 0\nv_permlane16_swap_b32 v217, v219\n"
    "global_store_dwordx4 v1, v[216:219], s[32:33] sc1\ns_setprio 0\nds_read_b32 v244, v62 offset:2048\nds_read_b32 v245, v62 offset:2560\nds_read_b32 v242, v62 offset:1024\n"
    "ds_read_b32 v243, v62 offset:1536\nds_read_b32 v240, v62 offset:0\nds_read_b32 v241, v62 offset:512\nv_bfe_u32 v30, v0, 5, 1\ns_lshl_b32 s15, s3, 11\n"
    "s_add_u32 s14, s15, 0x25600\nv_lshl_add_u32 v48, v30, 8, s14\nv_and_b32 v31, 31, v0\nv_lshl_add_u32 v48, v31, 2, v48\ns_add_u32 s16, s15, 0x25a00\n"
    "v_lshl_add_u32 v49, v30, 8, s16\nv_lshl_add_u32 v49, v31, 2, v49\nv_and_b32 v32, 63, v0\nv_lshl_add_u32 v50, v32, 4, s14\ns_mov_b32 s17, 0x1f600\n"
    "v_lshl_add_u32 v51, v32, 4, s17\nv_bfe_u32 v33, v0, 3, 3\ns_lshl_b32 s23, s40, 6\ns_lshl4_add_u32 s22, s3, s23\ns_lshl_b32 s21, s22, 13\ns_add_u32 s20, s7, s21\n"
    "s_lshl_b32 s24, s39, 7\ns_add_u32 s19, s20, s24\ns_add_u32 s18, s19, 0x200000\nv_lshl_add_u32 v52, v33, 13, s18\nv_and_b32 v34, 7, v0\nv_lshl_add_u32 v52, v34, 4, v52\n"
    "s_add_u32 s25, s19, 0x210000\nv_lshl_add_u32 v53, v33, 13, s25\nv_lshl_add_u32 v53, v34, 4, v53\ns_lshl_b32 s45, s3, 8\ns_add_u32 s44, s45, 0x27600\n"
    "v_lshl_add_u32 v54, v32, 2, s44\ns_add_u32 s14, s7, 0x401000\nv_lshl_add_u32 v20, v0, 4, s14\ns_lshl_b32 s17, s41, 13\ns_add_u32 s16, s7, s17\n"
    "s_add_u32 s15, s16, 0x200000\nv_lshl_add_u32 v21, v0, 4, s15\nv_add_u32 v22, 8192, v20\nv_add_u32 v23, 16384, v20\nv_add_u32 v24, 24576, v20\nv_add_u32 v25, 32768, v20\n"
    "v_add_u32 v26, 40960, v20\nv_add_u32 v27, 49152, v20\nv_add_u32 v28, 57344, v20\ns_cmp_eq_u32 s6, 0\ns_cbranch_scc0 .LL18\ns_cmp_eq_u32 s43, 1\ns_cselect_b64 vcc, -1, 0\n"
    "s_waitcnt vmcnt(14)\ns_mov_b64 exec, 1\nglobal_store_dword v2, v9, s[32:33] sc1\ns_mov_b64 exec, -1\ns_waitcnt lgkmcnt(0)\ns_cbranch_vccz .Lcn19\n"
    "global_load_dwordx4 v[164:167], v59, s[32:33] offset:-4096 sc1\nglobal_load_dwordx4 v[168:171], v59, s[32:33] offset:-3072 sc1\n"
    "global_load_dwordx4 v[172:175], v59, s[32:33] offset:-2048 sc1\nglobal_load_dwordx4 v[176:179], v59, s[32:33] offset:-1024 sc1\n.Lcn19:\nv_lshlrev_b32 v220, 16, v244\n"
    "v_and_b32 v221, 0xffff0000, v244\nv_lshlrev_b32 v222, 16, v245\nv_and_b32 v223, 0xffff0000, v245\nv_lshlrev_b32 v224, 16, v242\nv_and_b32 v225, 0xffff0000, v242\n"
    "v_lshlrev_b32 v226, 16, v243\nv_and_b32 v227, 0xffff0000, v243\nv_mul_f32 v212, v220, v224\nv_mul_f32 v214, v221, v225\ns_barrier\ns_setprio 0\nv_mul_f32 v213, v220, v226\n"
    "v_mul_f32 v215, v221, v227\nv_fma_f32 v212, -v222, v226, v212\nv_fma_f32 v214, -v223, v227, v214\nv_fma_f32 v213, -v222, v224, -v213\nv_fma_f32 v215, -v223, v225, -v215\n"
    "s_bitcmp1_b32 s3, 0\ns_cselect_b32 s86, 0x43800000, 1.0\ns_mov_b32 s87, 0x43810000\ns_cselect_b32 s87, 0x44010000, s87\nv_max3_f32 v236, |v212|, |v213|, |v214|\n"
    "v_max_f32_e64 v236, v236, |v215|\nv_lshlrev_b32 v196, 16, v240\nv_and_b32 v197, 0xffff0000, v240\nv_lshlrev_b32 v198, 16, v241\nv_and_b32 v199, 0xffff0000, v241\n"
    "v_mul_f32 v196, s93, v196\nv_mul_f32 v197, s93, v197\ns_cbranch_vccz .Lcn20\nglobal_load_dwordx4 v[180:183], v59, s[32:33] offset:0 sc1\n"
    "global_load_dwordx4 v[184:187], v59, s[32:33] offset:1024 sc1\nglobal_load_dwordx4 v[188:191], v59, s[32:33] offset:2048 sc1\n"
    "global_load_dwordx4 v[192:195], v59, s[32:33] offset:3072 sc1\n.Lcn20:\nv_mul_f32 v198, s94, v198\nv_mul_f32 v199, s94, v199\nv_mul_f32 v208, s86, v196\n"
    "v_mul_f32 v209, s86, v197\nv_mul_f32 v210, s87, v196\nv_mul_f32 v211, s87, v197\nv_max_f32_dpp v236, v236, v236 row_shr:1 row_mask:0xf bank_mask:0xf bound_ctrl:0\n"
    "v_mul_f32 v229, s86, v198\nv_mul_f32 v231, s86, v199\nv_mul_f32 v217, s87, v198\nv_mul_f32 v219, s87, v199\n"
    "v_max_f32_dpp v236, v236, v236 row_shr:2 row_mask:0xf bank_mask:0xf bound_ctrl:0\nv_exp_f32 v208, v208\nv_exp_f32 v209, v209\nv_exp_f32 v210, v210\nv_exp_f32 v211, v211\n"
    "v_max_f32_dpp v236, v236, v236 row_shr:4 row_mask:0xf bank_mask:0xf bound_ctrl:0\nv_fract_f32 v229, v229\nv_fract_f32 v231, v231\nv_fract_f32 v217, v217\n"
    "v_fract_f32 v219, v219\nv_max_f32_dpp v236, v236, v236 row_shr:8 row_mask:0xf bank_mask:0xf bound_ctrl:0\nv_cos_f32 v228, v229\nv_cos_f32 v230, v231\nv_cos_f32 v216, v217\n"
    "v_cos_f32 v218, v219\nv_max_f32_dpp v236, v236, v236 row_bcast:15 row_mask:0xf bank_mask:0xf bound_ctrl:0\nv_sin_f32 v229, v229\nv_sin_f32 v231, v231\n"
    "v_sin_f32 v217, v217\ns_cbranch_vccz .Lcn21\nglobal_load_dwordx4 v[64:67], v4, s[32:33] offset:-4096 sc1\nglobal_load_dwordx4 v[68:71], v4, s[32:33] offset:-3072 sc1\n"
    "global_load_dwordx4 v[72:75], v4, s[32:33] offset:-2048 sc1\nglobal_load_dwordx4 v[76:79], v4, s[32:33] offset:-1024 sc1\n.Lcn21:\nv_sin_f32 v219, v219\n"
    "v_max_f32_dpp v236, v236, v236 row_bcast:31 row_mask:0xf bank_mask:0xf bound_ctrl:0\nv_mul_f32 v228, v228, v208\nv_mul_f32 v230, v230, v209\nv_mul_f32 v216, v216, v210\n"
    "v_mul_f32 v218, v218, v211\nv_mul_f32 v229, v229, v208\nv_mul_f32 v231, v231, v209\nv_mul_f32 v217, v217, v210\nv_mul_f32 v219, v219, v211\nv_readlane_b32 s86, v236, 63\n"
    "s_nop 0\ns_lshr_b32 s86, s86, 23\ns_max_u32 s86, s86, 9\ns_min_u32 s86, s86, 254\ns_sub_u32 s87, 261, s86\ns_lshl_b32 s87, s87, 23\ns_sub_u32 s86, s86, 7\n"
    "s_lshl_b32 s86, s86, 23\nv_mul_f32 v212, s87, v212\nv_mul_f32 v213, s87, v213\nv_mul_f32 v214, s87, v214\nv_mul_f32 v215, s87, v215\nv_mov_b32 v236, s86\n"
    "v_mul_f32 v236, s92, v236\nv_mov_b32 v29, 0\nv_pk_mul_f32 v[200:201], v[216:217], v[212:213] op_sel_hi:[0,1]\n"
    "v_pk_mul_f32 v[202:203], v[218:219], v[214:215] op_sel_hi:[0,1]\n"
    "v_pk_fma_f32 v[216:217], v[216:217], v[212:213], v[200:201] op_sel:[1,1,0] op_sel_hi:[1,0,1] neg_lo:[0,1,0]\n"
    "v_pk_fma_f32 v[218:219], v[218:219], v[214:215], v[202:203] op_sel:[1,1,0] op_sel_hi:[1,0,1] neg_lo:[0,1,0]\ns_bitcmp1_b32 s3, 0\ns_cbranch_vccz .Lcn22\n"
    "global_load_dwordx4 v[80:83], v4, s[32:33] offset:0 sc1\nglobal_load_dwordx4 v[84:87], v4, s[32:33] offset:1024 sc1\n"
    "global_load_dwordx4 v[88:91], v4, s[32:33] offset:2048 sc1\nglobal_load_dwordx4 v[92:95], v4, s[32:33] offset:3072 sc1\n.Lcn22:\ns_cbranch_scc0 .LL9\n"
    "v_pk_mul_f32 v[200:201], v[212:213], v[228:229] op_sel_hi:[0,1]\nv_pk_mul_f32 v[202:203], v[214:215], v[230:231] op_sel_hi:[0,1]\n"
    "v_pk_fma_f32 v[212:213], v[212:213], v[228:229], v[200:201] op_sel:[1,1,0] op_sel_hi:[1,0,1] neg_lo:[0,1,0]\n"
    "v_pk_fma_f32 v[214:215], v[214:215], v[230:231], v[202:203] op_sel:[1,1,0] op_sel_hi:[1,0,1] neg_lo:[0,1,0]\n.LL9:\ns_mov_b64 exec, 1\nds_write_b32 v7, v236\n"
    "ds_write_b16 v8, v29\ns_mov_b64 exec, -1\nv_cvt_pk_f16_f32 v204, v212, v213\nv_cvt_pk_f16_f32 v205, v214, v215\n"
    "v_pk_mul_f32 v[200:201], v[212:213], v[228:229] op_sel_hi:[0,1]\nv_pk_mul_f32 v[202:203], v[214:215], v[230:231] op_sel_hi:[0,1]\nds_write_b64 v3, v[204:205] offset:0\n"
    "v_pk_fma_f32 v[212:213], v[212:213], v[228:229], v[200:201] op_sel:[1,1,0] op_sel_hi:[1,0,1] neg_lo:[0,1,0]\n"
    "v_pk_fma_f32 v[214:215], v[214:215], v[230:231], v[202:203] op_sel:[1,1,0] op_sel_hi:[1,0,1] neg_lo:[0,1,0]\nv_cvt_pk_f16_f32 v206, v212, v213\n"
    "v_cvt_pk_f16_f32 v207, v214, v215\nv_pk_mul_f32 v[200:201], v[212:213], v[228:229] op_sel_hi:[0,1]\nv_pk_mul_f32 v[202:203], v[214:215], v[230:231] op_sel_hi:[0,1]\n"
    "ds_write_b64 v3, v[206:207] offset:544\nv_pk_fma_f32 v[212:213], v[212:213], v[228:229], v[200:201] op_sel:[1,1,0] op_sel_hi:[1,0,1] neg_lo:[0,1,0]\n"
    "v_pk_fma_f32 v[214:215], v[214:215], v[230:231], v[202:203] op_sel:[1,1,0] op_sel_hi:[1,0,1] neg_lo:[0,1,0]\nv_cvt_pk_f16_f32 v204, v212, v213\n"
    "v_cvt_pk_f16_f32 v205, v214, v215\nv_pk_mul_f32 v[200:201], v[212:213], v[228:229] op_sel_hi:[0,1]\nv_pk_mul_f32 v[202:203], v[214:215], v[230:231] op_sel_hi:[0,1]\n"
    "ds_write_b64 v3, v[204:205] offset:1088\nv_pk_fma_f32 v[212:213], v[212:213], v[228:229], v[200:201] op_sel:[1,1,0] op_sel_hi:[1,0,1] neg_lo:[0,1,0]\n"
    "s_cbranch_vccz .Lcn23\nglobal_load_dwordx4 v[96:99], v5, s[32:33] offset:-4096 sc1\nglobal_load_dwordx4 v[100:103], v5, s[32:33] offset:-3072 sc1\n"
    "global_load_dwordx4 v[104:107], v5, s[32:33] offset:-2048 sc1\nglobal_load_dwordx4 v[108:111], v5, s[32:33] offset:-1024 sc1\n.Lcn23:\n"
    "v_pk_fma_f32 v[214:215], v[214:215], v[230:231], v[202:203] op_sel:[1,1,0] op_sel_hi:[1,0,1] neg_lo:[0,1,0]\nv_cvt_pk_f16_f32 v206, v212, v213\n"
    "v_cvt_pk_f16_f32 v207, v214, v215\nv_pk_mul_f32 v[200:201], v[212:213], v[228:229] op_sel_hi:[0,1]\nv_pk_mul_f32 v[202:203], v[214:215], v[230:231] op_sel_hi:[0,1]\n"
    "ds_write_b64 v3, v[206:207] offset:1632\nv_pk_fma_f32 v[212:213], v[212:213], v[228:229], v[200:201] op_sel:[1,1,0] op_sel_hi:[1,0,1] neg_lo:[0,1,0]\n"
    "v_pk_fma_f32 v[214:215], v[214:215], v[230:231], v[202:203] op_sel:[1,1,0] op_sel_hi:[1,0,1] neg_lo:[0,1,0]\nv_cvt_pk_f16_f32 v204, v212, v213\n"
    "v_cvt_pk_f16_f32 v205, v214, v215\nv_pk_mul_f32 v[200:201], v[212:213], v[228:229] op_sel_hi:[0,1]\nv_pk_mul_f32 v[202:203], v[214:215], v[230:231] op_sel_hi:[0,1]\n"
    "ds_write_b64 v3, v[204:205] offset:2176\nv_pk_fma_f32 v[212:213], v[212:213], v[228:229], v[200:201] op_sel:[1,1,0] op_sel_hi:[1,0,1] neg_lo:[0,1,0]\n"
    "v_pk_fma_f32 v[214:215], v[214:215], v[230:231], v[202:203] op_sel:[1,1,0] op_sel_hi:[1,0,1] neg_lo:[0,1,0]\nv_cvt_pk_f16_f32 v206, v212, v213\n"
    "v_cvt_pk_f16_f32 v207, v214, v215\nv_pk_mul_f32 v[200:201], v[212:213], v[228:229] op_sel_hi:[0,1]\nv_pk_mul_f32 v[202:203], v[214:215], v[230:231] op_sel_hi:[0,1]\n"
    "ds_write_b64 v3, v[206:207] offset:2720\nv_pk_fma_f32 v[212:213], v[212:213], v[228:229], v[200:201] op_sel:[1,1,0] op_sel_hi:[1,0,1] neg_lo:[0,1,0]\n"
    "v_pk_fma_f32 v[214:215], v[214:215], v[230:231], v[202:203] op_sel:[1,1,0] op_sel_hi:[1,0,1] neg_lo:[0,1,0]\nv_cvt_pk_f16_f32 v204, v212, v213\n"
    "v_cvt_pk_f16_f32 v205, v214, v215\nv_pk_mul_f32 v[200:201], v[212:213], v[228:229] op_sel_hi:[0,1]\nv_pk_mul_f32 v[202:203], v[214:215], v[230:231] op_sel_hi:[0,1]\n"
    "ds_write_b64 v3, v[204:205] offset:3264\nv_pk_fma_f32 v[212:213], v[212:213], v[228:229], v[200:201] op_sel:[1,1,0] op_sel_hi:[1,0,1] neg_lo:[0,1,0]\n"
    "v_pk_fma_f32 v[214:215], v[214:215], v[230:231], v[202:203] op_sel:[1,1,0] op_sel_hi:[1,0,1] neg_lo:[0,1,0]\nv_cvt_pk_f16_f32 v206, v212, v213\n"
    "v_cvt_pk_f16_f32 v207, v214, v215\nds_write_b64 v3, v[206:207] offset:3808\ns_bitcmp1_b32 s3, 0\ns_cbranch_scc1 .LL10\n"
    "v_pk_mul_f32 v[200:201], v[228:229], v[228:229] op_sel_hi:[0,1]\nv_pk_mul_f32 v[202:203], v[230:231], v[230:231] op_sel_hi:[0,1]\n"
    "v_pk_fma_f32 v[228:229], v[228:229], v[228:229], v[200:201] op_sel:[1,1,0] op_sel_hi:[1,0,1] neg_lo:[0,1,0]\n"
    "v_pk_fma_f32 v[230:231], v[230:231], v[230:231], v[202:203] op_sel:[1,1,0] op_sel_hi:[1,0,1] neg_lo:[0,1,0]\n.LL10:\nv_cvt_pk_f16_f32 v204, v216, v217\n"
    "v_cvt_pk_f16_f32 v205, v218, v219\nv_pk_mul_f32 v[200:201], v[216:217], v[228:229] op_sel_hi:[0,1]\nv_pk_mul_f32 v[202:203], v[218:219], v[230:231] op_sel_hi:[0,1]\n"
    "ds_write_b64 v3, v[204:205] offset:4352\nv_pk_fma_f32 v[216:217], v[216:217], v[228:229], v[200:201] op_sel:[1,1,0] op_sel_hi:[1,0,1] neg_lo:[0,1,0]\n"
    "v_pk_fma_f32 v[218:219], v[218:219], v[230:231], v[202:203] op_sel:[1,1,0] op_sel_hi:[1,0,1] neg_lo:[0,1,0]\nv_cvt_pk_f16_f32 v206, v216, v217\n"
    "v_cvt_pk_f16_f32 v207, v218, v219\nv_pk_mul_f32 v[200:201], v[216:217], v[228:229] op_sel_hi:[0,1]\nv_pk_mul_f32 v[202:203], v[218:219], v[230:231] op_sel_hi:[0,1]\n"
    "ds_write_b64 v3, v[206:207] offset:4896\nv_pk_fma_f32 v[216:217], v[216:217], v[228:229], v[200:201] op_sel:[1,1,0] op_sel_hi:[1,0,1] neg_lo:[0,1,0]\n"
    "v_pk_fma_f32 v[218:219], v[218:219], v[230:231], v[202:203] op_sel:[1,1,0] op_sel_hi:[1,0,1] neg_lo:[0,1,0]\nv_cvt_pk_f16_f32 v204, v216, v217\n"
    "v_cvt_pk_f16_f32 v205, v218, v219\nds_write_b64 v3, v[204:205] offset:5440\ns_setprio 0\nglobal_load_dwordx4 v[240:243], v6, s[8:9]\n"
    "global_load_dwordx4 v[244:247], v6, s[8:9] offset:32\ns_cmp_eq_u32 s43, 1\ns_cbranch_scc1 .LL17\nv_and_b32 v10, 3, v0\ns_lshl2_add_u32 s16, s40, s3\n"
    "s_lshl_b32 s15, s16, 9\ns_add_u32 s14, s15, 0x1002000\nv_lshl_add_u32 v3, v10, 7, s14\nv_and_b32 v11, 4, v0\nv_lshl_add_u32 v3, v11, 2, v3\ns_mov_b32 s80, 0x10000\n"
    "global_load_dwordx4 v[196:199], v3, s[32:33] sc1\ns_waitcnt vmcnt(0)\n.Ltp24:\nv_xor_b32 v196, s34, v196\nv_xor_b32 v197, s34, v197\nv_xor_b32 v198, s34, v198\n"
    "v_xor_b32 v199, s34, v199\nv_or3_b32 v196, v196, v197, v198\nv_or_b32 v196, v196, v199\nv_cmp_ne_u32 vcc, 0, v196\ns_cbranch_vccz .Ltq25\ns_sub_u32 s80, s80, 1\n"
    "s_cbranch_scc1 .Ltq25\ns_sleep 2\nglobal_load_dwordx4 v[196:199], v3, s[32:33] sc1\ns_waitcnt vmcnt(0)\ns_branch .Ltp24\n.Ltq25:\n"
    "global_load_dwordx4 v[132:135], v58, s[32:33] offset:-4096 sc1\nglobal_load_dwordx4 v[136:139], v58, s[32:33] offset:-3072 sc1\n"
    "global_load_dwordx4 v[140:143], v58, s[32:33] offset:-2048 sc1\nglobal_load_dwordx4 v[144:147], v58, s[32:33] offset:-1024 sc1\n"
    "global_load_dwordx4 v[148:151], v58, s[32:33] offset:0 sc1\nglobal_load_dwordx4 v[152:155], v58, s[32:33] offset:1024 sc1\n"
    "global_load_dwordx4 v[156:159], v58, s[32:33] offset:2048 sc1\nglobal_load_dwordx4 v[160:163], v58, s[32:33] offset:3072 sc1\n"
    "global_load_dwordx4 v[164:167], v59, s[32:33] offset:-4096 sc1\nglobal_load_dwordx4 v[168:171], v59, s[32:33] offset:-3072 sc1\n"
    "global_load_dwordx4 v[172:175], v59, s[32:33] offset:-2048 sc1\nglobal_load_dwordx4 v[176:179], v59, s[32:33] offset:-1024 sc1\n"
    "global_load_dwordx4 v[180:183], v59, s[32:33] offset:0 sc1\nglobal_load_dwordx4 v[184:187], v59, s[32:33] offset:1024 sc1\n"
    "global_load_dwordx4 v[188:191], v59, s[32:33] offset:2048 sc1\nglobal_load_dwordx4 v[192:195], v59, s[32:33] offset:3072 sc1\n"
    "global_load_dwordx4 v[64:67], v4, s[32:33] offset:-4096 sc1\nglobal_load_dwordx4 v[68:71], v4, s[32:33] offset:-3072 sc1\n"
    "global_load_dwordx4 v[72:75], v4, s[32:33] offset:-2048 sc1\nglobal_load_dwordx4 v[76:79], v4, s[32:33] offset:-1024 sc1\n"
    "global_load_dwordx4 v[80:83], v4, s[32:33] offset:0 sc1\nglobal_load_dwordx4 v[84:87], v4, s[32:33] offset:1024 sc1\n"
    "global_load_dwordx4 v[88:91], v4, s[32:33] offset:2048 sc1\nglobal_load_dwordx4 v[92:95], v4, s[32:33] offset:3072 sc1\n"
    "global_load_dwordx4 v[96:99], v5, s[32:33] offset:-4096 sc1\nglobal_load_dwordx4 v[100:103], v5, s[32:33] offset:-3072 sc1\n"
    "global_load_dwordx4 v[104:107], v5, s[32:33] offset:-2048 sc1\nglobal_load_dwordx4 v[108:111], v5, s[32:33] offset:-1024 sc1\n"
    "global_load_dwordx4 v[112:115], v5, s[32:33] offset:0 sc1\nglobal_load_dwordx4 v[116:119], v5, s[32:33] offset:1024 sc1\n"
    "global_load_dwordx4 v[120:123], v5, s[32:33] offset:2048 sc1\nglobal_load_dwordx4 v[124:127], v5, s[32:33] offset:3072 sc1\ns_waitcnt vmcnt(0)\ns_mov_b32 s43, 1\n.LL17:\n"
    "s_branch .LL16\n.LL18:\ns_waitcnt lgkmcnt(0)\ns_barrier\nv_and_b32 v10, 63, v0\ns_lshl_b32 s16, s39, 15\ns_lshl2_add_u32 s18, s40, s3\ns_lshl_b32 s17, s18, 11\n"
    "s_add_u32 s15, s16, s17\ns_add_u32 s14, s15, 0x600000\nv_lshl_add_u32 v2, v10, 4, s14\ns_lshl_b32 s20, s3, 11\ns_add_u32 s19, s20, 0x1c200\nv_lshl_add_u32 v3, v10, 4, s19\n"
    "v_bfe_u32 v12, v0, 5, 1\nv_bfe_u32 v13, v0, 2, 2\nv_lshl_add_u32 v11, v12, 2, v13\nv_lshl_add_u32 v4, v11, 6, s19\nv_bfe_u32 v14, v0, 4, 1\nv_lshl_add_u32 v4, v14, 5, v4\n"
    "v_and_b32 v15, 3, v0\nv_lshl_add_u32 v4, v15, 3, v4\nglobal_load_dwordx4 v[240:243], v2, s[32:33] offset:0 sc1\n"
    "global_load_dwordx4 v[244:247], v2, s[32:33] offset:1024 sc1\ns_mov_b32 s80, 0x10000\n.LL26:\ns_waitcnt vmcnt(0)\ns_mov_b64 s[82:83], 0\nv_max3_u32 v5, v240, v241, v242\n"
    "v_max_u32 v5, v5, v243\nv_cmp_eq_u32 vcc, -1, v5\ns_nop 0\ns_or_b64 s[82:83], s[82:83], vcc\ns_cbranch_vccz .Lag28\ns_mov_b64 exec, vcc\n"
    "global_load_dwordx4 v[240:243], v2, s[32:33] offset:0 sc1\ns_mov_b64 exec, -1\n.Lag28:\nv_max3_u32 v5, v244, v245, v246\nv_max_u32 v5, v5, v247\nv_cmp_eq_u32 vcc, -1, v5\n"
    "s_nop 0\ns_or_b64 s[82:83], s[82:83], vcc\ns_cbranch_vccz .Lag29\ns_mov_b64 exec, vcc\nglobal_load_dwordx4 v[244:247], v2, s[32:33] offset:1024 sc1\ns_mov_b64 exec, -1\n"
    ".Lag29:\ns_cmp_eq_u64 s[82:83], 0\ns_cbranch_scc1 .LL27\ns_sub_u32 s80, s80, 1\ns_cbranch_scc0 .LL26\ns_waitcnt vmcnt(0)\n.LL27:\nds_write_b128 v3, v[240:243]\n"
    "ds_write_b128 v3, v[244:247] offset:1024\nds_read_b64_tr_b16 v[240:241], v4 offset:0\nds_read_b64_tr_b16 v[242:243], v4 offset:512\n"
    "ds_read_b64_tr_b16 v[244:245], v4 offset:1024\nds_read_b64_tr_b16 v[246:247], v4 offset:1536\ns_waitcnt lgkmcnt(0)\n.LL16:\nv_mov_b32 v36, 0x3f803f80\n"
    "s_waitcnt vmcnt(2) lgkmcnt(0)\ns_setprio 2\ns_barrier\nds_read_b128 v[196:199], v51 offset:24576\nds_read_b128 v[200:203], v51 offset:25600\n"
    "ds_read_b128 v[204:207], v51 offset:26624\nds_read_b128 v[208:211], v51 offset:27648\nds_read_b128 v[212:215], v51 offset:28672\nds_read_b128 v[216:219], v51 offset:29696\n"
    "s_waitcnt lgkmcnt(5)\nv_mfma_f32_32x32x16_bf16 v[2:17], v[132:135], v[196:199], 0\nds_read_b128 v[196:199], v51 offset:30720\ns_waitcnt lgkmcnt(5)\n"
    "v_mfma_f32_32x32x16_bf16 v[2:17], v[136:139], v[200:203], v[2:17]\nds_read_b128 v[200:203], v51 offset:31744\ns_waitcnt lgkmcnt(5)\n"
    "v_mfma_f32_32x32x16_bf16 v[2:17], v[140:143], v[204:207], v[2:17]\ns_barrier\nds_read_b128 v[204:207], v51 offset:0\ns_waitcnt lgkmcnt(5)\n"
    "v_mfma_f32_32x32x16_bf16 v[2:17], v[144:147], v[208:211], v[2:17]\nds_read_b128 v[208:211], v51 offset:1024\ns_waitcnt lgkmcnt(5)\n"
    "v_mfma_f32_32x32x16_bf16 v[2:17], v[148:151], v[212:215], v[2:17]\nds_read_b128 v[212:215], v51 offset:2048\ns_waitcnt lgkmcnt(5)\n"
    "v_mfma_f32_32x32x16_bf16 v[2:17], v[152:155], v[216:219], v[2:17]\nds_read_b128 v[216:219], v51 offset:3072\ns_waitcnt lgkmcnt(5)\n"
    "v_mfma_f32_32x32x16_bf16 v[2:17], v[156:159], v[196:199], v[2:17]\nds_read_b128 v[196:199], v51 offset:4096\ns_waitcnt lgkmcnt(5)\n"
    "v_mfma_f32_32x32x16_bf16 v[2:17], v[160:163], v[200:203], v[2:17]\nds_read_b128 v[200:203], v51 offset:5120\ns_waitcnt lgkmcnt(5)\n"
    "v_mfma_f32_32x32x16_bf16 v[2:17], v[164:167], v[204:207], v[2:17]\nds_read_b128 v[204:207], v51 offset:6144\ns_waitcnt lgkmcnt(5)\n"
    "v_mfma_f32_32x32x16_bf16 v[2:17], v[168:171], v[208:211], v[2:17]\nds_read_b128 v[208:211], v51 offset:7168\ns_waitcnt lgkmcnt(5)\n"
    "v_mfma_f32_32x32x16_bf16 v[2:17], v[172:175], v[212:215], v[2:17]\ns_barrier\nds_read_b128 v[212:215], v51 offset:8192\ns_waitcnt lgkmcnt(5)\n"
    "v_mfma_f32_32x32x16_bf16 v[2:17], v[176:179], v[216:219], v[2:17]\nds_read_b128 v[216:219], v51 offset:9216\ns_waitcnt lgkmcnt(5)\n"
    "v_mfma_f32_32x32x16_bf16 v[2:17], v[180:183], v[196:199], v[2:17]\nds_read_b128 v[196:199], v51 offset:10240\ns_waitcnt lgkmcnt(5)\n"
    "v_mfma_f32_32x32x16_bf16 v[2:17], v[184:187], v[200:203], v[2:17]\nds_read_b128 v[200:203], v51 offset:11264\ns_waitcnt lgkmcnt(5)\n"
    "v_mfma_f32_32x32x16_bf16 v[2:17], v[188:191], v[204:207], v[2:17]\nds_read_b128 v[204:207], v51 offset:12288\ns_waitcnt lgkmcnt(5)\n"
    "v_mfma_f32_32x32x16_bf16 v[2:17], v[192:195], v[208:211], v[2:17]\nds_read_b128 v[208:211], v51 offset:13312\ns_waitcnt lgkmcnt(5)\n"
    "v_mfma_f32_32x32x16_bf16 v[2:17], v[64:67], v[212:215], v[2:17]\nds_read_b128 v[212:215], v51 offset:14336\ns_waitcnt lgkmcnt(5)\n"
    "v_mfma_f32_32x32x16_bf16 v[2:17], v[68:71], v[216:219], v[2:17]\nds_read_b128 v[216:219], v51 offset:15360\ns_waitcnt lgkmcnt(5)\n"
    "v_mfma_f32_32x32x16_bf16 v[2:17], v[72:75], v[196:199], v[2:17]\ns_barrier\nds_read_b128 v[196:199], v51 offset:16384\ns_waitcnt lgkmcnt(5)\n"
    "v_mfma_f32_32x32x16_bf16 v[2:17], v[76:79], v[200:203], v[2:17]\nds_read_b128 v[200:203], v51 offset:17408\ns_waitcnt lgkmcnt(5)\n"
    "v_mfma_f32_32x32x16_bf16 v[2:17], v[80:83], v[204:207], v[2:17]\nds_read_b128 v[204:207], v51 offset:18432\ns_waitcnt lgkmcnt(5)\n"
    "v_mfma_f32_32x32x16_bf16 v[2:17], v[84:87], v[208:211], v[2:17]\nds_read_b128 v[208:211], v51 offset:19456\ns_waitcnt lgkmcnt(5)\n"
    "v_mfma_f32_32x32x16_bf16 v[2:17], v[88:91], v[212:215], v[2:17]\nds_read_b128 v[212:215], v51 offset:20480\ns_waitcnt lgkmcnt(5)\n"
    "v_mfma_f32_32x32x16_bf16 v[2:17], v[92:95], v[216:219], v[2:17]\nds_read_b128 v[216:219], v51 offset:21504\ns_waitcnt lgkmcnt(5)\n"
    "v_mfma_f32_32x32x16_bf16 v[2:17], v[96:99], v[196:199], v[2:17]\nds_read_b128 v[196:199], v51 offset:22528\ns_waitcnt lgkmcnt(5)\n"
    "v_mfma_f32_32x32x16_bf16 v[2:17], v[100:103], v[200:203], v[2:17]\nds_read_b128 v[200:203], v51 offset:23552\ns_cmp_eq_u32 s6, 0\ns_cbranch_scc0 .LL30\ns_waitcnt vmcnt(0)\n"
    "v_permlane32_swap_b32 v240, v242\nv_permlane32_swap_b32 v241, v243\nv_permlane32_swap_b32 v244, v246\nv_permlane32_swap_b32 v245, v247\n.LL30:\ns_waitcnt lgkmcnt(5)\n"
    "v_mfma_f32_32x32x16_bf16 v[2:17], v[104:107], v[204:207], v[2:17]\ns_waitcnt lgkmcnt(4)\nv_mfma_f32_32x32x16_bf16 v[2:17], v[108:111], v[208:211], v[2:17]\n"
    "v_lshlrev_b32 v220, 16, v240\nv_and_b32 v221, 0xffff0000, v240\nv_lshlrev_b32 v222, 16, v241\nv_and_b32 v223, 0xffff0000, v241\ns_waitcnt lgkmcnt(3)\n"
    "v_mfma_f32_32x32x16_bf16 v[2:17], v[112:115], v[212:215], v[2:17]\nv_lshlrev_b32 v224, 16, v242\nv_and_b32 v225, 0xffff0000, v242\nv_lshlrev_b32 v226, 16, v243\n"
    "v_and_b32 v227, 0xffff0000, v243\ns_waitcnt lgkmcnt(2)\nv_mfma_f32_32x32x16_bf16 v[2:17], v[116:119], v[216:219], v[2:17]\nv_lshlrev_b32 v228, 16, v244\n"
    "v_and_b32 v229, 0xffff0000, v244\nv_lshlrev_b32 v230, 16, v245\nv_and_b32 v231, 0xffff0000, v245\ns_waitcnt lgkmcnt(1)\n"
    "v_mfma_f32_32x32x16_bf16 v[2:17], v[120:123], v[196:199], v[2:17]\nv_lshlrev_b32 v232, 16, v246\nv_and_b32 v233, 0xffff0000, v246\nv_lshlrev_b32 v234, 16, v247\n"
    "v_and_b32 v235, 0xffff0000, v247\ns_waitcnt lgkmcnt(0)\nv_mfma_f32_32x32x16_bf16 v[2:17], v[124:127], v[200:203], v[2:17]\ns_nop 11\n"
    "v_pk_add_f32 v[2:3], v[2:3], v[220:221]\nv_pk_add_f32 v[4:5], v[4:5], v[222:223]\nv_pk_add_f32 v[6:7], v[6:7], v[224:225]\nv_pk_add_f32 v[8:9], v[8:9], v[226:227]\n"
    "v_pk_add_f32 v[10:11], v[10:11], v[228:229]\nv_pk_add_f32 v[12:13], v[12:13], v[230:231]\nv_pk_add_f32 v[14:15], v[14:15], v[232:233]\n"
    "v_pk_add_f32 v[16:17], v[16:17], v[234:235]\nv_cvt_pk_bf16_f32 v40, v2, v3\nv_cvt_pk_bf16_f32 v41, v4, v5\nds_write2_b32 v48, v40, v41 offset0:0 offset1:32\n"
    "v_cvt_pk_bf16_f32 v42, v6, v7\nv_cvt_pk_bf16_f32 v43, v8, v9\nds_write2_b32 v48, v42, v43 offset0:128 offset1:160\nv_cvt_pk_bf16_f32 v44, v10, v11\n"
    "v_cvt_pk_bf16_f32 v45, v12, v13\nds_write2_b32 v49, v44, v45 offset0:0 offset1:32\nv_cvt_pk_bf16_f32 v46, v14, v15\nv_cvt_pk_bf16_f32 v47, v16, v17\n"
    "ds_write2_b32 v49, v46, v47 offset0:128 offset1:160\nds_read_b128 v[2:5], v50\nds_read_b128 v[6:9], v50 offset:1024\nv_dot2_f32_bf16 v32, v40, v36, 0\n"
    "v_dot2_f32_bf16 v33, v40, v40, 0\nv_dot2_f32_bf16 v34, v41, v36, 0\nv_dot2_f32_bf16 v35, v41, v41, 0\nv_dot2c_f32_bf16 v32, v36, v42\nv_dot2c_f32_bf16 v33, v42, v42\n"
    "v_dot2c_f32_bf16 v34, v36, v43\nv_dot2c_f32_bf16 v35, v43, v43\nv_dot2c_f32_bf16 v32, v36, v44\nv_dot2c_f32_bf16 v33, v44, v44\nv_dot2c_f32_bf16 v34, v36, v45\n"
    "v_dot2c_f32_bf16 v35, v45, v45\nv_dot2c_f32_bf16 v32, v36, v46\nv_dot2c_f32_bf16 v33, v46, v46\nv_dot2c_f32_bf16 v34, v36, v47\nv_dot2c_f32_bf16 v35, v47, v47\ns_nop 1\n"
    "v_add_f32 v32, v32, v34\nv_add_f32 v33, v33, v35\ns_nop 1\nv_permlane32_swap_b32 v32, v33\nv_add_f32 v32, v32, v33\nds_write_b32 v54, v32\ns_waitcnt lgkmcnt(0)\ns_barrier\n"
    "global_store_dwordx4 v52, v[2:5], s[32:33] sc1\nglobal_store_dwordx4 v53, v[6:9], s[32:33] sc1\ns_setprio 0\ns_mov_b32 s14, 0x1d550\nv_lshl_add_u32 v14, v0, 3, s14\n"
    "v_lshrrev_b32 v48, 3, v0\nv_lshl_add_u32 v14, v48, 4, v14\ns_mov_b32 s15, 0x20300\nv_lshl_add_u32 v15, v48, 7, s15\nv_xor_b32 v50, v0, v48\nv_and_b32 v49, 7, v50\n"
    "v_lshl_add_u32 v15, v49, 4, v15\ns_mov_b32 s16, 0x17800\nv_lshl_add_u32 v16, v0, 4, s16\ns_lshl_b32 s18, s41, 7\ns_add_u32 s17, s18, 0x600000\n"
    "v_lshl_add_u32 v51, v48, 15, s17\nv_and_b32 v52, 1, v0\nv_lshl_add_u32 v51, v52, 6, v51\nv_bfe_u32 v53, v0, 1, 2\nv_lshl_add_u32 v51, v53, 4, v51\n"
    "v_lshrrev_b32 v55, 4, v0\ns_lshl_b32 s21, s35, 18\ns_lshl_b32 s22, s36, 8\ns_add_u32 s20, s21, s22\ns_add_u32 s19, s20, 0x1200000\nv_lshl_add_u32 v54, v55, 13, s19\n"
    "v_and_b32 v56, 15, v0\nv_lshl_add_u32 v54, v56, 4, v54\ns_cmp_eq_u32 s6, 0\ns_cselect_b64 vcc, -1, 0\nv_cndmask_b32 v17, v54, v51, vcc\nv_bfe_u32 v59, v0, 5, 1\n"
    "v_and_b32 v61, 31, v0\nv_sub_u32 v60, 0x3ff, v61\nv_lshl_add_u32 v58, v59, 3, v60\nv_lshrrev_b32 v57, 2, v58\ns_lshr_b32 s25, s3, 1\ns_mul_i32 s24, s25, 0x2100\n"
    "s_add_u32 s23, s24, 0x17800\nv_lshl_add_u32 v19, v57, 3, s23\nv_and_b32 v62, 3, v58\ns_mov_b32 s44, 0x840\nv_mad_u32_u24 v19, v62, s44, v19\n"
    "s_lshl2_add_u32 s45, s3, 0x27f80\nv_mov_b32 v12, s45\ns_add_u32 s46, s6, 12\nv_mov_b32 v13, s46\ns_mov_b32 s47, 0x1cc34\nv_lshl_add_u32 v5, v0, 3, s47\n"
    "v_add_u32 v64, 0xffffff86, v0\nv_lshrrev_b32 v63, 3, v64\nv_lshl_add_u32 v5, v63, 4, v5\ns_mov_b32 s48, 0x1cc38\nv_lshl_add_u32 v6, v0, 3, s48\n"
    "v_add_u32 v66, 0xffffff87, v0\nv_lshrrev_b32 v65, 3, v66\nv_lshl_add_u32 v6, v65, 4, v6\nv_bfe_u32 v68, v0, 2, 2\nv_lshl_add_u32 v67, v59, 3, v68\n"
    "s_mul_i32 s50, s25, 0x3180\ns_add_u32 s49, s50, 0x20300\nv_lshl_add_u32 v2, v67, 4, s49\ns_cmp_eq_u32 s6, 0\ns_cbranch_scc1 .LL46\ns_sleep 9\ns_branch .LL47\n.LL46:\n"
    "s_barrier\ns_sleep 0\n.LL47:\nv_lshl_add_u32 v2, v52, 3, v2\ns_and_b32 s52, s3, 1\ns_xor_b32 s51, s52, 1\nv_and_b32 v70, 3, v55\nv_and_b32 v69, s51, v70\n"
    "s_mov_b32 s53, 0x480\nv_mad_u32_u24 v2, v69, s53, v2\nv_bfe_u32 v71, v0, 1, 1\ns_mov_b32 s54, 0x240\nv_mad_u32_u24 v2, v71, s54, v2\ns_add_u32 s55, s49, 0x1f80\n"
    "v_lshl_add_u32 v4, v67, 5, s55\nv_and_b32 v72, 3, v0\nv_lshl_add_u32 v4, v72, 3, v4\nv_and_b32 v73, 1, v55\nv_mad_u32_u24 v3, v73, s53, v4\ns_mul_i32 s57, s25, 0x2800\n"
    "s_add_u32 s56, s57, 0x18000\nv_lshl_add_u32 v7, v61, 5, s56\nv_lshl_add_u32 v7, v59, 4, v7\ns_add_u32 s58, s57, 0x19800\nv_lshl_add_u32 v8, v61, 6, s58\n"
    "v_lshrrev_b32 v76, 1, v0\nv_and_b32 v75, 2, v76\nv_xor_b32 v74, v59, v75\nv_lshl_add_u32 v8, v74, 4, v8\nv_lshlrev_b32 v78, 4, v0\nv_and_b32 v80, 16, v0\n"
    "v_lshlrev_b32 v79, 1, v80\nv_xor_b32 v77, v78, v79\nv_add_u32 v9, 0x18800, v77\ns_lshl_b32 s59, s51, 8\nv_add_u32 v10, s59, v19\nv_and_b32 v81, 63, v0\n"
    "s_mul_i32 s62, s25, 0x1950\ns_mul_i32 s63, s52, 0x500\ns_add_u32 s61, s62, s63\ns_add_u32 s60, s61, 0x1d530\nv_lshl_add_u32 v40, v81, 4, s60\nv_add_u32 v83, 3, v81\n"
    "v_lshrrev_b32 v82, 2, v83\nv_lshl_add_u32 v40, v82, 4, v40\ns_add_u32 s64, s61, 0x1d528\nv_lshl_add_u32 v41, v81, 4, s64\nv_add_u32 v85, 2, v81\nv_lshrrev_b32 v84, 2, v85\n"
    "v_lshl_add_u32 v41, v84, 4, v41\ns_add_u32 s65, s61, 0x1d550\nv_lshl_add_u32 v42, v81, 4, s65\nv_bfe_u32 v86, v0, 2, 4\nv_lshl_add_u32 v42, v86, 4, v42\n"
    "s_mul_i32 s68, s52, 0x480\ns_add_u32 s67, s49, s68\ns_add_u32 s66, s67, 0x250\nv_lshl_add_u32 v43, v81, 4, s66\nv_add_u32 v88, 1, v81\nv_lshrrev_b32 v87, 5, v88\n"
    "v_lshl_add_u32 v43, v87, 6, v43\ns_add_u32 s69, s67, 0x260\nv_lshl_add_u32 v44, v81, 4, s69\nv_lshrrev_b32 v89, 5, v85\nv_lshl_add_u32 v44, v89, 6, v44\n"
    "s_add_u32 s70, s67, 0x1f90\nv_lshl_add_u32 v90, v81, 4, s70\nv_lshrrev_b32 v91, 6, v88\nv_lshl_add_u32 v90, v91, 7, v90\ns_add_u32 s73, s67, 0x1f80\n"
    "s_add_u32 s72, s73, 0x400\ns_add_u32 s71, s72, 0xfffffb80\nv_mov_b32 v92, s71\nv_add_u32 v93, s52, v88\nv_cmp_gt_u32 vcc, 0x41, v93\ns_nop 1\n"
    "v_cndmask_b32 v45, v92, v90, vcc\ns_add_u32 s74, s73, 0x900\nv_lshl_add_u32 v29, v81, 4, s74\ns_add_u32 s76, s67, 0x440\ns_add_u32 s75, s76, 0xfffff700\n"
    "v_mov_b32 v94, s75\nv_cmp_gt_u32 vcc, 64, v88\ns_nop 1\nv_cndmask_b32 v30, v94, v43, vcc\nv_cmp_gt_u32 vcc, 64, v85\ns_nop 1\nv_cndmask_b32 v31, v94, v44, vcc\n"
    "s_lshl3_add_u32 s77, s25, 0x27fd0\nv_lshl_add_u32 v11, v52, 2, s77\nv_mov_b32 v34, 125952\nv_mov_b32 v32, 163216\ns_barrier\ns_cmp_eq_u32 s6, 0\ns_cbranch_scc0 .LL48\n"
    "ds_read_b64 v[32:33], v32\nds_read_u16 v35, v34 offset:1536\nds_read_u16 v34, v34\ns_waitcnt lgkmcnt(0)\ns_barrier\n.LL48:\n"
    "global_load_dwordx4 v[240:243], v21, s[32:33] sc1\nglobal_load_dwordx4 v[208:211], v20, s[32:33] offset:-4096 sc1\n"
    "global_load_dwordx4 v[212:215], v22, s[32:33] offset:-4096 sc1\nglobal_load_dwordx4 v[216:219], v23, s[32:33] offset:-4096 sc1\n"
    "global_load_dwordx4 v[220:223], v24, s[32:33] offset:-4096 sc1\nglobal_load_dwordx4 v[224:227], v25, s[32:33] offset:-4096 sc1\n"
    "global_load_dwordx4 v[228:231], v26, s[32:33] offset:-4096 sc1\nglobal_load_dwordx4 v[232:235], v27, s[32:33] offset:-4096 sc1\n"
    "global_load_dwordx4 v[236:239], v28, s[32:33] offset:-4096 sc1\ns_setprio 0\ns_cmp_eq_u32 s6, 0\ns_cbranch_scc0 .LL31\nv_cvt_f32_f16 v34, v34\nv_cvt_f32_f16 v35, v35\n"
    "v_mul_f32_e64 v34, -v34, v32\nv_mul_f32_e64 v35, -v35, v33\nv_readfirstlane_b32 s88, v32\nv_readfirstlane_b32 s90, v33\nv_readfirstlane_b32 s92, v34\n"
    "v_readfirstlane_b32 s94, v35\n.LL31:\nds_read_b64 v[64:65], v19 offset:896\nds_read_b64 v[66:67], v19 offset:904\nds_read_b64 v[68:69], v19 offset:864\n"
    "ds_read_b64 v[70:71], v19 offset:872\nds_read_b64 v[72:73], v19 offset:832\nds_read_b64 v[74:75], v19 offset:840\nds_read_b64 v[76:77], v19 offset:800\n"
    "ds_read_b64 v[78:79], v19 offset:808\nds_read_b64 v[80:81], v19 offset:704\nds_read_b64 v[82:83], v19 offset:712\nds_read_b64 v[84:85], v19 offset:672\n"
    "ds_read_b64 v[86:87], v19 offset:680\nds_read_b64 v[88:89], v19 offset:640\nds_read_b64 v[90:91], v19 offset:648\nds_read_b64 v[92:93], v19 offset:608\n"
    "ds_read_b64 v[94:95], v19 offset:616\nds_read_b64 v[96:97], v10 offset:512\nds_read_b64 v[98:99], v10 offset:520\nds_read_b64 v[100:101], v10 offset:480\n"
    "ds_read_b64 v[102:103], v10 offset:488\nds_read_b64 v[104:105], v10 offset:448\nds_read_b64 v[106:107], v10 offset:456\nds_read_b64 v[108:109], v10 offset:416\n"
    "ds_read_b64 v[110:111], v10 offset:424\nds_read_b64 v[112:113], v10 offset:320\nds_read_b64 v[114:115], v10 offset:328\nds_read_b64 v[116:117], v10 offset:288\n"
    "ds_read_b64 v[118:119], v10 offset:296\nds_read_b64 v[120:121], v10 offset:256\nds_read_b64 v[122:123], v10 offset:264\nds_read_b64 v[124:125], v10 offset:224\n"
    "ds_read_b64 v[126:127], v10 offset:232\ns_waitcnt lgkmcnt(0)\ns_mov_b32 s84, 0x3b000000\ns_mov_b32 s86, 0x3727c5ac\ns_mov_b32 s80, 0x10000\ns_waitcnt vmcnt(8)\n.Lln34:\n"
    "v_max3_u32 v60, v240, v241, v242\nv_max_u32 v60, v60, v243\nv_cmp_eq_u32 s[82:83], -1, v60\nv_lshlrev_b32 v32, 16, v240\nv_and_b32 v36, 0xffff0000, v240\n"
    "v_lshlrev_b32 v33, 16, v241\nv_and_b32 v37, 0xffff0000, v241\nv_lshlrev_b32 v34, 16, v242\nv_and_b32 v38, 0xffff0000, v242\nv_lshlrev_b32 v35, 16, v243\n"
    "v_and_b32 v39, 0xffff0000, v243\ns_waitcnt vmcnt(4)\nv_pk_add_f32 v[48:49], v[208:209], v[216:217]\nv_pk_add_f32 v[50:51], v[210:211], v[218:219]\n"
    "v_pk_add_f32 v[52:53], v[212:213], v[220:221]\nv_pk_add_f32 v[54:55], v[214:215], v[222:223]\ns_waitcnt vmcnt(1)\nv_pk_add_f32 v[56:57], v[224:225], v[232:233]\n"
    "v_pk_add_f32 v[58:59], v[226:227], v[234:235]\nv_pk_add_f32 v[48:49], v[48:49], v[56:57]\nv_pk_add_f32 v[50:51], v[50:51], v[58:59]\n"
    "v_pk_mul_f32 v[48:49], v[48:49], s[84:85] op_sel_hi:[1,0]\nv_pk_mul_f32 v[50:51], v[50:51], s[84:85] op_sel_hi:[1,0]\n"
    "v_pk_fma_f32 v[60:61], v[48:49], v[48:49], s[86:87] op_sel_hi:[1,1,0] neg_lo:[0,0,1] neg_hi:[0,0,1]\n"
    "v_pk_fma_f32 v[62:63], v[50:51], v[50:51], s[86:87] op_sel_hi:[1,1,0] neg_lo:[0,0,1] neg_hi:[0,0,1]\ns_waitcnt vmcnt(0)\nv_pk_add_f32 v[56:57], v[228:229], v[236:237]\n"
    "v_pk_add_f32 v[58:59], v[230:231], v[238:239]\nv_pk_add_f32 v[52:53], v[52:53], v[56:57]\nv_pk_add_f32 v[54:55], v[54:55], v[58:59]\n"
    "v_pk_fma_f32 v[52:53], v[52:53], s[84:85], v[60:61] op_sel_hi:[1,0,1] neg_lo:[0,0,1] neg_hi:[0,0,1]\n"
    "v_pk_fma_f32 v[54:55], v[54:55], s[84:85], v[62:63] op_sel_hi:[1,0,1] neg_lo:[0,0,1] neg_hi:[0,0,1]\nv_pk_add_f32 v[56:57], v[52:53], v[54:55]\nv_max_f32 v52, s86, v52\n"
    "v_max_f32 v53, s86, v53\nv_max_f32 v54, s86, v54\nv_max_f32 v55, s86, v55\nv_cmp_u_f32 vcc, v56, v57\nv_rsq_f32 v52, v52\nv_rsq_f32 v53, v53\nv_rsq_f32 v54, v54\n"
    "v_rsq_f32 v55, v55\ns_or_b64 vcc, vcc, s[82:83]\ns_cbranch_vccnz .Lls36\n.Llo35:\nv_pk_mul_f32 v[48:49], v[48:49], v[52:53]\nv_pk_mul_f32 v[50:51], v[50:51], v[54:55]\n"
    "v_pk_fma_f32 v[244:245], v[32:33], v[52:53], v[48:49] neg_lo:[0,0,1] neg_hi:[0,0,1]\nv_pk_fma_f32 v[246:247], v[34:35], v[54:55], v[50:51] neg_lo:[0,0,1] neg_hi:[0,0,1]\n"
    "v_pk_fma_f32 v[248:249], v[36:37], v[52:53], v[48:49] neg_lo:[0,0,1] neg_hi:[0,0,1]\nv_pk_fma_f32 v[250:251], v[38:39], v[54:55], v[50:51] neg_lo:[0,0,1] neg_hi:[0,0,1]\n"
    "v_cvt_pk_f16_f32 v56, v244, v245\nv_cvt_pk_f16_f32 v57, v246, v247\nds_write_b64 v14, v[56:57] offset:0\nv_cvt_pk_f16_f32 v58, v248, v249\n"
    "v_cvt_pk_f16_f32 v59, v250, v251\nds_write_b64 v14, v[58:59] offset:6480\ns_waitcnt lgkmcnt(0)\ns_barrier\ns_bitcmp1_b32 s3, 0\ns_cbranch_scc1 .LL50\ns_setprio 0\n"
    "ds_read_b64 v[196:197], v41\nds_read_b128 v[198:201], v40\nds_read_b64 v[202:203], v42\nds_read_b64 v[48:49], v41 offset:2560\nds_read_b128 v[50:53], v40 offset:2560\n"
    "ds_read_b64 v[54:55], v42 offset:2560\ns_waitcnt lgkmcnt(3)\nv_alignbit_b32 v204, v197, v196, 16\nv_alignbit_b32 v205, v198, v197, 16\nv_alignbit_b32 v206, v199, v198, 16\n"
    "v_alignbit_b32 v207, v200, v199, 16\nv_alignbit_b32 v208, v201, v200, 16\nv_alignbit_b32 v209, v202, v201, 16\nv_alignbit_b32 v210, v203, v202, 16\n"
    "v_pk_add_f16 v212, v196, v204\nv_pk_add_f16 v213, v197, v205\nv_pk_add_f16 v214, v198, v206\nv_pk_add_f16 v215, v199, v207\nv_pk_add_f16 v216, v200, v208\n"
    "v_pk_add_f16 v217, v201, v209\nv_pk_add_f16 v218, v202, v210\nv_pk_add_f16 v211, v214, v215\nv_pk_add_f16 v219, v216, v217\nv_pk_add_f16 v232, v205, v214\n"
    "v_pk_add_f16 v233, v206, v215\nv_pk_add_f16 v234, v207, v216\nv_pk_add_f16 v235, v208, v217\nds_write_b64 v43, v[232:233] offset:4040\nv_pk_add_f16 v228, v213, v214\n"
    "v_pk_add_f16 v230, v215, v216\nds_write_b64 v44, v[234:235] offset:4032\nv_pk_add_f16 v229, v211, v200\nv_pk_add_f16 v231, v219, v202\nv_pk_add_f16 v228, v228, v199\n"
    "v_pk_add_f16 v230, v230, v201\nv_pk_add_f16 v236, v213, v211\nv_pk_add_f16 v237, v211, v216\nds_write_b128 v43, v[228:231]\nv_pk_add_f16 v238, v215, v219\n"
    "v_pk_add_f16 v239, v219, v218\nv_pk_add_f16 v220, v212, v200\nv_pk_add_f16 v221, v213, v201\nv_pk_add_f16 v222, v214, v202\nv_pk_add_f16 v223, v215, v203\n"
    "v_pk_add_f16 v224, v236, v204\nv_pk_add_f16 v225, v237, v205\nv_pk_add_f16 v226, v238, v206\nv_pk_add_f16 v227, v239, v207\nds_write_b128 v29, v[224:227]\n"
    "v_pk_add_f16 v220, v220, v236\nv_pk_add_f16 v221, v221, v237\nv_pk_add_f16 v222, v222, v238\nv_pk_add_f16 v223, v223, v239\nds_write_b128 v45, v[220:223]\n"
    "s_mov_b64 exec, 1\nds_write_b32 v12, v13 offset:80\ns_mov_b64 exec, -1\ns_waitcnt lgkmcnt(6)\nv_alignbit_b32 v204, v50, v49, 16\nv_alignbit_b32 v205, v51, v50, 16\n"
    "v_alignbit_b32 v206, v52, v51, 16\nv_alignbit_b32 v207, v53, v52, 16\nv_alignbit_b32 v208, v54, v53, 16\nv_pk_add_f16 v212, v49, v204\nv_pk_add_f16 v213, v50, v205\n"
    "v_pk_add_f16 v214, v51, v206\nv_pk_add_f16 v215, v52, v207\nv_pk_add_f16 v216, v53, v208\nv_pk_add_f16 v60, v204, v213\nv_pk_add_f16 v61, v205, v214\n"
    "v_pk_add_f16 v62, v206, v215\nv_pk_add_f16 v63, v207, v216\nds_write_b64 v30, v[60:61] offset:6344\nv_pk_add_f16 v56, v212, v51\nv_pk_add_f16 v57, v213, v52\n"
    "v_pk_add_f16 v58, v214, v53\nv_pk_add_f16 v59, v215, v54\nds_write_b64 v31, v[62:63] offset:6336\nv_pk_add_f16 v56, v56, v213\nv_pk_add_f16 v57, v57, v214\n"
    "v_pk_add_f16 v58, v58, v215\nv_pk_add_f16 v59, v59, v216\nds_write_b128 v30, v[56:59] offset:2304\ns_mov_b32 s80, 0x100000\n.Lsl53:\nds_read_b32 v28, v11\n"
    "s_waitcnt lgkmcnt(0)\nv_cmp_gt_i32 vcc, v13, v28\ns_cbranch_vccz .Lsr52\ns_sleep 0\ns_sub_u32 s80, s80, 1\ns_cbranch_scc0 .Lsl53\n.Lsr52:\ns_setprio 0\n"
    "ds_read_b64_tr_b16 v[48:49], v2 offset:1984\nds_read_b64_tr_b16 v[50:51], v2 offset:2048\nds_read_b64_tr_b16 v[52:53], v2 offset:1728\n"
    "ds_read_b64_tr_b16 v[54:55], v2 offset:1792\nds_read_b64_tr_b16 v[56:57], v2 offset:1408\nds_read_b64_tr_b16 v[58:59], v2 offset:1472\n"
    "ds_read_b64_tr_b16 v[60:61], v2 offset:1152\nds_read_b64_tr_b16 v[62:63], v2 offset:1216\nds_read_b32 v22, v5\nds_read_b32 v23, v5 offset:6480\nds_read_b64 v[46:47], v6\n"
    "ds_read_b64 v[20:21], v6 offset:6480\ns_waitcnt lgkmcnt(10)\nv_mfma_f32_32x32x16_f16 v[196:211], v[48:51], v[64:67], 0\nds_read_b64_tr_b16 v[48:49], v2 offset:832\n"
    "ds_read_b64_tr_b16 v[50:51], v2 offset:896\nv_fmac_f32 v32, s96, v244\nv_fmac_f32 v33, s96, v245\ns_waitcnt lgkmcnt(10)\n"
    "v_mfma_f32_32x32x16_f16 v[196:211], v[52:55], v[68:71], v[196:211]\nds_read_b64_tr_b16 v[52:53], v2 offset:576\nds_read_b64_tr_b16 v[54:55], v2 offset:640\n"
    "v_fmac_f32 v34, s96, v246\nv_fmac_f32 v35, s96, v247\ns_waitcnt lgkmcnt(10)\nv_mfma_f32_32x32x16_f16 v[196:211], v[56:59], v[72:75], v[196:211]\n"
    "ds_read_b64_tr_b16 v[56:57], v2 offset:6016\nds_read_b64_tr_b16 v[58:59], v2 offset:6080\nv_fmac_f32 v36, s97, v248\nv_fmac_f32 v37, s97, v249\ns_waitcnt lgkmcnt(10)\n"
    "v_mfma_f32_32x32x16_f16 v[196:211], v[60:63], v[76:79], v[196:211]\nds_read_b64_tr_b16 v[60:61], v2 offset:5760\nds_read_b64_tr_b16 v[62:63], v2 offset:5824\n"
    "v_fmac_f32 v38, s97, v250\nv_fmac_f32 v39, s97, v251\ns_waitcnt lgkmcnt(6)\nv_mfma_f32_32x32x16_f16 v[196:211], v[48:51], v[96:99], v[196:211]\n"
    "ds_read_b64_tr_b16 v[48:49], v2 offset:5440\nds_read_b64_tr_b16 v[50:51], v2 offset:5504\nv_cvt_f32_f16_sdwa v24, v22 dst_sel:DWORD dst_unused:UNUSED_PAD src0_sel:WORD_1\n"
    "v_cvt_f32_f16_sdwa v26, v46 dst_sel:DWORD dst_unused:UNUSED_PAD src0_sel:WORD_1\ns_waitcnt lgkmcnt(6)\n"
    "v_mfma_f32_32x32x16_f16 v[196:211], v[52:55], v[100:103], v[196:211]\nds_read_b64_tr_b16 v[52:53], v2 offset:5184\nds_read_b64_tr_b16 v[54:55], v2 offset:5248\n"
    "v_cvt_f32_f16 v25, v46\nv_cvt_f32_f16 v27, v47\ns_waitcnt lgkmcnt(6)\nv_mfma_f32_32x32x16_f16 v[196:211], v[56:59], v[80:83], v[196:211]\n"
    "ds_read_b64_tr_b16 v[56:57], v2 offset:4864\nds_read_b64_tr_b16 v[58:59], v2 offset:4928\nv_cvt_f32_f16_sdwa v28, v23 dst_sel:DWORD dst_unused:UNUSED_PAD src0_sel:WORD_1\n"
    "v_cvt_f32_f16_sdwa v30, v20 dst_sel:DWORD dst_unused:UNUSED_PAD src0_sel:WORD_1\ns_waitcnt lgkmcnt(6)\nv_mfma_f32_32x32x16_f16 v[196:211], v[60:63], v[84:87], v[196:211]\n"
    "ds_read_b64_tr_b16 v[60:61], v2 offset:4608\nds_read_b64_tr_b16 v[62:63], v2 offset:4672\ns_mov_b64 exec, 1\nds_write_b32 v12, v13 offset:64\ns_mov_b64 exec, -1\n"
    "s_waitcnt lgkmcnt(7)\nv_mfma_f32_32x32x16_f16 v[196:211], v[48:51], v[88:91], v[196:211]\nv_cvt_f32_f16 v29, v20\nv_cvt_f32_f16 v31, v21\ns_waitcnt lgkmcnt(5)\n"
    "v_mfma_f32_32x32x16_f16 v[196:211], v[52:55], v[92:95], v[196:211]\nv_fmac_f32 v32, s92, v24\nv_fmac_f32 v33, s92, v25\ns_waitcnt lgkmcnt(3)\n"
    "v_mfma_f32_32x32x16_f16 v[196:211], v[56:59], v[112:115], v[196:211]\nv_fmac_f32 v34, s92, v26\nv_fmac_f32 v35, s92, v27\ns_waitcnt lgkmcnt(1)\n"
    "v_mfma_f32_32x32x16_f16 v[196:211], v[60:63], v[116:119], v[196:211]\nv_fmac_f32 v36, s94, v28\nv_fmac_f32 v37, s94, v29\nv_fmac_f32 v38, s94, v30\n"
    "v_fmac_f32 v39, s94, v31\ns_nop 7\nds_write_b128 v7, v[196:199] offset:2048\nds_write_b128 v7, v[200:203] offset:3072\nds_write_b128 v7, v[204:207] offset:4096\n"
    "ds_write_b128 v7, v[208:211] offset:5120\ns_setprio 0\ns_branch .LL51\n.LL50:\ns_setprio 0\nds_read_b64 v[196:197], v41\nds_read_b128 v[198:201], v40\n"
    "ds_read_b64 v[202:203], v42\ns_waitcnt lgkmcnt(0)\nv_alignbit_b32 v204, v197, v196, 16\nv_alignbit_b32 v205, v198, v197, 16\nv_alignbit_b32 v206, v199, v198, 16\n"
    "v_alignbit_b32 v207, v200, v199, 16\nv_alignbit_b32 v208, v201, v200, 16\nv_alignbit_b32 v209, v202, v201, 16\nv_alignbit_b32 v210, v203, v202, 16\n"
    "v_pk_add_f16 v212, v196, v204\nv_pk_add_f16 v213, v197, v205\nv_pk_add_f16 v214, v198, v206\nv_pk_add_f16 v215, v199, v207\nv_pk_add_f16 v216, v200, v208\n"
    "v_pk_add_f16 v217, v201, v209\nv_pk_add_f16 v218, v202, v210\nv_pk_add_f16 v211, v214, v215\nv_pk_add_f16 v219, v216, v217\nv_pk_add_f16 v232, v205, v214\n"
    "v_pk_add_f16 v233, v206, v215\nv_pk_add_f16 v234, v207, v216\nv_pk_add_f16 v235, v208, v217\nds_write_b64 v43, v[232:233] offset:4040\nv_pk_add_f16 v228, v213, v214\n"
    "v_pk_add_f16 v230, v215, v216\nds_write_b64 v44, v[234:235] offset:4032\nv_pk_add_f16 v229, v211, v200\nv_pk_add_f16 v231, v219, v202\nv_pk_add_f16 v228, v228, v199\n"
    "v_pk_add_f16 v230, v230, v201\nv_pk_add_f16 v236, v213, v211\nv_pk_add_f16 v237, v211, v216\nds_write_b128 v43, v[228:231]\nv_pk_add_f16 v238, v215, v219\n"
    "v_pk_add_f16 v239, v219, v218\nv_pk_add_f16 v220, v212, v200\nv_pk_add_f16 v221, v213, v201\nv_pk_add_f16 v222, v214, v202\nv_pk_add_f16 v223, v215, v203\n"
    "v_pk_add_f16 v224, v236, v204\nv_pk_add_f16 v225, v237, v205\nv_pk_add_f16 v226, v238, v206\nv_pk_add_f16 v227, v239, v207\nds_write_b128 v29, v[224:227]\n"
    "v_pk_add_f16 v220, v220, v236\nv_pk_add_f16 v221, v221, v237\nv_pk_add_f16 v222, v222, v238\nv_pk_add_f16 v223, v223, v239\nds_write_b128 v45, v[220:223]\n"
    "s_mov_b64 exec, 1\nds_write_b32 v12, v13 offset:80\ns_mov_b64 exec, -1\ns_mov_b32 s80, 0x100000\n.Lsl55:\nds_read_b32 v28, v11\ns_waitcnt lgkmcnt(0)\n"
    "v_cmp_gt_i32 vcc, v13, v28\ns_cbranch_vccz .Lsr54\ns_sleep 0\ns_sub_u32 s80, s80, 1\ns_cbranch_scc0 .Lsl55\n.Lsr54:\ns_setprio 0\n"
    "ds_read_b64_tr_b16 v[48:49], v3 offset:512\nds_read_b64_tr_b16 v[50:51], v3 offset:640\nds_read_b64_tr_b16 v[52:53], v3 offset:0\n"
    "ds_read_b64_tr_b16 v[54:55], v3 offset:128\nds_read_b64_tr_b16 v[56:57], v4 offset:512\nds_read_b64_tr_b16 v[58:59], v4 offset:640\n"
    "ds_read_b64_tr_b16 v[60:61], v4 offset:0\nds_read_b64_tr_b16 v[62:63], v4 offset:128\nds_read_b32 v22, v5\nds_read_b32 v23, v5 offset:6480\nds_read_b64 v[46:47], v6\n"
    "ds_read_b64 v[20:21], v6 offset:6480\ns_waitcnt lgkmcnt(10)\nv_mfma_f32_32x32x16_f16 v[228:243], v[48:51], v[96:99], 0\nds_read_b64_tr_b16 v[48:49], v3 offset:2816\n"
    "ds_read_b64_tr_b16 v[50:51], v3 offset:2944\nv_fmac_f32 v32, s96, v244\nv_fmac_f32 v33, s96, v245\ns_waitcnt lgkmcnt(10)\n"
    "v_mfma_f32_32x32x16_f16 v[228:243], v[52:55], v[100:103], v[228:243]\nds_read_b64_tr_b16 v[52:53], v3 offset:2304\nds_read_b64_tr_b16 v[54:55], v3 offset:2432\n"
    "v_fmac_f32 v34, s96, v246\nv_fmac_f32 v35, s96, v247\ns_waitcnt lgkmcnt(10)\nv_mfma_f32_32x32x16_f16 v[212:227], v[56:59], v[104:107], 0\n"
    "ds_read_b64_tr_b16 v[56:57], v4 offset:2816\nds_read_b64_tr_b16 v[58:59], v4 offset:2944\nv_fmac_f32 v36, s97, v248\nv_fmac_f32 v37, s97, v249\ns_waitcnt lgkmcnt(10)\n"
    "v_mfma_f32_32x32x16_f16 v[212:227], v[60:63], v[108:111], v[212:227]\nds_read_b64_tr_b16 v[60:61], v4 offset:2304\nds_read_b64_tr_b16 v[62:63], v4 offset:2432\n"
    "v_fmac_f32 v38, s97, v250\nv_fmac_f32 v39, s97, v251\ns_waitcnt lgkmcnt(6)\nv_mfma_f32_32x32x16_f16 v[228:243], v[48:51], v[112:115], v[228:243]\n"
    "ds_read_b64_tr_b16 v[48:49], v2 offset:832\nds_read_b64_tr_b16 v[50:51], v2 offset:896\nv_cvt_f32_f16_sdwa v24, v22 dst_sel:DWORD dst_unused:UNUSED_PAD src0_sel:WORD_1\n"
    "v_cvt_f32_f16_sdwa v26, v46 dst_sel:DWORD dst_unused:UNUSED_PAD src0_sel:WORD_1\ns_waitcnt lgkmcnt(6)\n"
    "v_mfma_f32_32x32x16_f16 v[228:243], v[52:55], v[116:119], v[228:243]\nds_read_b64_tr_b16 v[52:53], v2 offset:576\nds_read_b64_tr_b16 v[54:55], v2 offset:640\n"
    "v_cvt_f32_f16 v25, v46\nv_cvt_f32_f16 v27, v47\ns_waitcnt lgkmcnt(6)\nv_mfma_f32_32x32x16_f16 v[212:227], v[56:59], v[120:123], v[212:227]\n"
    "ds_read_b64_tr_b16 v[56:57], v2 offset:256\nds_read_b64_tr_b16 v[58:59], v2 offset:320\nv_cvt_f32_f16_sdwa v28, v23 dst_sel:DWORD dst_unused:UNUSED_PAD src0_sel:WORD_1\n"
    "v_cvt_f32_f16_sdwa v30, v20 dst_sel:DWORD dst_unused:UNUSED_PAD src0_sel:WORD_1\ns_waitcnt lgkmcnt(6)\n"
    "v_mfma_f32_32x32x16_f16 v[212:227], v[60:63], v[124:127], v[212:227]\nds_read_b64_tr_b16 v[60:61], v2 offset:0\nds_read_b64_tr_b16 v[62:63], v2 offset:64\n"
    "v_cvt_f32_f16 v29, v20\nv_cvt_f32_f16 v31, v21\ns_waitcnt lgkmcnt(6)\nv_mfma_f32_32x32x16_f16 v[196:211], v[48:51], v[64:67], 0\n"
    "ds_read_b64_tr_b16 v[48:49], v2 offset:4864\nds_read_b64_tr_b16 v[50:51], v2 offset:4928\nv_fmac_f32 v32, s92, v24\nv_fmac_f32 v33, s92, v25\ns_waitcnt lgkmcnt(6)\n"
    "v_mfma_f32_32x32x16_f16 v[196:211], v[52:55], v[68:71], v[196:211]\nds_read_b64_tr_b16 v[52:53], v2 offset:4608\nds_read_b64_tr_b16 v[54:55], v2 offset:4672\n"
    "v_fmac_f32 v34, s92, v26\nv_fmac_f32 v35, s92, v27\ns_waitcnt lgkmcnt(6)\nv_mfma_f32_32x32x16_f16 v[196:211], v[56:59], v[72:75], v[196:211]\n"
    "ds_read_b64_tr_b16 v[56:57], v2 offset:4288\nds_read_b64_tr_b16 v[58:59], v2 offset:4352\nv_fmac_f32 v36, s94, v28\nv_fmac_f32 v37, s94, v29\ns_waitcnt lgkmcnt(6)\n"
    "v_mfma_f32_32x32x16_f16 v[196:211], v[60:63], v[76:79], v[196:211]\nds_read_b64_tr_b16 v[60:61], v2 offset:4032\nds_read_b64_tr_b16 v[62:63], v2 offset:4096\n"
    "s_mov_b64 exec, 1\nds_write_b32 v12, v13 offset:64\ns_mov_b64 exec, -1\ns_waitcnt lgkmcnt(7)\nv_mfma_f32_32x32x16_f16 v[196:211], v[48:51], v[80:83], v[196:211]\n"
    "v_add_f32 v236, v236, v220\nv_add_f32 v237, v237, v221\nv_add_f32 v238, v238, v222\nv_add_f32 v239, v239, v223\ns_waitcnt lgkmcnt(5)\n"
    "v_mfma_f32_32x32x16_f16 v[196:211], v[52:55], v[84:87], v[196:211]\nv_add_f32 v240, v240, v224\nv_add_f32 v241, v241, v225\nv_add_f32 v242, v242, v226\n"
    "v_add_f32 v243, v243, v227\ns_waitcnt lgkmcnt(3)\nv_mfma_f32_32x32x16_f16 v[196:211], v[56:59], v[88:91], v[196:211]\nv_xor_b32 v45, 32, v8\ns_waitcnt lgkmcnt(1)\n"
    "v_mfma_f32_32x32x16_f16 v[196:211], v[60:63], v[92:95], v[196:211]\nv_fmac_f32 v38, s94, v30\nv_fmac_f32 v39, s94, v31\nds_write_b128 v8, v[228:231] offset:0\n"
    "ds_write_b128 v45, v[232:235] offset:0\nds_write_b128 v8, v[236:239] offset:2048\nds_write_b128 v45, v[240:243] offset:2048\ns_nop 5\nds_write_b128 v7, v[196:199]\n"
    "ds_write_b128 v7, v[200:203] offset:1024\ns_setprio 0\n.LL51:\ns_waitcnt lgkmcnt(0)\ns_barrier\nds_read_b128 v[48:51], v15\nds_read_b128 v[52:55], v15 offset:12672\n"
    "ds_read_b128 v[56:59], v16\nds_read_b128 v[60:63], v16 offset:10240\nds_read_b128 v[196:199], v9\nds_read_b128 v[200:203], v9 offset:10240\ns_mov_b32 vcc_lo, 0x55555555\n"
    "s_mov_b32 vcc_hi, 0x55555555\ns_waitcnt lgkmcnt(4)\nv_pk_fma_f32 v[32:33], v[48:49], s[88:89], v[32:33] op_sel_hi:[1,0,1]\n"
    "v_pk_fma_f32 v[34:35], v[50:51], s[88:89], v[34:35] op_sel_hi:[1,0,1]\nv_pk_fma_f32 v[36:37], v[52:53], s[90:91], v[36:37] op_sel_hi:[1,0,1]\n"
    "v_pk_fma_f32 v[38:39], v[54:55], s[90:91], v[38:39] op_sel_hi:[1,0,1]\ns_waitcnt lgkmcnt(2)\nv_pk_fma_f32 v[32:33], v[56:57], s[88:89], v[32:33] op_sel_hi:[1,0,1]\n"
    "v_pk_fma_f32 v[34:35], v[58:59], s[88:89], v[34:35] op_sel_hi:[1,0,1]\nv_pk_fma_f32 v[36:37], v[60:61], s[90:91], v[36:37] op_sel_hi:[1,0,1]\n"
    "v_pk_fma_f32 v[38:39], v[62:63], s[90:91], v[38:39] op_sel_hi:[1,0,1]\ns_waitcnt lgkmcnt(0)\nv_pk_fma_f32 v[32:33], v[196:197], s[88:89], v[32:33] op_sel_hi:[1,0,1]\n"
    "v_pk_fma_f32 v[34:35], v[198:199], s[88:89], v[34:35] op_sel_hi:[1,0,1]\nv_pk_fma_f32 v[36:37], v[200:201], s[90:91], v[36:37] op_sel_hi:[1,0,1]\n"
    "v_pk_fma_f32 v[38:39], v[202:203], s[90:91], v[38:39] op_sel_hi:[1,0,1]\ns_cmp_eq_u32 s6, 0\ns_cbranch_scc0 .LL57\nv_cvt_pk_bf16_f32 v52, v32, v33\n"
    "v_cvt_pk_bf16_f32 v53, v34, v35\nv_cvt_pk_bf16_f32 v54, v36, v37\nv_cvt_pk_bf16_f32 v55, v38, v39\ns_nop 0\n"
    "v_cndmask_b32_dpp v48, v54, v52, vcc quad_perm:[1,0,3,2] row_mask:0xf bank_mask:0xf\nv_cndmask_b32_dpp v49, v55, v53, vcc quad_perm:[1,0,3,2] row_mask:0xf bank_mask:0xf\n"
    "s_not_b64 vcc, vcc\nv_cndmask_b32_dpp v50, v52, v54, vcc quad_perm:[1,0,3,2] row_mask:0xf bank_mask:0xf\n"
    "v_cndmask_b32_dpp v51, v53, v55, vcc quad_perm:[1,0,3,2] row_mask:0xf bank_mask:0xf\nglobal_store_dwordx4 v17, v[48:51], s[32:33] sc1\ns_branch .LH213\n.LL57:\n"
    "v_cvt_pk_bf16_f32 v48, v32, v36\nv_cvt_pk_bf16_f32 v49, v33, v37\nv_cvt_pk_bf16_f32 v50, v34, v38\nv_cvt_pk_bf16_f32 v51, v35, v39\n"
    "global_store_dwordx4 v17, v[48:51], s[32:33] sc1\nv_and_b32 v4, 7, v0\ns_lshl_b32 s15, s41, 13\ns_add_u32 s14, s15, 0x1200000\nv_lshl_add_u32 v2, v4, 10, s14\n"
    "v_lshrrev_b32 v5, 3, v0\nv_lshl_add_u32 v2, v5, 2, v2\ns_lshl_b32 s17, s36, 16\ns_lshl_b32 s18, s35, 7\ns_add_u32 s16, s17, s18\nv_lshl_add_u32 v3, v5, 10, s16\n"
    "v_lshl_add_u32 v3, v4, 4, v3\ns_mov_b32 s80, 0x10000\ns_sleep 14\n.LL58:\nglobal_load_dword v52, v2, s[32:33] offset:0 sc1\n"
    "global_load_dword v53, v2, s[32:33] offset:256 sc1\nglobal_load_dword v54, v2, s[32:33] offset:512 sc1\nglobal_load_dword v55, v2, s[32:33] offset:768 sc1\n"
    "s_waitcnt vmcnt(0)\nv_max3_u32 v4, v52, v53, v54\nv_max_u32 v4, v4, v55\nv_cmp_eq_u32 vcc, -1, v4\ns_cbranch_vccz .LL59\ns_sub_u32 s80, s80, 1\ns_cbranch_scc0 .LL58\n"
    ".LL59:\nglobal_store_dwordx4 v3, v[52:55], s[30:31] sc1\ns_endpgm\n.LH213:\ns_mov_b32 s6, 1\ns_mov_b32 s7, 8388608\nv_and_b32 v196, 63, v0\nv_lshlrev_b32 v26, 4, v196\n"
    "v_bfe_u32 v197, v0, 1, 5\nv_lshl_add_u32 v27, v197, 6, 32\nv_and_b32 v198, 1, v0\nv_lshl_add_u32 v27, v198, 4, v27\ns_mov_b32 s14, 0x1020\nv_lshl_add_u32 v9, v197, 6, s14\n"
    "v_lshl_add_u32 v9, v198, 4, v9\nv_bfe_u32 v199, v0, 4, 2\ns_lshl_b32 s16, s3, 6\ns_add_u32 s15, s16, 0x27c80\nv_lshl_add_u32 v39, v199, 4, s15\nv_bfe_u32 v201, v0, 2, 2\n"
    "v_lshl_add_u32 v200, v199, 3, v201\ns_lshr_b32 s20, s3, 1\ns_add_u32 s19, s20, 1\ns_lshl_b32 s18, s19, 15\ns_and_b32 s22, s3, 3\ns_lshl_b32 s21, s22, 13\n"
    "s_add_u32 s17, s18, s21\nv_lshl_add_u32 v32, v200, 6, s17\nv_and_b32 v202, 3, v0\nv_lshl_add_u32 v32, v202, 3, v32\nv_xor_b32 v204, s3, v199\nv_and_b32 v203, 1, v204\n"
    "v_lshl_add_u32 v32, v203, 5, v32\ns_add_u32 s44, s3, 1\ns_and_b32 s25, s44, 3\ns_lshl_b32 s24, s25, 13\ns_add_u32 s23, s18, s24\nv_lshl_add_u32 v33, v200, 6, s23\n"
    "v_lshl_add_u32 v33, v202, 3, v33\nv_lshl_add_u32 v33, v203, 5, v33\ns_add_u32 s48, s3, 2\ns_and_b32 s47, s48, 3\ns_lshl_b32 s46, s47, 13\ns_add_u32 s45, s18, s46\n"
    "v_lshl_add_u32 v34, v200, 6, s45\nv_lshl_add_u32 v34, v202, 3, v34\nv_lshl_add_u32 v34, v203, 5, v34\ns_add_u32 s52, s3, 3\ns_and_b32 s51, s52, 3\ns_lshl_b32 s50, s51, 13\n"
    "s_add_u32 s49, s18, s50\nv_lshl_add_u32 v35, v200, 6, s49\nv_lshl_add_u32 v35, v202, 3, v35\nv_lshl_add_u32 v35, v203, 5, v35\nds_read_b128 v[248:251], v39\n"
    "s_lshl2_add_u32 s15, s37, s3\ns_lshl_b32 s14, s15, 15\ns_add_u32 s62, s14, 0x600000\ns_mul_i32 s16, s3, 0x3000\ns_add_u32 s64, s16, 0x18000\ns_lshl_b32 s21, s37, 2\n"
    "s_max_u32 s20, s21, 1\ns_add_u32 s19, s20, -1\ns_lshl_b32 s18, s19, 15\ns_add_u32 s17, s18, 0x600000\ns_lshl_b32 s22, s3, 13\ns_add_u32 s60, s17, s22\n"
    "s_lshl_b32 s24, s3, 12\ns_add_u32 s23, s24, 0x24000\ns_add_u32 s65, s23, 0xfffffc00\ns_and_b32 s66, s42, 7\ns_lshl_b32 s25, s66, 12\ns_add_u32 s44, s62, s25\n"
    "s_add_u32 s69, s66, 1\ns_and_b32 s68, s69, 7\ns_lshl_b32 s67, s68, 12\ns_add_u32 s45, s62, s67\ns_add_u32 s72, s66, 2\ns_and_b32 s71, s72, 7\ns_lshl_b32 s70, s71, 12\n"
    "s_add_u32 s46, s62, s70\ns_add_u32 s75, s66, 3\ns_and_b32 s74, s75, 7\ns_lshl_b32 s73, s74, 12\ns_add_u32 s47, s62, s73\ns_add_u32 s78, s66, 4\ns_and_b32 s77, s78, 7\n"
    "s_lshl_b32 s76, s77, 12\ns_add_u32 s48, s62, s76\ns_and_b32 s17, s42, 7\ns_add_u32 s16, s17, 5\ns_and_b32 s15, s16, 7\ns_lshl_b32 s14, s15, 12\ns_add_u32 s49, s62, s14\n"
    "s_add_u32 s20, s17, 6\ns_and_b32 s19, s20, 7\ns_lshl_b32 s18, s19, 12\ns_add_u32 s50, s62, s18\ns_add_u32 s23, s17, 7\ns_and_b32 s22, s23, 7\ns_lshl_b32 s21, s22, 12\n"
    "s_add_u32 s51, s62, s21\ns_add_u32 s60, s60, s32\ns_addc_u32 s61, s33, 0\nds_read_b64_tr_b16 v[64:65], v32 offset:0\nds_read_b64_tr_b16 v[66:67], v32 offset:256\n"
    "ds_read_b64_tr_b16 v[68:69], v32 offset:2048\nds_read_b64_tr_b16 v[70:71], v32 offset:2304\nds_read_b64_tr_b16 v[72:73], v32 offset:4096\n"
    "ds_read_b64_tr_b16 v[74:75], v32 offset:4352\nds_read_b64_tr_b16 v[76:77], v32 offset:6144\nds_read_b64_tr_b16 v[78:79], v32 offset:6400\n"
    "ds_read_b64_tr_b16 v[80:81], v33 offset:0\nds_read_b64_tr_b16 v[82:83], v33 offset:256\nds_read_b64_tr_b16 v[84:85], v33 offset:2048\n"
    "ds_read_b64_tr_b16 v[86:87], v33 offset:2304\nds_read_b64_tr_b16 v[88:89], v33 offset:4096\nds_read_b64_tr_b16 v[90:91], v33 offset:4352\n"
    "ds_read_b64_tr_b16 v[92:93], v33 offset:6144\nds_read_b64_tr_b16 v[94:95], v33 offset:6400\nds_read_b64_tr_b16 v[96:97], v34 offset:0\n"
    "ds_read_b64_tr_b16 v[98:99], v34 offset:256\nds_read_b64_tr_b16 v[100:101], v34 offset:2048\nds_read_b64_tr_b16 v[102:103], v34 offset:2304\n"
    "ds_read_b64_tr_b16 v[104:105], v34 offset:4096\nds_read_b64_tr_b16 v[106:107], v34 offset:4352\nds_read_b64_tr_b16 v[108:109], v34 offset:6144\n"
    "ds_read_b64_tr_b16 v[110:111], v34 offset:6400\nds_read_b64_tr_b16 v[112:113], v35 offset:0\nds_read_b64_tr_b16 v[114:115], v35 offset:256\n"
    "ds_read_b64_tr_b16 v[116:117], v35 offset:2048\nds_read_b64_tr_b16 v[118:119], v35 offset:2304\nds_read_b64_tr_b16 v[120:121], v35 offset:4096\n"
    "ds_read_b64_tr_b16 v[122:123], v35 offset:4352\nds_read_b64_tr_b16 v[124:125], v35 offset:6144\nds_read_b64_tr_b16 v[126:127], v35 offset:6400\ns_waitcnt lgkmcnt(0)\n"
    "s_sleep 11\ns_setprio 3\ns_barrier\ns_add_u32 m0, s64, 0\ns_add_u32 s52, s32, s44\ns_addc_u32 s53, s33, 0\nglobal_load_lds_dwordx4 v26, s[52:53] offset:0 sc1\n"
    "global_load_lds_dwordx4 v26, s[52:53] offset:1024 sc1\nglobal_load_lds_dwordx4 v26, s[52:53] offset:2048 sc1\nglobal_load_lds_dwordx4 v26, s[52:53] offset:3072 sc1\n"
    "v_mov_b64 v[10:11], 0\nv_mov_b64 v[12:13], 0\nv_mov_b64 v[14:15], 0\nv_mov_b64 v[16:17], 0\nv_mov_b64 v[18:19], 0\nv_mov_b64 v[20:21], 0\nv_mov_b64 v[22:23], 0\n"
    "v_mov_b64 v[24:25], 0\nv_add_u32 v1, 8388608, v1\nv_bfe_u32 v197, v0, 4, 2\nv_bfe_u32 v198, v0, 2, 2\nv_lshl_add_u32 v196, v197, 3, v198\ns_add_u32 m0, s64, 4096\n"
    "s_add_u32 s52, s32, s45\ns_addc_u32 s53, s33, 0\nglobal_load_lds_dwordx4 v26, s[52:53] offset:0 sc1\nglobal_load_lds_dwordx4 v26, s[52:53] offset:1024 sc1\n"
    "global_load_lds_dwordx4 v26, s[52:53] offset:2048 sc1\nglobal_load_lds_dwordx4 v26, s[52:53] offset:3072 sc1\ns_and_b32 s16, s3, 3\ns_lshl_b32 s15, s16, 12\n"
    "s_add_u32 s14, s15, 0x24000\nv_lshl_add_u32 v28, v196, 5, s14\nv_and_b32 v199, 3, v0\nv_lshl_add_u32 v28, v199, 3, v28\ns_add_u32 s20, s3, 1\ns_and_b32 s19, s20, 3\n"
    "s_lshl_b32 s18, s19, 12\ns_add_u32 s17, s18, 0x24000\nv_lshl_add_u32 v29, v196, 5, s17\nv_lshl_add_u32 v29, v199, 3, v29\ns_add_u32 s24, s3, 2\ns_and_b32 s23, s24, 3\n"
    "s_lshl_b32 s22, s23, 12\ns_add_u32 s21, s22, 0x24000\nv_lshl_add_u32 v30, v196, 5, s21\nv_lshl_add_u32 v30, v199, 3, v30\ns_add_u32 s68, s3, 3\ns_and_b32 s67, s68, 3\n"
    "s_lshl_b32 s66, s67, 12\ns_add_u32 s25, s66, 0x24000\nv_lshl_add_u32 v31, v196, 5, s25\nv_lshl_add_u32 v31, v199, 3, v31\nv_and_b32 v200, 63, v0\ns_lshl_b32 s72, s35, 15\n"
    "s_and_b32 s74, s36, 3\ns_lshl_b32 s73, s74, 13\ns_add_u32 s71, s72, s73\ns_lshl_b32 s75, s3, 11\ns_add_u32 s70, s71, s75\ns_add_u32 s69, s70, 0xd00000\n"
    "v_lshl_add_u32 v36, v200, 4, s69\nv_and_b32 v196, 63, v0\ns_max_u32 s20, s37, 1\ns_add_u32 s19, s20, -1\ns_lshr_b32 s18, s19, 2\ns_and_b32 s21, s35, 1\n"
    "s_lshl1_add_u32 s17, s18, s21\ns_lshl_b32 s16, s17, 15\ns_and_b32 s23, s19, 3\ns_lshl_b32 s22, s23, 13\ns_add_u32 s15, s16, s22\ns_add_u32 s14, s15, 0xd01800\n"
    "v_lshl_add_u32 v37, v196, 4, s14\nv_and_b32 v198, 7, v0\nv_bfe_u32 v199, v0, 4, 2\nv_lshl_add_u32 v197, v198, 2, v199\ns_lshl_b32 s25, s3, 9\ns_add_u32 s24, s25, 0x21000\n"
    "v_lshl_add_u32 v38, v197, 3, s24\nv_and_b32 v200, 8, v0\nv_lshl_add_u32 v38, v200, 5, v38\ns_lshl_b32 s67, s3, 12\ns_add_u32 s66, s67, 0x24000\n"
    "v_lshl_add_u32 v43, v197, 3, s66\nv_lshl_add_u32 v43, v200, 5, v43\nv_and_b32 v201, 15, v0\ns_add_u32 s68, s66, 0x80\nv_lshl_add_u32 v44, v201, 3, s68\n"
    "s_lshl_b32 s70, s3, 5\ns_add_u32 s69, s70, 0x18040\nv_lshl_add_u32 v40, v199, 3, s69\ns_mov_b32 s71, 0xd0\nv_mad_u32_u24 v40, v201, s71, v40\nv_and_b32 v202, 31, v0\n"
    "s_mov_b32 s72, 0x27f80\nv_lshl_add_u32 v41, v202, 2, s72\nv_mov_b32 v42, 2\ns_lshl2_add_u32 s75, s40, s3\ns_lshl_b32 s74, s75, 15\ns_add_u32 s73, s74, 0xe05000\n"
    "v_lshl_add_u32 v4, v196, 4, s73\ns_add_u32 s76, s74, 0xe07000\nv_lshl_add_u32 v5, v196, 4, s76\ns_mov_b32 s86, 0x3b000000\ns_add_u32 m0, s64, 8192\n"
    "s_add_u32 s52, s32, s46\ns_addc_u32 s53, s33, 0\nglobal_load_lds_dwordx4 v26, s[52:53] offset:0 sc1\nglobal_load_lds_dwordx4 v26, s[52:53] offset:1024 sc1\n"
    "global_load_lds_dwordx4 v26, s[52:53] offset:2048 sc1\nglobal_load_lds_dwordx4 v26, s[52:53] offset:3072 sc1\ns_setprio 0\ns_waitcnt vmcnt(8)\ns_barrier\ns_setprio 2\n"
    "s_add_u32 m0, s65, 1024\ns_nop 0\nglobal_load_lds_dwordx4 v27, s[60:61] offset:0 sc1\ns_add_u32 m0, s65, 0\ns_nop 0\nglobal_load_lds_dwordx4 v27, s[60:61] offset:2048 sc1\n"
    "s_add_u32 m0, s65, 3072\ns_nop 0\nglobal_load_lds_dwordx4 v9, s[60:61] offset:0 sc1\ns_add_u32 m0, s65, 2048\ns_nop 0\n"
    "global_load_lds_dwordx4 v9, s[60:61] offset:2048 sc1\ns_setprio 0\ns_waitcnt vmcnt(8)\ns_barrier\ns_setprio 2\ns_add_u32 m0, s64, 0\ns_add_u32 s52, s32, s47\n"
    "s_addc_u32 s53, s33, 0\nglobal_load_lds_dwordx4 v26, s[52:53] offset:0 sc1\nglobal_load_lds_dwordx4 v26, s[52:53] offset:1024 sc1\n"
    "global_load_lds_dwordx4 v26, s[52:53] offset:2048 sc1\nglobal_load_lds_dwordx4 v26, s[52:53] offset:3072 sc1\ns_setprio 0\ns_waitcnt vmcnt(8)\ns_barrier\ns_setprio 2\n"
    "s_add_u32 m0, s64, 4096\ns_add_u32 s52, s32, s48\ns_addc_u32 s53, s33, 0\nglobal_load_lds_dwordx4 v26, s[52:53] offset:0 sc1\n"
    "global_load_lds_dwordx4 v26, s[52:53] offset:1024 sc1\nglobal_load_lds_dwordx4 v26, s[52:53] offset:2048 sc1\nglobal_load_lds_dwordx4 v26, s[52:53] offset:3072 sc1\n"
    "s_setprio 0\ns_waitcnt vmcnt(8)\nds_read_b64_tr_b16 v[196:197], v28 offset:0\nds_read_b64_tr_b16 v[198:199], v28 offset:128\n"
    "ds_read_b64_tr_b16 v[200:201], v28 offset:1024\nds_read_b64_tr_b16 v[202:203], v28 offset:1152\nds_read_b64_tr_b16 v[204:205], v28 offset:2048\n"
    "ds_read_b64_tr_b16 v[206:207], v28 offset:2176\nds_read_b64_tr_b16 v[208:209], v28 offset:3072\nds_read_b64_tr_b16 v[210:211], v28 offset:3200\ns_waitcnt vmcnt(4)\n"
    "s_barrier\ns_setprio 2\ns_add_u32 m0, s64, 8192\ns_add_u32 s52, s32, s49\ns_addc_u32 s53, s33, 0\nglobal_load_lds_dwordx4 v26, s[52:53] offset:0 sc1\n"
    "global_load_lds_dwordx4 v26, s[52:53] offset:1024 sc1\nglobal_load_lds_dwordx4 v26, s[52:53] offset:2048 sc1\nglobal_load_lds_dwordx4 v26, s[52:53] offset:3072 sc1\n"
    "s_setprio 0\ns_waitcnt lgkmcnt(0)\nv_pk_maximum3_f16 v62, v196, v197, v198\nv_pk_maximum3_f16 v63, v199, v200, v201\nv_pk_maximum3_f16 v62, v62, v202, v203\n"
    "v_pk_maximum3_f16 v63, v63, v204, v205\nv_pk_maximum3_f16 v62, v62, v206, v207\nv_pk_maximum3_f16 v63, v63, v208, v209\nv_pk_maximum3_f16 v62, v62, v210, v211\n"
    "v_pk_maximum3_f16 v62, v62, v63, v63\nv_cmp_u_f16_sdwa vcc, v62, v62 src0_sel:WORD_0 src1_sel:WORD_1\ns_cbranch_vccnz .Lxs60\n.Lxr61:\ns_waitcnt vmcnt(4)\ns_barrier\n"
    "s_waitcnt lgkmcnt(0)\ns_setprio 2\nv_mfma_f32_16x16x32_bf16 v[46:49], v[64:67], v[196:199], 0\nv_mfma_f32_16x16x32_bf16 v[46:49], v[68:71], v[200:203], v[46:49]\n"
    "v_mfma_f32_16x16x32_bf16 v[46:49], v[72:75], v[204:207], v[46:49]\nv_mfma_f32_16x16x32_bf16 v[46:49], v[76:79], v[208:211], v[46:49]\ns_setprio 2\ns_add_u32 m0, s64, 0\n"
    "s_add_u32 s52, s32, s50\ns_addc_u32 s53, s33, 0\nglobal_load_lds_dwordx4 v26, s[52:53] offset:0 sc1\nglobal_load_lds_dwordx4 v26, s[52:53] offset:1024 sc1\n"
    "global_load_lds_dwordx4 v26, s[52:53] offset:2048 sc1\nglobal_load_lds_dwordx4 v26, s[52:53] offset:3072 sc1\ns_setprio 0\nds_read_b64_tr_b16 v[196:197], v29 offset:0\n"
    "ds_read_b64_tr_b16 v[198:199], v29 offset:128\nds_read_b64_tr_b16 v[200:201], v29 offset:1024\nds_read_b64_tr_b16 v[202:203], v29 offset:1152\n"
    "ds_read_b64_tr_b16 v[204:205], v29 offset:2048\nds_read_b64_tr_b16 v[206:207], v29 offset:2176\nds_read_b64_tr_b16 v[208:209], v29 offset:3072\n"
    "ds_read_b64_tr_b16 v[210:211], v29 offset:3200\ns_waitcnt vmcnt(4)\ns_barrier\ns_waitcnt lgkmcnt(0)\ns_setprio 2\n"
    "v_mfma_f32_16x16x32_bf16 v[46:49], v[80:83], v[196:199], v[46:49]\nv_mfma_f32_16x16x32_bf16 v[46:49], v[84:87], v[200:203], v[46:49]\n"
    "v_mfma_f32_16x16x32_bf16 v[46:49], v[88:91], v[204:207], v[46:49]\nv_mfma_f32_16x16x32_bf16 v[46:49], v[92:95], v[208:211], v[46:49]\ns_setprio 2\ns_add_u32 m0, s64, 4096\n"
    "s_add_u32 s52, s32, s51\ns_addc_u32 s53, s33, 0\nglobal_load_lds_dwordx4 v26, s[52:53] offset:0 sc1\nglobal_load_lds_dwordx4 v26, s[52:53] offset:1024 sc1\n"
    "global_load_lds_dwordx4 v26, s[52:53] offset:2048 sc1\nglobal_load_lds_dwordx4 v26, s[52:53] offset:3072 sc1\nglobal_load_dwordx4 v[212:215], v36, s[32:33] offset:0 sc1\n"
    "global_load_dwordx4 v[216:219], v36, s[32:33] offset:1024 sc1\nglobal_load_dwordx4 v[220:223], v37, s[32:33] offset:0 sc1\n"
    "global_load_dwordx4 v[224:227], v37, s[32:33] offset:1024 sc1\ns_setprio 0\nds_read_b64_tr_b16 v[196:197], v30 offset:0\nds_read_b64_tr_b16 v[198:199], v30 offset:128\n"
    "ds_read_b64_tr_b16 v[200:201], v30 offset:1024\nds_read_b64_tr_b16 v[202:203], v30 offset:1152\nds_read_b64_tr_b16 v[204:205], v30 offset:2048\n"
    "ds_read_b64_tr_b16 v[206:207], v30 offset:2176\nds_read_b64_tr_b16 v[208:209], v30 offset:3072\nds_read_b64_tr_b16 v[210:211], v30 offset:3200\ns_waitcnt vmcnt(8)\n"
    "s_barrier\ns_waitcnt lgkmcnt(0)\ns_setprio 2\nv_mfma_f32_16x16x32_bf16 v[46:49], v[96:99], v[196:199], v[46:49]\n"
    "v_mfma_f32_16x16x32_bf16 v[46:49], v[100:103], v[200:203], v[46:49]\nv_mfma_f32_16x16x32_bf16 v[46:49], v[104:107], v[204:207], v[46:49]\n"
    "v_mfma_f32_16x16x32_bf16 v[46:49], v[108:111], v[208:211], v[46:49]\ns_setprio 2\nglobal_load_dwordx4 v[64:67], v4, s[32:33] offset:-4096 sc1\n"
    "global_load_dwordx4 v[68:71], v4, s[32:33] offset:-3072 sc1\nglobal_load_dwordx4 v[72:75], v4, s[32:33] offset:-2048 sc1\n"
    "global_load_dwordx4 v[76:79], v4, s[32:33] offset:-1024 sc1\ns_setprio 0\nds_read_b64_tr_b16 v[196:197], v31 offset:0\nds_read_b64_tr_b16 v[198:199], v31 offset:128\n"
    "ds_read_b64_tr_b16 v[200:201], v31 offset:1024\nds_read_b64_tr_b16 v[202:203], v31 offset:1152\nds_read_b64_tr_b16 v[204:205], v31 offset:2048\n"
    "ds_read_b64_tr_b16 v[206:207], v31 offset:2176\nds_read_b64_tr_b16 v[208:209], v31 offset:3072\nds_read_b64_tr_b16 v[210:211], v31 offset:3200\ns_waitcnt vmcnt(8)\n"
    "s_barrier\ns_waitcnt lgkmcnt(0)\ns_setprio 2\nv_mfma_f32_16x16x32_bf16 v[46:49], v[112:115], v[196:199], v[46:49]\n"
    "v_mfma_f32_16x16x32_bf16 v[46:49], v[116:119], v[200:203], v[46:49]\nv_mfma_f32_16x16x32_bf16 v[46:49], v[120:123], v[204:207], v[46:49]\n"
    "v_mfma_f32_16x16x32_bf16 v[46:49], v[124:127], v[208:211], v[46:49]\ns_setprio 2\nglobal_load_dwordx4 v[80:83], v4, s[32:33] offset:0 sc1\n"
    "global_load_dwordx4 v[84:87], v4, s[32:33] offset:1024 sc1\nglobal_load_dwordx4 v[88:91], v4, s[32:33] offset:2048 sc1\n"
    "global_load_dwordx4 v[92:95], v4, s[32:33] offset:3072 sc1\ns_setprio 0\ns_waitcnt vmcnt(8)\ns_setprio 2\ns_mov_b32 s80, 0x10000\n.Lpl65:\nv_add_f32 v228, v212, v216\n"
    "v_add_f32 v229, v213, v217\nv_add_f32 v230, v214, v218\nv_add_f32 v231, v215, v219\nv_permlane16_swap_b32 v228, v229\ns_nop 0\nv_permlane16_swap_b32 v230, v231\n"
    "v_add_f32 v228, v228, v229\nv_add_f32 v230, v230, v231\ns_nop 1\nv_permlane32_swap_b32 v228, v230\nv_add_f32 v228, v228, v230\nv_cmp_u_f32 vcc, v228, v228\n"
    "s_cbranch_vccnz .Lps63\n.Lpr64:\nv_mov_b32 v229, v228\ns_nop 1\nv_mov_b32_dpp v229, v228 row_ror:8 row_mask:0xf bank_mask:0x3\n"
    "v_mov_b32_dpp v228, v228 row_ror:8 row_mask:0xf bank_mask:0xc\nv_mul_f32 v228, s86, v228\nv_mul_f32 v230, v228, v228\nv_fma_f32 v229, v229, s86, -v230\n"
    "v_max_f32 v229, 0, v229\nv_cvt_pk_bf16_f32 v229, 0, v229\nv_add_f32 v229, 0x3727c5ac, v229\nv_cvt_pk_bf16_f32 v229, 0, v229\nv_sqrt_f32 v229, v229\ns_nop 0\n"
    "v_cvt_pk_bf16_f32 v229, 0, v229\nv_rcp_f32 v229, v229\nv_cvt_pk_bf16_f32 v228, -v228, -v228\ns_mov_b32 exec_lo, 0xff00ff00\ns_mov_b32 exec_hi, 0xff00ff00\n"
    "v_mov_b32 v228, 0\ns_mov_b64 exec, -1\nds_write_b64 v38, v[228:229]\ns_waitcnt lgkmcnt(0)\ns_barrier\ns_setprio 2\n"
    "global_load_dwordx4 v[96:99], v5, s[32:33] offset:-4096 sc1\nglobal_load_dwordx4 v[100:103], v5, s[32:33] offset:-3072 sc1\n"
    "global_load_dwordx4 v[104:107], v5, s[32:33] offset:-2048 sc1\nglobal_load_dwordx4 v[108:111], v5, s[32:33] offset:-1024 sc1\ns_setprio 0\ns_setprio 0\n"
    "ds_write_b32 v41, v42\ns_mov_b32 s80, 0x10000\n.Lpl70:\nv_add_f32 v232, v220, v224\nv_add_f32 v233, v221, v225\nv_add_f32 v234, v222, v226\nv_add_f32 v235, v223, v227\n"
    "v_permlane16_swap_b32 v232, v233\ns_nop 0\nv_permlane16_swap_b32 v234, v235\nv_add_f32 v232, v232, v233\nv_add_f32 v234, v234, v235\ns_nop 1\n"
    "v_permlane32_swap_b32 v232, v234\nv_add_f32 v232, v232, v234\nv_cmp_u_f32 vcc, v232, v232\ns_cbranch_vccnz .Lps68\n.Lpr69:\nv_mov_b32 v233, v232\ns_nop 1\n"
    "v_mov_b32_dpp v233, v232 row_ror:8 row_mask:0xf bank_mask:0x3\nv_mov_b32_dpp v232, v232 row_ror:8 row_mask:0xf bank_mask:0xc\nv_mul_f32 v232, s86, v232\n"
    "v_mul_f32 v234, v232, v232\nv_fma_f32 v233, v233, s86, -v234\nv_max_f32 v233, 0, v233\nv_cvt_pk_bf16_f32 v233, 0, v233\nv_add_f32 v233, 0x3727c5ac, v233\n"
    "v_cvt_pk_bf16_f32 v233, 0, v233\nv_sqrt_f32 v233, v233\ns_nop 0\nv_cvt_pk_bf16_f32 v233, 0, v233\nv_rcp_f32 v233, v233\nv_cvt_pk_bf16_f32 v232, 0, v232\n"
    "ds_write_b64 v43, v[232:233]\nds_read_b64 v[232:233], v44\ns_waitcnt lgkmcnt(0)\nv_fma_f32 v46, -v232, v248, v46\nv_fma_f32 v47, -v232, v249, v47\n"
    "v_fma_f32 v48, -v232, v250, v48\nv_fma_f32 v49, -v232, v251, v49\nv_mul_f32 v46, v46, v233\nv_mul_f32 v47, v47, v233\nv_mul_f32 v48, v48, v233\nv_mul_f32 v49, v49, v233\n"
    "v_cvt_pk_bf16_f32 v46, v46, v47\nv_cvt_pk_bf16_f32 v47, v48, v49\nds_write_b64 v40, v[46:47]\nv_mov_b32 v62, 0\ns_branch .LHL12\n.LG1:\ns_waitcnt lgkmcnt(0)\n"
    "s_mov_b32 s86, 16777216\ns_load_dword s34, s[32:33], s86\ns_mov_b32 s6, 0\ns_mov_b32 s7, 0\ns_and_b32 s35, s2, 7\ns_lshr_b32 s36, s2, 3\ns_bfe_u32 s44, s2, 0x20001\n"
    "s_bfe_u32 s45, s2, 0x20003\ns_lshl2_add_u32 s37, s44, s45\ns_and_b32 s46, s2, 1\ns_lshr_b32 s47, s2, 5\ns_lshl3_add_u32 s38, s46, s47\ns_bfe_u32 s48, s2, 0x30003\n"
    "s_lshl3_add_u32 s39, s35, s48\ns_lshr_b32 s40, s2, 6\ns_lshl_b32 s49, s35, 5\ns_add_u32 s41, s49, s36\ns_lshl1_add_u32 s50, s45, s47\ns_and_b32 s42, s50, 7\n"
    "s_lshl_b32 s86, s41, 2\ns_load_dword s96, s[26:27], s86\ns_load_dword s95, s[28:29], s86\nv_mov_b32 v64, 0\nv_mov_b32 v65, 0xf149f2ca\nv_bfe_u32 v68, v0, 4, 2\n"
    "v_lshlrev_b32 v67, 2, v68\ns_add_u32 s45, s37, s3\ns_cmp_eq_u32 s45, 0\ns_cselect_b32 s44, 8, 0\nv_and_b32 v71, 7, v0\nv_add_u32 v70, 1, v71\nv_max_u32 v69, s44, v70\n"
    "v_sub_u32 v66, v67, v69\nv_add_u32 v73, 8, v71\nv_sub_u32 v72, v73, v69\nv_cmp_le_u32 vcc, v66, v72\ns_nop 1\nv_cndmask_b32 v2, v65, v64, vcc\nv_add_u32 v75, 1, v67\n"
    "v_sub_u32 v74, v75, v69\nv_cmp_le_u32 vcc, v74, v72\ns_nop 1\nv_cndmask_b32 v3, v65, v64, vcc\nv_add_u32 v77, 2, v67\nv_sub_u32 v76, v77, v69\nv_cmp_le_u32 vcc, v76, v72\n"
    "s_nop 1\nv_cndmask_b32 v4, v65, v64, vcc\nv_add_u32 v79, 3, v67\nv_sub_u32 v78, v79, v69\nv_cmp_le_u32 vcc, v78, v72\ns_nop 1\nv_cndmask_b32 v5, v65, v64, vcc\n"
    "s_mul_i32 s47, s3, 0xd00\ns_add_u32 s46, s47, 0x18040\nv_lshl_add_u32 v6, v68, 4, s46\nv_and_b32 v81, 15, v0\nv_max_u32 v80, 1, v81\ns_mov_b32 s48, 0x1a0\n"
    "v_mad_u32_u24 v6, v80, s48, v6\ns_add_u32 s49, s47, 0x18d00\nv_lshl_add_u32 v7, v68, 4, s49\nv_mad_u32_u24 v7, v71, s48, v7\nv_and_b32 v82, 3, v0\n"
    "s_add_u32 s50, s47, 0x18080\nv_lshl_add_u32 v8, v82, 3, s50\nv_bfe_u32 v85, v0, 2, 2\nv_add_u32 v84, v67, v85\nv_max_u32 v83, 1, v84\nv_mad_u32_u24 v8, v83, s48, v8\n"
    "v_bfe_u32 v86, v0, 3, 1\ns_lshr_b32 s55, s3, 1\ns_lshl2_add_u32 s54, s37, s55\ns_lshl_b32 s53, s54, 15\ns_lshl_b32 s56, s38, 11\ns_add_u32 s52, s53, s56\n"
    "s_and_b32 s58, s3, 1\ns_lshl_b32 s57, s58, 8\ns_add_u32 s51, s52, s57\nv_lshl_add_u32 v9, v86, 10, s51\nv_bfe_u32 v87, v0, 5, 1\nv_lshl_add_u32 v9, v87, 9, v9\n"
    "v_bfe_u32 v88, v0, 4, 1\nv_lshl_add_u32 v9, v88, 4, v9\nv_lshl_add_u32 v9, v71, 5, v9\nv_mov_b64 v[10:11], 0\nv_mov_b64 v[12:13], 0\nv_mov_b64 v[14:15], 0\n"
    "v_mov_b64 v[16:17], 0\nv_mov_b64 v[18:19], 0\nv_mov_b64 v[20:21], 0\nv_mov_b64 v[22:23], 0\nv_mov_b64 v[24:25], 0\nv_bfe_u32 v65, v0, 4, 1\nv_bfe_u32 v66, v0, 5, 1\n"
    "v_xor_b32 v64, v65, v66\nv_bfe_u32 v69, v0, 2, 2\nv_lshl_add_u32 v68, v66, 3, v69\nv_lshlrev_b32 v67, 6, v68\nv_lshl_add_u32 v26, v64, 5, v67\nv_and_b32 v70, 1, v0\n"
    "v_lshl_add_u32 v26, v70, 4, v26\nv_and_b32 v71, 2, v0\nv_lshl_add_u32 v26, v71, 2, v26\ns_mul_i32 s45, s3, 0x1a00\ns_add_u32 s44, s45, 0x18d00\n"
    "v_lshl_add_u32 v27, v66, 4, s44\nv_and_b32 v72, 31, v0\ns_mov_b32 s46, 0xd0\nv_mad_u32_u24 v27, v72, s46, v27\nv_and_b32 v73, 19, v0\ns_mov_b32 s47, 0x27c00\n"
    "v_lshl_add_u32 v28, v73, 2, s47\nv_and_b32 v74, 4, v0\nv_lshl_add_u32 v28, v74, 3, v28\nv_and_b32 v75, 8, v0\nv_lshl_add_u32 v28, v75, 1, v28\ns_mul_i32 s49, s3, 0x3000\n"
    "s_add_u32 s48, s49, 0x18000\nv_lshl_add_u32 v29, v72, 7, s48\nv_bfe_u32 v77, v0, 1, 3\nv_xor_b32 v76, v66, v77\nv_lshl_add_u32 v29, v76, 4, v29\ns_and_b32 s52, s41, 3\n"
    "s_lshr_b32 s55, s41, 2\ns_lshl_b32 s54, s55, 7\ns_add_u32 s53, s54, 0x1002000\ns_lshl3_add_u32 s51, s52, s53\ns_lshl2_add_u32 s50, s3, s51\nv_mov_b32 v30, s50\n"
    "v_and_b32 v78, 0x7f, v0\ns_mov_b32 s56, 0x23800\nv_lshl_add_u32 v31, v78, 3, s56\ns_mov_b32 s57, 0x23000\nv_lshl_add_u32 v32, v78, 4, s57\nv_and_b32 v79, 63, v0\n"
    "s_lshr_b32 s60, s3, 1\ns_lshl_b32 s59, s60, 8\ns_add_u32 s58, s59, 0x20000\nv_lshl_add_u32 v33, v79, 2, s58\ns_lshl_b32 s62, s41, 11\ns_add_u32 s61, s62, 0xe00000\n"
    "v_lshl_add_u32 v38, v0, 4, s61\ns_add_u32 s63, s49, 0x1a000\nv_add_u32 v39, s63, v67\nv_lshl_add_u32 v39, v64, 5, v39\nv_and_b32 v80, 3, v0\n"
    "v_lshl_add_u32 v39, v80, 3, v39\ns_mul_i32 s65, s3, 0x2200\ns_add_u32 s64, s65, 0x1f400\nv_lshl_add_u32 v188, v79, 3, s64\ns_lshl_b32 s67, s39, 9\n"
    "s_add_u32 s66, s67, 0x1008000\nv_lshl_add_u32 v187, v80, 7, s66\nv_mov_b32 v191, 0x10000\ns_lshl_b32 s70, s41, 13\ns_lshl_b32 s71, s3, 11\ns_add_u32 s69, s70, s71\n"
    "s_add_u32 s68, s69, 0x600000\nv_lshl_add_u32 v200, v79, 4, s68\ns_lshl_b32 s74, s41, 10\ns_lshl_b32 s75, s3, 8\ns_add_u32 s73, s74, s75\ns_add_u32 s72, s73, 0xd00000\n"
    "v_lshl_add_u32 v201, v79, 4, s72\nv_and_b32 v81, 15, v0\ns_add_u32 s76, s71, 0x1004000\nv_lshl_add_u32 v202, v81, 7, s76\nv_bfe_u32 v82, v0, 4, 2\n"
    "v_lshl_add_u32 v202, v82, 4, v202\nv_mov_b32 v196, -1\nv_mov_b32 v197, -1\nv_mov_b32 v198, -1\nv_mov_b32 v199, -1\ns_lshl_b32 s78, s39, 15\ns_add_u32 s77, s78, 0x1000\n"
    "v_lshl_add_u32 v182, v0, 4, s77\ns_add_u32 s79, s77, 0x2000\nv_lshl_add_u32 v183, v0, 4, s79\ns_lshl_b32 s45, s39, 15\ns_add_u32 s44, s45, 0x5000\n"
    "v_lshl_add_u32 v184, v0, 4, s44\ns_add_u32 s46, s45, 0x7000\nv_lshl_add_u32 v185, v0, 4, s46\ns_mov_b32 s47, 0x1f600\nv_lshl_add_u32 v186, v0, 4, s47\n"
    "v_bfe_u32 v64, v0, 4, 2\ns_mul_i32 s49, s3, 0x1760\ns_add_u32 s48, s49, 0x18000\nv_lshl_add_u32 v189, v64, 4, s48\nv_and_b32 v65, 15, v0\ns_mov_b32 s50, 0x220\n"
    "v_mad_u32_u24 v189, v65, s50, v189\ns_lshr_b32 s53, s3, 1\ns_mul_i32 s52, s53, 0x4400\ns_add_u32 s51, s52, 0x1f400\nv_lshl_add_u32 v190, v64, 4, s51\n"
    "v_mad_u32_u24 v190, v65, s50, v190\ns_cmp_eq_u32 s3, 3\ns_cbranch_scc0 .LL76\nv_mov_b32 v64, 0\nv_lshlrev_b32 v65, 2, v0\nv_add_u32 v65, 162048, v65\ns_mov_b32 exec_hi, 0\n"
    "ds_write_b32 v65, v64\ns_mov_b64 exec, 15\nv_mov_b32 v64, 0x3f803f80\ns_mov_b32 exec_lo, -1\ns_mov_b32 exec_hi, 15\nds_write_b32 v65, v64 offset:880\ns_mov_b64 exec, -1\n"
    ".LL76:\ns_and_b32 s54, s42, 7\ns_lshl_b32 s44, s54, 12\ns_add_u32 s56, s54, 1\ns_and_b32 s55, s56, 7\ns_lshl_b32 s45, s55, 12\ns_add_u32 s58, s54, 2\n"
    "s_and_b32 s57, s58, 7\ns_lshl_b32 s46, s57, 12\ns_add_u32 s60, s54, 3\ns_and_b32 s59, s60, 7\ns_lshl_b32 s47, s59, 12\ns_add_u32 s62, s54, 4\ns_and_b32 s61, s62, 7\n"
    "s_lshl_b32 s48, s61, 12\ns_add_u32 s64, s54, 5\ns_and_b32 s63, s64, 7\ns_lshl_b32 s49, s63, 12\ns_add_u32 s66, s54, 6\ns_and_b32 s65, s66, 7\ns_lshl_b32 s50, s65, 12\n"
    "s_add_u32 s68, s54, 7\ns_and_b32 s67, s68, 7\ns_lshl_b32 s51, s67, 12\ns_mov_b32 s81, 0x3b000000\ns_mov_b32 s84, -1\ns_mov_b32 s85, 0\nv_mov_b32 v143, 0x3f803f80\n"
    "v_add_u32 v116, s44, v26\nv_add_u32 v118, 0x10000, v116\nv_mov_b32 v112, 0\nv_mov_b32 v113, 0\nv_mov_b32 v114, 0\nv_mov_b32 v115, 0\nv_mov_b32 v121, 0\nv_mov_b32 v122, 0\n"
    "v_mov_b32 v123, 0\nv_mov_b32 v125, 0\nv_mov_b32 v126, 0\nv_mov_b32 v127, 0\nv_mov_b32 v129, 0\nv_mov_b32 v130, 0\nv_mov_b32 v131, 0\nv_mov_b32 v133, 0\nv_mov_b32 v134, 0\n"
    "v_mov_b32 v135, 0\nv_xor_b32 v176, 32, v29\nv_xor_b32 v177, 64, v29\nv_xor_b32 v178, 0x60, v29\ns_waitcnt lgkmcnt(0)\ns_lshl_b32 s97, s96, 16\n"
    "s_and_b32 s96, s96, 0xffff0000\ns_cmp_lt_u32 s3, 2\ns_cselect_b32 s96, s97, s96\ns_mov_b32 s97, 0x3fb8aa3b\nv_mov_b32 v66, s96\nv_mul_f32 v67, s97, v66\n"
    "v_fma_f32 v68, s97, v66, -v67\nv_exp_f32 v67, v67\nv_fmac_f32 v68, 0x32a57060, v66\nv_mul_f32 v68, 0x3f317218, v68\nv_fmac_f32 v67, v67, v68\n"
    "v_cvt_pk_bf16_f32 v67, 0, v67\nv_mul_f32 v66, s97, v67\nv_mul_f32 v68, 0x3e22f983, v67\nv_readfirstlane_b32 s92, v67\nv_readfirstlane_b32 s93, v66\n"
    "v_readfirstlane_b32 s94, v68\ns_and_b32 s97, s95, 0xffff0000\ns_lshl_b32 s96, s95, 16\ns_add_u32 s34, s34, 1\nv_mov_b32 v180, s34\ns_cmp_lt_u32 s3, 2\n"
    "s_cbranch_scc0 .LL75\ns_waitcnt vmcnt(0)\nds_read_b64_tr_b16 v[244:245], v39\nds_read_b64_tr_b16 v[246:247], v39 offset:256\ns_waitcnt lgkmcnt(0)\n.LL75:\n"
    "s_waitcnt lgkmcnt(0)\ns_barrier\nds_read_b128 v[144:147], v29 offset:0\nds_read_b128 v[148:151], v176 offset:0\nds_read_b128 v[152:155], v177 offset:0\n"
    "ds_read_b128 v[156:159], v178 offset:0\nds_read_b64_tr_b16 v[44:45], v116 offset:0\nds_read_b64_tr_b16 v[46:47], v116 offset:256\n"
    "ds_read_b64_tr_b16 v[48:49], v116 offset:1024\nds_read_b64_tr_b16 v[50:51], v116 offset:1280\nds_read_b64_tr_b16 v[52:53], v116 offset:2048\n"
    "ds_read_b64_tr_b16 v[54:55], v116 offset:2304\nds_read_b64_tr_b16 v[56:57], v116 offset:3072\nds_read_b64_tr_b16 v[58:59], v116 offset:3328\n"
    "ds_read_b64_tr_b16 v[60:61], v116 offset:32768\nds_read_b64_tr_b16 v[62:63], v116 offset:33024\ns_waitcnt lgkmcnt(13)\nv_dot2c_f32_bf16 v112, v143, v144\n"
    "v_dot2c_f32_bf16 v113, v144, v144\nv_dot2c_f32_bf16 v114, v143, v145\nv_dot2c_f32_bf16 v115, v145, v145\nv_dot2c_f32_bf16 v112, v143, v146\n"
    "v_dot2c_f32_bf16 v113, v146, v146\nv_dot2c_f32_bf16 v114, v143, v147\nv_dot2c_f32_bf16 v115, v147, v147\ns_waitcnt lgkmcnt(12)\nv_dot2c_f32_bf16 v112, v143, v148\n"
    "v_dot2c_f32_bf16 v113, v148, v148\nv_dot2c_f32_bf16 v114, v143, v149\nv_dot2c_f32_bf16 v115, v149, v149\nv_dot2c_f32_bf16 v112, v143, v150\n"
    "v_dot2c_f32_bf16 v113, v150, v150\nv_dot2c_f32_bf16 v114, v143, v151\nv_dot2c_f32_bf16 v115, v151, v151\ns_waitcnt lgkmcnt(11)\nv_dot2c_f32_bf16 v112, v143, v152\n"
    "v_dot2c_f32_bf16 v113, v152, v152\nv_dot2c_f32_bf16 v114, v143, v153\nv_dot2c_f32_bf16 v115, v153, v153\nv_dot2c_f32_bf16 v112, v143, v154\n"
    "v_dot2c_f32_bf16 v113, v154, v154\nv_dot2c_f32_bf16 v114, v143, v155\nv_dot2c_f32_bf16 v115, v155, v155\ns_waitcnt lgkmcnt(10)\nv_dot2c_f32_bf16 v112, v143, v156\n"
    "v_dot2c_f32_bf16 v113, v156, v156\nv_dot2c_f32_bf16 v114, v143, v157\nv_dot2c_f32_bf16 v115, v157, v157\nv_dot2c_f32_bf16 v112, v143, v158\n"
    "v_dot2c_f32_bf16 v113, v158, v158\nv_dot2c_f32_bf16 v114, v143, v159\nv_dot2c_f32_bf16 v115, v159, v159\ns_waitcnt lgkmcnt(8)\n"
    "v_mfma_f32_32x32x16_bf16 v[64:79], v[44:47], v[144:147], 0\nds_read_b64_tr_b16 v[44:45], v116 offset:33792\nds_read_b64_tr_b16 v[46:47], v116 offset:34048\n"
    "v_add_u32 v117, s45, v26\nv_add_u32 v119, 0x10000, v117\ns_waitcnt lgkmcnt(8)\nv_mfma_f32_32x32x16_bf16 v[64:79], v[48:51], v[148:151], v[64:79]\n"
    "ds_read_b64_tr_b16 v[48:49], v116 offset:34816\nds_read_b64_tr_b16 v[50:51], v116 offset:35072\ns_cmp_lt_u32 s3, 2\ns_cbranch_scc0 .LL73\n"
    "global_store_dwordx4 v38, v[244:247], s[32:33] sc1\n.LL73:\ns_waitcnt lgkmcnt(8)\nv_mfma_f32_32x32x16_bf16 v[64:79], v[52:55], v[152:155], v[64:79]\n"
    "ds_read_b64_tr_b16 v[52:53], v116 offset:35840\nds_read_b64_tr_b16 v[54:55], v116 offset:36096\ns_waitcnt lgkmcnt(8)\n"
    "v_mfma_f32_32x32x16_bf16 v[64:79], v[56:59], v[156:159], v[64:79]\nds_read_b64_tr_b16 v[56:57], v118 offset:0\nds_read_b64_tr_b16 v[58:59], v118 offset:256\n"
    "s_waitcnt lgkmcnt(8)\nv_mfma_f32_32x32x16_bf16 v[80:95], v[60:63], v[144:147], 0\nds_read_b64_tr_b16 v[60:61], v118 offset:1024\n"
    "ds_read_b64_tr_b16 v[62:63], v118 offset:1280\ns_waitcnt lgkmcnt(8)\nv_mfma_f32_32x32x16_bf16 v[80:95], v[44:47], v[148:151], v[80:95]\n"
    "ds_read_b64_tr_b16 v[44:45], v118 offset:2048\nds_read_b64_tr_b16 v[46:47], v118 offset:2304\ns_waitcnt lgkmcnt(8)\n"
    "v_mfma_f32_32x32x16_bf16 v[80:95], v[48:51], v[152:155], v[80:95]\nds_read_b64_tr_b16 v[48:49], v118 offset:3072\nds_read_b64_tr_b16 v[50:51], v118 offset:3328\ns_barrier\n"
    "s_waitcnt lgkmcnt(8)\nv_mfma_f32_32x32x16_bf16 v[80:95], v[52:55], v[156:159], v[80:95]\nds_read_b128 v[160:163], v29 offset:4096\n"
    "ds_read_b64_tr_b16 v[52:53], v117 offset:0\nds_read_b64_tr_b16 v[54:55], v117 offset:256\ns_waitcnt lgkmcnt(9)\n"
    "v_mfma_f32_32x32x16_bf16 v[96:111], v[56:59], v[144:147], 0\nds_read_b128 v[164:167], v176 offset:4096\nds_read_b64_tr_b16 v[56:57], v117 offset:1024\n"
    "ds_read_b64_tr_b16 v[58:59], v117 offset:1280\ns_waitcnt lgkmcnt(10)\nv_mfma_f32_32x32x16_bf16 v[96:111], v[60:63], v[148:151], v[96:111]\n"
    "ds_read_b128 v[168:171], v177 offset:4096\nds_read_b64_tr_b16 v[60:61], v117 offset:2048\nds_read_b64_tr_b16 v[62:63], v117 offset:2304\ns_waitcnt lgkmcnt(11)\n"
    "v_mfma_f32_32x32x16_bf16 v[96:111], v[44:47], v[152:155], v[96:111]\nds_read_b128 v[172:175], v178 offset:4096\nds_read_b64_tr_b16 v[44:45], v117 offset:3072\n"
    "ds_read_b64_tr_b16 v[46:47], v117 offset:3328\ns_waitcnt lgkmcnt(12)\nv_mfma_f32_32x32x16_bf16 v[96:111], v[48:51], v[156:159], v[96:111]\n"
    "ds_read_b64_tr_b16 v[48:49], v117 offset:32768\nds_read_b64_tr_b16 v[50:51], v117 offset:33024\ns_waitcnt lgkmcnt(13)\nv_dot2c_f32_bf16 v112, v143, v160\n"
    "v_dot2c_f32_bf16 v113, v160, v160\nv_dot2c_f32_bf16 v114, v143, v161\nv_dot2c_f32_bf16 v115, v161, v161\nv_dot2c_f32_bf16 v112, v143, v162\n"
    "v_dot2c_f32_bf16 v113, v162, v162\nv_dot2c_f32_bf16 v114, v143, v163\nv_dot2c_f32_bf16 v115, v163, v163\ns_waitcnt lgkmcnt(10)\nv_dot2c_f32_bf16 v112, v143, v164\n"
    "v_dot2c_f32_bf16 v113, v164, v164\nv_dot2c_f32_bf16 v114, v143, v165\nv_dot2c_f32_bf16 v115, v165, v165\nv_dot2c_f32_bf16 v112, v143, v166\n"
    "v_dot2c_f32_bf16 v113, v166, v166\nv_dot2c_f32_bf16 v114, v143, v167\nv_dot2c_f32_bf16 v115, v167, v167\ns_waitcnt lgkmcnt(7)\nv_dot2c_f32_bf16 v112, v143, v168\n"
    "v_dot2c_f32_bf16 v113, v168, v168\nv_dot2c_f32_bf16 v114, v143, v169\nv_dot2c_f32_bf16 v115, v169, v169\nv_dot2c_f32_bf16 v112, v143, v170\n"
    "v_dot2c_f32_bf16 v113, v170, v170\nv_dot2c_f32_bf16 v114, v143, v171\nv_dot2c_f32_bf16 v115, v171, v171\ns_waitcnt lgkmcnt(4)\nv_dot2c_f32_bf16 v112, v143, v172\n"
    "v_dot2c_f32_bf16 v113, v172, v172\nv_dot2c_f32_bf16 v114, v143, v173\nv_dot2c_f32_bf16 v115, v173, v173\nv_dot2c_f32_bf16 v112, v143, v174\n"
    "v_dot2c_f32_bf16 v113, v174, v174\nv_dot2c_f32_bf16 v114, v143, v175\nv_dot2c_f32_bf16 v115, v175, v175\nv_mfma_f32_32x32x16_bf16 v[64:79], v[52:55], v[160:163], v[64:79]\n"
    "ds_read_b64_tr_b16 v[52:53], v117 offset:33792\nds_read_b64_tr_b16 v[54:55], v117 offset:34048\nv_add_u32 v116, s46, v26\nv_add_u32 v118, 0x10000, v116\n"
    "v_mfma_f32_32x32x16_bf16 v[64:79], v[56:59], v[164:167], v[64:79]\nds_read_b64_tr_b16 v[56:57], v117 offset:34816\nds_read_b64_tr_b16 v[58:59], v117 offset:35072\n"
    "v_mfma_f32_32x32x16_bf16 v[64:79], v[60:63], v[168:171], v[64:79]\nds_read_b64_tr_b16 v[60:61], v117 offset:35840\nds_read_b64_tr_b16 v[62:63], v117 offset:36096\n"
    "s_waitcnt lgkmcnt(8)\nv_mfma_f32_32x32x16_bf16 v[64:79], v[44:47], v[172:175], v[64:79]\nds_read_b64_tr_b16 v[44:45], v119 offset:0\n"
    "ds_read_b64_tr_b16 v[46:47], v119 offset:256\ns_waitcnt lgkmcnt(8)\nv_mfma_f32_32x32x16_bf16 v[80:95], v[48:51], v[160:163], v[80:95]\n"
    "ds_read_b64_tr_b16 v[48:49], v119 offset:1024\nds_read_b64_tr_b16 v[50:51], v119 offset:1280\ns_waitcnt lgkmcnt(8)\n"
    "v_mfma_f32_32x32x16_bf16 v[80:95], v[52:55], v[164:167], v[80:95]\nds_read_b64_tr_b16 v[52:53], v119 offset:2048\nds_read_b64_tr_b16 v[54:55], v119 offset:2304\n"
    "s_waitcnt lgkmcnt(8)\nv_mfma_f32_32x32x16_bf16 v[80:95], v[56:59], v[168:171], v[80:95]\nds_read_b64_tr_b16 v[56:57], v119 offset:3072\n"
    "ds_read_b64_tr_b16 v[58:59], v119 offset:3328\ns_barrier\ns_waitcnt lgkmcnt(8)\nv_mfma_f32_32x32x16_bf16 v[80:95], v[60:63], v[172:175], v[80:95]\n"
    "ds_read_b128 v[144:147], v29 offset:8192\nds_read_b64_tr_b16 v[60:61], v116 offset:0\nds_read_b64_tr_b16 v[62:63], v116 offset:256\ns_waitcnt lgkmcnt(9)\n"
    "v_mfma_f32_32x32x16_bf16 v[96:111], v[44:47], v[160:163], v[96:111]\nds_read_b128 v[148:151], v176 offset:8192\nds_read_b64_tr_b16 v[44:45], v116 offset:1024\n"
    "ds_read_b64_tr_b16 v[46:47], v116 offset:1280\ns_waitcnt lgkmcnt(10)\nv_mfma_f32_32x32x16_bf16 v[96:111], v[48:51], v[164:167], v[96:111]\n"
    "ds_read_b128 v[152:155], v177 offset:8192\nds_read_b64_tr_b16 v[48:49], v116 offset:2048\nds_read_b64_tr_b16 v[50:51], v116 offset:2304\ns_waitcnt lgkmcnt(11)\n"
    "v_mfma_f32_32x32x16_bf16 v[96:111], v[52:55], v[168:171], v[96:111]\nds_read_b128 v[156:159], v178 offset:8192\nds_read_b64_tr_b16 v[52:53], v116 offset:3072\n"
    "ds_read_b64_tr_b16 v[54:55], v116 offset:3328\ns_waitcnt lgkmcnt(12)\nv_mfma_f32_32x32x16_bf16 v[96:111], v[56:59], v[172:175], v[96:111]\n"
    "ds_read_b64_tr_b16 v[56:57], v116 offset:32768\nds_read_b64_tr_b16 v[58:59], v116 offset:33024\ns_waitcnt lgkmcnt(13)\nv_dot2c_f32_bf16 v112, v143, v144\n"
    "v_dot2c_f32_bf16 v113, v144, v144\nv_dot2c_f32_bf16 v114, v143, v145\nv_dot2c_f32_bf16 v115, v145, v145\nv_dot2c_f32_bf16 v112, v143, v146\n"
    "v_dot2c_f32_bf16 v113, v146, v146\nv_dot2c_f32_bf16 v114, v143, v147\nv_dot2c_f32_bf16 v115, v147, v147\ns_waitcnt lgkmcnt(10)\nv_dot2c_f32_bf16 v112, v143, v148\n"
    "v_dot2c_f32_bf16 v113, v148, v148\nv_dot2c_f32_bf16 v114, v143, v149\nv_dot2c_f32_bf16 v115, v149, v149\nv_dot2c_f32_bf16 v112, v143, v150\n"
    "v_dot2c_f32_bf16 v113, v150, v150\nv_dot2c_f32_bf16 v114, v143, v151\nv_dot2c_f32_bf16 v115, v151, v151\ns_waitcnt lgkmcnt(7)\nv_dot2c_f32_bf16 v112, v143, v152\n"
    "v_dot2c_f32_bf16 v113, v152, v152\nv_dot2c_f32_bf16 v114, v143, v153\nv_dot2c_f32_bf16 v115, v153, v153\nv_dot2c_f32_bf16 v112, v143, v154\n"
    "v_dot2c_f32_bf16 v113, v154, v154\nv_dot2c_f32_bf16 v114, v143, v155\nv_dot2c_f32_bf16 v115, v155, v155\ns_waitcnt lgkmcnt(4)\nv_dot2c_f32_bf16 v112, v143, v156\n"
    "v_dot2c_f32_bf16 v113, v156, v156\nv_dot2c_f32_bf16 v114, v143, v157\nv_dot2c_f32_bf16 v115, v157, v157\nv_dot2c_f32_bf16 v112, v143, v158\n"
    "v_dot2c_f32_bf16 v113, v158, v158\nv_dot2c_f32_bf16 v114, v143, v159\nv_dot2c_f32_bf16 v115, v159, v159\nv_mfma_f32_32x32x16_bf16 v[64:79], v[60:63], v[144:147], v[64:79]\n"
    "ds_read_b64_tr_b16 v[60:61], v116 offset:33792\nds_read_b64_tr_b16 v[62:63], v116 offset:34048\nv_add_u32 v117, s47, v26\nv_add_u32 v119, 0x10000, v117\n"
    "v_mfma_f32_32x32x16_bf16 v[64:79], v[44:47], v[148:151], v[64:79]\nds_read_b64_tr_b16 v[44:45], v116 offset:34816\nds_read_b64_tr_b16 v[46:47], v116 offset:35072\n"
    "v_mfma_f32_32x32x16_bf16 v[64:79], v[48:51], v[152:155], v[64:79]\nds_read_b64_tr_b16 v[48:49], v116 offset:35840\nds_read_b64_tr_b16 v[50:51], v116 offset:36096\n"
    "s_waitcnt lgkmcnt(8)\nv_mfma_f32_32x32x16_bf16 v[64:79], v[52:55], v[156:159], v[64:79]\nds_read_b64_tr_b16 v[52:53], v118 offset:0\n"
    "ds_read_b64_tr_b16 v[54:55], v118 offset:256\ns_waitcnt lgkmcnt(8)\nv_mfma_f32_32x32x16_bf16 v[80:95], v[56:59], v[144:147], v[80:95]\n"
    "ds_read_b64_tr_b16 v[56:57], v118 offset:1024\nds_read_b64_tr_b16 v[58:59], v118 offset:1280\ns_waitcnt lgkmcnt(8)\n"
    "v_mfma_f32_32x32x16_bf16 v[80:95], v[60:63], v[148:151], v[80:95]\nds_read_b64_tr_b16 v[60:61], v118 offset:2048\nds_read_b64_tr_b16 v[62:63], v118 offset:2304\n"
    "s_waitcnt lgkmcnt(8)\nv_mfma_f32_32x32x16_bf16 v[80:95], v[44:47], v[152:155], v[80:95]\nds_read_b64_tr_b16 v[44:45], v118 offset:3072\n"
    "ds_read_b64_tr_b16 v[46:47], v118 offset:3328\ns_barrier\ns_waitcnt lgkmcnt(8)\nv_mfma_f32_32x32x16_bf16 v[80:95], v[48:51], v[156:159], v[80:95]\n"
    "ds_read_b128 v[160:163], v29 offset:0\nds_read_b64_tr_b16 v[48:49], v117 offset:0\nds_read_b64_tr_b16 v[50:51], v117 offset:256\ns_waitcnt lgkmcnt(9)\n"
    "v_mfma_f32_32x32x16_bf16 v[96:111], v[52:55], v[144:147], v[96:111]\nds_read_b128 v[164:167], v176 offset:0\nds_read_b64_tr_b16 v[52:53], v117 offset:1024\n"
    "ds_read_b64_tr_b16 v[54:55], v117 offset:1280\ns_waitcnt lgkmcnt(10)\nv_mfma_f32_32x32x16_bf16 v[96:111], v[56:59], v[148:151], v[96:111]\n"
    "ds_read_b128 v[168:171], v177 offset:0\nds_read_b64_tr_b16 v[56:57], v117 offset:2048\nds_read_b64_tr_b16 v[58:59], v117 offset:2304\ns_waitcnt lgkmcnt(11)\n"
    "v_mfma_f32_32x32x16_bf16 v[96:111], v[60:63], v[152:155], v[96:111]\nds_read_b128 v[172:175], v178 offset:0\nds_read_b64_tr_b16 v[60:61], v117 offset:3072\n"
    "ds_read_b64_tr_b16 v[62:63], v117 offset:3328\ns_waitcnt lgkmcnt(12)\nv_mfma_f32_32x32x16_bf16 v[96:111], v[44:47], v[156:159], v[96:111]\n"
    "ds_read_b64_tr_b16 v[44:45], v117 offset:32768\nds_read_b64_tr_b16 v[46:47], v117 offset:33024\ns_waitcnt lgkmcnt(13)\nv_dot2c_f32_bf16 v112, v143, v160\n"
    "v_dot2c_f32_bf16 v113, v160, v160\nv_dot2c_f32_bf16 v114, v143, v161\nv_dot2c_f32_bf16 v115, v161, v161\nv_dot2c_f32_bf16 v112, v143, v162\n"
    "v_dot2c_f32_bf16 v113, v162, v162\nv_dot2c_f32_bf16 v114, v143, v163\nv_dot2c_f32_bf16 v115, v163, v163\ns_waitcnt lgkmcnt(10)\nv_dot2c_f32_bf16 v112, v143, v164\n"
    "v_dot2c_f32_bf16 v113, v164, v164\nv_dot2c_f32_bf16 v114, v143, v165\nv_dot2c_f32_bf16 v115, v165, v165\nv_dot2c_f32_bf16 v112, v143, v166\n"
    "v_dot2c_f32_bf16 v113, v166, v166\nv_dot2c_f32_bf16 v114, v143, v167\nv_dot2c_f32_bf16 v115, v167, v167\ns_waitcnt lgkmcnt(7)\nv_dot2c_f32_bf16 v112, v143, v168\n"
    "v_dot2c_f32_bf16 v113, v168, v168\nv_dot2c_f32_bf16 v114, v143, v169\nv_dot2c_f32_bf16 v115, v169, v169\nv_dot2c_f32_bf16 v112, v143, v170\n"
    "v_dot2c_f32_bf16 v113, v170, v170\nv_dot2c_f32_bf16 v114, v143, v171\nv_dot2c_f32_bf16 v115, v171, v171\ns_waitcnt lgkmcnt(4)\nv_dot2c_f32_bf16 v112, v143, v172\n"
    "v_dot2c_f32_bf16 v113, v172, v172\nv_dot2c_f32_bf16 v114, v143, v173\nv_dot2c_f32_bf16 v115, v173, v173\nv_dot2c_f32_bf16 v112, v143, v174\n"
    "v_dot2c_f32_bf16 v113, v174, v174\nv_dot2c_f32_bf16 v114, v143, v175\nv_dot2c_f32_bf16 v115, v175, v175\nv_mfma_f32_32x32x16_bf16 v[64:79], v[48:51], v[160:163], v[64:79]\n"
    "ds_read_b64_tr_b16 v[48:49], v117 offset:33792\nds_read_b64_tr_b16 v[50:51], v117 offset:34048\nv_add_u32 v116, s48, v26\nv_add_u32 v118, 0x10000, v116\n"
    "v_mfma_f32_32x32x16_bf16 v[64:79], v[52:55], v[164:167], v[64:79]\nds_read_b64_tr_b16 v[52:53], v117 offset:34816\nds_read_b64_tr_b16 v[54:55], v117 offset:35072\n"
    "s_cmp_lt_u32 s3, 2\ns_cbranch_scc0 .LL74\ns_waitcnt vmcnt(0)\ns_mov_b64 exec, 1\nglobal_store_dword v30, v180, s[32:33] sc1\ns_mov_b64 exec, -1\n.LL74:\n"
    "v_mfma_f32_32x32x16_bf16 v[64:79], v[56:59], v[168:171], v[64:79]\nds_read_b64_tr_b16 v[56:57], v117 offset:35840\nds_read_b64_tr_b16 v[58:59], v117 offset:36096\n"
    "s_waitcnt lgkmcnt(8)\nv_mfma_f32_32x32x16_bf16 v[64:79], v[60:63], v[172:175], v[64:79]\nds_read_b64_tr_b16 v[60:61], v119 offset:0\n"
    "ds_read_b64_tr_b16 v[62:63], v119 offset:256\ns_waitcnt lgkmcnt(8)\nv_mfma_f32_32x32x16_bf16 v[80:95], v[44:47], v[160:163], v[80:95]\n"
    "ds_read_b64_tr_b16 v[44:45], v119 offset:1024\nds_read_b64_tr_b16 v[46:47], v119 offset:1280\ns_waitcnt lgkmcnt(8)\n"
    "v_mfma_f32_32x32x16_bf16 v[80:95], v[48:51], v[164:167], v[80:95]\nds_read_b64_tr_b16 v[48:49], v119 offset:2048\nds_read_b64_tr_b16 v[50:51], v119 offset:2304\n"
    "s_waitcnt lgkmcnt(8)\nv_mfma_f32_32x32x16_bf16 v[80:95], v[52:55], v[168:171], v[80:95]\nds_read_b64_tr_b16 v[52:53], v119 offset:3072\n"
    "ds_read_b64_tr_b16 v[54:55], v119 offset:3328\ns_barrier\ns_waitcnt lgkmcnt(8)\nv_mfma_f32_32x32x16_bf16 v[80:95], v[56:59], v[172:175], v[80:95]\n"
    "ds_read_b128 v[144:147], v29 offset:4096\nds_read_b64_tr_b16 v[56:57], v116 offset:0\nds_read_b64_tr_b16 v[58:59], v116 offset:256\ns_waitcnt lgkmcnt(9)\n"
    "v_mfma_f32_32x32x16_bf16 v[96:111], v[60:63], v[160:163], v[96:111]\nds_read_b128 v[148:151], v176 offset:4096\nds_read_b64_tr_b16 v[60:61], v116 offset:1024\n"
    "ds_read_b64_tr_b16 v[62:63], v116 offset:1280\ns_waitcnt lgkmcnt(10)\nv_mfma_f32_32x32x16_bf16 v[96:111], v[44:47], v[164:167], v[96:111]\n"
    "ds_read_b128 v[152:155], v177 offset:4096\nds_read_b64_tr_b16 v[44:45], v116 offset:2048\nds_read_b64_tr_b16 v[46:47], v116 offset:2304\ns_waitcnt lgkmcnt(11)\n"
    "v_mfma_f32_32x32x16_bf16 v[96:111], v[48:51], v[168:171], v[96:111]\nds_read_b128 v[156:159], v178 offset:4096\nds_read_b64_tr_b16 v[48:49], v116 offset:3072\n"
    "ds_read_b64_tr_b16 v[50:51], v116 offset:3328\ns_waitcnt lgkmcnt(12)\nv_mfma_f32_32x32x16_bf16 v[96:111], v[52:55], v[172:175], v[96:111]\n"
    "ds_read_b64_tr_b16 v[52:53], v116 offset:32768\nds_read_b64_tr_b16 v[54:55], v116 offset:33024\ns_waitcnt lgkmcnt(13)\nv_dot2c_f32_bf16 v112, v143, v144\n"
    "v_dot2c_f32_bf16 v113, v144, v144\nv_dot2c_f32_bf16 v114, v143, v145\nv_dot2c_f32_bf16 v115, v145, v145\nv_dot2c_f32_bf16 v112, v143, v146\n"
    "v_dot2c_f32_bf16 v113, v146, v146\nv_dot2c_f32_bf16 v114, v143, v147\nv_dot2c_f32_bf16 v115, v147, v147\ns_waitcnt lgkmcnt(10)\nv_dot2c_f32_bf16 v112, v143, v148\n"
    "v_dot2c_f32_bf16 v113, v148, v148\nv_dot2c_f32_bf16 v114, v143, v149\nv_dot2c_f32_bf16 v115, v149, v149\nv_dot2c_f32_bf16 v112, v143, v150\n"
    "v_dot2c_f32_bf16 v113, v150, v150\nv_dot2c_f32_bf16 v114, v143, v151\nv_dot2c_f32_bf16 v115, v151, v151\ns_waitcnt lgkmcnt(7)\nv_dot2c_f32_bf16 v112, v143, v152\n"
    "v_dot2c_f32_bf16 v113, v152, v152\nv_dot2c_f32_bf16 v114, v143, v153\nv_dot2c_f32_bf16 v115, v153, v153\nv_dot2c_f32_bf16 v112, v143, v154\n"
    "v_dot2c_f32_bf16 v113, v154, v154\nv_dot2c_f32_bf16 v114, v143, v155\nv_dot2c_f32_bf16 v115, v155, v155\ns_waitcnt lgkmcnt(4)\nv_dot2c_f32_bf16 v112, v143, v156\n"
    "v_dot2c_f32_bf16 v113, v156, v156\nv_dot2c_f32_bf16 v114, v143, v157\nv_dot2c_f32_bf16 v115, v157, v157\nv_dot2c_f32_bf16 v112, v143, v158\n"
    "v_dot2c_f32_bf16 v113, v158, v158\nv_dot2c_f32_bf16 v114, v143, v159\nv_dot2c_f32_bf16 v115, v159, v159\nv_mfma_f32_32x32x16_bf16 v[64:79], v[56:59], v[144:147], v[64:79]\n"
    "ds_read_b64_tr_b16 v[56:57], v116 offset:33792\nds_read_b64_tr_b16 v[58:59], v116 offset:34048\nv_add_u32 v117, s49, v26\nv_add_u32 v119, 0x10000, v117\n"
    "v_mfma_f32_32x32x16_bf16 v[64:79], v[60:63], v[148:151], v[64:79]\nds_read_b64_tr_b16 v[60:61], v116 offset:34816\nds_read_b64_tr_b16 v[62:63], v116 offset:35072\n"
    "v_mfma_f32_32x32x16_bf16 v[64:79], v[44:47], v[152:155], v[64:79]\nds_read_b64_tr_b16 v[44:45], v116 offset:35840\nds_read_b64_tr_b16 v[46:47], v116 offset:36096\n"
    "s_waitcnt lgkmcnt(8)\nv_mfma_f32_32x32x16_bf16 v[64:79], v[48:51], v[156:159], v[64:79]\nds_read_b64_tr_b16 v[48:49], v118 offset:0\n"
    "ds_read_b64_tr_b16 v[50:51], v118 offset:256\ns_waitcnt lgkmcnt(8)\nv_mfma_f32_32x32x16_bf16 v[80:95], v[52:55], v[144:147], v[80:95]\n"
    "ds_read_b64_tr_b16 v[52:53], v118 offset:1024\nds_read_b64_tr_b16 v[54:55], v118 offset:1280\ns_waitcnt lgkmcnt(8)\n"
    "v_mfma_f32_32x32x16_bf16 v[80:95], v[56:59], v[148:151], v[80:95]\nds_read_b64_tr_b16 v[56:57], v118 offset:2048\nds_read_b64_tr_b16 v[58:59], v118 offset:2304\n"
    "s_waitcnt lgkmcnt(8)\nv_mfma_f32_32x32x16_bf16 v[80:95], v[60:63], v[152:155], v[80:95]\nds_read_b64_tr_b16 v[60:61], v118 offset:3072\n"
    "ds_read_b64_tr_b16 v[62:63], v118 offset:3328\ns_barrier\ns_waitcnt lgkmcnt(8)\nv_mfma_f32_32x32x16_bf16 v[80:95], v[44:47], v[156:159], v[80:95]\n"
    "ds_read_b128 v[160:163], v29 offset:8192\nds_read_b64_tr_b16 v[44:45], v117 offset:0\nds_read_b64_tr_b16 v[46:47], v117 offset:256\ns_waitcnt lgkmcnt(9)\n"
    "v_mfma_f32_32x32x16_bf16 v[96:111], v[48:51], v[144:147], v[96:111]\nds_read_b128 v[164:167], v176 offset:8192\nds_read_b64_tr_b16 v[48:49], v117 offset:1024\n"
    "ds_read_b64_tr_b16 v[50:51], v117 offset:1280\ns_waitcnt lgkmcnt(10)\nv_mfma_f32_32x32x16_bf16 v[96:111], v[52:55], v[148:151], v[96:111]\n"
    "ds_read_b128 v[168:171], v177 offset:8192\nds_read_b64_tr_b16 v[52:53], v117 offset:2048\nds_read_b64_tr_b16 v[54:55], v117 offset:2304\ns_waitcnt lgkmcnt(11)\n"
    "v_mfma_f32_32x32x16_bf16 v[96:111], v[56:59], v[152:155], v[96:111]\nds_read_b128 v[172:175], v178 offset:8192\nds_read_b64_tr_b16 v[56:57], v117 offset:3072\n"
    "ds_read_b64_tr_b16 v[58:59], v117 offset:3328\ns_waitcnt lgkmcnt(12)\nv_mfma_f32_32x32x16_bf16 v[96:111], v[60:63], v[156:159], v[96:111]\n"
    "ds_read_b64_tr_b16 v[60:61], v117 offset:32768\nds_read_b64_tr_b16 v[62:63], v117 offset:33024\ns_waitcnt lgkmcnt(13)\nv_dot2c_f32_bf16 v112, v143, v160\n"
    "v_dot2c_f32_bf16 v113, v160, v160\nv_dot2c_f32_bf16 v114, v143, v161\nv_dot2c_f32_bf16 v115, v161, v161\nv_dot2c_f32_bf16 v112, v143, v162\n"
    "v_dot2c_f32_bf16 v113, v162, v162\nv_dot2c_f32_bf16 v114, v143, v163\nv_dot2c_f32_bf16 v115, v163, v163\ns_waitcnt lgkmcnt(10)\nv_dot2c_f32_bf16 v112, v143, v164\n"
    "v_dot2c_f32_bf16 v113, v164, v164\nv_dot2c_f32_bf16 v114, v143, v165\nv_dot2c_f32_bf16 v115, v165, v165\nv_dot2c_f32_bf16 v112, v143, v166\n"
    "v_dot2c_f32_bf16 v113, v166, v166\nv_dot2c_f32_bf16 v114, v143, v167\nv_dot2c_f32_bf16 v115, v167, v167\ns_waitcnt lgkmcnt(7)\nv_dot2c_f32_bf16 v112, v143, v168\n"
    "v_dot2c_f32_bf16 v113, v168, v168\nv_dot2c_f32_bf16 v114, v143, v169\nv_dot2c_f32_bf16 v115, v169, v169\nv_dot2c_f32_bf16 v112, v143, v170\n"
    "v_dot2c_f32_bf16 v113, v170, v170\nv_dot2c_f32_bf16 v114, v143, v171\nv_dot2c_f32_bf16 v115, v171, v171\ns_waitcnt lgkmcnt(4)\nv_dot2c_f32_bf16 v112, v143, v172\n"
    "v_dot2c_f32_bf16 v113, v172, v172\nv_dot2c_f32_bf16 v114, v143, v173\nv_dot2c_f32_bf16 v115, v173, v173\nv_dot2c_f32_bf16 v112, v143, v174\n"
    "v_dot2c_f32_bf16 v113, v174, v174\nv_dot2c_f32_bf16 v114, v143, v175\nv_dot2c_f32_bf16 v115, v175, v175\nv_mfma_f32_32x32x16_bf16 v[64:79], v[44:47], v[160:163], v[64:79]\n"
    "ds_read_b64_tr_b16 v[44:45], v117 offset:33792\nds_read_b64_tr_b16 v[46:47], v117 offset:34048\nv_add_u32 v116, s50, v26\nv_add_u32 v118, 0x10000, v116\n"
    "v_mfma_f32_32x32x16_bf16 v[64:79], v[48:51], v[164:167], v[64:79]\nds_read_b64_tr_b16 v[48:49], v117 offset:34816\nds_read_b64_tr_b16 v[50:51], v117 offset:35072\n"
    "v_mfma_f32_32x32x16_bf16 v[64:79], v[52:55], v[168:171], v[64:79]\nds_read_b64_tr_b16 v[52:53], v117 offset:35840\nds_read_b64_tr_b16 v[54:55], v117 offset:36096\n"
    "s_waitcnt lgkmcnt(8)\nv_mfma_f32_32x32x16_bf16 v[64:79], v[56:59], v[172:175], v[64:79]\nds_read_b64_tr_b16 v[56:57], v119 offset:0\n"
    "ds_read_b64_tr_b16 v[58:59], v119 offset:256\ns_waitcnt lgkmcnt(8)\nv_mfma_f32_32x32x16_bf16 v[80:95], v[60:63], v[160:163], v[80:95]\n"
    "ds_read_b64_tr_b16 v[60:61], v119 offset:1024\nds_read_b64_tr_b16 v[62:63], v119 offset:1280\ns_waitcnt lgkmcnt(8)\n"
    "v_mfma_f32_32x32x16_bf16 v[80:95], v[44:47], v[164:167], v[80:95]\nds_read_b64_tr_b16 v[44:45], v119 offset:2048\nds_read_b64_tr_b16 v[46:47], v119 offset:2304\n"
    "s_waitcnt lgkmcnt(8)\nv_mfma_f32_32x32x16_bf16 v[80:95], v[48:51], v[168:171], v[80:95]\nds_read_b64_tr_b16 v[48:49], v119 offset:3072\n"
    "ds_read_b64_tr_b16 v[50:51], v119 offset:3328\ns_barrier\ns_waitcnt lgkmcnt(8)\nv_mfma_f32_32x32x16_bf16 v[80:95], v[52:55], v[172:175], v[80:95]\n"
    "ds_read_b128 v[144:147], v29 offset:0\nds_read_b64_tr_b16 v[52:53], v116 offset:0\nds_read_b64_tr_b16 v[54:55], v116 offset:256\ns_waitcnt lgkmcnt(9)\n"
    "v_mfma_f32_32x32x16_bf16 v[96:111], v[56:59], v[160:163], v[96:111]\nds_read_b128 v[148:151], v176 offset:0\nds_read_b64_tr_b16 v[56:57], v116 offset:1024\n"
    "ds_read_b64_tr_b16 v[58:59], v116 offset:1280\ns_waitcnt lgkmcnt(10)\nv_mfma_f32_32x32x16_bf16 v[96:111], v[60:63], v[164:167], v[96:111]\n"
    "ds_read_b128 v[152:155], v177 offset:0\nds_read_b64_tr_b16 v[60:61], v116 offset:2048\nds_read_b64_tr_b16 v[62:63], v116 offset:2304\ns_waitcnt lgkmcnt(11)\n"
    "v_mfma_f32_32x32x16_bf16 v[96:111], v[44:47], v[168:171], v[96:111]\nds_read_b128 v[156:159], v178 offset:0\nds_read_b64_tr_b16 v[44:45], v116 offset:3072\n"
    "ds_read_b64_tr_b16 v[46:47], v116 offset:3328\ns_waitcnt lgkmcnt(12)\nv_mfma_f32_32x32x16_bf16 v[96:111], v[48:51], v[172:175], v[96:111]\n"
    "ds_read_b64_tr_b16 v[48:49], v116 offset:32768\nds_read_b64_tr_b16 v[50:51], v116 offset:33024\ns_waitcnt lgkmcnt(13)\nv_dot2c_f32_bf16 v112, v143, v144\n"
    "v_dot2c_f32_bf16 v113, v144, v144\nv_dot2c_f32_bf16 v114, v143, v145\nv_dot2c_f32_bf16 v115, v145, v145\nv_dot2c_f32_bf16 v112, v143, v146\n"
    "v_dot2c_f32_bf16 v113, v146, v146\nv_dot2c_f32_bf16 v114, v143, v147\nv_dot2c_f32_bf16 v115, v147, v147\ns_waitcnt lgkmcnt(10)\nv_dot2c_f32_bf16 v112, v143, v148\n"
    "v_dot2c_f32_bf16 v113, v148, v148\nv_dot2c_f32_bf16 v114, v143, v149\nv_dot2c_f32_bf16 v115, v149, v149\nv_dot2c_f32_bf16 v112, v143, v150\n"
    "v_dot2c_f32_bf16 v113, v150, v150\nv_dot2c_f32_bf16 v114, v143, v151\nv_dot2c_f32_bf16 v115, v151, v151\ns_waitcnt lgkmcnt(7)\nv_dot2c_f32_bf16 v112, v143, v152\n"
    "v_dot2c_f32_bf16 v113, v152, v152\nv_dot2c_f32_bf16 v114, v143, v153\nv_dot2c_f32_bf16 v115, v153, v153\nv_dot2c_f32_bf16 v112, v143, v154\n"
    "v_dot2c_f32_bf16 v113, v154, v154\nv_dot2c_f32_bf16 v114, v143, v155\nv_dot2c_f32_bf16 v115, v155, v155\ns_waitcnt lgkmcnt(4)\nv_dot2c_f32_bf16 v112, v143, v156\n"
    "v_dot2c_f32_bf16 v113, v156, v156\nv_dot2c_f32_bf16 v114, v143, v157\nv_dot2c_f32_bf16 v115, v157, v157\nv_dot2c_f32_bf16 v112, v143, v158\n"
    "v_dot2c_f32_bf16 v113, v158, v158\nv_dot2c_f32_bf16 v114, v143, v159\nv_dot2c_f32_bf16 v115, v159, v159\nv_mfma_f32_32x32x16_bf16 v[64:79], v[52:55], v[144:147], v[64:79]\n"
    "ds_read_b64_tr_b16 v[52:53], v116 offset:33792\nds_read_b64_tr_b16 v[54:55], v116 offset:34048\nv_add_u32 v117, s51, v26\nv_add_u32 v119, 0x10000, v117\n"
    "v_mfma_f32_32x32x16_bf16 v[64:79], v[56:59], v[148:151], v[64:79]\nds_read_b64_tr_b16 v[56:57], v116 offset:34816\nds_read_b64_tr_b16 v[58:59], v116 offset:35072\n"
    "v_mfma_f32_32x32x16_bf16 v[64:79], v[60:63], v[152:155], v[64:79]\nds_read_b64_tr_b16 v[60:61], v116 offset:35840\nds_read_b64_tr_b16 v[62:63], v116 offset:36096\n"
    "s_waitcnt lgkmcnt(8)\nv_mfma_f32_32x32x16_bf16 v[64:79], v[44:47], v[156:159], v[64:79]\nds_read_b64_tr_b16 v[44:45], v118 offset:0\n"
    "ds_read_b64_tr_b16 v[46:47], v118 offset:256\ns_waitcnt lgkmcnt(8)\nv_mfma_f32_32x32x16_bf16 v[80:95], v[48:51], v[144:147], v[80:95]\n"
    "ds_read_b64_tr_b16 v[48:49], v118 offset:1024\nds_read_b64_tr_b16 v[50:51], v118 offset:1280\ns_waitcnt lgkmcnt(8)\n"
    "v_mfma_f32_32x32x16_bf16 v[80:95], v[52:55], v[148:151], v[80:95]\nds_read_b64_tr_b16 v[52:53], v118 offset:2048\nds_read_b64_tr_b16 v[54:55], v118 offset:2304\n"
    "s_waitcnt lgkmcnt(8)\nv_mfma_f32_32x32x16_bf16 v[80:95], v[56:59], v[152:155], v[80:95]\nds_read_b64_tr_b16 v[56:57], v118 offset:3072\n"
    "ds_read_b64_tr_b16 v[58:59], v118 offset:3328\ns_waitcnt lgkmcnt(8)\nv_mfma_f32_32x32x16_bf16 v[80:95], v[60:63], v[156:159], v[80:95]\ns_waitcnt lgkmcnt(6)\n"
    "v_mfma_f32_32x32x16_bf16 v[96:111], v[44:47], v[144:147], v[96:111]\ns_waitcnt lgkmcnt(4)\nv_mfma_f32_32x32x16_bf16 v[96:111], v[48:51], v[148:151], v[96:111]\n"
    "s_waitcnt lgkmcnt(2)\nv_mfma_f32_32x32x16_bf16 v[96:111], v[52:55], v[152:155], v[96:111]\ns_waitcnt lgkmcnt(0)\n"
    "v_mfma_f32_32x32x16_bf16 v[96:111], v[56:59], v[156:159], v[96:111]\ns_barrier\nds_read_b128 v[160:163], v29 offset:4096\nds_read_b128 v[164:167], v176 offset:4096\n"
    "ds_read_b128 v[168:171], v177 offset:4096\nds_read_b128 v[172:175], v178 offset:4096\nds_read_b64_tr_b16 v[60:61], v117 offset:0\n"
    "ds_read_b64_tr_b16 v[62:63], v117 offset:256\nds_read_b64_tr_b16 v[44:45], v117 offset:1024\nds_read_b64_tr_b16 v[46:47], v117 offset:1280\n"
    "ds_read_b64_tr_b16 v[48:49], v117 offset:2048\nds_read_b64_tr_b16 v[50:51], v117 offset:2304\nds_read_b64_tr_b16 v[52:53], v117 offset:3072\n"
    "ds_read_b64_tr_b16 v[54:55], v117 offset:3328\nds_read_b64_tr_b16 v[56:57], v117 offset:32768\nds_read_b64_tr_b16 v[58:59], v117 offset:33024\ns_waitcnt lgkmcnt(13)\n"
    "v_dot2c_f32_bf16 v112, v143, v160\nv_dot2c_f32_bf16 v113, v160, v160\nv_dot2c_f32_bf16 v114, v143, v161\nv_dot2c_f32_bf16 v115, v161, v161\n"
    "v_dot2c_f32_bf16 v112, v143, v162\nv_dot2c_f32_bf16 v113, v162, v162\nv_dot2c_f32_bf16 v114, v143, v163\nv_dot2c_f32_bf16 v115, v163, v163\ns_waitcnt lgkmcnt(12)\n"
    "v_dot2c_f32_bf16 v112, v143, v164\nv_dot2c_f32_bf16 v113, v164, v164\nv_dot2c_f32_bf16 v114, v143, v165\nv_dot2c_f32_bf16 v115, v165, v165\n"
    "v_dot2c_f32_bf16 v112, v143, v166\nv_dot2c_f32_bf16 v113, v166, v166\nv_dot2c_f32_bf16 v114, v143, v167\nv_dot2c_f32_bf16 v115, v167, v167\ns_waitcnt lgkmcnt(11)\n"
    "v_dot2c_f32_bf16 v112, v143, v168\nv_dot2c_f32_bf16 v113, v168, v168\nv_dot2c_f32_bf16 v114, v143, v169\nv_dot2c_f32_bf16 v115, v169, v169\n"
    "v_dot2c_f32_bf16 v112, v143, v170\nv_dot2c_f32_bf16 v113, v170, v170\nv_dot2c_f32_bf16 v114, v143, v171\nv_dot2c_f32_bf16 v115, v171, v171\ns_waitcnt lgkmcnt(10)\n"
    "v_dot2c_f32_bf16 v112, v143, v172\nv_dot2c_f32_bf16 v113, v172, v172\nv_dot2c_f32_bf16 v114, v143, v173\nv_dot2c_f32_bf16 v115, v173, v173\n"
    "v_dot2c_f32_bf16 v112, v143, v174\nv_dot2c_f32_bf16 v113, v174, v174\nv_dot2c_f32_bf16 v114, v143, v175\nv_dot2c_f32_bf16 v115, v175, v175\ns_waitcnt lgkmcnt(8)\n"
    "v_mfma_f32_32x32x16_bf16 v[64:79], v[60:63], v[160:163], v[64:79]\nds_read_b64_tr_b16 v[60:61], v117 offset:33792\nds_read_b64_tr_b16 v[62:63], v117 offset:34048\n"
    "v_add_f32 v112, v112, v114\nv_add_f32 v113, v113, v115\ns_waitcnt lgkmcnt(8)\nv_mfma_f32_32x32x16_bf16 v[64:79], v[44:47], v[164:167], v[64:79]\n"
    "ds_read_b64_tr_b16 v[44:45], v117 offset:34816\nds_read_b64_tr_b16 v[46:47], v117 offset:35072\nv_permlane32_swap_b32 v112, v113\nv_add_f32 v138, v112, v113\n"
    "v_add_f32 v137, v112, v113\ns_waitcnt lgkmcnt(8)\nv_mfma_f32_32x32x16_bf16 v[64:79], v[48:51], v[168:171], v[64:79]\nds_read_b64_tr_b16 v[48:49], v117 offset:35840\n"
    "ds_read_b64_tr_b16 v[50:51], v117 offset:36096\nv_permlane32_swap_b32 v138, v137\nv_mul_f32 v138, s81, v138\nv_mul_f32 v112, v138, v138\ns_waitcnt lgkmcnt(8)\n"
    "v_mfma_f32_32x32x16_bf16 v[64:79], v[52:55], v[172:175], v[64:79]\nds_read_b64_tr_b16 v[52:53], v119 offset:0\nds_read_b64_tr_b16 v[54:55], v119 offset:256\n"
    "v_fma_f32 v137, v137, s81, -v112\nv_max_f32 v137, 0, v137\nv_cvt_pk_bf16_f32 v137, 0, v137\ns_waitcnt lgkmcnt(8)\n"
    "v_mfma_f32_32x32x16_bf16 v[80:95], v[56:59], v[160:163], v[80:95]\nds_read_b64_tr_b16 v[56:57], v119 offset:1024\nds_read_b64_tr_b16 v[58:59], v119 offset:1280\n"
    "v_add_f32 v137, 0x3727c5ac, v137\nv_cvt_pk_bf16_f32 v137, 0, v137\ns_waitcnt lgkmcnt(8)\nv_mfma_f32_32x32x16_bf16 v[80:95], v[60:63], v[164:167], v[80:95]\n"
    "ds_read_b64_tr_b16 v[60:61], v119 offset:2048\nds_read_b64_tr_b16 v[62:63], v119 offset:2304\nv_sqrt_f32 v137, v137\nv_cvt_pk_bf16_f32 v138, -v138, -v138\ns_barrier\n"
    "ds_read_b32 v124, v28 offset:0\nds_read_b32 v128, v28 offset:128\nds_read_b32 v132, v28 offset:256\ns_waitcnt lgkmcnt(11)\n"
    "v_mfma_f32_32x32x16_bf16 v[80:95], v[44:47], v[168:171], v[80:95]\nds_read_b64_tr_b16 v[44:45], v119 offset:3072\nds_read_b64_tr_b16 v[46:47], v119 offset:3328\n"
    "v_cndmask_b32 v120, 0, v138, s[84:85]\nv_cvt_pk_bf16_f32 v137, 0, v137\nv_rcp_f32 v137, v137\ns_waitcnt lgkmcnt(11)\n"
    "v_mfma_f32_32x32x16_bf16 v[80:95], v[48:51], v[172:175], v[80:95]\ns_waitcnt lgkmcnt(0)\nv_cvt_pk_bf16_f32 v140, 0, v124\nv_cvt_pk_bf16_f32 v141, 0, v128\n"
    "v_cvt_pk_bf16_f32 v142, 0, v132\nv_mfma_f32_32x32x16_bf16 v[96:111], v[52:55], v[160:163], v[96:111]\nv_sub_f32 v124, v124, v140\nv_sub_f32 v128, v128, v141\n"
    "v_sub_f32 v132, v132, v142\nv_cvt_pk_bf16_f32 v124, v140, v124\nv_cvt_pk_bf16_f32 v128, v141, v128\nv_cvt_pk_bf16_f32 v132, v142, v132\n"
    "v_mfma_f32_32x32x16_bf16 v[64:79], v[124:127], v[120:123], v[64:79]\nv_mfma_f32_32x32x16_bf16 v[80:95], v[128:131], v[120:123], v[80:95]\n"
    "v_mfma_f32_32x32x16_bf16 v[96:111], v[56:59], v[164:167], v[96:111]\ns_nop 9\nv_mul_f32 v64, v64, v137\nv_mul_f32 v65, v65, v137\nv_mul_f32 v66, v66, v137\n"
    "v_mul_f32 v67, v67, v137\nv_mul_f32 v68, v68, v137\nv_mul_f32 v69, v69, v137\nv_mfma_f32_32x32x16_bf16 v[96:111], v[60:63], v[168:171], v[96:111]\n"
    "v_mul_f32 v70, v70, v137\nv_mul_f32 v71, v71, v137\nv_cvt_pk_bf16_f32 v64, v64, v65\nv_cvt_pk_bf16_f32 v65, v66, v67\nv_cvt_pk_bf16_f32 v66, v68, v69\n"
    "v_cvt_pk_bf16_f32 v67, v70, v71\nds_write_b128 v27, v[64:67] offset:0\nv_mfma_f32_32x32x16_bf16 v[96:111], v[44:47], v[172:175], v[96:111]\nv_mul_f32 v72, v72, v137\n"
    "v_mul_f32 v73, v73, v137\nv_mul_f32 v74, v74, v137\nv_mul_f32 v75, v75, v137\nv_mul_f32 v76, v76, v137\nv_mul_f32 v77, v77, v137\n"
    "v_mfma_f32_32x32x16_bf16 v[96:111], v[132:135], v[120:123], v[96:111]\nv_mul_f32 v78, v78, v137\nv_mul_f32 v79, v79, v137\nv_cvt_pk_bf16_f32 v72, v72, v73\n"
    "v_cvt_pk_bf16_f32 v73, v74, v75\nv_cvt_pk_bf16_f32 v74, v76, v77\nv_cvt_pk_bf16_f32 v75, v78, v79\nds_write_b128 v27, v[72:75] offset:32\n"
    "v_pk_mul_f32 v[80:81], v[80:81], v[136:137] op_sel:[0,1]\nv_pk_mul_f32 v[82:83], v[82:83], v[136:137] op_sel:[0,1]\n"
    "v_pk_mul_f32 v[84:85], v[84:85], v[136:137] op_sel:[0,1]\nv_pk_mul_f32 v[86:87], v[86:87], v[136:137] op_sel:[0,1]\nv_cvt_pk_bf16_f32 v80, v80, v81\n"
    "v_cvt_pk_bf16_f32 v81, v82, v83\nv_cvt_pk_bf16_f32 v82, v84, v85\nv_cvt_pk_bf16_f32 v83, v86, v87\nds_write_b128 v27, v[80:83] offset:64\n"
    "v_pk_mul_f32 v[88:89], v[88:89], v[136:137] op_sel:[0,1]\nv_pk_mul_f32 v[90:91], v[90:91], v[136:137] op_sel:[0,1]\n"
    "v_pk_mul_f32 v[92:93], v[92:93], v[136:137] op_sel:[0,1]\nv_pk_mul_f32 v[94:95], v[94:95], v[136:137] op_sel:[0,1]\nv_cvt_pk_bf16_f32 v88, v88, v89\n"
    "v_cvt_pk_bf16_f32 v89, v90, v91\nv_cvt_pk_bf16_f32 v90, v92, v93\nv_cvt_pk_bf16_f32 v91, v94, v95\nds_write_b128 v27, v[88:91] offset:96\n"
    "v_pk_mul_f32 v[96:97], v[96:97], v[136:137] op_sel:[0,1]\nv_pk_mul_f32 v[98:99], v[98:99], v[136:137] op_sel:[0,1]\n"
    "v_pk_mul_f32 v[100:101], v[100:101], v[136:137] op_sel:[0,1]\nv_pk_mul_f32 v[102:103], v[102:103], v[136:137] op_sel:[0,1]\nv_cvt_pk_bf16_f32 v96, v96, v97\n"
    "v_cvt_pk_bf16_f32 v97, v98, v99\nv_cvt_pk_bf16_f32 v98, v100, v101\nv_cvt_pk_bf16_f32 v99, v102, v103\nds_write_b128 v27, v[96:99] offset:128\n"
    "v_pk_mul_f32 v[104:105], v[104:105], v[136:137] op_sel:[0,1]\nv_pk_mul_f32 v[106:107], v[106:107], v[136:137] op_sel:[0,1]\n"
    "v_pk_mul_f32 v[108:109], v[108:109], v[136:137] op_sel:[0,1]\nv_pk_mul_f32 v[110:111], v[110:111], v[136:137] op_sel:[0,1]\nv_cvt_pk_bf16_f32 v104, v104, v105\n"
    "v_cvt_pk_bf16_f32 v105, v106, v107\nv_cvt_pk_bf16_f32 v106, v108, v109\nv_cvt_pk_bf16_f32 v107, v110, v111\nds_write_b128 v27, v[104:107] offset:160\n.LGL77:\n"
    "s_waitcnt lgkmcnt(0)\ns_barrier\nds_read_b128 v[64:67], v6\nds_read_b128 v[68:71], v6 offset:208\ns_mov_b32 exec_lo, 0x00ff00ff\ns_mov_b32 exec_hi, 0xff00ff00\n"
    "ds_read_b128 v[10:13], v7\nds_read_b128 v[14:17], v7 offset:208\ns_mov_b64 exec, -1\nds_read_b64_tr_b16 v[72:73], v8 offset:0\nds_read_b64_tr_b16 v[74:75], v8 offset:32\n"
    "ds_read_b64_tr_b16 v[76:77], v8 offset:208\nds_read_b64_tr_b16 v[78:79], v8 offset:240\ns_mov_b32 s81, 0x3eb8aa3b\ns_waitcnt lgkmcnt(5)\n"
    "v_mfma_f32_16x16x32_bf16 v[80:83], v[64:67], v[10:13], v[2:5]\ns_waitcnt lgkmcnt(4)\nv_mfma_f32_16x16x32_bf16 v[84:87], v[68:71], v[14:17], v[2:5]\ns_nop 5\n"
    "v_max_f32 v88, v80, v81\ns_nop 0\nv_max_f32 v89, v84, v85\nv_max3_f32 v88, v88, v82, v83\nv_max3_f32 v89, v89, v86, v87\ns_nop 1\nv_permlane16_swap_b32 v88, v89\n"
    "v_max_f32 v90, v88, v89\nv_max_f32 v89, v88, v89\ns_nop 1\nv_permlane32_swap_b32 v90, v89\nv_max_f32 v90, v90, v89\nv_mul_f32 v88, 0xbeb8aa3b, v90\n"
    "v_mul_f32 v89, 0xbeb8aa3b, v90\ns_nop 1\nv_permlane16_swap_b32 v88, v89\nv_pk_fma_f32 v[80:81], v[80:81], s[80:81], v[88:89] op_sel:[0,1,0] op_sel_hi:[1,1,0]\n"
    "v_pk_fma_f32 v[82:83], v[82:83], s[80:81], v[88:89] op_sel:[0,1,0] op_sel_hi:[1,1,0]\nv_pk_fma_f32 v[84:85], v[84:85], s[80:81], v[88:89] op_sel:[0,1,1] op_sel_hi:[1,1,1]\n"
    "v_pk_fma_f32 v[86:87], v[86:87], s[80:81], v[88:89] op_sel:[0,1,1] op_sel_hi:[1,1,1]\nv_exp_f32 v80, v80\nv_exp_f32 v81, v81\nv_exp_f32 v82, v82\nv_exp_f32 v83, v83\n"
    "v_exp_f32 v84, v84\nv_exp_f32 v85, v85\nv_exp_f32 v86, v86\nv_exp_f32 v87, v87\ns_mov_b32 exec_lo, 0x00ff00ff\ns_mov_b32 exec_hi, 0x00ff00ff\n"
    "v_cvt_pk_bf16_f32 v18, v80, v81\nv_cvt_pk_bf16_f32 v19, v82, v83\nv_cvt_pk_bf16_f32 v22, v84, v85\nv_cvt_pk_bf16_f32 v23, v86, v87\ns_not_b64 exec, exec\n"
    "v_cvt_pk_bf16_f32 v20, v80, v81\nv_cvt_pk_bf16_f32 v21, v82, v83\nv_cvt_pk_bf16_f32 v24, v84, v85\nv_cvt_pk_bf16_f32 v25, v86, v87\ns_mov_b64 exec, -1\n"
    "v_pk_add_f32 v[80:81], v[80:81], v[82:83]\nv_pk_add_f32 v[84:85], v[84:85], v[86:87]\nv_add_f32 v88, v80, v81\nv_add_f32 v89, v84, v85\ns_waitcnt lgkmcnt(0)\n"
    "v_mfma_f32_16x16x32_bf16 v[64:67], v[72:75], v[18:21], 0\nv_mfma_f32_16x16x32_bf16 v[68:71], v[76:79], v[22:25], 0\nv_permlane16_swap_b32 v88, v89\n"
    "v_add_f32 v90, v88, v89\nv_add_f32 v89, v88, v89\ns_nop 1\nv_permlane32_swap_b32 v90, v89\nv_add_f32 v90, v90, v89\nv_rcp_f32 v88, v90\nv_rcp_f32 v89, v90\ns_nop 1\n"
    "v_permlane16_swap_b32 v88, v89\nv_pk_mul_f32 v[64:65], v[64:65], v[88:89] op_sel:[0,0] op_sel_hi:[1,0]\n"
    "v_pk_mul_f32 v[68:69], v[68:69], v[88:89] op_sel:[0,1] op_sel_hi:[1,1]\nv_pk_mul_f32 v[66:67], v[66:67], v[88:89] op_sel:[0,0] op_sel_hi:[1,0]\n"
    "v_pk_mul_f32 v[70:71], v[70:71], v[88:89] op_sel:[0,1] op_sel_hi:[1,1]\nv_cvt_pk_bf16_f32 v80, v64, v65\nv_cvt_pk_bf16_f32 v82, v68, v69\nv_cvt_pk_bf16_f32 v81, v66, v67\n"
    "v_cvt_pk_bf16_f32 v83, v70, v71\nv_permlane16_swap_b32 v80, v82\ns_nop 0\nv_permlane16_swap_b32 v81, v83\nglobal_store_dwordx4 v9, v[80:83], s[32:33] sc1\n"
    "s_cmp_eq_u32 s6, 0\ns_cbranch_scc1 .LL79\ns_lshl_b32 s15, s39, 15\ns_add_u32 s14, s15, 0x801000\nv_lshl_add_u32 v182, v0, 4, s14\ns_add_u32 s16, s15, 0x803000\n"
    "v_lshl_add_u32 v183, v0, 4, s16\ns_add_u32 s17, s15, 0x805000\nv_lshl_add_u32 v184, v0, 4, s17\ns_add_u32 s18, s15, 0x807000\nv_lshl_add_u32 v185, v0, 4, s18\n"
    "s_mov_b32 s19, 0x1f600\nv_lshl_add_u32 v186, v0, 4, s19\nv_and_b32 v100, 63, v0\ns_lshl_b32 s22, s41, 13\ns_lshl_b32 s23, s3, 11\ns_add_u32 s21, s22, s23\n"
    "s_add_u32 s20, s21, 0x1200000\nv_lshl_add_u32 v200, v100, 4, s20\nv_mov_b32 v201, v200\ns_mov_b32 s24, 0x1004000\nv_lshl_add_u32 v202, v100, 4, s24\nv_mov_b32 v196, -1\n"
    "v_mov_b32 v197, -1\nv_mov_b32 v198, -1\nv_mov_b32 v199, -1\ns_barrier\ns_mov_b32 s14, 0x1d550\nv_lshl_add_u32 v14, v0, 3, s14\nv_lshrrev_b32 v48, 3, v0\n"
    "v_lshl_add_u32 v14, v48, 4, v14\ns_mov_b32 s15, 0x20300\nv_lshl_add_u32 v15, v48, 7, s15\nv_xor_b32 v50, v0, v48\nv_and_b32 v49, 7, v50\nv_lshl_add_u32 v15, v49, 4, v15\n"
    "s_mov_b32 s16, 0x17800\nv_lshl_add_u32 v16, v0, 4, s16\ns_lshl_b32 s18, s41, 7\ns_add_u32 s17, s18, 0x600000\nv_lshl_add_u32 v51, v48, 15, s17\nv_and_b32 v52, 1, v0\n"
    "v_lshl_add_u32 v51, v52, 6, v51\nv_bfe_u32 v53, v0, 1, 2\nv_lshl_add_u32 v51, v53, 4, v51\nv_lshrrev_b32 v55, 4, v0\ns_lshl_b32 s21, s35, 18\ns_lshl_b32 s22, s36, 8\n"
    "s_add_u32 s20, s21, s22\ns_add_u32 s19, s20, 0x1200000\nv_lshl_add_u32 v54, v55, 13, s19\nv_and_b32 v56, 15, v0\nv_lshl_add_u32 v54, v56, 4, v54\ns_cmp_eq_u32 s6, 0\n"
    "s_cselect_b64 vcc, -1, 0\nv_cndmask_b32 v17, v54, v51, vcc\nv_bfe_u32 v59, v0, 5, 1\nv_and_b32 v61, 31, v0\nv_sub_u32 v60, 0x3ff, v61\nv_lshl_add_u32 v58, v59, 3, v60\n"
    "v_lshrrev_b32 v57, 2, v58\ns_lshr_b32 s25, s3, 1\ns_mul_i32 s24, s25, 0x2100\ns_add_u32 s23, s24, 0x17800\nv_lshl_add_u32 v19, v57, 3, s23\nv_and_b32 v62, 3, v58\n"
    "s_mov_b32 s44, 0x840\nv_mad_u32_u24 v19, v62, s44, v19\ns_lshl2_add_u32 s45, s3, 0x27f80\nv_mov_b32 v12, s45\ns_add_u32 s46, s6, 12\nv_mov_b32 v13, s46\n"
    "v_and_b32 v63, 0xff, v0\nv_mov_b32 v65, 0x1d000\nv_mov_b32 v67, 0x1fdb0\nv_mov_b32 v69, 0x20b10\nv_mov_b32 v70, 0x21870\nv_cmp_gt_u32 vcc, 0xa1, v63\ns_nop 1\n"
    "v_cndmask_b32 v68, v70, v69, vcc\nv_cmp_gt_u32 vcc, 0x7b, v63\ns_nop 1\nv_cndmask_b32 v66, v68, v67, vcc\nv_cmp_gt_u32 vcc, 0x55, v63\ns_nop 1\n"
    "v_cndmask_b32 v64, v66, v65, vcc\nv_lshl_add_u32 v18, v63, 4, v64\nv_mov_b32 v72, 0x1950\nv_mov_b32 v73, 0x3180\nv_cmp_gt_u32 vcc, 0x55, v63\ns_nop 1\n"
    "v_cndmask_b32 v71, v73, v72, vcc\nv_add_u32 v1, v18, v71\ns_mul_i32 s49, s25, 0x1950\ns_and_b32 s51, s3, 1\ns_mul_i32 s50, s51, 0xa00\ns_add_u32 s48, s49, s50\n"
    "s_add_u32 s47, s48, 0x1d000\nv_lshl_add_u32 v2, v59, 4, s47\ns_mov_b32 s52, 0x50\nv_mad_u32_u24 v2, v61, s52, v2\ns_mul_i32 s55, s25, 0x3180\ns_lshl_b32 s56, s51, 12\n"
    "s_add_u32 s54, s55, s56\ns_add_u32 s53, s54, 0x20300\nv_lshl_add_u32 v3, v61, 7, s53\nv_and_b32 v75, 7, v0\nv_xor_b32 v74, v59, v75\nv_lshl_add_u32 v3, v74, 4, v3\n"
    "s_lshl3_add_u32 s57, s25, 0x27fc0\nv_lshl_add_u32 v7, v52, 2, s57\ns_add_u32 s14, s7, 0x401000\nv_lshl_add_u32 v20, v0, 4, s14\ns_lshl_b32 s17, s41, 13\n"
    "s_add_u32 s16, s7, s17\ns_add_u32 s15, s16, 0x200000\nv_lshl_add_u32 v21, v0, 4, s15\nv_add_u32 v22, 8192, v20\nv_add_u32 v23, 16384, v20\nv_add_u32 v24, 24576, v20\n"
    "v_add_u32 v25, 32768, v20\nv_add_u32 v26, 40960, v20\nv_add_u32 v27, 49152, v20\nv_add_u32 v28, 57344, v20\nv_and_b32 v56, 63, v0\ns_lshl_b32 s16, s41, 13\n"
    "s_lshl_b32 s17, s3, 11\ns_add_u32 s15, s16, s17\ns_add_u32 s14, s15, 0x800000\nv_lshl_add_u32 v4, v56, 4, s14\ns_add_u32 s18, s15, 0xa00000\n"
    "v_lshl_add_u32 v5, v56, 4, s18\nv_and_b32 v57, 3, v0\ns_lshl_b32 s21, s2, 8\ns_lshl_b32 s22, s3, 6\ns_add_u32 s20, s21, s22\ns_add_u32 s19, s20, 0xc00000\n"
    "v_lshl_add_u32 v6, v57, 4, s19\nv_and_b32 v58, 0x7f, v0\ns_lshr_b32 s25, s3, 1\ns_mul_i32 s24, s25, 0x2100\ns_add_u32 s23, s24, 0x18000\nv_lshl_add_u32 v47, v58, 4, s23\n"
    "v_and_b32 v59, 15, v0\ns_mov_b32 s44, 0x27600\nv_lshl_add_u32 v29, v59, 4, s44\nv_bfe_u32 v60, v0, 3, 1\ns_lshl_b32 s48, s40, 14\ns_add_u32 s47, s7, s48\n"
    "s_lshl_b32 s49, s39, 7\ns_add_u32 s46, s47, s49\ns_add_u32 s45, s46, 0x400000\nv_lshl_add_u32 v30, v60, 13, s45\nv_and_b32 v61, 7, v0\nv_lshl_add_u32 v30, v61, 4, v30\n"
    "v_mov_b32 v8, -1\nv_mov_b32 v9, -1\nv_mov_b32 v10, -1\nv_mov_b32 v11, -1\ns_sleep 6\nglobal_load_dwordx4 v[208:211], v182, s[32:33] offset:-4096 sc1\n"
    "global_load_dwordx4 v[212:215], v182, s[32:33] offset:0 sc1\nglobal_load_dwordx4 v[216:219], v183, s[32:33] offset:-4096 sc1\n"
    "global_load_dwordx4 v[220:223], v183, s[32:33] offset:0 sc1\nglobal_load_dwordx4 v[224:227], v184, s[32:33] offset:-4096 sc1\n"
    "global_load_dwordx4 v[228:231], v184, s[32:33] offset:0 sc1\nglobal_load_dwordx4 v[232:235], v185, s[32:33] offset:-4096 sc1\n"
    "global_load_dwordx4 v[236:239], v185, s[32:33] offset:0 sc1\ns_branch .LL80\n.LL79:\nglobal_load_dwordx4 v[22:25], v187, s[32:33] sc1\ns_barrier\nds_read_b32 v242, v33\n"
    "ds_read_b32 v243, v33 offset:512\nds_read_b64 v[240:241], v31\nds_read_b64 v[244:245], v31 offset:1024\nds_read_b128 v[246:249], v32\n"
    "ds_read_b64 v[250:251], v32 offset:16\ns_bitcmp1_b32 s3, 0\ns_cselect_b32 s82, 0x43000000, 0\ns_mov_b32 s86, 0x41000000\ns_waitcnt lgkmcnt(0)\n"
    "v_lshlrev_b32 v112, 16, v242\nv_and_b32 v113, 0xffff0000, v242\nv_lshlrev_b32 v114, 16, v243\nv_and_b32 v115, 0xffff0000, v243\nv_mul_f32 v112, s93, v112\n"
    "v_mul_f32 v113, s93, v113\nv_mul_f32 v114, s94, v114\nv_mul_f32 v115, s94, v115\nv_mul_f32 v132, s82, v112\nv_mul_f32 v133, s82, v113\nv_mul_f32 v134, s86, v112\n"
    "v_mul_f32 v135, s86, v113\nv_mul_f32 v117, s82, v114\nv_mul_f32 v119, s82, v115\nv_mul_f32 v121, s86, v114\nv_mul_f32 v123, s86, v115\nv_exp_f32 v132, v132\n"
    "v_exp_f32 v133, v133\nv_exp_f32 v134, v134\nv_exp_f32 v135, v135\nv_fract_f32 v117, v117\nv_fract_f32 v119, v119\nv_fract_f32 v121, v121\nv_fract_f32 v123, v123\n"
    "v_cos_f32 v116, v117\nv_cos_f32 v118, v119\nv_cos_f32 v120, v121\nv_cos_f32 v122, v123\nv_sin_f32 v117, v117\nv_sin_f32 v119, v119\nv_sin_f32 v121, v121\n"
    "v_sin_f32 v123, v123\nv_mul_f32 v116, v116, v132\nv_mul_f32 v118, v118, v133\nv_mul_f32 v120, v120, v134\nv_mul_f32 v122, v122, v135\nv_mul_f32 v117, v117, v132\n"
    "v_mul_f32 v119, v119, v133\nv_mul_f32 v121, v121, v134\nv_mul_f32 v123, v123, v135\ns_lshr_b32 s83, s3, 1\ns_mul_i32 s83, s83, 0x6000600\ns_mov_b32 s84, 0xffff\n"
    "v_add_u32 v240, s83, v240\nv_add_u32 v241, s83, v241\nv_add_u32 v244, s83, v244\nv_add_u32 v245, s83, v245\nv_add_u32 v246, s83, v246\nv_add_u32 v247, s83, v247\n"
    "v_add_u32 v248, s83, v248\nv_add_u32 v249, s83, v249\nv_add_u32 v250, s83, v250\nv_add_u32 v251, s83, v251\nv_cvt_pk_f16_f32 v144, v116, v117\n"
    "v_cvt_pk_f16_f32 v145, v118, v119\nv_pk_mul_f32 v[124:125], v[116:117], v[120:121] op_sel_hi:[0,1]\nv_pk_mul_f32 v[126:127], v[118:119], v[122:123] op_sel_hi:[0,1]\n"
    "v_pk_fma_f32 v[116:117], v[116:117], v[120:121], v[124:125] op_sel:[1,1,0] op_sel_hi:[1,0,1] neg_lo:[0,1,0]\n"
    "v_pk_fma_f32 v[118:119], v[118:119], v[122:123], v[126:127] op_sel:[1,1,0] op_sel_hi:[1,0,1] neg_lo:[0,1,0]\nv_cvt_pk_f16_f32 v146, v116, v117\n"
    "v_cvt_pk_f16_f32 v147, v118, v119\nv_pk_mul_f32 v[124:125], v[116:117], v[120:121] op_sel_hi:[0,1]\nv_pk_mul_f32 v[126:127], v[118:119], v[122:123] op_sel_hi:[0,1]\n"
    "v_pk_fma_f32 v[116:117], v[116:117], v[120:121], v[124:125] op_sel:[1,1,0] op_sel_hi:[1,0,1] neg_lo:[0,1,0]\n"
    "v_pk_fma_f32 v[118:119], v[118:119], v[122:123], v[126:127] op_sel:[1,1,0] op_sel_hi:[1,0,1] neg_lo:[0,1,0]\nv_cvt_pk_f16_f32 v148, v116, v117\n"
    "v_cvt_pk_f16_f32 v149, v118, v119\nv_pk_mul_f32 v[124:125], v[116:117], v[120:121] op_sel_hi:[0,1]\nv_pk_mul_f32 v[126:127], v[118:119], v[122:123] op_sel_hi:[0,1]\n"
    "v_pk_fma_f32 v[116:117], v[116:117], v[120:121], v[124:125] op_sel:[1,1,0] op_sel_hi:[1,0,1] neg_lo:[0,1,0]\n"
    "v_pk_fma_f32 v[118:119], v[118:119], v[122:123], v[126:127] op_sel:[1,1,0] op_sel_hi:[1,0,1] neg_lo:[0,1,0]\nv_cvt_pk_f16_f32 v150, v116, v117\n"
    "v_cvt_pk_f16_f32 v151, v118, v119\nv_pk_mul_f32 v[124:125], v[116:117], v[120:121] op_sel_hi:[0,1]\nv_pk_mul_f32 v[126:127], v[118:119], v[122:123] op_sel_hi:[0,1]\n"
    "v_pk_fma_f32 v[116:117], v[116:117], v[120:121], v[124:125] op_sel:[1,1,0] op_sel_hi:[1,0,1] neg_lo:[0,1,0]\n"
    "v_pk_fma_f32 v[118:119], v[118:119], v[122:123], v[126:127] op_sel:[1,1,0] op_sel_hi:[1,0,1] neg_lo:[0,1,0]\nv_cvt_pk_f16_f32 v152, v116, v117\n"
    "v_cvt_pk_f16_f32 v153, v118, v119\nv_pk_mul_f32 v[124:125], v[116:117], v[120:121] op_sel_hi:[0,1]\nv_pk_mul_f32 v[126:127], v[118:119], v[122:123] op_sel_hi:[0,1]\n"
    "v_pk_fma_f32 v[116:117], v[116:117], v[120:121], v[124:125] op_sel:[1,1,0] op_sel_hi:[1,0,1] neg_lo:[0,1,0]\n"
    "v_pk_fma_f32 v[118:119], v[118:119], v[122:123], v[126:127] op_sel:[1,1,0] op_sel_hi:[1,0,1] neg_lo:[0,1,0]\nv_cvt_pk_f16_f32 v154, v116, v117\n"
    "v_cvt_pk_f16_f32 v155, v118, v119\nv_pk_mul_f32 v[124:125], v[116:117], v[120:121] op_sel_hi:[0,1]\nv_pk_mul_f32 v[126:127], v[118:119], v[122:123] op_sel_hi:[0,1]\n"
    "v_pk_fma_f32 v[116:117], v[116:117], v[120:121], v[124:125] op_sel:[1,1,0] op_sel_hi:[1,0,1] neg_lo:[0,1,0]\n"
    "v_pk_fma_f32 v[118:119], v[118:119], v[122:123], v[126:127] op_sel:[1,1,0] op_sel_hi:[1,0,1] neg_lo:[0,1,0]\nv_cvt_pk_f16_f32 v156, v116, v117\n"
    "v_cvt_pk_f16_f32 v157, v118, v119\nv_pk_mul_f32 v[124:125], v[116:117], v[120:121] op_sel_hi:[0,1]\nv_pk_mul_f32 v[126:127], v[118:119], v[122:123] op_sel_hi:[0,1]\n"
    "v_pk_fma_f32 v[116:117], v[116:117], v[120:121], v[124:125] op_sel:[1,1,0] op_sel_hi:[1,0,1] neg_lo:[0,1,0]\n"
    "v_pk_fma_f32 v[118:119], v[118:119], v[122:123], v[126:127] op_sel:[1,1,0] op_sel_hi:[1,0,1] neg_lo:[0,1,0]\nv_cvt_pk_f16_f32 v158, v116, v117\n"
    "v_cvt_pk_f16_f32 v159, v118, v119\nv_pk_mul_f32 v[124:125], v[116:117], v[120:121] op_sel_hi:[0,1]\nv_pk_mul_f32 v[126:127], v[118:119], v[122:123] op_sel_hi:[0,1]\n"
    "v_pk_fma_f32 v[116:117], v[116:117], v[120:121], v[124:125] op_sel:[1,1,0] op_sel_hi:[1,0,1] neg_lo:[0,1,0]\n"
    "v_pk_fma_f32 v[118:119], v[118:119], v[122:123], v[126:127] op_sel:[1,1,0] op_sel_hi:[1,0,1] neg_lo:[0,1,0]\nv_cvt_pk_f16_f32 v160, v116, v117\n"
    "v_cvt_pk_f16_f32 v161, v118, v119\nv_pk_mul_f32 v[124:125], v[116:117], v[120:121] op_sel_hi:[0,1]\nv_pk_mul_f32 v[126:127], v[118:119], v[122:123] op_sel_hi:[0,1]\n"
    "v_pk_fma_f32 v[116:117], v[116:117], v[120:121], v[124:125] op_sel:[1,1,0] op_sel_hi:[1,0,1] neg_lo:[0,1,0]\n"
    "v_pk_fma_f32 v[118:119], v[118:119], v[122:123], v[126:127] op_sel:[1,1,0] op_sel_hi:[1,0,1] neg_lo:[0,1,0]\nv_cvt_pk_f16_f32 v162, v116, v117\n"
    "v_cvt_pk_f16_f32 v163, v118, v119\nv_pk_mul_f32 v[124:125], v[116:117], v[120:121] op_sel_hi:[0,1]\nv_pk_mul_f32 v[126:127], v[118:119], v[122:123] op_sel_hi:[0,1]\n"
    "v_pk_fma_f32 v[116:117], v[116:117], v[120:121], v[124:125] op_sel:[1,1,0] op_sel_hi:[1,0,1] neg_lo:[0,1,0]\n"
    "v_pk_fma_f32 v[118:119], v[118:119], v[122:123], v[126:127] op_sel:[1,1,0] op_sel_hi:[1,0,1] neg_lo:[0,1,0]\nv_cvt_pk_f16_f32 v164, v116, v117\n"
    "v_cvt_pk_f16_f32 v165, v118, v119\nv_pk_mul_f32 v[124:125], v[116:117], v[120:121] op_sel_hi:[0,1]\nv_pk_mul_f32 v[126:127], v[118:119], v[122:123] op_sel_hi:[0,1]\n"
    "v_pk_fma_f32 v[116:117], v[116:117], v[120:121], v[124:125] op_sel:[1,1,0] op_sel_hi:[1,0,1] neg_lo:[0,1,0]\n"
    "v_pk_fma_f32 v[118:119], v[118:119], v[122:123], v[126:127] op_sel:[1,1,0] op_sel_hi:[1,0,1] neg_lo:[0,1,0]\nv_cvt_pk_f16_f32 v166, v116, v117\n"
    "v_cvt_pk_f16_f32 v167, v118, v119\nv_pk_mul_f32 v[124:125], v[116:117], v[120:121] op_sel_hi:[0,1]\nv_pk_mul_f32 v[126:127], v[118:119], v[122:123] op_sel_hi:[0,1]\n"
    "v_pk_fma_f32 v[116:117], v[116:117], v[120:121], v[124:125] op_sel:[1,1,0] op_sel_hi:[1,0,1] neg_lo:[0,1,0]\n"
    "v_pk_fma_f32 v[118:119], v[118:119], v[122:123], v[126:127] op_sel:[1,1,0] op_sel_hi:[1,0,1] neg_lo:[0,1,0]\nv_cvt_pk_f16_f32 v168, v116, v117\n"
    "v_cvt_pk_f16_f32 v169, v118, v119\nv_pk_mul_f32 v[124:125], v[116:117], v[120:121] op_sel_hi:[0,1]\nv_pk_mul_f32 v[126:127], v[118:119], v[122:123] op_sel_hi:[0,1]\n"
    "v_pk_fma_f32 v[116:117], v[116:117], v[120:121], v[124:125] op_sel:[1,1,0] op_sel_hi:[1,0,1] neg_lo:[0,1,0]\n"
    "v_pk_fma_f32 v[118:119], v[118:119], v[122:123], v[126:127] op_sel:[1,1,0] op_sel_hi:[1,0,1] neg_lo:[0,1,0]\nv_cvt_pk_f16_f32 v170, v116, v117\n"
    "v_cvt_pk_f16_f32 v171, v118, v119\nv_pk_mul_f32 v[124:125], v[116:117], v[120:121] op_sel_hi:[0,1]\nv_pk_mul_f32 v[126:127], v[118:119], v[122:123] op_sel_hi:[0,1]\n"
    "v_pk_fma_f32 v[116:117], v[116:117], v[120:121], v[124:125] op_sel:[1,1,0] op_sel_hi:[1,0,1] neg_lo:[0,1,0]\n"
    "v_pk_fma_f32 v[118:119], v[118:119], v[122:123], v[126:127] op_sel:[1,1,0] op_sel_hi:[1,0,1] neg_lo:[0,1,0]\nv_cvt_pk_f16_f32 v172, v116, v117\n"
    "v_cvt_pk_f16_f32 v173, v118, v119\nv_pk_mul_f32 v[124:125], v[116:117], v[120:121] op_sel_hi:[0,1]\nv_pk_mul_f32 v[126:127], v[118:119], v[122:123] op_sel_hi:[0,1]\n"
    "v_pk_fma_f32 v[116:117], v[116:117], v[120:121], v[124:125] op_sel:[1,1,0] op_sel_hi:[1,0,1] neg_lo:[0,1,0]\n"
    "v_pk_fma_f32 v[118:119], v[118:119], v[122:123], v[126:127] op_sel:[1,1,0] op_sel_hi:[1,0,1] neg_lo:[0,1,0]\nv_cvt_pk_f16_f32 v174, v116, v117\n"
    "v_cvt_pk_f16_f32 v175, v118, v119\ns_waitcnt vmcnt(0)\ns_mov_b32 s80, 0x10000\n.Ltp81:\nv_xor_b32 v22, s34, v22\nv_xor_b32 v23, s34, v23\nv_xor_b32 v24, s34, v24\n"
    "v_xor_b32 v25, s34, v25\nv_or3_b32 v22, v22, v23, v24\nv_or_b32 v22, v22, v25\nv_cmp_ne_u32 vcc, 0, v22\ns_cbranch_vccz .Ltq82\ns_sub_u32 s80, s80, 1\n"
    "s_cbranch_scc1 .Ltq82\ns_sleep 2\nglobal_load_dwordx4 v[22:25], v187, s[32:33] sc1\ns_waitcnt vmcnt(0)\ns_branch .Ltp81\n.Ltq82:\ns_sleep 4\n"
    "global_load_dwordx4 v[208:211], v182, s[32:33] offset:-4096 sc1\nglobal_load_dwordx4 v[212:215], v182, s[32:33] offset:0 sc1\n"
    "global_load_dwordx4 v[216:219], v183, s[32:33] offset:-4096 sc1\nglobal_load_dwordx4 v[220:223], v183, s[32:33] offset:0 sc1\n"
    "global_load_dwordx4 v[224:227], v184, s[32:33] offset:-4096 sc1\nglobal_load_dwordx4 v[228:231], v184, s[32:33] offset:0 sc1\n"
    "global_load_dwordx4 v[232:235], v185, s[32:33] offset:-4096 sc1\nglobal_load_dwordx4 v[236:239], v185, s[32:33] offset:0 sc1\ns_mov_b32 s14, 0x1d550\n"
    "v_lshl_add_u32 v14, v0, 3, s14\nv_lshrrev_b32 v48, 3, v0\nv_lshl_add_u32 v14, v48, 4, v14\ns_mov_b32 s15, 0x20300\nv_lshl_add_u32 v15, v48, 7, s15\nv_xor_b32 v50, v0, v48\n"
    "v_and_b32 v49, 7, v50\nv_lshl_add_u32 v15, v49, 4, v15\ns_mov_b32 s16, 0x17800\nv_lshl_add_u32 v16, v0, 4, s16\ns_lshl_b32 s18, s41, 7\ns_add_u32 s17, s18, 0x600000\n"
    "v_lshl_add_u32 v51, v48, 15, s17\nv_and_b32 v52, 1, v0\nv_lshl_add_u32 v51, v52, 6, v51\nv_bfe_u32 v53, v0, 1, 2\nv_lshl_add_u32 v51, v53, 4, v51\n"
    "v_lshrrev_b32 v55, 4, v0\ns_lshl_b32 s21, s35, 18\ns_lshl_b32 s22, s36, 8\ns_add_u32 s20, s21, s22\ns_add_u32 s19, s20, 0x1200000\nv_lshl_add_u32 v54, v55, 13, s19\n"
    "v_and_b32 v56, 15, v0\nv_lshl_add_u32 v54, v56, 4, v54\ns_cmp_eq_u32 s6, 0\ns_cselect_b64 vcc, -1, 0\nv_cndmask_b32 v17, v54, v51, vcc\nv_bfe_u32 v59, v0, 5, 1\n"
    "v_and_b32 v61, 31, v0\nv_sub_u32 v60, 0x3ff, v61\nv_lshl_add_u32 v58, v59, 3, v60\nv_lshrrev_b32 v57, 2, v58\ns_lshr_b32 s25, s3, 1\ns_mul_i32 s24, s25, 0x2100\n"
    "s_add_u32 s23, s24, 0x17800\nv_lshl_add_u32 v19, v57, 3, s23\nv_and_b32 v62, 3, v58\ns_mov_b32 s44, 0x840\nv_mad_u32_u24 v19, v62, s44, v19\n"
    "s_lshl2_add_u32 s45, s3, 0x27f80\nv_mov_b32 v12, s45\ns_add_u32 s46, s6, 12\nv_mov_b32 v13, s46\nv_and_b32 v63, 0xff, v0\nv_mov_b32 v65, 0x1d000\nv_mov_b32 v67, 0x1fdb0\n"
    "v_mov_b32 v69, 0x20b10\nv_mov_b32 v70, 0x21870\nv_cmp_gt_u32 vcc, 0xa1, v63\ns_nop 1\nv_cndmask_b32 v68, v70, v69, vcc\nv_cmp_gt_u32 vcc, 0x7b, v63\ns_nop 1\n"
    "v_cndmask_b32 v66, v68, v67, vcc\nv_cmp_gt_u32 vcc, 0x55, v63\ns_nop 1\nv_cndmask_b32 v64, v66, v65, vcc\nv_lshl_add_u32 v18, v63, 4, v64\nv_mov_b32 v72, 0x1950\n"
    "v_mov_b32 v73, 0x3180\nv_cmp_gt_u32 vcc, 0x55, v63\ns_nop 1\nv_cndmask_b32 v71, v73, v72, vcc\nv_add_u32 v1, v18, v71\ns_mul_i32 s49, s25, 0x1950\ns_and_b32 s51, s3, 1\n"
    "s_mul_i32 s50, s51, 0xa00\ns_add_u32 s48, s49, s50\ns_add_u32 s47, s48, 0x1d000\nv_lshl_add_u32 v2, v59, 4, s47\ns_mov_b32 s52, 0x50\nv_mad_u32_u24 v2, v61, s52, v2\n"
    "s_mul_i32 s55, s25, 0x3180\ns_lshl_b32 s56, s51, 12\ns_add_u32 s54, s55, s56\ns_add_u32 s53, s54, 0x20300\nv_lshl_add_u32 v3, v61, 7, s53\nv_and_b32 v75, 7, v0\n"
    "v_xor_b32 v74, v59, v75\nv_lshl_add_u32 v3, v74, 4, v3\ns_lshl3_add_u32 s57, s25, 0x27fc0\nv_lshl_add_u32 v7, v52, 2, s57\ns_add_u32 s14, s7, 0x401000\n"
    "v_lshl_add_u32 v20, v0, 4, s14\ns_lshl_b32 s17, s41, 13\ns_add_u32 s16, s7, s17\ns_add_u32 s15, s16, 0x200000\nv_lshl_add_u32 v21, v0, 4, s15\nv_add_u32 v22, 8192, v20\n"
    "v_add_u32 v23, 16384, v20\nv_add_u32 v24, 24576, v20\nv_add_u32 v25, 32768, v20\nv_add_u32 v26, 40960, v20\nv_add_u32 v27, 49152, v20\nv_add_u32 v28, 57344, v20\n"
    "v_and_b32 v56, 63, v0\ns_lshl_b32 s16, s41, 13\ns_lshl_b32 s17, s3, 11\ns_add_u32 s15, s16, s17\ns_add_u32 s14, s15, 0x800000\nv_lshl_add_u32 v4, v56, 4, s14\n"
    "s_add_u32 s18, s15, 0xa00000\nv_lshl_add_u32 v5, v56, 4, s18\nv_and_b32 v57, 3, v0\ns_lshl_b32 s21, s2, 8\ns_lshl_b32 s22, s3, 6\ns_add_u32 s20, s21, s22\n"
    "s_add_u32 s19, s20, 0xc00000\nv_lshl_add_u32 v6, v57, 4, s19\nv_and_b32 v58, 0x7f, v0\ns_lshr_b32 s25, s3, 1\ns_mul_i32 s24, s25, 0x2100\ns_add_u32 s23, s24, 0x18000\n"
    "v_lshl_add_u32 v47, v58, 4, s23\nv_and_b32 v59, 15, v0\ns_mov_b32 s44, 0x27600\nv_lshl_add_u32 v29, v59, 4, s44\nv_bfe_u32 v60, v0, 3, 1\ns_lshl_b32 s48, s40, 14\n"
    "s_add_u32 s47, s7, s48\ns_lshl_b32 s49, s39, 7\ns_add_u32 s46, s47, s49\ns_add_u32 s45, s46, 0x400000\nv_lshl_add_u32 v30, v60, 13, s45\nv_and_b32 v61, 7, v0\n"
    "v_lshl_add_u32 v30, v61, 4, v30\nv_mov_b32 v8, -1\nv_mov_b32 v9, -1\nv_mov_b32 v10, -1\nv_mov_b32 v11, -1\n.LL80:\ns_mov_b32 s80, 0x10000\ns_setprio 0\n.LL85:\n"
    "s_waitcnt vmcnt(7)\nds_write_b128 v186, v[208:211] offset:24576\nv_max3_u32 v31, v208, v209, 0\nv_max3_u32 v31, v210, v211, v31\ns_waitcnt vmcnt(6)\n"
    "ds_write_b128 v186, v[212:215] offset:28672\nv_max3_u32 v31, v212, v213, v31\nv_max3_u32 v31, v214, v215, v31\nv_cmp_eq_u32 vcc, -1, v31\ns_cbranch_vccnz .LL87\n.LL86:\n"
    "s_waitcnt lgkmcnt(0)\ns_barrier\nglobal_load_dwordx4 v[112:115], v202, s[32:33] sc1\nglobal_store_dwordx4 v200, v[196:199], s[32:33] offset:0 sc1\n"
    "global_store_dwordx4 v200, v[196:199], s[32:33] offset:1024 sc1\ns_mov_b64 exec, 0xffff\nglobal_store_dwordx4 v201, v[196:199], s[32:33] sc1\ns_mov_b64 exec, -1\n.LL96:\n"
    "s_waitcnt vmcnt(9)\nds_write_b128 v186, v[216:219] offset:0\nv_max3_u32 v31, v216, v217, 0\nv_max3_u32 v31, v218, v219, v31\ns_waitcnt vmcnt(8)\n"
    "ds_write_b128 v186, v[220:223] offset:4096\nv_max3_u32 v31, v220, v221, v31\nv_max3_u32 v31, v222, v223, v31\nv_cmp_eq_u32 vcc, -1, v31\ns_cbranch_vccnz .LL98\n.LL97:\n"
    "s_waitcnt lgkmcnt(0)\ns_barrier\n.LL105:\ns_waitcnt vmcnt(7)\nds_write_b128 v186, v[224:227] offset:8192\nv_max3_u32 v31, v224, v225, 0\nv_max3_u32 v31, v226, v227, v31\n"
    "s_waitcnt vmcnt(6)\nds_write_b128 v186, v[228:231] offset:12288\nv_max3_u32 v31, v228, v229, v31\nv_max3_u32 v31, v230, v231, v31\nv_cmp_eq_u32 vcc, -1, v31\n"
    "s_cbranch_vccnz .LL107\n.LL106:\ns_waitcnt lgkmcnt(0)\ns_barrier\n.LL112:\ns_waitcnt vmcnt(5)\nds_write_b128 v186, v[232:235] offset:16384\nv_max3_u32 v31, v232, v233, 0\n"
    "v_max3_u32 v31, v234, v235, v31\ns_waitcnt vmcnt(4)\nds_write_b128 v186, v[236:239] offset:20480\nv_max3_u32 v31, v236, v237, v31\nv_max3_u32 v31, v238, v239, v31\n"
    "v_cmp_eq_u32 vcc, -1, v31\ns_cbranch_vccnz .LL114\n.LL113:\ns_waitcnt lgkmcnt(0)\ns_barrier\ns_setprio 0\ns_cmp_eq_u32 s6, 0\ns_cbranch_scc1 .LL117\n"
    "ds_write_b128 v47, v[40:43]\nv_alignbit_b32 v48, v41, v40, 16\nv_alignbit_b32 v49, v42, v41, 16\nv_alignbit_b32 v50, v43, v42, 16\nv_alignbit_b32 v51, v44, v43, 16\n"
    "ds_write_b128 v47, v[48:51] offset:2112\nv_mov_b32 v52, v41\nv_mov_b32 v53, v42\nv_mov_b32 v54, v43\nv_mov_b32 v55, v44\nds_write_b128 v47, v[52:55] offset:4224\n"
    "v_alignbit_b32 v56, v42, v41, 16\nv_alignbit_b32 v57, v43, v42, 16\nv_alignbit_b32 v58, v44, v43, 16\nv_alignbit_b32 v59, v45, v44, 16\n"
    "ds_write_b128 v47, v[56:59] offset:6336\ns_waitcnt vmcnt(0)\ns_branch .LL118\n.LL117:\nv_and_or_b32 v124, v240, s84, v191\nv_alignbit_b32 v125, 1, v240, 16\n"
    "v_and_or_b32 v126, v241, s84, v191\nv_alignbit_b32 v127, 1, v241, 16\nv_and_or_b32 v128, v244, s84, v191\nv_alignbit_b32 v129, 1, v244, 16\n"
    "v_and_or_b32 v130, v245, s84, v191\nv_alignbit_b32 v131, 1, v245, 16\nv_and_or_b32 v132, v246, s84, v191\nv_alignbit_b32 v133, 1, v246, 16\n"
    "v_and_or_b32 v134, v247, s84, v191\nv_alignbit_b32 v135, 1, v247, 16\nv_and_or_b32 v136, v248, s84, v191\nv_alignbit_b32 v137, 1, v248, 16\n"
    "v_and_or_b32 v138, v249, s84, v191\nv_alignbit_b32 v139, 1, v249, 16\nv_and_or_b32 v140, v250, s84, v191\nv_alignbit_b32 v141, 1, v250, 16\n"
    "v_and_or_b32 v142, v251, s84, v191\nv_alignbit_b32 v143, 1, v251, 16\ns_waitcnt vmcnt(0)\ns_mov_b32 s80, 0x10000\n.Ltp138:\nv_xor_b32 v112, s34, v112\n"
    "v_xor_b32 v113, s34, v113\nv_xor_b32 v114, s34, v114\nv_xor_b32 v115, s34, v115\nv_or3_b32 v112, v112, v113, v114\nv_or_b32 v112, v112, v115\nv_cmp_ne_u32 vcc, 0, v112\n"
    "s_cbranch_vccz .Ltq139\ns_sub_u32 s80, s80, 1\ns_cbranch_scc1 .Ltq139\ns_sleep 2\nglobal_load_dwordx4 v[112:115], v202, s[32:33] sc1\ns_waitcnt vmcnt(0)\ns_branch .Ltp138\n"
    ".Ltq139:\n.LL118:\ns_waitcnt lgkmcnt(0)\ns_barrier\ns_cmp_eq_u32 s3, 3\ns_cbranch_scc0 .LL136\nds_read_b128 v[208:211], v29 offset:0\n"
    "ds_read_b128 v[212:215], v29 offset:256\nds_read_b128 v[216:219], v29 offset:512\nds_read_b128 v[220:223], v29 offset:768\ns_waitcnt lgkmcnt(2)\n"
    "v_pk_add_f32 v[208:209], v[208:209], v[212:213]\nv_pk_add_f32 v[210:211], v[210:211], v[214:215]\ns_waitcnt lgkmcnt(0)\nv_pk_add_f32 v[216:217], v[216:217], v[220:221]\n"
    "v_pk_add_f32 v[218:219], v[218:219], v[222:223]\nv_pk_add_f32 v[208:209], v[208:209], v[216:217]\nv_pk_add_f32 v[210:211], v[210:211], v[218:219]\ns_mov_b64 exec, 0xffff\n"
    "global_store_dwordx4 v30, v[208:211], s[32:33] sc1\ns_mov_b64 exec, -1\n.LL136:\ns_cmp_eq_u32 s6, 0\ns_cbranch_scc0 .LL137\nds_write_b64 v188, v[144:145] offset:0\n"
    "ds_write_b64 v188, v[146:147] offset:544\nds_write_b64 v188, v[148:149] offset:1088\nds_write_b64 v188, v[150:151] offset:1632\nds_write_b64 v188, v[152:153] offset:2176\n"
    "ds_write_b64 v188, v[154:155] offset:2720\nds_write_b64 v188, v[156:157] offset:3264\nds_write_b64 v188, v[158:159] offset:3808\nds_write_b64 v188, v[160:161] offset:4352\n"
    "ds_write_b64 v188, v[162:163] offset:4896\nds_write_b64 v188, v[164:165] offset:5440\nds_write_b64 v188, v[166:167] offset:5984\nds_write_b64 v188, v[168:169] offset:6528\n"
    "ds_write_b64 v188, v[170:171] offset:7072\nds_write_b64 v188, v[172:173] offset:7616\nds_write_b64 v188, v[174:175] offset:8160\ns_waitcnt lgkmcnt(0)\ns_barrier\n"
    "ds_read_b128 v[48:51], v189 offset:0\nds_read_b128 v[52:55], v190 offset:0\nds_read_b128 v[56:59], v190 offset:8704\nds_read_b128 v[60:63], v189 offset:64\n"
    "ds_read_b128 v[64:67], v190 offset:64\nds_read_b128 v[68:71], v190 offset:8768\nds_read_b128 v[72:75], v189 offset:128\nds_read_b128 v[76:79], v190 offset:128\n"
    "ds_read_b128 v[80:83], v190 offset:8832\nds_read_b128 v[84:87], v189 offset:192\nds_read_b128 v[88:91], v190 offset:192\nds_read_b128 v[92:95], v190 offset:8896\n"
    "ds_read_b128 v[96:99], v189 offset:256\nds_read_b128 v[100:103], v190 offset:256\nds_read_b128 v[104:107], v190 offset:8960\ns_waitcnt lgkmcnt(12)\n"
    "v_mfma_f32_16x16x32_f16 v[108:111], v[48:51], v[52:55], 0\nv_mfma_f32_16x16x32_f16 v[112:115], v[48:51], v[56:59], 0\nds_read_b128 v[48:51], v189 offset:320\n"
    "ds_read_b128 v[52:55], v190 offset:320\nds_read_b128 v[56:59], v190 offset:9024\ns_waitcnt lgkmcnt(12)\nv_mfma_f32_16x16x32_f16 v[108:111], v[60:63], v[64:67], v[108:111]\n"
    "v_mfma_f32_16x16x32_f16 v[112:115], v[60:63], v[68:71], v[112:115]\nds_read_b128 v[60:63], v189 offset:384\nds_read_b128 v[64:67], v190 offset:384\n"
    "ds_read_b128 v[68:71], v190 offset:9088\ns_waitcnt lgkmcnt(12)\nv_mfma_f32_16x16x32_f16 v[108:111], v[72:75], v[76:79], v[108:111]\n"
    "v_mfma_f32_16x16x32_f16 v[112:115], v[72:75], v[80:83], v[112:115]\nds_read_b128 v[72:75], v189 offset:448\nds_read_b128 v[76:79], v190 offset:448\n"
    "ds_read_b128 v[80:83], v190 offset:9152\ns_waitcnt lgkmcnt(12)\nv_mfma_f32_16x16x32_f16 v[108:111], v[84:87], v[88:91], v[108:111]\n"
    "v_mfma_f32_16x16x32_f16 v[112:115], v[84:87], v[92:95], v[112:115]\ns_waitcnt lgkmcnt(9)\nv_mfma_f32_16x16x32_f16 v[108:111], v[96:99], v[100:103], v[108:111]\n"
    "v_mfma_f32_16x16x32_f16 v[112:115], v[96:99], v[104:107], v[112:115]\ns_waitcnt lgkmcnt(6)\nv_mfma_f32_16x16x32_f16 v[108:111], v[48:51], v[52:55], v[108:111]\n"
    "v_mfma_f32_16x16x32_f16 v[112:115], v[48:51], v[56:59], v[112:115]\ns_waitcnt lgkmcnt(3)\nv_mfma_f32_16x16x32_f16 v[108:111], v[60:63], v[64:67], v[108:111]\n"
    "v_mfma_f32_16x16x32_f16 v[112:115], v[60:63], v[68:71], v[112:115]\ns_waitcnt lgkmcnt(0)\nv_mfma_f32_16x16x32_f16 v[108:111], v[72:75], v[76:79], v[108:111]\n"
    "v_mfma_f32_16x16x32_f16 v[112:115], v[72:75], v[80:83], v[112:115]\ns_nop 6\nv_cvt_pk_f16_f32 v48, v108, v109\nds_write_b16 v124, v48 offset:59392\n"
    "ds_write_b16_d16_hi v125, v48 offset:59392\nv_cvt_pk_f16_f32 v49, v110, v111\nds_write_b16 v126, v49 offset:59392\nds_write_b16_d16_hi v127, v49 offset:59392\n"
    "v_cvt_pk_f16_f32 v50, v112, v113\nds_write_b16 v128, v50 offset:59392\nds_write_b16_d16_hi v129, v50 offset:59392\nv_cvt_pk_f16_f32 v51, v114, v115\n"
    "ds_write_b16 v130, v51 offset:59392\nds_write_b16_d16_hi v131, v51 offset:59392\n.LL137:\ns_waitcnt lgkmcnt(0)\ns_barrier\ns_or_b32 s81, s2, s3\ns_or_b32 s81, s81, s6\n"
    "s_cbranch_scc1 .LL134\nv_mov_b32 v60, 16777216\nv_mov_b32 v61, s34\ns_mov_b64 exec, 1\nglobal_store_dword v60, v61, s[32:33] sc1\ns_mov_b64 exec, -1\n.LL134:\n"
    "global_load_dwordx4 v[240:243], v21, s[32:33] sc1\nglobal_load_dwordx4 v[208:211], v20, s[32:33] offset:-4096 sc1\n"
    "global_load_dwordx4 v[212:215], v22, s[32:33] offset:-4096 sc1\nglobal_load_dwordx4 v[216:219], v23, s[32:33] offset:-4096 sc1\n"
    "global_load_dwordx4 v[220:223], v24, s[32:33] offset:-4096 sc1\nglobal_load_dwordx4 v[224:227], v25, s[32:33] offset:-4096 sc1\n"
    "global_load_dwordx4 v[228:231], v26, s[32:33] offset:-4096 sc1\nglobal_load_dwordx4 v[232:235], v27, s[32:33] offset:-4096 sc1\n"
    "global_load_dwordx4 v[236:239], v28, s[32:33] offset:-4096 sc1\ns_cmp_eq_u32 s6, 0\ns_cbranch_scc0 .LL119\nv_mov_b32 v60, 163216\nds_read_b64 v[60:61], v60\n"
    "ds_read_u16 v64, v132 offset:59392\nds_read_u16 v65, v133 offset:59392\nds_read_u16 v66, v134 offset:59392\nds_read_u16 v67, v135 offset:59392\n"
    "ds_read_u16 v68, v136 offset:59392\nds_read_u16 v69, v137 offset:59392\nds_read_u16 v70, v138 offset:59392\nds_read_u16 v71, v139 offset:59392\n"
    "ds_read_u16 v72, v140 offset:59392\nds_read_u16 v73, v141 offset:59392\nds_read_u16 v74, v142 offset:59392\nds_read_u16 v75, v143 offset:59392\ns_waitcnt lgkmcnt(0)\n"
    "v_lshl_or_b32 v40, v65, 16, v64\nv_lshl_or_b32 v41, v67, 16, v66\nv_lshl_or_b32 v42, v69, 16, v68\nv_lshl_or_b32 v43, v71, 16, v70\nv_lshl_or_b32 v44, v73, 16, v72\n"
    "v_lshl_or_b32 v45, v75, 16, v74\nv_readfirstlane_b32 s88, v60\nv_readfirstlane_b32 s90, v61\nds_write_b128 v47, v[40:43]\nv_alignbit_b32 v48, v41, v40, 16\n"
    "v_alignbit_b32 v49, v42, v41, 16\nv_alignbit_b32 v50, v43, v42, 16\nv_alignbit_b32 v51, v44, v43, 16\nds_write_b128 v47, v[48:51] offset:2112\nv_mov_b32 v52, v41\n"
    "v_mov_b32 v53, v42\nv_mov_b32 v54, v43\nv_mov_b32 v55, v44\nds_write_b128 v47, v[52:55] offset:4224\nv_alignbit_b32 v56, v42, v41, 16\nv_alignbit_b32 v57, v43, v42, 16\n"
    "v_alignbit_b32 v58, v44, v43, 16\nv_alignbit_b32 v59, v45, v44, 16\nds_write_b128 v47, v[56:59] offset:6336\ns_waitcnt lgkmcnt(0)\ns_barrier\n.LL119:\n"
    "v_mov_b64 v[60:61], 0\nv_mov_b64 v[62:63], 0\ns_cmp_eq_u32 s3, 3\ns_cbranch_scc1 .LL121\ns_cmp_eq_u32 s3, 2\ns_cselect_b32 exec_hi, 3, -1\nds_write_b128 v18, v[60:63]\n"
    "ds_write_b128 v1, v[60:63]\ns_mov_b64 exec, -1\n.LL121:\nds_read_b64 v[64:65], v19 offset:2016\nds_read_b64 v[66:67], v19 offset:2024\n"
    "ds_read_b64 v[68:69], v19 offset:1984\nds_read_b64 v[70:71], v19 offset:1992\nds_read_b64 v[72:73], v19 offset:1952\nds_read_b64 v[74:75], v19 offset:1960\n"
    "ds_read_b64 v[76:77], v19 offset:1920\nds_read_b64 v[78:79], v19 offset:1928\nds_read_b64 v[80:81], v19 offset:1888\nds_read_b64 v[82:83], v19 offset:1896\n"
    "ds_read_b64 v[84:85], v19 offset:1856\nds_read_b64 v[86:87], v19 offset:1864\nds_read_b64 v[88:89], v19 offset:1824\nds_read_b64 v[90:91], v19 offset:1832\n"
    "ds_read_b64 v[92:93], v19 offset:1792\nds_read_b64 v[94:95], v19 offset:1800\nds_read_b64 v[96:97], v19 offset:1760\nds_read_b64 v[98:99], v19 offset:1768\n"
    "ds_read_b64 v[100:101], v19 offset:1728\nds_read_b64 v[102:103], v19 offset:1736\nds_read_b64 v[104:105], v19 offset:1696\nds_read_b64 v[106:107], v19 offset:1704\n"
    "ds_read_b64 v[108:109], v19 offset:1664\nds_read_b64 v[110:111], v19 offset:1672\nds_read_b64 v[112:113], v19 offset:1632\nds_read_b64 v[114:115], v19 offset:1640\n"
    "ds_read_b64 v[116:117], v19 offset:1600\nds_read_b64 v[118:119], v19 offset:1608\nds_read_b64 v[120:121], v19 offset:1568\nds_read_b64 v[122:123], v19 offset:1576\n"
    "ds_read_b64 v[124:125], v19 offset:1536\nds_read_b64 v[126:127], v19 offset:1544\nds_read_b64 v[128:129], v19 offset:1504\nds_read_b64 v[130:131], v19 offset:1512\n"
    "ds_read_b64 v[132:133], v19 offset:1472\nds_read_b64 v[134:135], v19 offset:1480\nds_read_b64 v[136:137], v19 offset:1440\nds_read_b64 v[138:139], v19 offset:1448\n"
    "ds_read_b64 v[140:141], v19 offset:1408\nds_read_b64 v[142:143], v19 offset:1416\nds_read_b64 v[144:145], v19 offset:1376\nds_read_b64 v[146:147], v19 offset:1384\n"
    "ds_read_b64 v[148:149], v19 offset:1344\nds_read_b64 v[150:151], v19 offset:1352\nds_read_b64 v[152:153], v19 offset:1312\nds_read_b64 v[154:155], v19 offset:1320\n"
    "ds_read_b64 v[156:157], v19 offset:1280\nds_read_b64 v[158:159], v19 offset:1288\nds_read_b64 v[160:161], v19 offset:1248\nds_read_b64 v[162:163], v19 offset:1256\n"
    "ds_read_b64 v[164:165], v19 offset:1216\nds_read_b64 v[166:167], v19 offset:1224\nds_read_b64 v[168:169], v19 offset:1184\nds_read_b64 v[170:171], v19 offset:1192\n"
    "ds_read_b64 v[172:173], v19 offset:1152\nds_read_b64 v[174:175], v19 offset:1160\nds_read_b64 v[176:177], v19 offset:1120\nds_read_b64 v[178:179], v19 offset:1128\n"
    "ds_read_b64 v[180:181], v19 offset:1088\nds_read_b64 v[182:183], v19 offset:1096\nds_read_b64 v[184:185], v19 offset:1056\nds_read_b64 v[186:187], v19 offset:1064\n"
    "ds_read_b64 v[188:189], v19 offset:1024\nds_read_b64 v[190:191], v19 offset:1032\nds_read_b64 v[192:193], v19 offset:992\nds_read_b64 v[194:195], v19 offset:1000\n"
    "ds_read_b64 v[196:197], v19 offset:960\nds_read_b64 v[198:199], v19 offset:968\nds_read_b64 v[200:201], v19 offset:928\nds_read_b64 v[202:203], v19 offset:936\n"
    "s_waitcnt lgkmcnt(0)\ns_mov_b32 s84, 0x3b000000\ns_mov_b32 s86, 0x3727c5ac\ns_mov_b32 s80, 0x10000\ns_waitcnt vmcnt(8)\n.Lln122:\nv_max3_u32 v60, v240, v241, v242\n"
    "v_max_u32 v60, v60, v243\nv_cmp_eq_u32 s[82:83], -1, v60\nv_lshlrev_b32 v32, 16, v240\nv_and_b32 v36, 0xffff0000, v240\nv_lshlrev_b32 v33, 16, v241\n"
    "v_and_b32 v37, 0xffff0000, v241\nv_lshlrev_b32 v34, 16, v242\nv_and_b32 v38, 0xffff0000, v242\nv_lshlrev_b32 v35, 16, v243\nv_and_b32 v39, 0xffff0000, v243\n"
    "s_waitcnt vmcnt(4)\nv_pk_add_f32 v[48:49], v[208:209], v[216:217]\nv_pk_add_f32 v[50:51], v[210:211], v[218:219]\nv_pk_add_f32 v[52:53], v[212:213], v[220:221]\n"
    "v_pk_add_f32 v[54:55], v[214:215], v[222:223]\ns_waitcnt vmcnt(1)\nv_pk_add_f32 v[56:57], v[224:225], v[232:233]\nv_pk_add_f32 v[58:59], v[226:227], v[234:235]\n"
    "v_pk_add_f32 v[48:49], v[48:49], v[56:57]\nv_pk_add_f32 v[50:51], v[50:51], v[58:59]\nv_pk_mul_f32 v[48:49], v[48:49], s[84:85] op_sel_hi:[1,0]\n"
    "v_pk_mul_f32 v[50:51], v[50:51], s[84:85] op_sel_hi:[1,0]\nv_pk_fma_f32 v[60:61], v[48:49], v[48:49], s[86:87] op_sel_hi:[1,1,0] neg_lo:[0,0,1] neg_hi:[0,0,1]\n"
    "v_pk_fma_f32 v[62:63], v[50:51], v[50:51], s[86:87] op_sel_hi:[1,1,0] neg_lo:[0,0,1] neg_hi:[0,0,1]\ns_waitcnt vmcnt(0)\nv_pk_add_f32 v[56:57], v[228:229], v[236:237]\n"
    "v_pk_add_f32 v[58:59], v[230:231], v[238:239]\nv_pk_add_f32 v[52:53], v[52:53], v[56:57]\nv_pk_add_f32 v[54:55], v[54:55], v[58:59]\n"
    "v_pk_fma_f32 v[52:53], v[52:53], s[84:85], v[60:61] op_sel_hi:[1,0,1] neg_lo:[0,0,1] neg_hi:[0,0,1]\n"
    "v_pk_fma_f32 v[54:55], v[54:55], s[84:85], v[62:63] op_sel_hi:[1,0,1] neg_lo:[0,0,1] neg_hi:[0,0,1]\nv_pk_add_f32 v[56:57], v[52:53], v[54:55]\nv_max_f32 v52, s86, v52\n"
    "v_max_f32 v53, s86, v53\nv_max_f32 v54, s86, v54\nv_max_f32 v55, s86, v55\nv_cmp_u_f32 vcc, v56, v57\nv_rsq_f32 v52, v52\nv_rsq_f32 v53, v53\nv_rsq_f32 v54, v54\n"
    "v_rsq_f32 v55, v55\ns_or_b64 vcc, vcc, s[82:83]\ns_cbranch_vccnz .Lls124\n.Llo123:\nv_pk_mul_f32 v[48:49], v[48:49], v[52:53]\nv_pk_mul_f32 v[50:51], v[50:51], v[54:55]\n"
    "v_pk_fma_f32 v[244:245], v[32:33], v[52:53], v[48:49] neg_lo:[0,0,1] neg_hi:[0,0,1]\nv_pk_fma_f32 v[246:247], v[34:35], v[54:55], v[50:51] neg_lo:[0,0,1] neg_hi:[0,0,1]\n"
    "v_pk_fma_f32 v[248:249], v[36:37], v[52:53], v[48:49] neg_lo:[0,0,1] neg_hi:[0,0,1]\nv_pk_fma_f32 v[250:251], v[38:39], v[54:55], v[50:51] neg_lo:[0,0,1] neg_hi:[0,0,1]\n"
    "v_cvt_pk_f16_f32 v56, v244, v245\nv_cvt_pk_f16_f32 v57, v246, v247\nds_write_b64 v14, v[56:57] offset:0\nv_cvt_pk_f16_f32 v58, v248, v249\n"
    "v_cvt_pk_f16_f32 v59, v250, v251\nds_write_b64 v14, v[58:59] offset:6480\ns_waitcnt lgkmcnt(0)\ns_barrier\nds_read_b128 v[220:223], v2 offset:1392\n"
    "ds_read_b128 v[224:227], v2 offset:1360\nds_read_b128 v[228:231], v2 offset:1312\nds_read_b128 v[232:235], v2 offset:1280\ns_cmp_eq_u32 s6, 0\ns_waitcnt lgkmcnt(3)\n"
    "v_mfma_f32_32x32x16_f16 v[204:219], v[64:67], v[220:223], 0\nds_read_b128 v[220:223], v2 offset:1232\ns_waitcnt lgkmcnt(3)\n"
    "v_mfma_f32_32x32x16_f16 v[204:219], v[68:71], v[224:227], v[204:219]\nds_read_b128 v[224:227], v2 offset:1200\ns_cbranch_scc0 .LL142\n"
    "global_store_dwordx4 v4, v[8:11], s[32:33] offset:0 sc1\n.LL142:\ns_waitcnt lgkmcnt(3)\nv_mfma_f32_32x32x16_f16 v[204:219], v[72:75], v[228:231], v[204:219]\n"
    "ds_read_b128 v[68:71], v2 offset:1152\ns_waitcnt lgkmcnt(3)\nv_mfma_f32_32x32x16_f16 v[204:219], v[76:79], v[232:235], v[204:219]\nds_read_b128 v[228:231], v2 offset:1120\n"
    "s_waitcnt lgkmcnt(3)\nv_mfma_f32_32x32x16_f16 v[204:219], v[80:83], v[220:223], v[204:219]\nds_read_b128 v[72:75], v2 offset:1072\ns_cbranch_scc0 .LL143\n"
    "global_store_dwordx4 v4, v[8:11], s[32:33] offset:1024 sc1\n.LL143:\ns_waitcnt lgkmcnt(3)\nv_mfma_f32_32x32x16_f16 v[204:219], v[84:87], v[224:227], v[204:219]\n"
    "ds_read_b128 v[232:235], v2 offset:1040\ns_waitcnt lgkmcnt(3)\nv_mfma_f32_32x32x16_f16 v[204:219], v[88:91], v[68:71], v[204:219]\nds_read_b128 v[76:79], v2 offset:992\n"
    "s_waitcnt lgkmcnt(3)\nv_mfma_f32_32x32x16_f16 v[204:219], v[92:95], v[228:231], v[204:219]\nds_read_b128 v[220:223], v2 offset:960\ns_cbranch_scc0 .LL144\n"
    "global_store_dwordx4 v5, v[8:11], s[32:33] offset:0 sc1\n.LL144:\ns_waitcnt lgkmcnt(3)\nv_mfma_f32_32x32x16_f16 v[204:219], v[96:99], v[72:75], v[204:219]\n"
    "ds_read_b128 v[80:83], v2 offset:912\ns_waitcnt lgkmcnt(3)\nv_mfma_f32_32x32x16_f16 v[204:219], v[100:103], v[232:235], v[204:219]\nds_read_b128 v[224:227], v2 offset:880\n"
    "s_waitcnt lgkmcnt(3)\nv_mfma_f32_32x32x16_f16 v[204:219], v[104:107], v[76:79], v[204:219]\nds_read_b128 v[84:87], v2 offset:832\ns_cbranch_scc0 .LL145\n"
    "global_store_dwordx4 v5, v[8:11], s[32:33] offset:1024 sc1\n.LL145:\ns_waitcnt lgkmcnt(3)\nv_mfma_f32_32x32x16_f16 v[204:219], v[108:111], v[220:223], v[204:219]\n"
    "ds_read_b128 v[68:71], v2 offset:800\ns_waitcnt lgkmcnt(3)\nv_mfma_f32_32x32x16_f16 v[204:219], v[112:115], v[80:83], v[204:219]\nds_read_b128 v[88:91], v2 offset:752\n"
    "s_waitcnt lgkmcnt(3)\nv_mfma_f32_32x32x16_f16 v[204:219], v[116:119], v[224:227], v[204:219]\nds_read_b128 v[228:231], v2 offset:720\ns_cbranch_scc0 .LL146\n"
    "s_mov_b64 exec, 15\nglobal_store_dwordx4 v6, v[8:11], s[32:33] sc1\ns_mov_b64 exec, -1\n.LL146:\ns_waitcnt lgkmcnt(3)\n"
    "v_mfma_f32_32x32x16_f16 v[204:219], v[120:123], v[84:87], v[204:219]\nds_read_b128 v[92:95], v2 offset:672\ns_waitcnt lgkmcnt(3)\n"
    "v_mfma_f32_32x32x16_f16 v[204:219], v[124:127], v[68:71], v[204:219]\nds_read_b128 v[72:75], v2 offset:640\ns_waitcnt lgkmcnt(3)\n"
    "v_mfma_f32_32x32x16_f16 v[204:219], v[128:131], v[88:91], v[204:219]\nds_read_b128 v[96:99], v2 offset:592\ns_waitcnt lgkmcnt(3)\n"
    "v_mfma_f32_32x32x16_f16 v[204:219], v[132:135], v[228:231], v[204:219]\nds_read_b128 v[232:235], v2 offset:560\ns_waitcnt lgkmcnt(3)\n"
    "v_mfma_f32_32x32x16_f16 v[204:219], v[136:139], v[92:95], v[204:219]\nds_read_b128 v[100:103], v2 offset:512\ns_waitcnt lgkmcnt(3)\n"
    "v_mfma_f32_32x32x16_f16 v[204:219], v[140:143], v[72:75], v[204:219]\nds_read_b128 v[76:79], v2 offset:480\ns_waitcnt lgkmcnt(3)\n"
    "v_mfma_f32_32x32x16_f16 v[204:219], v[144:147], v[96:99], v[204:219]\nds_read_b128 v[104:107], v2 offset:432\ns_waitcnt lgkmcnt(3)\n"
    "v_mfma_f32_32x32x16_f16 v[204:219], v[148:151], v[232:235], v[204:219]\nds_read_b128 v[220:223], v2 offset:400\ns_waitcnt lgkmcnt(3)\n"
    "v_mfma_f32_32x32x16_f16 v[204:219], v[152:155], v[100:103], v[204:219]\nds_read_b128 v[108:111], v2 offset:352\nv_fmac_f32 v32, s96, v244\nv_fmac_f32 v33, s96, v245\n"
    "v_fmac_f32 v34, s96, v246\ns_waitcnt lgkmcnt(3)\nv_mfma_f32_32x32x16_f16 v[204:219], v[156:159], v[76:79], v[204:219]\nds_read_b128 v[80:83], v2 offset:320\n"
    "v_fmac_f32 v35, s96, v247\nv_fmac_f32 v36, s97, v248\nv_fmac_f32 v37, s97, v249\ns_waitcnt lgkmcnt(3)\n"
    "v_mfma_f32_32x32x16_f16 v[204:219], v[160:163], v[104:107], v[204:219]\nds_read_b128 v[112:115], v2 offset:272\nv_fmac_f32 v38, s97, v250\nv_fmac_f32 v39, s97, v251\n"
    "v_xor_b32 v238, 32, v3\ns_waitcnt lgkmcnt(3)\nv_mfma_f32_32x32x16_f16 v[204:219], v[164:167], v[220:223], v[204:219]\nds_read_b128 v[224:227], v2 offset:240\n"
    "v_xor_b32 v239, 64, v3\nv_xor_b32 v240, 0x60, v3\ns_waitcnt lgkmcnt(3)\nv_mfma_f32_32x32x16_f16 v[204:219], v[168:171], v[108:111], v[204:219]\n"
    "ds_read_b128 v[116:119], v2 offset:192\ns_waitcnt lgkmcnt(3)\nv_mfma_f32_32x32x16_f16 v[204:219], v[172:175], v[80:83], v[204:219]\nds_read_b128 v[84:87], v2 offset:160\n"
    "s_waitcnt lgkmcnt(3)\nv_mfma_f32_32x32x16_f16 v[204:219], v[176:179], v[112:115], v[204:219]\nds_read_b128 v[120:123], v2 offset:112\ns_waitcnt lgkmcnt(3)\n"
    "v_mfma_f32_32x32x16_f16 v[204:219], v[180:183], v[224:227], v[204:219]\nds_read_b128 v[68:71], v2 offset:80\ns_waitcnt lgkmcnt(3)\n"
    "v_mfma_f32_32x32x16_f16 v[204:219], v[184:187], v[116:119], v[204:219]\nds_read_b128 v[124:127], v2 offset:32\ns_waitcnt lgkmcnt(3)\n"
    "v_mfma_f32_32x32x16_f16 v[204:219], v[188:191], v[84:87], v[204:219]\ns_waitcnt lgkmcnt(2)\nv_mfma_f32_32x32x16_f16 v[204:219], v[192:195], v[120:123], v[204:219]\n"
    "ds_read_b32 v241, v7\ns_waitcnt lgkmcnt(2)\nv_mfma_f32_32x32x16_f16 v[204:219], v[196:199], v[68:71], v[204:219]\ns_waitcnt lgkmcnt(1)\n"
    "v_mfma_f32_32x32x16_f16 v[204:219], v[200:203], v[124:127], v[204:219]\ns_waitcnt lgkmcnt(0)\nv_cmp_gt_i32 vcc, v13, v241\ns_cbranch_vccnz .Lfs147\n.Lfr148:\ns_nop 8\n"
    "ds_write_b128 v3, v[204:207]\nds_write_b128 v238, v[208:211]\nds_write_b128 v239, v[212:215]\nds_write_b128 v240, v[216:219]\ns_waitcnt vmcnt(0)\ns_waitcnt lgkmcnt(0)\n"
    "s_barrier\nds_read_b128 v[48:51], v15\nds_read_b128 v[52:55], v15 offset:12672\ns_cmp_lt_u32 s3, 2\ns_cbranch_scc1 .LL150\nds_read_b128 v[56:59], v16\n"
    "ds_read_b128 v[60:63], v16 offset:10240\ns_waitcnt lgkmcnt(0)\nv_pk_fma_f32 v[32:33], v[56:57], s[88:89], v[32:33] op_sel_hi:[1,0,1]\n"
    "v_pk_fma_f32 v[34:35], v[58:59], s[88:89], v[34:35] op_sel_hi:[1,0,1]\nv_pk_fma_f32 v[36:37], v[60:61], s[90:91], v[36:37] op_sel_hi:[1,0,1]\n"
    "v_pk_fma_f32 v[38:39], v[62:63], s[90:91], v[38:39] op_sel_hi:[1,0,1]\n.LL150:\ns_mov_b32 vcc_lo, 0x55555555\ns_mov_b32 vcc_hi, 0x55555555\ns_waitcnt lgkmcnt(0)\n"
    "v_pk_fma_f32 v[32:33], v[48:49], s[88:89], v[32:33] op_sel_hi:[1,0,1]\nv_pk_fma_f32 v[34:35], v[50:51], s[88:89], v[34:35] op_sel_hi:[1,0,1]\n"
    "v_pk_fma_f32 v[36:37], v[52:53], s[90:91], v[36:37] op_sel_hi:[1,0,1]\nv_pk_fma_f32 v[38:39], v[54:55], s[90:91], v[38:39] op_sel_hi:[1,0,1]\ns_cmp_eq_u32 s6, 0\n"
    "s_cbranch_scc0 .LL151\nv_cvt_pk_bf16_f32 v52, v32, v33\nv_cvt_pk_bf16_f32 v53, v34, v35\nv_cvt_pk_bf16_f32 v54, v36, v37\nv_cvt_pk_bf16_f32 v55, v38, v39\ns_nop 0\n"
    "v_cndmask_b32_dpp v48, v54, v52, vcc quad_perm:[1,0,3,2] row_mask:0xf bank_mask:0xf\nv_cndmask_b32_dpp v49, v55, v53, vcc quad_perm:[1,0,3,2] row_mask:0xf bank_mask:0xf\n"
    "s_not_b64 vcc, vcc\nv_cndmask_b32_dpp v50, v52, v54, vcc quad_perm:[1,0,3,2] row_mask:0xf bank_mask:0xf\n"
    "v_cndmask_b32_dpp v51, v53, v55, vcc quad_perm:[1,0,3,2] row_mask:0xf bank_mask:0xf\nglobal_store_dwordx4 v17, v[48:51], s[32:33] sc1\ns_branch .LG278\n.LL151:\n"
    "v_cvt_pk_bf16_f32 v48, v32, v36\nv_cvt_pk_bf16_f32 v49, v33, v37\nv_cvt_pk_bf16_f32 v50, v34, v38\nv_cvt_pk_bf16_f32 v51, v35, v39\n"
    "global_store_dwordx4 v17, v[48:51], s[32:33] sc1\nv_and_b32 v4, 7, v0\ns_lshl_b32 s15, s41, 13\ns_add_u32 s14, s15, 0x1200000\nv_lshl_add_u32 v2, v4, 10, s14\n"
    "v_lshrrev_b32 v5, 3, v0\nv_lshl_add_u32 v2, v5, 2, v2\ns_lshl_b32 s17, s36, 16\ns_lshl_b32 s18, s35, 7\ns_add_u32 s16, s17, s18\nv_lshl_add_u32 v3, v5, 10, s16\n"
    "v_lshl_add_u32 v3, v4, 4, v3\ns_mov_b32 s80, 0x10000\ns_sleep 16\n.LL152:\nglobal_load_dword v52, v2, s[32:33] offset:0 sc1\n"
    "global_load_dword v53, v2, s[32:33] offset:256 sc1\nglobal_load_dword v54, v2, s[32:33] offset:512 sc1\nglobal_load_dword v55, v2, s[32:33] offset:768 sc1\n"
    "s_waitcnt vmcnt(0)\nv_max3_u32 v4, v52, v53, v54\nv_max_u32 v4, v4, v55\nv_cmp_eq_u32 vcc, -1, v4\ns_cbranch_vccz .LL153\ns_sub_u32 s80, s80, 1\ns_cbranch_scc0 .LL152\n"
    ".LL153:\nglobal_store_dwordx4 v3, v[52:55], s[30:31] sc1\ns_endpgm\n.LG278:\ns_mov_b32 s6, 1\ns_mov_b32 s7, 8388608\nv_bfe_u32 v65, v0, 4, 1\nv_bfe_u32 v66, v0, 5, 1\n"
    "v_xor_b32 v64, v65, v66\nv_bfe_u32 v69, v0, 2, 2\nv_lshl_add_u32 v68, v66, 3, v69\nv_lshlrev_b32 v67, 6, v68\nv_lshl_add_u32 v26, v64, 5, v67\nv_and_b32 v70, 1, v0\n"
    "v_lshl_add_u32 v26, v70, 4, v26\nv_and_b32 v71, 2, v0\nv_lshl_add_u32 v26, v71, 2, v26\ns_mul_i32 s15, s3, 0x1a00\ns_add_u32 s14, s15, 0x18d00\n"
    "v_lshl_add_u32 v27, v66, 4, s14\nv_and_b32 v72, 31, v0\ns_mov_b32 s16, 0xd0\nv_mad_u32_u24 v27, v72, s16, v27\nv_and_b32 v73, 19, v0\ns_mov_b32 s17, 0x27c00\n"
    "v_lshl_add_u32 v28, v73, 2, s17\nv_and_b32 v74, 4, v0\nv_lshl_add_u32 v28, v74, 3, v28\nv_and_b32 v75, 8, v0\nv_lshl_add_u32 v28, v75, 1, v28\ns_mul_i32 s19, s3, 0x3000\n"
    "s_add_u32 s18, s19, 0x18000\nv_add_u32 v29, s18, v67\nv_lshl_add_u32 v29, v65, 5, v29\nv_and_b32 v76, 3, v0\nv_lshl_add_u32 v29, v76, 3, v29\nv_and_b32 v77, 63, v0\n"
    "s_lshl_b32 s23, s35, 15\ns_and_b32 s25, s36, 3\ns_lshl_b32 s24, s25, 13\ns_add_u32 s22, s23, s24\ns_lshl_b32 s44, s3, 11\ns_add_u32 s21, s22, s44\n"
    "s_add_u32 s20, s21, 0xd00000\nv_lshl_add_u32 v34, v77, 2, s20\nv_lshlrev_b32 v35, 4, v77\ns_mov_b32 s45, 0x21000\nv_lshl_add_u32 v36, v0, 3, s45\ns_and_b32 s54, s42, 7\n"
    "s_lshl_b32 s44, s54, 12\ns_add_u32 s56, s54, 1\ns_and_b32 s55, s56, 7\ns_lshl_b32 s45, s55, 12\ns_add_u32 s58, s54, 2\ns_and_b32 s57, s58, 7\ns_lshl_b32 s46, s57, 12\n"
    "s_add_u32 s60, s54, 3\ns_and_b32 s59, s60, 7\ns_lshl_b32 s47, s59, 12\ns_add_u32 s62, s54, 4\ns_and_b32 s61, s62, 7\ns_lshl_b32 s48, s61, 12\ns_add_u32 s64, s54, 5\n"
    "s_and_b32 s63, s64, 7\ns_lshl_b32 s49, s63, 12\ns_add_u32 s66, s54, 6\ns_and_b32 s65, s66, 7\ns_lshl_b32 s50, s65, 12\ns_add_u32 s68, s54, 7\ns_and_b32 s67, s68, 7\n"
    "s_lshl_b32 s51, s67, 12\ns_mov_b32 s81, 0x3b000000\ns_mov_b32 s84, -1\ns_mov_b32 s85, 0\nv_mov_b32 v143, 0x3f803f80\nv_add_u32 v116, s44, v26\n"
    "v_add_u32 v118, 0x10000, v116\nv_mov_b32 v112, 0\nv_mov_b32 v113, 0\nv_mov_b32 v114, 0\nv_mov_b32 v115, 0\nv_mov_b32 v121, 0\nv_mov_b32 v122, 0\nv_mov_b32 v123, 0\n"
    "v_mov_b32 v125, 0\nv_mov_b32 v126, 0\nv_mov_b32 v127, 0\nv_mov_b32 v129, 0\nv_mov_b32 v130, 0\nv_mov_b32 v131, 0\nv_mov_b32 v133, 0\nv_mov_b32 v134, 0\nv_mov_b32 v135, 0\n"
    "ds_read_b32 v124, v28 offset:0\nds_read_b32 v128, v28 offset:128\nds_read_b32 v132, v28 offset:256\ns_waitcnt lgkmcnt(0)\nv_cvt_pk_bf16_f32 v140, 0, v124\n"
    "v_cvt_pk_bf16_f32 v141, 0, v128\nv_cvt_pk_bf16_f32 v142, 0, v132\nv_sub_f32 v124, v124, v140\nv_sub_f32 v128, v128, v141\nv_sub_f32 v132, v132, v142\n"
    "v_cvt_pk_bf16_f32 v124, v140, v124\nv_cvt_pk_bf16_f32 v128, v141, v128\nv_cvt_pk_bf16_f32 v132, v142, v132\ns_lshl_b32 s86, s42, 8\nv_add_u32 v34, s86, v34\n"
    "s_and_b32 s86, s42, 0\ns_lshl2_add_u32 s15, s37, s3\ns_lshl_b32 s14, s15, 15\ns_add_u32 s82, s14, 0x600000\ns_mul_i32 s16, s3, 0x3000\ns_add_u32 s87, s16, 0x18000\n"
    "s_add_u32 s82, s82, s32\ns_addc_u32 s83, s33, 0\nds_read_b64_tr_b16 v[192:193], v116 offset:0\nds_read_b64_tr_b16 v[194:195], v116 offset:256\n"
    "ds_read_b64_tr_b16 v[196:197], v116 offset:1024\nds_read_b64_tr_b16 v[198:199], v116 offset:1280\nds_read_b64_tr_b16 v[200:201], v116 offset:2048\n"
    "ds_read_b64_tr_b16 v[202:203], v116 offset:2304\nds_read_b64_tr_b16 v[204:205], v116 offset:3072\nds_read_b64_tr_b16 v[206:207], v116 offset:3328\n"
    "ds_read_b64_tr_b16 v[208:209], v116 offset:32768\nds_read_b64_tr_b16 v[210:211], v116 offset:33024\nds_read_b64_tr_b16 v[212:213], v116 offset:33792\n"
    "ds_read_b64_tr_b16 v[214:215], v116 offset:34048\ns_barrier\nv_mov_b32 v64, 0\nv_mov_b32 v65, 0xf149f2ca\nv_bfe_u32 v68, v0, 4, 2\nv_lshlrev_b32 v67, 2, v68\n"
    "s_add_u32 s15, s37, s3\ns_cmp_eq_u32 s15, 0\ns_cselect_b32 s14, 8, 0\nv_and_b32 v71, 7, v0\nv_add_u32 v70, 1, v71\nv_max_u32 v69, s14, v70\nv_sub_u32 v66, v67, v69\n"
    "v_add_u32 v73, 8, v71\nv_sub_u32 v72, v73, v69\nv_cmp_le_u32 vcc, v66, v72\ns_nop 1\nv_cndmask_b32 v2, v65, v64, vcc\nv_add_u32 v75, 1, v67\nv_sub_u32 v74, v75, v69\n"
    "v_cmp_le_u32 vcc, v74, v72\ns_nop 1\nv_cndmask_b32 v3, v65, v64, vcc\nv_add_u32 v77, 2, v67\nv_sub_u32 v76, v77, v69\nv_cmp_le_u32 vcc, v76, v72\ns_nop 1\n"
    "v_cndmask_b32 v4, v65, v64, vcc\nv_add_u32 v79, 3, v67\nv_sub_u32 v78, v79, v69\nv_cmp_le_u32 vcc, v78, v72\ns_nop 1\nv_cndmask_b32 v5, v65, v64, vcc\n"
    "s_mul_i32 s17, s3, 0xd00\ns_add_u32 s16, s17, 0x18040\nv_lshl_add_u32 v6, v68, 4, s16\nv_and_b32 v81, 15, v0\nv_max_u32 v80, 1, v81\ns_mov_b32 s18, 0x1a0\n"
    "v_mad_u32_u24 v6, v80, s18, v6\ns_add_u32 s19, s17, 0x18d00\nv_lshl_add_u32 v7, v68, 4, s19\nv_mad_u32_u24 v7, v71, s18, v7\nv_and_b32 v82, 3, v0\n"
    "s_add_u32 s20, s17, 0x18080\nv_lshl_add_u32 v8, v82, 3, s20\nv_bfe_u32 v85, v0, 2, 2\nv_add_u32 v84, v67, v85\nv_max_u32 v83, 1, v84\nv_mad_u32_u24 v8, v83, s18, v8\n"
    "v_bfe_u32 v86, v0, 3, 1\ns_lshr_b32 s54, s3, 1\ns_lshl2_add_u32 s25, s37, s54\ns_lshl_b32 s24, s25, 15\ns_lshl_b32 s55, s38, 11\ns_add_u32 s23, s24, s55\n"
    "s_and_b32 s57, s3, 1\ns_lshl_b32 s56, s57, 8\ns_add_u32 s22, s23, s56\ns_add_u32 s21, s22, 0x800000\nv_lshl_add_u32 v9, v86, 10, s21\nv_bfe_u32 v87, v0, 5, 1\n"
    "v_lshl_add_u32 v9, v87, 9, v9\nv_bfe_u32 v88, v0, 4, 1\nv_lshl_add_u32 v9, v88, 4, v9\nv_lshl_add_u32 v9, v71, 5, v9\nv_mov_b64 v[10:11], 0\nv_mov_b64 v[12:13], 0\n"
    "v_mov_b64 v[14:15], 0\nv_mov_b64 v[16:17], 0\nv_mov_b64 v[18:19], 0\nv_mov_b64 v[20:21], 0\nv_mov_b64 v[22:23], 0\nv_mov_b64 v[24:25], 0\ns_mov_b32 s80, 0x10000\n"
    "s_barrier\nds_read_b64_tr_b16 v[144:145], v29 offset:0\nds_read_b64_tr_b16 v[146:147], v29 offset:256\nds_read_b64_tr_b16 v[148:149], v29 offset:1024\n"
    "ds_read_b64_tr_b16 v[150:151], v29 offset:1280\nds_read_b64_tr_b16 v[152:153], v29 offset:2048\nds_read_b64_tr_b16 v[154:155], v29 offset:2304\n"
    "ds_read_b64_tr_b16 v[156:157], v29 offset:3072\nds_read_b64_tr_b16 v[158:159], v29 offset:3328\n.LL154:\ns_waitcnt lgkmcnt(6)\nv_dot2_f32_bf16 v112, v143, v144, 0\n"
    "v_dot2_f32_bf16 v113, v144, v144, 0\nv_dot2_f32_bf16 v114, v143, v145, 0\nv_dot2_f32_bf16 v115, v145, v145, 0\nv_dot2c_f32_bf16 v112, v143, v146\n"
    "v_dot2c_f32_bf16 v113, v146, v146\nv_dot2c_f32_bf16 v114, v143, v147\nv_dot2c_f32_bf16 v115, v147, v147\ns_waitcnt lgkmcnt(4)\nv_dot2c_f32_bf16 v112, v143, v148\n"
    "v_dot2c_f32_bf16 v113, v148, v148\nv_dot2c_f32_bf16 v114, v143, v149\nv_dot2c_f32_bf16 v115, v149, v149\nv_dot2c_f32_bf16 v112, v143, v150\n"
    "v_dot2c_f32_bf16 v113, v150, v150\nv_dot2c_f32_bf16 v114, v143, v151\nv_dot2c_f32_bf16 v115, v151, v151\ns_waitcnt lgkmcnt(2)\nv_dot2c_f32_bf16 v112, v143, v152\n"
    "v_dot2c_f32_bf16 v113, v152, v152\nv_dot2c_f32_bf16 v114, v143, v153\nv_dot2c_f32_bf16 v115, v153, v153\nv_dot2c_f32_bf16 v112, v143, v154\n"
    "v_dot2c_f32_bf16 v113, v154, v154\nv_dot2c_f32_bf16 v114, v143, v155\nv_dot2c_f32_bf16 v115, v155, v155\ns_waitcnt lgkmcnt(0)\nv_dot2c_f32_bf16 v112, v143, v156\n"
    "v_dot2c_f32_bf16 v113, v156, v156\nv_dot2c_f32_bf16 v114, v143, v157\nv_dot2c_f32_bf16 v115, v157, v157\nv_dot2c_f32_bf16 v112, v143, v158\n"
    "v_dot2c_f32_bf16 v113, v158, v158\nv_dot2c_f32_bf16 v114, v143, v159\nv_dot2c_f32_bf16 v115, v159, v159\ns_nop 1\nv_add_f32 v112, v112, v114\nv_add_f32 v113, v113, v115\n"
    "v_cmp_u_f32 vcc, v113, v113\ns_cbranch_vccnz .Lxs155\n.Lxr156:\nv_mfma_f32_32x32x16_bf16 v[64:79], v[192:195], v[144:147], 0\n"
    "ds_read_b64_tr_b16 v[192:193], v116 offset:34816\nds_read_b64_tr_b16 v[194:195], v116 offset:35072\nv_add_u32 v117, s45, v26\nv_add_u32 v119, 0x10000, v117\n"
    "v_mfma_f32_32x32x16_bf16 v[64:79], v[196:199], v[148:151], v[64:79]\nds_read_b64_tr_b16 v[196:197], v116 offset:35840\nds_read_b64_tr_b16 v[198:199], v116 offset:36096\n"
    "v_permlane32_swap_b32 v112, v113\nv_add_f32 v112, v112, v113\nv_mfma_f32_32x32x16_bf16 v[64:79], v[200:203], v[152:155], v[64:79]\n"
    "ds_read_b64_tr_b16 v[200:201], v118 offset:0\nds_read_b64_tr_b16 v[202:203], v118 offset:256\nglobal_store_dword v34, v112, s[32:33] sc1\ns_barrier\n"
    "v_mfma_f32_32x32x16_bf16 v[64:79], v[204:207], v[156:159], v[64:79]\nds_read_b64_tr_b16 v[204:205], v118 offset:1024\nds_read_b64_tr_b16 v[206:207], v118 offset:1280\n"
    "ds_read_b64_tr_b16 v[160:161], v29 offset:4096\nds_read_b64_tr_b16 v[162:163], v29 offset:4352\nv_mfma_f32_32x32x16_bf16 v[80:95], v[208:211], v[144:147], 0\n"
    "ds_read_b64_tr_b16 v[208:209], v118 offset:2048\nds_read_b64_tr_b16 v[210:211], v118 offset:2304\nds_read_b64_tr_b16 v[164:165], v29 offset:5120\n"
    "ds_read_b64_tr_b16 v[166:167], v29 offset:5376\nv_mfma_f32_32x32x16_bf16 v[80:95], v[212:215], v[148:151], v[80:95]\nds_read_b64_tr_b16 v[212:213], v118 offset:3072\n"
    "ds_read_b64_tr_b16 v[214:215], v118 offset:3328\nds_read_b64_tr_b16 v[168:169], v29 offset:6144\nds_read_b64_tr_b16 v[170:171], v29 offset:6400\ns_waitcnt lgkmcnt(14)\n"
    "v_mfma_f32_32x32x16_bf16 v[80:95], v[192:195], v[152:155], v[80:95]\nds_read_b64_tr_b16 v[192:193], v117 offset:0\nds_read_b64_tr_b16 v[194:195], v117 offset:256\n"
    "ds_read_b64_tr_b16 v[172:173], v29 offset:7168\nds_read_b64_tr_b16 v[174:175], v29 offset:7424\nv_mfma_f32_32x32x16_bf16 v[80:95], v[196:199], v[156:159], v[80:95]\n"
    "ds_read_b64_tr_b16 v[196:197], v117 offset:1024\nds_read_b64_tr_b16 v[198:199], v117 offset:1280\ns_waitcnt lgkmcnt(10)\nv_pk_maximum3_f16 v138, v160, v161, v162\n"
    "v_pk_maximum3_f16 v139, v163, v164, v165\nv_mfma_f32_32x32x16_bf16 v[96:111], v[200:203], v[144:147], 0\nds_read_b64_tr_b16 v[200:201], v117 offset:2048\n"
    "ds_read_b64_tr_b16 v[202:203], v117 offset:2304\nv_pk_maximum3_f16 v138, v138, v166, v167\ns_waitcnt lgkmcnt(4)\nv_pk_maximum3_f16 v139, v139, v168, v169\n"
    "v_mfma_f32_32x32x16_bf16 v[96:111], v[204:207], v[148:151], v[96:111]\nds_read_b64_tr_b16 v[204:205], v117 offset:3072\nds_read_b64_tr_b16 v[206:207], v117 offset:3328\n"
    "v_pk_maximum3_f16 v138, v138, v170, v171\nv_pk_maximum3_f16 v139, v139, v172, v173\nv_mfma_f32_32x32x16_bf16 v[96:111], v[208:211], v[152:155], v[96:111]\n"
    "ds_read_b64_tr_b16 v[208:209], v117 offset:32768\nds_read_b64_tr_b16 v[210:211], v117 offset:33024\nv_pk_maximum3_f16 v138, v138, v174, v175\n"
    "v_pk_maximum3_f16 v138, v138, v139, v139\nv_mfma_f32_32x32x16_bf16 v[96:111], v[212:215], v[156:159], v[96:111]\nds_read_b64_tr_b16 v[212:213], v117 offset:33792\n"
    "ds_read_b64_tr_b16 v[214:215], v117 offset:34048\nv_cmp_u_f16_sdwa vcc, v138, v138 src0_sel:WORD_0 src1_sel:WORD_1\ns_cbranch_vccnz .Lxs158\n.Lxr159:\n"
    "s_waitcnt lgkmcnt(8)\nv_mfma_f32_32x32x16_bf16 v[64:79], v[192:195], v[160:163], v[64:79]\nds_read_b64_tr_b16 v[192:193], v117 offset:34816\n"
    "ds_read_b64_tr_b16 v[194:195], v117 offset:35072\nv_add_u32 v116, s46, v26\nv_add_u32 v118, 0x10000, v116\n"
    "v_mfma_f32_32x32x16_bf16 v[64:79], v[196:199], v[164:167], v[64:79]\nds_read_b64_tr_b16 v[196:197], v117 offset:35840\nds_read_b64_tr_b16 v[198:199], v117 offset:36096\n"
    "s_waitcnt lgkmcnt(8)\nv_mfma_f32_32x32x16_bf16 v[64:79], v[200:203], v[168:171], v[64:79]\nds_read_b64_tr_b16 v[200:201], v119 offset:0\n"
    "ds_read_b64_tr_b16 v[202:203], v119 offset:256\ns_barrier\nv_mfma_f32_32x32x16_bf16 v[64:79], v[204:207], v[172:175], v[64:79]\n"
    "ds_read_b64_tr_b16 v[204:205], v119 offset:1024\nds_read_b64_tr_b16 v[206:207], v119 offset:1280\nds_read_b64_tr_b16 v[144:145], v29 offset:8192\n"
    "ds_read_b64_tr_b16 v[146:147], v29 offset:8448\ns_waitcnt lgkmcnt(10)\nv_mfma_f32_32x32x16_bf16 v[80:95], v[208:211], v[160:163], v[80:95]\n"
    "ds_read_b64_tr_b16 v[208:209], v119 offset:2048\nds_read_b64_tr_b16 v[210:211], v119 offset:2304\nds_read_b64_tr_b16 v[148:149], v29 offset:9216\n"
    "ds_read_b64_tr_b16 v[150:151], v29 offset:9472\nv_mfma_f32_32x32x16_bf16 v[80:95], v[212:215], v[164:167], v[80:95]\nds_read_b64_tr_b16 v[212:213], v119 offset:3072\n"
    "ds_read_b64_tr_b16 v[214:215], v119 offset:3328\nds_read_b64_tr_b16 v[152:153], v29 offset:10240\nds_read_b64_tr_b16 v[154:155], v29 offset:10496\ns_waitcnt lgkmcnt(14)\n"
    "v_mfma_f32_32x32x16_bf16 v[80:95], v[192:195], v[168:171], v[80:95]\nds_read_b64_tr_b16 v[192:193], v116 offset:0\nds_read_b64_tr_b16 v[194:195], v116 offset:256\n"
    "ds_read_b64_tr_b16 v[156:157], v29 offset:11264\nds_read_b64_tr_b16 v[158:159], v29 offset:11520\nv_mfma_f32_32x32x16_bf16 v[80:95], v[196:199], v[172:175], v[80:95]\n"
    "ds_read_b64_tr_b16 v[196:197], v116 offset:1024\nds_read_b64_tr_b16 v[198:199], v116 offset:1280\ns_waitcnt lgkmcnt(10)\nv_pk_maximum3_f16 v138, v144, v145, v146\n"
    "v_pk_maximum3_f16 v139, v147, v148, v149\nv_mfma_f32_32x32x16_bf16 v[96:111], v[200:203], v[160:163], v[96:111]\nds_read_b64_tr_b16 v[200:201], v116 offset:2048\n"
    "ds_read_b64_tr_b16 v[202:203], v116 offset:2304\nv_pk_maximum3_f16 v138, v138, v150, v151\ns_waitcnt lgkmcnt(4)\nv_pk_maximum3_f16 v139, v139, v152, v153\n"
    "v_mfma_f32_32x32x16_bf16 v[96:111], v[204:207], v[164:167], v[96:111]\nds_read_b64_tr_b16 v[204:205], v116 offset:3072\nds_read_b64_tr_b16 v[206:207], v116 offset:3328\n"
    "v_pk_maximum3_f16 v138, v138, v154, v155\nv_pk_maximum3_f16 v139, v139, v156, v157\nv_mfma_f32_32x32x16_bf16 v[96:111], v[208:211], v[168:171], v[96:111]\n"
    "ds_read_b64_tr_b16 v[208:209], v116 offset:32768\nds_read_b64_tr_b16 v[210:211], v116 offset:33024\nv_pk_maximum3_f16 v138, v138, v158, v159\n"
    "v_pk_maximum3_f16 v138, v138, v139, v139\nv_mfma_f32_32x32x16_bf16 v[96:111], v[212:215], v[172:175], v[96:111]\nds_read_b64_tr_b16 v[212:213], v116 offset:33792\n"
    "ds_read_b64_tr_b16 v[214:215], v116 offset:34048\nv_cmp_u_f16_sdwa vcc, v138, v138 src0_sel:WORD_0 src1_sel:WORD_1\ns_cbranch_vccnz .Lxs161\n.Lxr162:\n"
    "s_waitcnt lgkmcnt(8)\nv_mfma_f32_32x32x16_bf16 v[64:79], v[192:195], v[144:147], v[64:79]\nds_read_b64_tr_b16 v[192:193], v116 offset:34816\n"
    "ds_read_b64_tr_b16 v[194:195], v116 offset:35072\nv_add_u32 v117, s47, v26\nv_add_u32 v119, 0x10000, v117\n"
    "v_mfma_f32_32x32x16_bf16 v[64:79], v[196:199], v[148:151], v[64:79]\nds_read_b64_tr_b16 v[196:197], v116 offset:35840\nds_read_b64_tr_b16 v[198:199], v116 offset:36096\n"
    "s_waitcnt lgkmcnt(8)\nv_mfma_f32_32x32x16_bf16 v[64:79], v[200:203], v[152:155], v[64:79]\nds_read_b64_tr_b16 v[200:201], v118 offset:0\n"
    "ds_read_b64_tr_b16 v[202:203], v118 offset:256\ns_barrier\nv_mfma_f32_32x32x16_bf16 v[64:79], v[204:207], v[156:159], v[64:79]\n"
    "ds_read_b64_tr_b16 v[204:205], v118 offset:1024\nds_read_b64_tr_b16 v[206:207], v118 offset:1280\nds_read_b64_tr_b16 v[160:161], v29 offset:0\n"
    "ds_read_b64_tr_b16 v[162:163], v29 offset:256\ns_waitcnt lgkmcnt(10)\nv_mfma_f32_32x32x16_bf16 v[80:95], v[208:211], v[144:147], v[80:95]\n"
    "ds_read_b64_tr_b16 v[208:209], v118 offset:2048\nds_read_b64_tr_b16 v[210:211], v118 offset:2304\nds_read_b64_tr_b16 v[164:165], v29 offset:1024\n"
    "ds_read_b64_tr_b16 v[166:167], v29 offset:1280\nv_mfma_f32_32x32x16_bf16 v[80:95], v[212:215], v[148:151], v[80:95]\nds_read_b64_tr_b16 v[212:213], v118 offset:3072\n"
    "ds_read_b64_tr_b16 v[214:215], v118 offset:3328\nds_read_b64_tr_b16 v[168:169], v29 offset:2048\nds_read_b64_tr_b16 v[170:171], v29 offset:2304\ns_waitcnt lgkmcnt(14)\n"
    "v_mfma_f32_32x32x16_bf16 v[80:95], v[192:195], v[152:155], v[80:95]\nds_read_b64_tr_b16 v[192:193], v117 offset:0\nds_read_b64_tr_b16 v[194:195], v117 offset:256\n"
    "ds_read_b64_tr_b16 v[172:173], v29 offset:3072\nds_read_b64_tr_b16 v[174:175], v29 offset:3328\nv_mfma_f32_32x32x16_bf16 v[80:95], v[196:199], v[156:159], v[80:95]\n"
    "ds_read_b64_tr_b16 v[196:197], v117 offset:1024\nds_read_b64_tr_b16 v[198:199], v117 offset:1280\ns_waitcnt lgkmcnt(10)\nv_pk_maximum3_f16 v138, v160, v161, v162\n"
    "v_pk_maximum3_f16 v139, v163, v164, v165\nv_mfma_f32_32x32x16_bf16 v[96:111], v[200:203], v[144:147], v[96:111]\nds_read_b64_tr_b16 v[200:201], v117 offset:2048\n"
    "ds_read_b64_tr_b16 v[202:203], v117 offset:2304\nv_pk_maximum3_f16 v138, v138, v166, v167\ns_waitcnt lgkmcnt(4)\nv_pk_maximum3_f16 v139, v139, v168, v169\n"
    "v_mfma_f32_32x32x16_bf16 v[96:111], v[204:207], v[148:151], v[96:111]\nds_read_b64_tr_b16 v[204:205], v117 offset:3072\nds_read_b64_tr_b16 v[206:207], v117 offset:3328\n"
    "v_pk_maximum3_f16 v138, v138, v170, v171\nv_pk_maximum3_f16 v139, v139, v172, v173\nv_mfma_f32_32x32x16_bf16 v[96:111], v[208:211], v[152:155], v[96:111]\n"
    "ds_read_b64_tr_b16 v[208:209], v117 offset:32768\nds_read_b64_tr_b16 v[210:211], v117 offset:33024\nv_pk_maximum3_f16 v138, v138, v174, v175\n"
    "v_pk_maximum3_f16 v138, v138, v139, v139\nv_mfma_f32_32x32x16_bf16 v[96:111], v[212:215], v[156:159], v[96:111]\nds_read_b64_tr_b16 v[212:213], v117 offset:33792\n"
    "ds_read_b64_tr_b16 v[214:215], v117 offset:34048\nv_cmp_u_f16_sdwa vcc, v138, v138 src0_sel:WORD_0 src1_sel:WORD_1\ns_cbranch_vccnz .Lxs164\n.Lxr165:\n"
    "s_waitcnt lgkmcnt(8)\nv_mfma_f32_32x32x16_bf16 v[64:79], v[192:195], v[160:163], v[64:79]\nds_read_b64_tr_b16 v[192:193], v117 offset:34816\n"
    "ds_read_b64_tr_b16 v[194:195], v117 offset:35072\nv_add_u32 v116, s48, v26\nv_add_u32 v118, 0x10000, v116\n"
    "v_mfma_f32_32x32x16_bf16 v[64:79], v[196:199], v[164:167], v[64:79]\nds_read_b64_tr_b16 v[196:197], v117 offset:35840\nds_read_b64_tr_b16 v[198:199], v117 offset:36096\n"
    "s_waitcnt lgkmcnt(8)\nv_mfma_f32_32x32x16_bf16 v[64:79], v[200:203], v[168:171], v[64:79]\nds_read_b64_tr_b16 v[200:201], v119 offset:0\n"
    "ds_read_b64_tr_b16 v[202:203], v119 offset:256\ns_barrier\nv_mfma_f32_32x32x16_bf16 v[64:79], v[204:207], v[172:175], v[64:79]\n"
    "ds_read_b64_tr_b16 v[204:205], v119 offset:1024\nds_read_b64_tr_b16 v[206:207], v119 offset:1280\nds_read_b64_tr_b16 v[144:145], v29 offset:4096\n"
    "ds_read_b64_tr_b16 v[146:147], v29 offset:4352\ns_waitcnt lgkmcnt(10)\nv_mfma_f32_32x32x16_bf16 v[80:95], v[208:211], v[160:163], v[80:95]\n"
    "ds_read_b64_tr_b16 v[208:209], v119 offset:2048\nds_read_b64_tr_b16 v[210:211], v119 offset:2304\nds_read_b64_tr_b16 v[148:149], v29 offset:5120\n"
    "ds_read_b64_tr_b16 v[150:151], v29 offset:5376\nv_mfma_f32_32x32x16_bf16 v[80:95], v[212:215], v[164:167], v[80:95]\nds_read_b64_tr_b16 v[212:213], v119 offset:3072\n"
    "ds_read_b64_tr_b16 v[214:215], v119 offset:3328\nds_read_b64_tr_b16 v[152:153], v29 offset:6144\nds_read_b64_tr_b16 v[154:155], v29 offset:6400\ns_waitcnt lgkmcnt(14)\n"
    "v_mfma_f32_32x32x16_bf16 v[80:95], v[192:195], v[168:171], v[80:95]\nds_read_b64_tr_b16 v[192:193], v116 offset:0\nds_read_b64_tr_b16 v[194:195], v116 offset:256\n"
    "ds_read_b64_tr_b16 v[156:157], v29 offset:7168\nds_read_b64_tr_b16 v[158:159], v29 offset:7424\nv_mfma_f32_32x32x16_bf16 v[80:95], v[196:199], v[172:175], v[80:95]\n"
    "ds_read_b64_tr_b16 v[196:197], v116 offset:1024\nds_read_b64_tr_b16 v[198:199], v116 offset:1280\ns_waitcnt lgkmcnt(10)\nv_pk_maximum3_f16 v138, v144, v145, v146\n"
    "v_pk_maximum3_f16 v139, v147, v148, v149\nv_mfma_f32_32x32x16_bf16 v[96:111], v[200:203], v[160:163], v[96:111]\nds_read_b64_tr_b16 v[200:201], v116 offset:2048\n"
    "ds_read_b64_tr_b16 v[202:203], v116 offset:2304\nv_pk_maximum3_f16 v138, v138, v150, v151\ns_waitcnt lgkmcnt(4)\nv_pk_maximum3_f16 v139, v139, v152, v153\n"
    "v_mfma_f32_32x32x16_bf16 v[96:111], v[204:207], v[164:167], v[96:111]\nds_read_b64_tr_b16 v[204:205], v116 offset:3072\nds_read_b64_tr_b16 v[206:207], v116 offset:3328\n"
    "v_pk_maximum3_f16 v138, v138, v154, v155\nv_pk_maximum3_f16 v139, v139, v156, v157\nv_mfma_f32_32x32x16_bf16 v[96:111], v[208:211], v[168:171], v[96:111]\n"
    "ds_read_b64_tr_b16 v[208:209], v116 offset:32768\nds_read_b64_tr_b16 v[210:211], v116 offset:33024\nv_pk_maximum3_f16 v138, v138, v158, v159\n"
    "v_pk_maximum3_f16 v138, v138, v139, v139\nv_mfma_f32_32x32x16_bf16 v[96:111], v[212:215], v[172:175], v[96:111]\nds_read_b64_tr_b16 v[212:213], v116 offset:33792\n"
    "ds_read_b64_tr_b16 v[214:215], v116 offset:34048\nv_cmp_u_f16_sdwa vcc, v138, v138 src0_sel:WORD_0 src1_sel:WORD_1\ns_cbranch_vccnz .Lxs167\n.Lxr168:\n"
    "s_waitcnt lgkmcnt(8)\nv_mfma_f32_32x32x16_bf16 v[64:79], v[192:195], v[144:147], v[64:79]\nds_read_b64_tr_b16 v[192:193], v116 offset:34816\n"
    "ds_read_b64_tr_b16 v[194:195], v116 offset:35072\nv_add_u32 v117, s49, v26\nv_add_u32 v119, 0x10000, v117\n"
    "v_mfma_f32_32x32x16_bf16 v[64:79], v[196:199], v[148:151], v[64:79]\nds_read_b64_tr_b16 v[196:197], v116 offset:35840\nds_read_b64_tr_b16 v[198:199], v116 offset:36096\n"
    "s_waitcnt lgkmcnt(8)\nv_mfma_f32_32x32x16_bf16 v[64:79], v[200:203], v[152:155], v[64:79]\nds_read_b64_tr_b16 v[200:201], v118 offset:0\n"
    "ds_read_b64_tr_b16 v[202:203], v118 offset:256\ns_barrier\nv_mfma_f32_32x32x16_bf16 v[64:79], v[204:207], v[156:159], v[64:79]\n"
    "ds_read_b64_tr_b16 v[204:205], v118 offset:1024\nds_read_b64_tr_b16 v[206:207], v118 offset:1280\nds_read_b64_tr_b16 v[160:161], v29 offset:8192\n"
    "ds_read_b64_tr_b16 v[162:163], v29 offset:8448\ns_waitcnt lgkmcnt(10)\nv_mfma_f32_32x32x16_bf16 v[80:95], v[208:211], v[144:147], v[80:95]\n"
    "ds_read_b64_tr_b16 v[208:209], v118 offset:2048\nds_read_b64_tr_b16 v[210:211], v118 offset:2304\nds_read_b64_tr_b16 v[164:165], v29 offset:9216\n"
    "ds_read_b64_tr_b16 v[166:167], v29 offset:9472\nv_mfma_f32_32x32x16_bf16 v[80:95], v[212:215], v[148:151], v[80:95]\nds_read_b64_tr_b16 v[212:213], v118 offset:3072\n"
    "ds_read_b64_tr_b16 v[214:215], v118 offset:3328\nds_read_b64_tr_b16 v[168:169], v29 offset:10240\nds_read_b64_tr_b16 v[170:171], v29 offset:10496\ns_waitcnt lgkmcnt(14)\n"
    "v_mfma_f32_32x32x16_bf16 v[80:95], v[192:195], v[152:155], v[80:95]\nds_read_b64_tr_b16 v[192:193], v117 offset:0\nds_read_b64_tr_b16 v[194:195], v117 offset:256\n"
    "ds_read_b64_tr_b16 v[172:173], v29 offset:11264\nds_read_b64_tr_b16 v[174:175], v29 offset:11520\nv_mfma_f32_32x32x16_bf16 v[80:95], v[196:199], v[156:159], v[80:95]\n"
    "ds_read_b64_tr_b16 v[196:197], v117 offset:1024\nds_read_b64_tr_b16 v[198:199], v117 offset:1280\ns_waitcnt lgkmcnt(10)\nv_pk_maximum3_f16 v138, v160, v161, v162\n"
    "v_pk_maximum3_f16 v139, v163, v164, v165\nv_mfma_f32_32x32x16_bf16 v[96:111], v[200:203], v[144:147], v[96:111]\nds_read_b64_tr_b16 v[200:201], v117 offset:2048\n"
    "ds_read_b64_tr_b16 v[202:203], v117 offset:2304\nv_pk_maximum3_f16 v138, v138, v166, v167\ns_waitcnt lgkmcnt(4)\nv_pk_maximum3_f16 v139, v139, v168, v169\n"
    "v_mfma_f32_32x32x16_bf16 v[96:111], v[204:207], v[148:151], v[96:111]\nds_read_b64_tr_b16 v[204:205], v117 offset:3072\nds_read_b64_tr_b16 v[206:207], v117 offset:3328\n"
    "v_pk_maximum3_f16 v138, v138, v170, v171\nv_pk_maximum3_f16 v139, v139, v172, v173\nv_mfma_f32_32x32x16_bf16 v[96:111], v[208:211], v[152:155], v[96:111]\n"
    "ds_read_b64_tr_b16 v[208:209], v117 offset:32768\nds_read_b64_tr_b16 v[210:211], v117 offset:33024\nv_pk_maximum3_f16 v138, v138, v174, v175\n"
    "v_pk_maximum3_f16 v138, v138, v139, v139\nv_mfma_f32_32x32x16_bf16 v[96:111], v[212:215], v[156:159], v[96:111]\nds_read_b64_tr_b16 v[212:213], v117 offset:33792\n"
    "ds_read_b64_tr_b16 v[214:215], v117 offset:34048\nv_cmp_u_f16_sdwa vcc, v138, v138 src0_sel:WORD_0 src1_sel:WORD_1\ns_cbranch_vccnz .Lxs170\n.Lxr171:\n"
    "s_waitcnt lgkmcnt(8)\nv_mfma_f32_32x32x16_bf16 v[64:79], v[192:195], v[160:163], v[64:79]\nds_read_b64_tr_b16 v[192:193], v117 offset:34816\n"
    "ds_read_b64_tr_b16 v[194:195], v117 offset:35072\nv_add_u32 v116, s50, v26\nv_add_u32 v118, 0x10000, v116\n"
    "v_mfma_f32_32x32x16_bf16 v[64:79], v[196:199], v[164:167], v[64:79]\nds_read_b64_tr_b16 v[196:197], v117 offset:35840\nds_read_b64_tr_b16 v[198:199], v117 offset:36096\n"
    "s_waitcnt lgkmcnt(8)\nv_mfma_f32_32x32x16_bf16 v[64:79], v[200:203], v[168:171], v[64:79]\nds_read_b64_tr_b16 v[200:201], v119 offset:0\n"
    "ds_read_b64_tr_b16 v[202:203], v119 offset:256\ns_barrier\nv_mfma_f32_32x32x16_bf16 v[64:79], v[204:207], v[172:175], v[64:79]\n"
    "ds_read_b64_tr_b16 v[204:205], v119 offset:1024\nds_read_b64_tr_b16 v[206:207], v119 offset:1280\nds_read_b64_tr_b16 v[144:145], v29 offset:0\n"
    "ds_read_b64_tr_b16 v[146:147], v29 offset:256\ns_waitcnt lgkmcnt(10)\nv_mfma_f32_32x32x16_bf16 v[80:95], v[208:211], v[160:163], v[80:95]\n"
    "ds_read_b64_tr_b16 v[208:209], v119 offset:2048\nds_read_b64_tr_b16 v[210:211], v119 offset:2304\nds_read_b64_tr_b16 v[148:149], v29 offset:1024\n"
    "ds_read_b64_tr_b16 v[150:151], v29 offset:1280\nv_mfma_f32_32x32x16_bf16 v[80:95], v[212:215], v[164:167], v[80:95]\nds_read_b64_tr_b16 v[212:213], v119 offset:3072\n"
    "ds_read_b64_tr_b16 v[214:215], v119 offset:3328\nds_read_b64_tr_b16 v[152:153], v29 offset:2048\nds_read_b64_tr_b16 v[154:155], v29 offset:2304\ns_waitcnt lgkmcnt(14)\n"
    "v_mfma_f32_32x32x16_bf16 v[80:95], v[192:195], v[168:171], v[80:95]\nds_read_b64_tr_b16 v[192:193], v116 offset:0\nds_read_b64_tr_b16 v[194:195], v116 offset:256\n"
    "ds_read_b64_tr_b16 v[156:157], v29 offset:3072\nds_read_b64_tr_b16 v[158:159], v29 offset:3328\nv_mfma_f32_32x32x16_bf16 v[80:95], v[196:199], v[172:175], v[80:95]\n"
    "ds_read_b64_tr_b16 v[196:197], v116 offset:1024\nds_read_b64_tr_b16 v[198:199], v116 offset:1280\ns_waitcnt lgkmcnt(10)\nv_pk_maximum3_f16 v138, v144, v145, v146\n"
    "v_pk_maximum3_f16 v139, v147, v148, v149\nv_mfma_f32_32x32x16_bf16 v[96:111], v[200:203], v[160:163], v[96:111]\nds_read_b64_tr_b16 v[200:201], v116 offset:2048\n"
    "ds_read_b64_tr_b16 v[202:203], v116 offset:2304\nv_pk_maximum3_f16 v138, v138, v150, v151\ns_waitcnt lgkmcnt(4)\nv_pk_maximum3_f16 v139, v139, v152, v153\n"
    "v_mfma_f32_32x32x16_bf16 v[96:111], v[204:207], v[164:167], v[96:111]\nds_read_b64_tr_b16 v[204:205], v116 offset:3072\nds_read_b64_tr_b16 v[206:207], v116 offset:3328\n"
    "v_pk_maximum3_f16 v138, v138, v154, v155\nv_pk_maximum3_f16 v139, v139, v156, v157\nv_mfma_f32_32x32x16_bf16 v[96:111], v[208:211], v[168:171], v[96:111]\n"
    "ds_read_b64_tr_b16 v[208:209], v116 offset:32768\nds_read_b64_tr_b16 v[210:211], v116 offset:33024\nv_pk_maximum3_f16 v138, v138, v158, v159\n"
    "v_pk_maximum3_f16 v138, v138, v139, v139\nv_mfma_f32_32x32x16_bf16 v[96:111], v[212:215], v[172:175], v[96:111]\nds_read_b64_tr_b16 v[212:213], v116 offset:33792\n"
    "ds_read_b64_tr_b16 v[214:215], v116 offset:34048\nv_cmp_u_f16_sdwa vcc, v138, v138 src0_sel:WORD_0 src1_sel:WORD_1\ns_cbranch_vccnz .Lxs173\n.Lxr174:\n"
    "s_waitcnt lgkmcnt(8)\nv_mfma_f32_32x32x16_bf16 v[64:79], v[192:195], v[144:147], v[64:79]\nds_read_b64_tr_b16 v[192:193], v116 offset:34816\n"
    "ds_read_b64_tr_b16 v[194:195], v116 offset:35072\nv_add_u32 v117, s51, v26\nv_add_u32 v119, 0x10000, v117\n"
    "v_mfma_f32_32x32x16_bf16 v[64:79], v[196:199], v[148:151], v[64:79]\nds_read_b64_tr_b16 v[196:197], v116 offset:35840\nds_read_b64_tr_b16 v[198:199], v116 offset:36096\n"
    "s_waitcnt lgkmcnt(8)\nv_mfma_f32_32x32x16_bf16 v[64:79], v[200:203], v[152:155], v[64:79]\nds_read_b64_tr_b16 v[200:201], v118 offset:0\n"
    "ds_read_b64_tr_b16 v[202:203], v118 offset:256\ns_barrier\nv_mfma_f32_32x32x16_bf16 v[64:79], v[204:207], v[156:159], v[64:79]\n"
    "ds_read_b64_tr_b16 v[204:205], v118 offset:1024\nds_read_b64_tr_b16 v[206:207], v118 offset:1280\nds_read_b64_tr_b16 v[160:161], v29 offset:4096\n"
    "ds_read_b64_tr_b16 v[162:163], v29 offset:4352\ns_waitcnt lgkmcnt(10)\nv_mfma_f32_32x32x16_bf16 v[80:95], v[208:211], v[144:147], v[80:95]\n"
    "ds_read_b64_tr_b16 v[208:209], v118 offset:2048\nds_read_b64_tr_b16 v[210:211], v118 offset:2304\nds_read_b64_tr_b16 v[164:165], v29 offset:5120\n"
    "ds_read_b64_tr_b16 v[166:167], v29 offset:5376\nv_mfma_f32_32x32x16_bf16 v[80:95], v[212:215], v[148:151], v[80:95]\nds_read_b64_tr_b16 v[212:213], v118 offset:3072\n"
    "ds_read_b64_tr_b16 v[214:215], v118 offset:3328\nds_read_b64_tr_b16 v[168:169], v29 offset:6144\nds_read_b64_tr_b16 v[170:171], v29 offset:6400\ns_waitcnt lgkmcnt(14)\n"
    "v_mfma_f32_32x32x16_bf16 v[80:95], v[192:195], v[152:155], v[80:95]\nds_read_b64_tr_b16 v[192:193], v117 offset:0\nds_read_b64_tr_b16 v[194:195], v117 offset:256\n"
    "ds_read_b64_tr_b16 v[172:173], v29 offset:7168\nds_read_b64_tr_b16 v[174:175], v29 offset:7424\nv_mfma_f32_32x32x16_bf16 v[80:95], v[196:199], v[156:159], v[80:95]\n"
    "ds_read_b64_tr_b16 v[196:197], v117 offset:1024\nds_read_b64_tr_b16 v[198:199], v117 offset:1280\ns_waitcnt lgkmcnt(10)\nv_pk_maximum3_f16 v138, v160, v161, v162\n"
    "v_pk_maximum3_f16 v139, v163, v164, v165\nv_mfma_f32_32x32x16_bf16 v[96:111], v[200:203], v[144:147], v[96:111]\nds_read_b64_tr_b16 v[200:201], v117 offset:2048\n"
    "ds_read_b64_tr_b16 v[202:203], v117 offset:2304\nv_pk_maximum3_f16 v138, v138, v166, v167\ns_waitcnt lgkmcnt(4)\nv_pk_maximum3_f16 v139, v139, v168, v169\n"
    "v_mfma_f32_32x32x16_bf16 v[96:111], v[204:207], v[148:151], v[96:111]\nds_read_b64_tr_b16 v[204:205], v117 offset:3072\nds_read_b64_tr_b16 v[206:207], v117 offset:3328\n"
    "v_pk_maximum3_f16 v138, v138, v170, v171\nv_pk_maximum3_f16 v139, v139, v172, v173\nv_mfma_f32_32x32x16_bf16 v[96:111], v[208:211], v[152:155], v[96:111]\n"
    "ds_read_b64_tr_b16 v[208:209], v117 offset:32768\nds_read_b64_tr_b16 v[210:211], v117 offset:33024\nv_pk_maximum3_f16 v138, v138, v174, v175\n"
    "v_pk_maximum3_f16 v138, v138, v139, v139\nv_mfma_f32_32x32x16_bf16 v[96:111], v[212:215], v[156:159], v[96:111]\nds_read_b64_tr_b16 v[212:213], v117 offset:33792\n"
    "ds_read_b64_tr_b16 v[214:215], v117 offset:34048\nv_cmp_u_f16_sdwa vcc, v138, v138 src0_sel:WORD_0 src1_sel:WORD_1\ns_cbranch_vccnz .Lxs176\n.Lxr177:\n"
    "s_waitcnt lgkmcnt(8)\nv_mfma_f32_32x32x16_bf16 v[64:79], v[192:195], v[160:163], v[64:79]\nds_read_b64_tr_b16 v[192:193], v117 offset:34816\n"
    "ds_read_b64_tr_b16 v[194:195], v117 offset:35072\nv_mfma_f32_32x32x16_bf16 v[64:79], v[196:199], v[164:167], v[64:79]\nds_read_b64_tr_b16 v[196:197], v117 offset:35840\n"
    "ds_read_b64_tr_b16 v[198:199], v117 offset:36096\ns_waitcnt lgkmcnt(8)\nv_mfma_f32_32x32x16_bf16 v[64:79], v[200:203], v[168:171], v[64:79]\n"
    "ds_read_b64_tr_b16 v[200:201], v119 offset:0\nds_read_b64_tr_b16 v[202:203], v119 offset:256\nv_mfma_f32_32x32x16_bf16 v[64:79], v[204:207], v[172:175], v[64:79]\n"
    "ds_read_b64_tr_b16 v[204:205], v119 offset:1024\nds_read_b64_tr_b16 v[206:207], v119 offset:1280\ns_waitcnt lgkmcnt(8)\n"
    "v_mfma_f32_32x32x16_bf16 v[80:95], v[208:211], v[160:163], v[80:95]\nds_read_b64_tr_b16 v[208:209], v119 offset:2048\nds_read_b64_tr_b16 v[210:211], v119 offset:2304\n"
    "s_barrier\nds_read_b32 v120, v36\nds_read_b32 v137, v36 offset:4\nv_mfma_f32_32x32x16_bf16 v[80:95], v[212:215], v[164:167], v[80:95]\n"
    "ds_read_b64_tr_b16 v[212:213], v119 offset:3072\nds_read_b64_tr_b16 v[214:215], v119 offset:3328\ns_waitcnt lgkmcnt(10)\n"
    "v_mfma_f32_32x32x16_bf16 v[80:95], v[192:195], v[168:171], v[80:95]\nv_mfma_f32_32x32x16_bf16 v[80:95], v[196:199], v[172:175], v[80:95]\ns_waitcnt lgkmcnt(2)\n"
    "v_mfma_f32_32x32x16_bf16 v[64:79], v[124:127], v[120:123], v[64:79]\nv_mfma_f32_32x32x16_bf16 v[80:95], v[128:131], v[120:123], v[80:95]\n"
    "v_mfma_f32_32x32x16_bf16 v[96:111], v[200:203], v[160:163], v[96:111]\nv_mfma_f32_32x32x16_bf16 v[96:111], v[204:207], v[164:167], v[96:111]\ns_nop 8\n"
    "v_mul_f32 v64, v64, v137\nv_mul_f32 v65, v65, v137\nv_mul_f32 v66, v66, v137\nv_mul_f32 v67, v67, v137\nv_mul_f32 v68, v68, v137\nv_mul_f32 v69, v69, v137\n"
    "s_waitcnt lgkmcnt(0)\nv_mfma_f32_32x32x16_bf16 v[96:111], v[208:211], v[168:171], v[96:111]\nv_mul_f32 v70, v70, v137\nv_mul_f32 v71, v71, v137\n"
    "v_cvt_pk_bf16_f32 v64, v64, v65\nv_cvt_pk_bf16_f32 v65, v66, v67\nv_cvt_pk_bf16_f32 v66, v68, v69\nv_cvt_pk_bf16_f32 v67, v70, v71\n"
    "v_mfma_f32_32x32x16_bf16 v[96:111], v[212:215], v[172:175], v[96:111]\nds_write_b128 v27, v[64:67] offset:0\nv_mul_f32 v72, v72, v137\nv_mul_f32 v73, v73, v137\n"
    "v_mul_f32 v74, v74, v137\nv_mul_f32 v75, v75, v137\nv_mul_f32 v76, v76, v137\nv_mfma_f32_32x32x16_bf16 v[96:111], v[132:135], v[120:123], v[96:111]\n"
    "v_mul_f32 v77, v77, v137\nv_mul_f32 v78, v78, v137\nv_mul_f32 v79, v79, v137\nv_cvt_pk_bf16_f32 v72, v72, v73\nv_cvt_pk_bf16_f32 v73, v74, v75\n"
    "v_cvt_pk_bf16_f32 v74, v76, v77\nv_cvt_pk_bf16_f32 v75, v78, v79\nds_write_b128 v27, v[72:75] offset:32\nv_pk_mul_f32 v[80:81], v[80:81], v[136:137] op_sel:[0,1]\n"
    "v_pk_mul_f32 v[82:83], v[82:83], v[136:137] op_sel:[0,1]\nv_pk_mul_f32 v[84:85], v[84:85], v[136:137] op_sel:[0,1]\n"
    "v_pk_mul_f32 v[86:87], v[86:87], v[136:137] op_sel:[0,1]\nv_cvt_pk_bf16_f32 v80, v80, v81\nv_cvt_pk_bf16_f32 v81, v82, v83\nv_cvt_pk_bf16_f32 v82, v84, v85\n"
    "v_cvt_pk_bf16_f32 v83, v86, v87\nds_write_b128 v27, v[80:83] offset:64\nv_pk_mul_f32 v[88:89], v[88:89], v[136:137] op_sel:[0,1]\n"
    "v_pk_mul_f32 v[90:91], v[90:91], v[136:137] op_sel:[0,1]\nv_pk_mul_f32 v[92:93], v[92:93], v[136:137] op_sel:[0,1]\n"
    "v_pk_mul_f32 v[94:95], v[94:95], v[136:137] op_sel:[0,1]\nv_cvt_pk_bf16_f32 v88, v88, v89\nv_cvt_pk_bf16_f32 v89, v90, v91\nv_cvt_pk_bf16_f32 v90, v92, v93\n"
    "v_cvt_pk_bf16_f32 v91, v94, v95\nds_write_b128 v27, v[88:91] offset:96\nv_pk_mul_f32 v[96:97], v[96:97], v[136:137] op_sel:[0,1]\n"
    "v_pk_mul_f32 v[98:99], v[98:99], v[136:137] op_sel:[0,1]\nv_pk_mul_f32 v[100:101], v[100:101], v[136:137] op_sel:[0,1]\n"
    "v_pk_mul_f32 v[102:103], v[102:103], v[136:137] op_sel:[0,1]\nv_cvt_pk_bf16_f32 v96, v96, v97\nv_cvt_pk_bf16_f32 v97, v98, v99\nv_cvt_pk_bf16_f32 v98, v100, v101\n"
    "v_cvt_pk_bf16_f32 v99, v102, v103\nds_write_b128 v27, v[96:99] offset:128\nv_pk_mul_f32 v[104:105], v[104:105], v[136:137] op_sel:[0,1]\n"
    "v_pk_mul_f32 v[106:107], v[106:107], v[136:137] op_sel:[0,1]\nv_pk_mul_f32 v[108:109], v[108:109], v[136:137] op_sel:[0,1]\n"
    "v_pk_mul_f32 v[110:111], v[110:111], v[136:137] op_sel:[0,1]\nv_cvt_pk_bf16_f32 v104, v104, v105\nv_cvt_pk_bf16_f32 v105, v106, v107\nv_cvt_pk_bf16_f32 v106, v108, v109\n"
    "v_cvt_pk_bf16_f32 v107, v110, v111\nds_write_b128 v27, v[104:107] offset:160\ns_branch .LGL77\n.Lls36:\ns_sub_u32 s80, s80, 1\ns_cbranch_scc1 .Llo35\n"
    "s_mov_b64 s[82:83], 0\nv_max3_u32 v60, v208, v209, v210\nv_max_u32 v60, v60, v211\nv_cmp_eq_u32 vcc, -1, v60\ns_nop 0\ns_or_b64 s[82:83], s[82:83], vcc\n"
    "s_cbranch_vccz .Lag37\ns_mov_b64 exec, vcc\nglobal_load_dwordx4 v[208:211], v20, s[32:33] offset:-4096 sc1\ns_mov_b64 exec, -1\n.Lag37:\nv_max3_u32 v60, v212, v213, v214\n"
    "v_max_u32 v60, v60, v215\nv_cmp_eq_u32 vcc, -1, v60\ns_nop 0\ns_or_b64 s[82:83], s[82:83], vcc\ns_cbranch_vccz .Lag38\ns_mov_b64 exec, vcc\n"
    "global_load_dwordx4 v[212:215], v22, s[32:33] offset:-4096 sc1\ns_mov_b64 exec, -1\n.Lag38:\nv_max3_u32 v60, v216, v217, v218\nv_max_u32 v60, v60, v219\n"
    "v_cmp_eq_u32 vcc, -1, v60\ns_nop 0\ns_or_b64 s[82:83], s[82:83], vcc\ns_cbranch_vccz .Lag39\ns_mov_b64 exec, vcc\n"
    "global_load_dwordx4 v[216:219], v23, s[32:33] offset:-4096 sc1\ns_mov_b64 exec, -1\n.Lag39:\nv_max3_u32 v60, v220, v221, v222\nv_max_u32 v60, v60, v223\n"
    "v_cmp_eq_u32 vcc, -1, v60\ns_nop 0\ns_or_b64 s[82:83], s[82:83], vcc\ns_cbranch_vccz .Lag40\ns_mov_b64 exec, vcc\n"
    "global_load_dwordx4 v[220:223], v24, s[32:33] offset:-4096 sc1\ns_mov_b64 exec, -1\n.Lag40:\nv_max3_u32 v60, v224, v225, v226\nv_max_u32 v60, v60, v227\n"
    "v_cmp_eq_u32 vcc, -1, v60\ns_nop 0\ns_or_b64 s[82:83], s[82:83], vcc\ns_cbranch_vccz .Lag41\ns_mov_b64 exec, vcc\n"
    "global_load_dwordx4 v[224:227], v25, s[32:33] offset:-4096 sc1\ns_mov_b64 exec, -1\n.Lag41:\nv_max3_u32 v60, v228, v229, v230\nv_max_u32 v60, v60, v231\n"
    "v_cmp_eq_u32 vcc, -1, v60\ns_nop 0\ns_or_b64 s[82:83], s[82:83], vcc\ns_cbranch_vccz .Lag42\ns_mov_b64 exec, vcc\n"
    "global_load_dwordx4 v[228:231], v26, s[32:33] offset:-4096 sc1\ns_mov_b64 exec, -1\n.Lag42:\nv_max3_u32 v60, v232, v233, v234\nv_max_u32 v60, v60, v235\n"
    "v_cmp_eq_u32 vcc, -1, v60\ns_nop 0\ns_or_b64 s[82:83], s[82:83], vcc\ns_cbranch_vccz .Lag43\ns_mov_b64 exec, vcc\n"
    "global_load_dwordx4 v[232:235], v27, s[32:33] offset:-4096 sc1\ns_mov_b64 exec, -1\n.Lag43:\nv_max3_u32 v60, v236, v237, v238\nv_max_u32 v60, v60, v239\n"
    "v_cmp_eq_u32 vcc, -1, v60\ns_nop 0\ns_or_b64 s[82:83], s[82:83], vcc\ns_cbranch_vccz .Lag44\ns_mov_b64 exec, vcc\n"
    "global_load_dwordx4 v[236:239], v28, s[32:33] offset:-4096 sc1\ns_mov_b64 exec, -1\n.Lag44:\nv_max3_u32 v60, v240, v241, v242\nv_max_u32 v60, v60, v243\n"
    "v_cmp_eq_u32 vcc, -1, v60\ns_nop 0\ns_or_b64 s[82:83], s[82:83], vcc\ns_cbranch_vccz .Lag45\ns_mov_b64 exec, vcc\nglobal_load_dwordx4 v[240:243], v21, s[32:33] sc1\n"
    "s_mov_b64 exec, -1\n.Lag45:\ns_cmp_eq_u64 s[82:83], 0\ns_cbranch_scc1 .Llo35\ns_waitcnt vmcnt(0)\ns_branch .Lln34\n.Lxs60:\ns_mov_b32 s80, 0x10000\n.Lxl62:\n"
    "v_pk_max_u16 v62, v196, v197\nv_pk_max_u16 v62, v62, v198\nv_pk_max_u16 v62, v62, v199\nv_pk_max_u16 v62, v62, v200\nv_pk_max_u16 v62, v62, v201\n"
    "v_pk_max_u16 v62, v62, v202\nv_pk_max_u16 v62, v62, v203\nv_pk_max_u16 v62, v62, v204\nv_pk_max_u16 v62, v62, v205\nv_pk_max_u16 v62, v62, v206\n"
    "v_pk_max_u16 v62, v62, v207\nv_pk_max_u16 v62, v62, v208\nv_pk_max_u16 v62, v62, v209\nv_pk_max_u16 v62, v62, v210\nv_pk_max_u16 v62, v62, v211\n"
    "v_pk_max_u16 v62, v62, v62 op_sel:[0,1] op_sel_hi:[1,0]\nv_cmp_eq_u32 vcc, -1, v62\ns_cbranch_vccz .Lxr61\ns_sub_u32 s80, s80, 1\ns_cbranch_scc1 .Lxr61\ns_sleep 2\n"
    "s_add_u32 m0, s65, 1024\ns_nop 0\nglobal_load_lds_dwordx4 v27, s[60:61] offset:0 sc1\ns_add_u32 m0, s65, 0\ns_nop 0\nglobal_load_lds_dwordx4 v27, s[60:61] offset:2048 sc1\n"
    "s_add_u32 m0, s65, 3072\ns_nop 0\nglobal_load_lds_dwordx4 v9, s[60:61] offset:0 sc1\ns_add_u32 m0, s65, 2048\ns_nop 0\n"
    "global_load_lds_dwordx4 v9, s[60:61] offset:2048 sc1\ns_waitcnt vmcnt(0)\nds_read_b64_tr_b16 v[196:197], v28 offset:0\nds_read_b64_tr_b16 v[198:199], v28 offset:128\n"
    "ds_read_b64_tr_b16 v[200:201], v28 offset:1024\nds_read_b64_tr_b16 v[202:203], v28 offset:1152\nds_read_b64_tr_b16 v[204:205], v28 offset:2048\n"
    "ds_read_b64_tr_b16 v[206:207], v28 offset:2176\nds_read_b64_tr_b16 v[208:209], v28 offset:3072\nds_read_b64_tr_b16 v[210:211], v28 offset:3200\ns_waitcnt lgkmcnt(0)\n"
    "s_branch .Lxl62\n.Lps63:\ns_sub_u32 s80, s80, 1\ns_cbranch_scc1 .Lpr64\ns_mov_b64 s[82:83], 0\nv_max3_u32 v229, v212, v213, v214\nv_max_u32 v229, v229, v215\n"
    "v_cmp_eq_u32 vcc, -1, v229\ns_nop 0\ns_or_b64 s[82:83], s[82:83], vcc\ns_cbranch_vccz .Lag66\ns_mov_b64 exec, vcc\n"
    "global_load_dwordx4 v[212:215], v36, s[32:33] offset:0 sc1\ns_mov_b64 exec, -1\n.Lag66:\nv_max3_u32 v229, v216, v217, v218\nv_max_u32 v229, v229, v219\n"
    "v_cmp_eq_u32 vcc, -1, v229\ns_nop 0\ns_or_b64 s[82:83], s[82:83], vcc\ns_cbranch_vccz .Lag67\ns_mov_b64 exec, vcc\n"
    "global_load_dwordx4 v[216:219], v36, s[32:33] offset:1024 sc1\ns_mov_b64 exec, -1\n.Lag67:\ns_cmp_eq_u64 s[82:83], 0\ns_cbranch_scc1 .Lpr64\ns_waitcnt vmcnt(0)\n"
    "s_branch .Lpl65\n.Lps68:\ns_sub_u32 s80, s80, 1\ns_cbranch_scc1 .Lpr69\ns_mov_b64 s[82:83], 0\nv_max3_u32 v233, v220, v221, v222\nv_max_u32 v233, v233, v223\n"
    "v_cmp_eq_u32 vcc, -1, v233\ns_nop 0\ns_or_b64 s[82:83], s[82:83], vcc\ns_cbranch_vccz .Lag71\ns_mov_b64 exec, vcc\n"
    "global_load_dwordx4 v[220:223], v37, s[32:33] offset:0 sc1\ns_mov_b64 exec, -1\n.Lag71:\nv_max3_u32 v233, v224, v225, v226\nv_max_u32 v233, v233, v227\n"
    "v_cmp_eq_u32 vcc, -1, v233\ns_nop 0\ns_or_b64 s[82:83], s[82:83], vcc\ns_cbranch_vccz .Lag72\ns_mov_b64 exec, vcc\n"
    "global_load_dwordx4 v[224:227], v37, s[32:33] offset:1024 sc1\ns_mov_b64 exec, -1\n.Lag72:\ns_cmp_eq_u64 s[82:83], 0\ns_cbranch_scc1 .Lpr69\ns_waitcnt vmcnt(0)\n"
    "s_branch .Lpl70\n.LL87:\ns_sub_u32 s80, s80, 1\ns_cbranch_scc1 .LL86\ns_waitcnt vmcnt(0) lgkmcnt(0)\nv_max3_u32 v31, v208, v209, v210\nv_max_u32 v31, v31, v211\n"
    "v_cmp_eq_u32 vcc, -1, v31\ns_nop 0\ns_or_b64 s[82:83], s[82:83], vcc\ns_cbranch_vccz .Lag88\ns_mov_b64 exec, vcc\n"
    "global_load_dwordx4 v[208:211], v182, s[32:33] offset:-4096 sc1\ns_mov_b64 exec, -1\n.Lag88:\nv_max3_u32 v31, v212, v213, v214\nv_max_u32 v31, v31, v215\n"
    "v_cmp_eq_u32 vcc, -1, v31\ns_nop 0\ns_or_b64 s[82:83], s[82:83], vcc\ns_cbranch_vccz .Lag89\ns_mov_b64 exec, vcc\n"
    "global_load_dwordx4 v[212:215], v182, s[32:33] offset:0 sc1\ns_mov_b64 exec, -1\n.Lag89:\nv_max3_u32 v31, v216, v217, v218\nv_max_u32 v31, v31, v219\n"
    "v_cmp_eq_u32 vcc, -1, v31\ns_nop 0\ns_or_b64 s[82:83], s[82:83], vcc\ns_cbranch_vccz .Lag90\ns_mov_b64 exec, vcc\n"
    "global_load_dwordx4 v[216:219], v183, s[32:33] offset:-4096 sc1\ns_mov_b64 exec, -1\n.Lag90:\nv_max3_u32 v31, v220, v221, v222\nv_max_u32 v31, v31, v223\n"
    "v_cmp_eq_u32 vcc, -1, v31\ns_nop 0\ns_or_b64 s[82:83], s[82:83], vcc\ns_cbranch_vccz .Lag91\ns_mov_b64 exec, vcc\n"
    "global_load_dwordx4 v[220:223], v183, s[32:33] offset:0 sc1\ns_mov_b64 exec, -1\n.Lag91:\nv_max3_u32 v31, v224, v225, v226\nv_max_u32 v31, v31, v227\n"
    "v_cmp_eq_u32 vcc, -1, v31\ns_nop 0\ns_or_b64 s[82:83], s[82:83], vcc\ns_cbranch_vccz .Lag92\ns_mov_b64 exec, vcc\n"
    "global_load_dwordx4 v[224:227], v184, s[32:33] offset:-4096 sc1\ns_mov_b64 exec, -1\n.Lag92:\nv_max3_u32 v31, v228, v229, v230\nv_max_u32 v31, v31, v231\n"
    "v_cmp_eq_u32 vcc, -1, v31\ns_nop 0\ns_or_b64 s[82:83], s[82:83], vcc\ns_cbranch_vccz .Lag93\ns_mov_b64 exec, vcc\n"
    "global_load_dwordx4 v[228:231], v184, s[32:33] offset:0 sc1\ns_mov_b64 exec, -1\n.Lag93:\nv_max3_u32 v31, v232, v233, v234\nv_max_u32 v31, v31, v235\n"
    "v_cmp_eq_u32 vcc, -1, v31\ns_nop 0\ns_or_b64 s[82:83], s[82:83], vcc\ns_cbranch_vccz .Lag94\ns_mov_b64 exec, vcc\n"
    "global_load_dwordx4 v[232:235], v185, s[32:33] offset:-4096 sc1\ns_mov_b64 exec, -1\n.Lag94:\nv_max3_u32 v31, v236, v237, v238\nv_max_u32 v31, v31, v239\n"
    "v_cmp_eq_u32 vcc, -1, v31\ns_nop 0\ns_or_b64 s[82:83], s[82:83], vcc\ns_cbranch_vccz .Lag95\ns_mov_b64 exec, vcc\n"
    "global_load_dwordx4 v[236:239], v185, s[32:33] offset:0 sc1\ns_mov_b64 exec, -1\n.Lag95:\ns_waitcnt vmcnt(0)\ns_branch .LL85\n.LL98:\ns_sub_u32 s80, s80, 1\n"
    "s_cbranch_scc1 .LL97\ns_waitcnt vmcnt(0) lgkmcnt(0)\nv_max3_u32 v31, v216, v217, v218\nv_max_u32 v31, v31, v219\nv_cmp_eq_u32 vcc, -1, v31\ns_nop 0\n"
    "s_or_b64 s[82:83], s[82:83], vcc\ns_cbranch_vccz .Lag99\ns_mov_b64 exec, vcc\nglobal_load_dwordx4 v[216:219], v183, s[32:33] offset:-4096 sc1\ns_mov_b64 exec, -1\n.Lag99:\n"
    "v_max3_u32 v31, v220, v221, v222\nv_max_u32 v31, v31, v223\nv_cmp_eq_u32 vcc, -1, v31\ns_nop 0\ns_or_b64 s[82:83], s[82:83], vcc\ns_cbranch_vccz .Lag100\n"
    "s_mov_b64 exec, vcc\nglobal_load_dwordx4 v[220:223], v183, s[32:33] offset:0 sc1\ns_mov_b64 exec, -1\n.Lag100:\nv_max3_u32 v31, v224, v225, v226\nv_max_u32 v31, v31, v227\n"
    "v_cmp_eq_u32 vcc, -1, v31\ns_nop 0\ns_or_b64 s[82:83], s[82:83], vcc\ns_cbranch_vccz .Lag101\ns_mov_b64 exec, vcc\n"
    "global_load_dwordx4 v[224:227], v184, s[32:33] offset:-4096 sc1\ns_mov_b64 exec, -1\n.Lag101:\nv_max3_u32 v31, v228, v229, v230\nv_max_u32 v31, v31, v231\n"
    "v_cmp_eq_u32 vcc, -1, v31\ns_nop 0\ns_or_b64 s[82:83], s[82:83], vcc\ns_cbranch_vccz .Lag102\ns_mov_b64 exec, vcc\n"
    "global_load_dwordx4 v[228:231], v184, s[32:33] offset:0 sc1\ns_mov_b64 exec, -1\n.Lag102:\nv_max3_u32 v31, v232, v233, v234\nv_max_u32 v31, v31, v235\n"
    "v_cmp_eq_u32 vcc, -1, v31\ns_nop 0\ns_or_b64 s[82:83], s[82:83], vcc\ns_cbranch_vccz .Lag103\ns_mov_b64 exec, vcc\n"
    "global_load_dwordx4 v[232:235], v185, s[32:33] offset:-4096 sc1\ns_mov_b64 exec, -1\n.Lag103:\nv_max3_u32 v31, v236, v237, v238\nv_max_u32 v31, v31, v239\n"
    "v_cmp_eq_u32 vcc, -1, v31\ns_nop 0\ns_or_b64 s[82:83], s[82:83], vcc\ns_cbranch_vccz .Lag104\ns_mov_b64 exec, vcc\n"
    "global_load_dwordx4 v[236:239], v185, s[32:33] offset:0 sc1\ns_mov_b64 exec, -1\n.Lag104:\ns_waitcnt vmcnt(0)\ns_branch .LL96\n.LL107:\ns_sub_u32 s80, s80, 1\n"
    "s_cbranch_scc1 .LL106\ns_waitcnt vmcnt(0) lgkmcnt(0)\nv_max3_u32 v31, v224, v225, v226\nv_max_u32 v31, v31, v227\nv_cmp_eq_u32 vcc, -1, v31\ns_nop 0\n"
    "s_or_b64 s[82:83], s[82:83], vcc\ns_cbranch_vccz .Lag108\ns_mov_b64 exec, vcc\nglobal_load_dwordx4 v[224:227], v184, s[32:33] offset:-4096 sc1\ns_mov_b64 exec, -1\n"
    ".Lag108:\nv_max3_u32 v31, v228, v229, v230\nv_max_u32 v31, v31, v231\nv_cmp_eq_u32 vcc, -1, v31\ns_nop 0\ns_or_b64 s[82:83], s[82:83], vcc\ns_cbranch_vccz .Lag109\n"
    "s_mov_b64 exec, vcc\nglobal_load_dwordx4 v[228:231], v184, s[32:33] offset:0 sc1\ns_mov_b64 exec, -1\n.Lag109:\nv_max3_u32 v31, v232, v233, v234\nv_max_u32 v31, v31, v235\n"
    "v_cmp_eq_u32 vcc, -1, v31\ns_nop 0\ns_or_b64 s[82:83], s[82:83], vcc\ns_cbranch_vccz .Lag110\ns_mov_b64 exec, vcc\n"
    "global_load_dwordx4 v[232:235], v185, s[32:33] offset:-4096 sc1\ns_mov_b64 exec, -1\n.Lag110:\nv_max3_u32 v31, v236, v237, v238\nv_max_u32 v31, v31, v239\n"
    "v_cmp_eq_u32 vcc, -1, v31\ns_nop 0\ns_or_b64 s[82:83], s[82:83], vcc\ns_cbranch_vccz .Lag111\ns_mov_b64 exec, vcc\n"
    "global_load_dwordx4 v[236:239], v185, s[32:33] offset:0 sc1\ns_mov_b64 exec, -1\n.Lag111:\ns_waitcnt vmcnt(0)\ns_branch .LL105\n.LL114:\ns_sub_u32 s80, s80, 1\n"
    "s_cbranch_scc1 .LL113\ns_waitcnt vmcnt(0) lgkmcnt(0)\nv_max3_u32 v31, v232, v233, v234\nv_max_u32 v31, v31, v235\nv_cmp_eq_u32 vcc, -1, v31\ns_nop 0\n"
    "s_or_b64 s[82:83], s[82:83], vcc\ns_cbranch_vccz .Lag115\ns_mov_b64 exec, vcc\nglobal_load_dwordx4 v[232:235], v185, s[32:33] offset:-4096 sc1\ns_mov_b64 exec, -1\n"
    ".Lag115:\nv_max3_u32 v31, v236, v237, v238\nv_max_u32 v31, v31, v239\nv_cmp_eq_u32 vcc, -1, v31\ns_nop 0\ns_or_b64 s[82:83], s[82:83], vcc\ns_cbranch_vccz .Lag116\n"
    "s_mov_b64 exec, vcc\nglobal_load_dwordx4 v[236:239], v185, s[32:33] offset:0 sc1\ns_mov_b64 exec, -1\n.Lag116:\ns_waitcnt vmcnt(0)\ns_branch .LL112\n.Lls124:\n"
    "s_sub_u32 s80, s80, 1\ns_cbranch_scc1 .Llo123\ns_mov_b64 s[82:83], 0\nv_max3_u32 v60, v208, v209, v210\nv_max_u32 v60, v60, v211\nv_cmp_eq_u32 vcc, -1, v60\ns_nop 0\n"
    "s_or_b64 s[82:83], s[82:83], vcc\ns_cbranch_vccz .Lag125\ns_mov_b64 exec, vcc\nglobal_load_dwordx4 v[208:211], v20, s[32:33] offset:-4096 sc1\ns_mov_b64 exec, -1\n"
    ".Lag125:\nv_max3_u32 v60, v212, v213, v214\nv_max_u32 v60, v60, v215\nv_cmp_eq_u32 vcc, -1, v60\ns_nop 0\ns_or_b64 s[82:83], s[82:83], vcc\ns_cbranch_vccz .Lag126\n"
    "s_mov_b64 exec, vcc\nglobal_load_dwordx4 v[212:215], v22, s[32:33] offset:-4096 sc1\ns_mov_b64 exec, -1\n.Lag126:\nv_max3_u32 v60, v216, v217, v218\n"
    "v_max_u32 v60, v60, v219\nv_cmp_eq_u32 vcc, -1, v60\ns_nop 0\ns_or_b64 s[82:83], s[82:83], vcc\ns_cbranch_vccz .Lag127\ns_mov_b64 exec, vcc\n"
    "global_load_dwordx4 v[216:219], v23, s[32:33] offset:-4096 sc1\ns_mov_b64 exec, -1\n.Lag127:\nv_max3_u32 v60, v220, v221, v222\nv_max_u32 v60, v60, v223\n"
    "v_cmp_eq_u32 vcc, -1, v60\ns_nop 0\ns_or_b64 s[82:83], s[82:83], vcc\ns_cbranch_vccz .Lag128\ns_mov_b64 exec, vcc\n"
    "global_load_dwordx4 v[220:223], v24, s[32:33] offset:-4096 sc1\ns_mov_b64 exec, -1\n.Lag128:\nv_max3_u32 v60, v224, v225, v226\nv_max_u32 v60, v60, v227\n"
    "v_cmp_eq_u32 vcc, -1, v60\ns_nop 0\ns_or_b64 s[82:83], s[82:83], vcc\ns_cbranch_vccz .Lag129\ns_mov_b64 exec, vcc\n"
    "global_load_dwordx4 v[224:227], v25, s[32:33] offset:-4096 sc1\ns_mov_b64 exec, -1\n.Lag129:\nv_max3_u32 v60, v228, v229, v230\nv_max_u32 v60, v60, v231\n"
    "v_cmp_eq_u32 vcc, -1, v60\ns_nop 0\ns_or_b64 s[82:83], s[82:83], vcc\ns_cbranch_vccz .Lag130\ns_mov_b64 exec, vcc\n"
    "global_load_dwordx4 v[228:231], v26, s[32:33] offset:-4096 sc1\ns_mov_b64 exec, -1\n.Lag130:\nv_max3_u32 v60, v232, v233, v234\nv_max_u32 v60, v60, v235\n"
    "v_cmp_eq_u32 vcc, -1, v60\ns_nop 0\ns_or_b64 s[82:83], s[82:83], vcc\ns_cbranch_vccz .Lag131\ns_mov_b64 exec, vcc\n"
    "global_load_dwordx4 v[232:235], v27, s[32:33] offset:-4096 sc1\ns_mov_b64 exec, -1\n.Lag131:\nv_max3_u32 v60, v236, v237, v238\nv_max_u32 v60, v60, v239\n"
    "v_cmp_eq_u32 vcc, -1, v60\ns_nop 0\ns_or_b64 s[82:83], s[82:83], vcc\ns_cbranch_vccz .Lag132\ns_mov_b64 exec, vcc\n"
    "global_load_dwordx4 v[236:239], v28, s[32:33] offset:-4096 sc1\ns_mov_b64 exec, -1\n.Lag132:\nv_max3_u32 v60, v240, v241, v242\nv_max_u32 v60, v60, v243\n"
    "v_cmp_eq_u32 vcc, -1, v60\ns_nop 0\ns_or_b64 s[82:83], s[82:83], vcc\ns_cbranch_vccz .Lag133\ns_mov_b64 exec, vcc\nglobal_load_dwordx4 v[240:243], v21, s[32:33] sc1\n"
    "s_mov_b64 exec, -1\n.Lag133:\ns_cmp_eq_u64 s[82:83], 0\ns_cbranch_scc1 .Llo123\ns_waitcnt vmcnt(0)\ns_branch .Lln122\n.Lfs147:\ns_mov_b32 s80, 0x100000\n.Lfl149:\n"
    "s_sleep 1\nds_read_b32 v241, v7\ns_waitcnt lgkmcnt(0)\nv_cmp_gt_i32 vcc, v13, v241\ns_cbranch_vccz .Lfr148\ns_sub_u32 s80, s80, 1\ns_cbranch_scc0 .Lfl149\n"
    "s_branch .Lfr148\n.Lxs155:\nv_pk_max_u16 v138, v144, v145\nv_pk_max_u16 v138, v138, v146\nv_pk_max_u16 v138, v138, v147\nv_pk_max_u16 v138, v138, v148\n"
    "v_pk_max_u16 v138, v138, v149\nv_pk_max_u16 v138, v138, v150\nv_pk_max_u16 v138, v138, v151\nv_pk_max_u16 v138, v138, v152\nv_pk_max_u16 v138, v138, v153\n"
    "v_pk_max_u16 v138, v138, v154\nv_pk_max_u16 v138, v138, v155\nv_pk_max_u16 v138, v138, v156\nv_pk_max_u16 v138, v138, v157\nv_pk_max_u16 v138, v138, v158\n"
    "v_pk_max_u16 v138, v138, v159\nv_pk_max_u16 v138, v138, v138 op_sel:[0,1] op_sel_hi:[1,0]\nv_cmp_eq_u32 vcc, -1, v138\ns_cbranch_vccz .Lxr156\ns_sub_u32 s80, s80, 1\n"
    "s_cbranch_scc1 .Lxr156\ns_sleep 2\ns_add_u32 m0, s87, 0\ns_add_u32 s52, s82, s44\ns_addc_u32 s53, s83, 0\nglobal_load_lds_dwordx4 v35, s[52:53] offset:0 sc1\n"
    "global_load_lds_dwordx4 v35, s[52:53] offset:1024 sc1\nglobal_load_lds_dwordx4 v35, s[52:53] offset:2048 sc1\nglobal_load_lds_dwordx4 v35, s[52:53] offset:3072 sc1\n"
    "s_waitcnt vmcnt(0)\nds_read_b64_tr_b16 v[144:145], v29 offset:0\nds_read_b64_tr_b16 v[146:147], v29 offset:256\nds_read_b64_tr_b16 v[148:149], v29 offset:1024\n"
    "ds_read_b64_tr_b16 v[150:151], v29 offset:1280\nds_read_b64_tr_b16 v[152:153], v29 offset:2048\nds_read_b64_tr_b16 v[154:155], v29 offset:2304\n"
    "ds_read_b64_tr_b16 v[156:157], v29 offset:3072\nds_read_b64_tr_b16 v[158:159], v29 offset:3328\ns_waitcnt lgkmcnt(0)\ns_branch .LL154\n.Lxs158:\ns_mov_b32 s80, 0x10000\n"
    ".Lxl160:\nv_pk_max_u16 v138, v160, v161\nv_pk_max_u16 v138, v138, v162\nv_pk_max_u16 v138, v138, v163\nv_pk_max_u16 v138, v138, v164\nv_pk_max_u16 v138, v138, v165\n"
    "v_pk_max_u16 v138, v138, v166\nv_pk_max_u16 v138, v138, v167\nv_pk_max_u16 v138, v138, v168\nv_pk_max_u16 v138, v138, v169\nv_pk_max_u16 v138, v138, v170\n"
    "v_pk_max_u16 v138, v138, v171\nv_pk_max_u16 v138, v138, v172\nv_pk_max_u16 v138, v138, v173\nv_pk_max_u16 v138, v138, v174\nv_pk_max_u16 v138, v138, v175\n"
    "v_pk_max_u16 v138, v138, v138 op_sel:[0,1] op_sel_hi:[1,0]\nv_cmp_eq_u32 vcc, -1, v138\ns_cbranch_vccz .Lxr159\ns_sub_u32 s80, s80, 1\ns_cbranch_scc1 .Lxr159\ns_sleep 2\n"
    "s_add_u32 m0, s87, 4096\ns_add_u32 s52, s82, s45\ns_addc_u32 s53, s83, 0\nglobal_load_lds_dwordx4 v35, s[52:53] offset:0 sc1\n"
    "global_load_lds_dwordx4 v35, s[52:53] offset:1024 sc1\nglobal_load_lds_dwordx4 v35, s[52:53] offset:2048 sc1\nglobal_load_lds_dwordx4 v35, s[52:53] offset:3072 sc1\n"
    "s_waitcnt vmcnt(0)\nds_read_b64_tr_b16 v[160:161], v29 offset:4096\nds_read_b64_tr_b16 v[162:163], v29 offset:4352\nds_read_b64_tr_b16 v[164:165], v29 offset:5120\n"
    "ds_read_b64_tr_b16 v[166:167], v29 offset:5376\nds_read_b64_tr_b16 v[168:169], v29 offset:6144\nds_read_b64_tr_b16 v[170:171], v29 offset:6400\n"
    "ds_read_b64_tr_b16 v[172:173], v29 offset:7168\nds_read_b64_tr_b16 v[174:175], v29 offset:7424\ns_waitcnt lgkmcnt(0)\ns_branch .Lxl160\n.Lxs161:\ns_mov_b32 s80, 0x10000\n"
    ".Lxl163:\nv_pk_max_u16 v138, v144, v145\nv_pk_max_u16 v138, v138, v146\nv_pk_max_u16 v138, v138, v147\nv_pk_max_u16 v138, v138, v148\nv_pk_max_u16 v138, v138, v149\n"
    "v_pk_max_u16 v138, v138, v150\nv_pk_max_u16 v138, v138, v151\nv_pk_max_u16 v138, v138, v152\nv_pk_max_u16 v138, v138, v153\nv_pk_max_u16 v138, v138, v154\n"
    "v_pk_max_u16 v138, v138, v155\nv_pk_max_u16 v138, v138, v156\nv_pk_max_u16 v138, v138, v157\nv_pk_max_u16 v138, v138, v158\nv_pk_max_u16 v138, v138, v159\n"
    "v_pk_max_u16 v138, v138, v138 op_sel:[0,1] op_sel_hi:[1,0]\nv_cmp_eq_u32 vcc, -1, v138\ns_cbranch_vccz .Lxr162\ns_sub_u32 s80, s80, 1\ns_cbranch_scc1 .Lxr162\ns_sleep 2\n"
    "s_add_u32 m0, s87, 8192\ns_add_u32 s52, s82, s46\ns_addc_u32 s53, s83, 0\nglobal_load_lds_dwordx4 v35, s[52:53] offset:0 sc1\n"
    "global_load_lds_dwordx4 v35, s[52:53] offset:1024 sc1\nglobal_load_lds_dwordx4 v35, s[52:53] offset:2048 sc1\nglobal_load_lds_dwordx4 v35, s[52:53] offset:3072 sc1\n"
    "s_waitcnt vmcnt(0)\nds_read_b64_tr_b16 v[144:145], v29 offset:8192\nds_read_b64_tr_b16 v[146:147], v29 offset:8448\nds_read_b64_tr_b16 v[148:149], v29 offset:9216\n"
    "ds_read_b64_tr_b16 v[150:151], v29 offset:9472\nds_read_b64_tr_b16 v[152:153], v29 offset:10240\nds_read_b64_tr_b16 v[154:155], v29 offset:10496\n"
    "ds_read_b64_tr_b16 v[156:157], v29 offset:11264\nds_read_b64_tr_b16 v[158:159], v29 offset:11520\ns_waitcnt lgkmcnt(0)\ns_branch .Lxl163\n.Lxs164:\ns_mov_b32 s80, 0x10000\n"
    ".Lxl166:\nv_pk_max_u16 v138, v160, v161\nv_pk_max_u16 v138, v138, v162\nv_pk_max_u16 v138, v138, v163\nv_pk_max_u16 v138, v138, v164\nv_pk_max_u16 v138, v138, v165\n"
    "v_pk_max_u16 v138, v138, v166\nv_pk_max_u16 v138, v138, v167\nv_pk_max_u16 v138, v138, v168\nv_pk_max_u16 v138, v138, v169\nv_pk_max_u16 v138, v138, v170\n"
    "v_pk_max_u16 v138, v138, v171\nv_pk_max_u16 v138, v138, v172\nv_pk_max_u16 v138, v138, v173\nv_pk_max_u16 v138, v138, v174\nv_pk_max_u16 v138, v138, v175\n"
    "v_pk_max_u16 v138, v138, v138 op_sel:[0,1] op_sel_hi:[1,0]\nv_cmp_eq_u32 vcc, -1, v138\ns_cbranch_vccz .Lxr165\ns_sub_u32 s80, s80, 1\ns_cbranch_scc1 .Lxr165\ns_sleep 2\n"
    "s_add_u32 m0, s87, 0\ns_add_u32 s52, s82, s47\ns_addc_u32 s53, s83, 0\nglobal_load_lds_dwordx4 v35, s[52:53] offset:0 sc1\n"
    "global_load_lds_dwordx4 v35, s[52:53] offset:1024 sc1\nglobal_load_lds_dwordx4 v35, s[52:53] offset:2048 sc1\nglobal_load_lds_dwordx4 v35, s[52:53] offset:3072 sc1\n"
    "s_waitcnt vmcnt(0)\nds_read_b64_tr_b16 v[160:161], v29 offset:0\nds_read_b64_tr_b16 v[162:163], v29 offset:256\nds_read_b64_tr_b16 v[164:165], v29 offset:1024\n"
    "ds_read_b64_tr_b16 v[166:167], v29 offset:1280\nds_read_b64_tr_b16 v[168:169], v29 offset:2048\nds_read_b64_tr_b16 v[170:171], v29 offset:2304\n"
    "ds_read_b64_tr_b16 v[172:173], v29 offset:3072\nds_read_b64_tr_b16 v[174:175], v29 offset:3328\ns_waitcnt lgkmcnt(0)\ns_branch .Lxl166\n.Lxs167:\ns_mov_b32 s80, 0x10000\n"
    ".Lxl169:\nv_pk_max_u16 v138, v144, v145\nv_pk_max_u16 v138, v138, v146\nv_pk_max_u16 v138, v138, v147\nv_pk_max_u16 v138, v138, v148\nv_pk_max_u16 v138, v138, v149\n"
    "v_pk_max_u16 v138, v138, v150\nv_pk_max_u16 v138, v138, v151\nv_pk_max_u16 v138, v138, v152\nv_pk_max_u16 v138, v138, v153\nv_pk_max_u16 v138, v138, v154\n"
    "v_pk_max_u16 v138, v138, v155\nv_pk_max_u16 v138, v138, v156\nv_pk_max_u16 v138, v138, v157\nv_pk_max_u16 v138, v138, v158\nv_pk_max_u16 v138, v138, v159\n"
    "v_pk_max_u16 v138, v138, v138 op_sel:[0,1] op_sel_hi:[1,0]\nv_cmp_eq_u32 vcc, -1, v138\ns_cbranch_vccz .Lxr168\ns_sub_u32 s80, s80, 1\ns_cbranch_scc1 .Lxr168\ns_sleep 2\n"
    "s_add_u32 m0, s87, 4096\ns_add_u32 s52, s82, s48\ns_addc_u32 s53, s83, 0\nglobal_load_lds_dwordx4 v35, s[52:53] offset:0 sc1\n"
    "global_load_lds_dwordx4 v35, s[52:53] offset:1024 sc1\nglobal_load_lds_dwordx4 v35, s[52:53] offset:2048 sc1\nglobal_load_lds_dwordx4 v35, s[52:53] offset:3072 sc1\n"
    "s_waitcnt vmcnt(0)\nds_read_b64_tr_b16 v[144:145], v29 offset:4096\nds_read_b64_tr_b16 v[146:147], v29 offset:4352\nds_read_b64_tr_b16 v[148:149], v29 offset:5120\n"
    "ds_read_b64_tr_b16 v[150:151], v29 offset:5376\nds_read_b64_tr_b16 v[152:153], v29 offset:6144\nds_read_b64_tr_b16 v[154:155], v29 offset:6400\n"
    "ds_read_b64_tr_b16 v[156:157], v29 offset:7168\nds_read_b64_tr_b16 v[158:159], v29 offset:7424\ns_waitcnt lgkmcnt(0)\ns_branch .Lxl169\n.Lxs170:\ns_mov_b32 s80, 0x10000\n"
    ".Lxl172:\nv_pk_max_u16 v138, v160, v161\nv_pk_max_u16 v138, v138, v162\nv_pk_max_u16 v138, v138, v163\nv_pk_max_u16 v138, v138, v164\nv_pk_max_u16 v138, v138, v165\n"
    "v_pk_max_u16 v138, v138, v166\nv_pk_max_u16 v138, v138, v167\nv_pk_max_u16 v138, v138, v168\nv_pk_max_u16 v138, v138, v169\nv_pk_max_u16 v138, v138, v170\n"
    "v_pk_max_u16 v138, v138, v171\nv_pk_max_u16 v138, v138, v172\nv_pk_max_u16 v138, v138, v173\nv_pk_max_u16 v138, v138, v174\nv_pk_max_u16 v138, v138, v175\n"
    "v_pk_max_u16 v138, v138, v138 op_sel:[0,1] op_sel_hi:[1,0]\nv_cmp_eq_u32 vcc, -1, v138\ns_cbranch_vccz .Lxr171\ns_sub_u32 s80, s80, 1\ns_cbranch_scc1 .Lxr171\ns_sleep 2\n"
    "s_add_u32 m0, s87, 8192\ns_add_u32 s52, s82, s49\ns_addc_u32 s53, s83, 0\nglobal_load_lds_dwordx4 v35, s[52:53] offset:0 sc1\n"
    "global_load_lds_dwordx4 v35, s[52:53] offset:1024 sc1\nglobal_load_lds_dwordx4 v35, s[52:53] offset:2048 sc1\nglobal_load_lds_dwordx4 v35, s[52:53] offset:3072 sc1\n"
    "s_waitcnt vmcnt(0)\nds_read_b64_tr_b16 v[160:161], v29 offset:8192\nds_read_b64_tr_b16 v[162:163], v29 offset:8448\nds_read_b64_tr_b16 v[164:165], v29 offset:9216\n"
    "ds_read_b64_tr_b16 v[166:167], v29 offset:9472\nds_read_b64_tr_b16 v[168:169], v29 offset:10240\nds_read_b64_tr_b16 v[170:171], v29 offset:10496\n"
    "ds_read_b64_tr_b16 v[172:173], v29 offset:11264\nds_read_b64_tr_b16 v[174:175], v29 offset:11520\ns_waitcnt lgkmcnt(0)\ns_branch .Lxl172\n.Lxs173:\ns_mov_b32 s80, 0x10000\n"
    ".Lxl175:\nv_pk_max_u16 v138, v144, v145\nv_pk_max_u16 v138, v138, v146\nv_pk_max_u16 v138, v138, v147\nv_pk_max_u16 v138, v138, v148\nv_pk_max_u16 v138, v138, v149\n"
    "v_pk_max_u16 v138, v138, v150\nv_pk_max_u16 v138, v138, v151\nv_pk_max_u16 v138, v138, v152\nv_pk_max_u16 v138, v138, v153\nv_pk_max_u16 v138, v138, v154\n"
    "v_pk_max_u16 v138, v138, v155\nv_pk_max_u16 v138, v138, v156\nv_pk_max_u16 v138, v138, v157\nv_pk_max_u16 v138, v138, v158\nv_pk_max_u16 v138, v138, v159\n"
    "v_pk_max_u16 v138, v138, v138 op_sel:[0,1] op_sel_hi:[1,0]\nv_cmp_eq_u32 vcc, -1, v138\ns_cbranch_vccz .Lxr174\ns_sub_u32 s80, s80, 1\ns_cbranch_scc1 .Lxr174\ns_sleep 2\n"
    "s_add_u32 m0, s87, 0\ns_add_u32 s52, s82, s50\ns_addc_u32 s53, s83, 0\nglobal_load_lds_dwordx4 v35, s[52:53] offset:0 sc1\n"
    "global_load_lds_dwordx4 v35, s[52:53] offset:1024 sc1\nglobal_load_lds_dwordx4 v35, s[52:53] offset:2048 sc1\nglobal_load_lds_dwordx4 v35, s[52:53] offset:3072 sc1\n"
    "s_waitcnt vmcnt(0)\nds_read_b64_tr_b16 v[144:145], v29 offset:0\nds_read_b64_tr_b16 v[146:147], v29 offset:256\nds_read_b64_tr_b16 v[148:149], v29 offset:1024\n"
    "ds_read_b64_tr_b16 v[150:151], v29 offset:1280\nds_read_b64_tr_b16 v[152:153], v29 offset:2048\nds_read_b64_tr_b16 v[154:155], v29 offset:2304\n"
    "ds_read_b64_tr_b16 v[156:157], v29 offset:3072\nds_read_b64_tr_b16 v[158:159], v29 offset:3328\ns_waitcnt lgkmcnt(0)\ns_branch .Lxl175\n.Lxs176:\ns_mov_b32 s80, 0x10000\n"
    ".Lxl178:\nv_pk_max_u16 v138, v160, v161\nv_pk_max_u16 v138, v138, v162\nv_pk_max_u16 v138, v138, v163\nv_pk_max_u16 v138, v138, v164\nv_pk_max_u16 v138, v138, v165\n"
    "v_pk_max_u16 v138, v138, v166\nv_pk_max_u16 v138, v138, v167\nv_pk_max_u16 v138, v138, v168\nv_pk_max_u16 v138, v138, v169\nv_pk_max_u16 v138, v138, v170\n"
    "v_pk_max_u16 v138, v138, v171\nv_pk_max_u16 v138, v138, v172\nv_pk_max_u16 v138, v138, v173\nv_pk_max_u16 v138, v138, v174\nv_pk_max_u16 v138, v138, v175\n"
    "v_pk_max_u16 v138, v138, v138 op_sel:[0,1] op_sel_hi:[1,0]\nv_cmp_eq_u32 vcc, -1, v138\ns_cbranch_vccz .Lxr177\ns_sub_u32 s80, s80, 1\ns_cbranch_scc1 .Lxr177\ns_sleep 2\n"
    "s_add_u32 m0, s87, 4096\ns_add_u32 s52, s82, s51\ns_addc_u32 s53, s83, 0\nglobal_load_lds_dwordx4 v35, s[52:53] offset:0 sc1\n"
    "global_load_lds_dwordx4 v35, s[52:53] offset:1024 sc1\nglobal_load_lds_dwordx4 v35, s[52:53] offset:2048 sc1\nglobal_load_lds_dwordx4 v35, s[52:53] offset:3072 sc1\n"
    "s_waitcnt vmcnt(0)\nds_read_b64_tr_b16 v[160:161], v29 offset:4096\nds_read_b64_tr_b16 v[162:163], v29 offset:4352\nds_read_b64_tr_b16 v[164:165], v29 offset:5120\n"
    "ds_read_b64_tr_b16 v[166:167], v29 offset:5376\nds_read_b64_tr_b16 v[168:169], v29 offset:6144\nds_read_b64_tr_b16 v[170:171], v29 offset:6400\n"
    "ds_read_b64_tr_b16 v[172:173], v29 offset:7168\nds_read_b64_tr_b16 v[174:175], v29 offset:7424\ns_waitcnt lgkmcnt(0)\ns_branch .Lxl178\n.Lend:\n"
    :: "{s[0:1]}"(__builtin_amdgcn_kernarg_segment_ptr()), "{s2}"(blockIdx.x), "{v0}"(threadIdx.x),
       "i"((__attribute__((address_space(3))) char*)smem)
    : "v255", "s101", "vcc", "scc", "memory");
}

extern "C" void kernel_launch(void* const* d_in, const int* in_sizes, int n_in,
                              void* d_out, int out_size, void* d_ws, size_t ws_size,
                              hipStream_t stream) {
  Args a;
  a.x = (const u16*)d_in[0]; a.w = (const u16*)d_in[1]; a.wo = (const u16*)d_in[2];
  a.A_re = (const u16*)d_in[4]; a.A_im = (const u16*)d_in[5]; a.B_re = (const u16*)d_in[6]; a.B_im = (const u16*)d_in[7];
  a.C_re = (const u16*)d_in[8]; a.C_im = (const u16*)d_in[9]; a.log_dt = (const u16*)d_in[10]; a.Dskip = (const u16*)d_in[11];
  a.out = (u16*)d_out; a.ws = (char*)d_ws;
  k_all<<<256, 512, 0, stream>>>(a);
}
```

```cpp
#include <hip/hip_runtime.h>
#include <stdint.h>
typedef unsigned short u16;

struct Args { const u16 *x, *w, *wo, *A_re, *A_im, *B_re, *B_im, *C_re, *C_im, *log_dt, *Dskip; u16* out; char* ws; };

__global__ __launch_bounds__(512, 2) __attribute__((aligned(4096))) void k_all(const Args ar) {
  __shared__ __attribute__((aligned(128))) char smem[163840];
  asm volatile(
    ".pushsection .rodata\n.p2align 7\n.Ltabs:\n"
    ".short 1400,1400,1400,1400,1400,1400,1400,1400,1400,1400,1400,1400,1400,1400,1400,1400,1400,1400,1400,1400,1400,1400,1400,1400,1400,1400,1400,1400,1400,1400,1400,1400\n"
    ".short 1400,1400,1400,1400,1400,1400,1400,1400,1400,1400,1400,1400,1400,1400,1400,1400,1400,1400,1400,1400,1400,1400,1400,1400,1400,1400,1400,1400,1400,1400,1400,1400\n"
    ".short 1400,1400,1400,1400,1400,1400,1400,1400,1400,1400,1400,1400,1400,1400,1400,1400,1278,1274,1270,1266,1262,1258,1254,1250,1246,1242,1238,1234,1230,1226,1222,1218\n"
    ".short 1214,1210,1206,1202,1198,1194,1190,1186,1182,1178,1174,1170,1166,1162,1158,1154,1150,1146,1142,1138,1134,1130,1126,1122,1118,1114,1110,1106,1102,1098,1094,1090\n"
    ".short 1086,1082,1078,1074,1070,1066,1062,1058,1054,1050,1046,1042,1038,1034,1030,1026,1400,1400,1400,1400,1400,1400,1400,1400,1400,1400,1400,1400,1400,1400,1400,1400\n"
    ".short 1400,1400,1400,1400,1400,1400,1400,1400,1400,1400,1400,1400,1400,1400,1400,1400,1280,1276,1272,1268,1264,1260,1256,1252,1248,1244,1240,1236,1232,1228,1224,1220\n"
    ".short 1216,1212,1208,1204,1200,1196,1192,1188,1184,1180,1176,1172,1168,1164,1160,1156,1152,1148,1144,1140,1136,1132,1128,1124,1120,1116,1112,1108,1104,1100,1096,1092\n"
    ".short 1088,1084,1080,1076,1072,1068,1064,1060,1056,1052,1048,1044,1040,1036,1032,1028,1400,1400,1400,1400,1400,1400,1400,1400,1400,1400,1400,1400,1400,1400,1400,1400\n"
    ".short 1400,1400,1400,1400,1400,1400,1400,1400,1400,1400,1400,1400,1400,1400,1400,1400,1400,1024,1022,1018,1014,1010,1006,1002,998,994,990,986,982,978,974,970\n"
    ".short 966,962,958,954,950,946,942,938,934,930,926,922,918,914,910,906,902,898,894,890,886,882,878,874,870,866,862,858,854,850,846,842\n"
    ".short 838,834,830,826,822,818,814,810,806,802,798,794,790,786,782,778,1400,1400,1400,1400,1400,1400,1400,1400,1400,1400,1400,1400,1400,1400,1400,1400\n"
    ".short 1400,1400,1400,1400,1400,1400,1400,1400,1400,1400,1400,1400,1400,1400,1400,1400,1400,1024,1020,1016,1012,1008,1004,1000,996,992,988,984,980,976,972,968\n"
    ".short 964,960,956,952,948,944,940,936,932,928,924,920,916,912,908,904,900,896,892,888,884,880,876,872,868,864,860,856,852,848,844,840\n"
    ".short 836,832,828,824,820,816,812,808,804,800,796,792,788,784,780,776,1400,1400,1400,1400,1400,1400,1400,1400,1400,1400,1400,1400,1400,1400,1400,1400\n"
    ".short 1400,1400,1400,1400,1400,1400,1400,1400,1400,1400,1400,1400,1400,1400,1400,1400,1400,1400,774,774,774,772,772,772,772,772,770,770,770,768,768,768\n"
    ".short 768,766,764,764,764,762,760,760,760,758,756,756,756,754,752,752,752,750,748,748,748,746,744,744,744,742,740,740,740,738,736,736\n"
    ".short 736,734,732,732,732,730,728,728,728,726,724,724,724,722,720,720,720,718,716,716,716,714,712,712,712,710,708,708,708,706,704,704\n"
    ".short 704,702,700,700,700,698,696,696,696,694,692,692,692,690,688,688,688,686,684,684,684,682,680,680,680,678,676,676,676,674,672,672\n"
    ".short 672,670,668,668,668,666,664,664,664,662,660,660,660,658,656,656,656,654,652,652,652,650,648,648,648,646,644,644,644,642,640,640\n"
    ".short 640,638,636,636,636,634,632,632,632,630,628,628,628,626,624,624,624,622,620,620,620,618,616,616,616,614,612,612,612,610,608,608\n"
    ".short 608,606,604,604,604,602,600,600,600,598,596,596,596,594,592,592,592,590,588,588,588,586,584,584,584,582,580,580,580,578,576,576\n"
    ".short 576,574,572,572,572,570,568,568,568,566,564,564,564,562,560,560,560,558,556,556,556,554,552,552,552,550,548,548,548,546,544,544\n"
    ".short 544,542,540,540,540,538,536,536,536,534,532,532,532,530,528,528,528,526,524,524,524,522,520,520,520,518,516,516,516,514,512,512\n"
    ".short 510,508,506,504,502,500,498,496,494,492,490,488,486,484,482,480,478,476,474,472,470,468,466,464,462,460,458,456,454,452,450,448\n"
    ".short 446,444,442,440,438,436,434,432,430,428,426,424,422,420,418,416,414,412,410,408,406,404,402,400,398,396,394,392,390,388,386,384\n"
    ".short 382,380,378,376,374,372,370,368,366,364,362,360,358,356,354,352,350,348,346,344,342,340,338,336,334,332,330,328,326,324,322,320\n"
    ".short 318,316,314,312,310,308,306,304,302,300,298,296,294,292,290,288,286,284,282,280,278,276,274,272,270,268,266,264,262,260,258,256\n"
    ".short 254,252,250,248,246,244,242,240,238,236,234,232,230,228,226,224,222,220,218,216,214,212,210,208,206,204,202,200,198,196,194,192\n"
    ".short 190,188,186,184,182,180,178,176,174,172,170,168,166,164,162,160,158,156,154,152,150,148,146,144,142,140,138,136,134,132,130,128\n"
    ".short 126,124,122,120,118,116,114,112,110,108,106,104,102,100,98,96,94,92,90,88,86,84,82,80,78,76,74,72,70,68,66,64\n"
    ".short 62,60,58,56,54,52,50,48,46,44,42,40,38,36,34,32,30,28,26,24,22,20,18,16,14,12,10,8,6,4,2,0\n"
    ".short 1400,1400,1400,1400,1400,1400,1400,1400,1400,1400,1400,1400,1400,1400,1400,1400,1400,1400,1400,1400,1400,1400,1400,1400,1400,1400,1400,1400,1400,1400,1400,1400\n"
    ".short 0,2,4,6,16,18,20,22,32,34,36,38,48,50,52,54,64,66,68,70,80,82,84,86,96,98,100,102,112,114,116,118\n"
    ".short 128,130,132,134,144,146,148,150,160,162,164,166,176,178,180,182,192,194,196,198,208,210,212,214,224,226,228,230,240,242,244,246\n"
    ".short 8,10,12,14,24,26,28,30,40,42,44,46,56,58,60,62,72,74,76,78,88,90,92,94,104,106,108,110,120,122,124,126\n"
    ".short 136,138,140,142,152,154,156,158,168,170,172,174,184,186,188,190,200,202,204,206,216,218,220,222,232,234,236,238,248,250,252,254\n"
    ".short 514,516,518,1472,522,524,526,1474,530,532,534,1476,538,540,542,1478,546,548,550,1480,554,556,558,1482,562,564,566,1484,570,572,574,1486\n"
    ".short 578,580,582,1488,586,588,590,1490,594,596,598,1492,602,604,606,1494,610,612,614,1496,618,620,622,1498,626,628,630,1500,634,636,638,1502\n"
    ".short 1504,1504,1504,1504,1506,1506,1506,1506,1508,1508,1508,1508,1510,1510,1510,1510,1512,1512,1512,1512,1514,1514,1514,1514,1516,1516,1516,1516,1518,1518,1518,1518\n"
    ".short 1520,1520,1520,1520,1522,1522,1522,1522,1524,1524,1524,1524,1526,1526,1526,1526,1528,1528,1528,1528,1530,1530,1530,1530,1532,1532,1532,1532,1534,1534,1534,1534\n"
    ".short 512,768,896,1024,520,772,900,1026,528,776,904,1028,536,780,908,1030,544,784,912,1032,552,788,916,1034,560,792,920,1036,568,796,924,1038\n"
    ".short 576,800,928,1040,584,804,932,1042,592,808,936,1044,600,812,940,1046,608,816,944,1048,616,820,948,1050,624,824,952,1052,632,828,956,1054\n"
    ".short 1088,1152,1216,1280,1090,1154,1218,1442,1092,1156,1220,1444,1094,1158,1222,1446,1096,1160,1224,1448,1098,1162,1226,1450,1100,1164,1228,1452,1102,1166,1230,1454\n"
    ".short 1104,1168,1232,1456,1106,1170,1234,1458,1108,1172,1236,1460,1110,1174,1238,1462,1112,1176,1240,1464,1114,1178,1242,1466,1116,1180,1244,1468,1118,1182,1246,1470\n"
    ".short 770,898,1472,1472,774,902,1474,1474,778,906,1476,1476,782,910,1478,1478,786,914,1480,1480,790,918,1482,1482,794,922,1484,1484,798,926,1486,1486\n"
    ".short 802,930,1488,1488,806,934,1490,1490,810,938,1492,1492,814,942,1494,1494,818,946,1496,1496,822,950,1498,1498,826,954,1500,1500,830,958,1502,1502\n"
    ".short 1504,1504,1504,1504,1506,1506,1506,1506,1508,1508,1508,1508,1510,1510,1510,1510,1512,1512,1512,1512,1514,1514,1514,1514,1516,1516,1516,1516,1518,1518,1518,1518\n"
    ".short 1520,1520,1520,1520,1522,1522,1522,1522,1524,1524,1524,1524,1526,1526,1526,1526,1528,1528,1528,1528,1530,1530,1530,1530,1532,1532,1532,1532,1534,1534,1534,1534\n"
    ".short 256,258,260,262,272,274,276,278,288,290,292,294,304,306,308,310,320,322,324,326,336,338,340,342,352,354,356,358,368,370,372,374\n"
    ".short 384,386,388,390,400,402,404,406,416,418,420,422,432,434,436,438,448,450,452,454,464,466,468,470,480,482,484,486,496,498,500,502\n"
    ".short 264,266,268,270,280,282,284,286,296,298,300,302,312,314,316,318,328,330,332,334,344,346,348,350,360,362,364,366,376,378,380,382\n"
    ".short 392,394,396,398,408,410,412,414,424,426,428,430,440,442,444,446,456,458,460,462,472,474,476,478,488,490,492,494,504,506,508,510\n"
    ".short 642,644,646,1472,650,652,654,1474,658,660,662,1476,666,668,670,1478,674,676,678,1480,682,684,686,1482,690,692,694,1484,698,700,702,1486\n"
    ".short 706,708,710,1488,714,716,718,1490,722,724,726,1492,730,732,734,1494,738,740,742,1496,746,748,750,1498,754,756,758,1500,762,764,766,1502\n"
    ".short 1504,1504,1504,1504,1506,1506,1506,1506,1508,1508,1508,1508,1510,1510,1510,1510,1512,1512,1512,1512,1514,1514,1514,1514,1516,1516,1516,1516,1518,1518,1518,1518\n"
    ".short 1520,1520,1520,1520,1522,1522,1522,1522,1524,1524,1524,1524,1526,1526,1526,1526,1528,1528,1528,1528,1530,1530,1530,1530,1532,1532,1532,1532,1534,1534,1534,1534\n"
    ".short 640,832,960,1056,648,836,964,1058,656,840,968,1060,664,844,972,1062,672,848,976,1064,680,852,980,1066,688,856,984,1068,696,860,988,1070\n"
    ".short 704,864,992,1072,712,868,996,1074,720,872,1000,1076,728,876,1004,1078,736,880,1008,1080,744,884,1012,1082,752,888,1016,1084,760,892,1020,1086\n"
    ".short 1120,1184,1248,1440,1122,1186,1250,1442,1124,1188,1252,1444,1126,1190,1254,1446,1128,1192,1256,1448,1130,1194,1258,1450,1132,1196,1260,1452,1134,1198,1262,1454\n"
    ".short 1136,1200,1264,1456,1138,1202,1266,1458,1140,1204,1268,1460,1142,1206,1270,1462,1144,1208,1272,1464,1146,1210,1274,1466,1148,1212,1276,1468,1150,1214,1278,1470\n"
    ".short 834,962,1472,1472,838,966,1474,1474,842,970,1476,1476,846,974,1478,1478,850,978,1480,1480,854,982,1482,1482,858,986,1484,1484,862,990,1486,1486\n"
    ".short 866,994,1488,1488,870,998,1490,1490,874,1002,1492,1492,878,1006,1494,1494,882,1010,1496,1496,886,1014,1498,1498,890,1018,1500,1500,894,1022,1502,1502\n"
    ".short 1504,1504,1504,1504,1506,1506,1506,1506,1508,1508,1508,1508,1510,1510,1510,1510,1512,1512,1512,1512,1514,1514,1514,1514,1516,1516,1516,1516,1518,1518,1518,1518\n"
    ".short 1520,1520,1520,1520,1522,1522,1522,1522,1524,1524,1524,1524,1526,1526,1526,1526,1528,1528,1528,1528,1530,1530,1530,1530,1532,1532,1532,1532,1534,1534,1534,1534\n"
    ".popsection\ns_load_dwordx16 s[8:23], s[0:1], 0\ns_load_dwordx8 s[24:31], s[0:1], 64\ns_load_dwordx2 s[32:33], s[0:1], 96\nv_readfirstlane_b32 s3, v0\ns_nop 0\n"
    "s_cmpk_gt_u32 s3, 255\ns_cbranch_scc1 .LH2\ns_lshr_b32 s3, s3, 6\ns_cmp_lt_u32 s3, 2\ns_cbranch_scc0 .LG1\ns_lshr_b32 s77, s2, 3\ns_cmp_eq_u32 s3, 0\ns_cbranch_scc0 .LL4\n"
    "v_lshl_add_u32 v243, v0, 5, s77\nv_lshlrev_b32 v243, 7, v243\ns_getpc_b64 s[4:5]\ns_add_u32 s4, s4, .Ltabs@rel32@lo+4\ns_addc_u32 s5, s5, .Ltabs@rel32@hi+12\n"
    "s_getpc_b64 vcc\n.Lpc:\ns_sub_u32 vcc_lo, vcc_lo, s4\ns_sub_u32 vcc_hi, vcc_lo, 4096\ns_add_u32 vcc_lo, vcc_lo, .Lend-.Lpc-4\ns_mov_b64 exec, -2\n"
    "v_add_u32 v243, vcc_hi, v243\nv_min_u32 v243, vcc_lo, v243\ns_mov_b64 exec, 0xffff\nglobal_load_dword v243, v243, s[4:5]\ns_mov_b64 exec, -1\n.LL4:\ns_and_b32 s79, s2, 7\n"
    "s_mul_i32 s78, s3, 12288\ns_add_u32 m0, s78, 106496\nv_lshrrev_b32 v241, 4, v0\nv_xor_b32 v241, v241, v0\nv_and_b32 v241, 2, v241\nv_and_or_b32 v241, v0, 1, v241\n"
    "v_lshlrev_b32 v241, 4, v241\nv_lshrrev_b32 v242, 2, v0\nv_lshl_or_b32 v241, v242, 10, v241\ns_bfe_u32 s78, s2, 0x40003\nv_lshl_or_b32 v241, s78, 15, v241\n"
    "v_lshl_or_b32 v241, s79, 7, v241\ns_lshr_b32 s78, s2, 7\nv_lshl_or_b32 v241, s78, 6, v241\ns_waitcnt lgkmcnt(0)\nglobal_load_lds_dwordx4 v241, s[12:13]\ns_branch .LG1\n"
    ".LH2:\ns_bfe_u32 s3, s3, 0x20006\nv_and_b32 v77, 63, v0\ns_bfe_u32 s86, s2, 0x20003\ns_bfe_u32 s63, s2, 0x20001\ns_lshl2_add_u32 s63, s63, s86\ns_lshr_b32 s87, s2, 5\n"
    "s_lshl1_add_u32 s58, s86, s87\ns_and_b32 s58, s58, 7\ns_mov_b32 s60, s58\ns_and_b32 s86, s2, 1\ns_lshl3_add_u32 s87, s86, s87\ns_lshl_b32 s87, s87, 6\n"
    "s_mul_i32 s86, s3, 49152\ns_add_u32 s67, s87, s86\ns_lshl2_add_u32 s86, s63, s3\ns_lshl_b32 s68, s86, 15\nv_lshrrev_b32 v80, 4, v77\nv_xor_b32 v81, v80, v77\n"
    "v_and_b32 v83, 2, v81\nv_and_or_b32 v83, v77, 1, v83\nv_lshlrev_b32 v83, 4, v83\nv_lshrrev_b32 v82, 2, v77\ns_movk_i32 s86, 0xc00\nv_mad_u32_u24 v76, v82, s86, v83\n"
    "v_and_b32 v81, 7, v81\nv_lshlrev_b32 v81, 4, v81\nv_lshrrev_b32 v83, 3, v77\nv_lshl_or_b32 v72, v83, 10, v81\nv_xor_b32 v73, 0x2040, v72\nv_add_u32 v74, 0x4000, v72\n"
    "v_add_u32 v75, 0x4000, v73\ns_mul_i32 s64, s3, 12288\ns_add_u32 s64, s64, 98304\ns_lshl_b32 s69, s3, 10\ns_waitcnt lgkmcnt(0)\ns_add_u32 s10, s10, s67\n"
    "s_addc_u32 s11, s11, 0\ns_add_u32 s70, s8, s68\ns_addc_u32 s71, s9, 0\ns_mul_i32 s86, s60, 196608\ns_add_u32 s54, s10, s86\ns_addc_u32 s55, s11, 0\n"
    "s_lshl_b32 s86, s60, 12\ns_add_u32 m0, s69, s86\ns_add_u32 s60, s60, 1\nglobal_load_lds_dwordx4 v76, s[54:55]\ns_add_u32 m0, m0, 31744\ns_and_b32 s60, s60, 7\n"
    "global_load_lds_dwordx4 v76, s[54:55] offset:1024\ns_add_u32 m0, m0, 31744\ns_nop 0\nglobal_load_lds_dwordx4 v76, s[54:55] offset:2048\ns_add_u32 m0, s64, 0\n"
    "s_lshl_b32 s86, s58, 7\ns_add_u32 s52, s70, s86\ns_addc_u32 s53, s71, 0\nglobal_load_lds_dwordx4 v72, s[52:53]\ns_add_u32 m0, s64, 1024\ns_add_u32 s58, s58, 1\n"
    "global_load_lds_dwordx4 v73, s[52:53]\ns_add_u32 m0, s64, 2048\ns_and_b32 s58, s58, 7\nglobal_load_lds_dwordx4 v74, s[52:53]\ns_add_u32 m0, s64, 3072\ns_nop 0\n"
    "global_load_lds_dwordx4 v75, s[52:53]\ns_setprio 3\ns_getpc_b64 s[4:5]\ns_add_u32 s4, s4, .Ltabs@rel32@lo+4\ns_addc_u32 s5, s5, .Ltabs@rel32@hi+12\ns_waitcnt lgkmcnt(0)\n"
    "s_mov_b32 s86, 16777216\ns_load_dword s34, s[32:33], s86\ns_mov_b32 s6, 0\ns_mov_b32 s7, 0\ns_and_b32 s35, s2, 7\ns_lshr_b32 s36, s2, 3\ns_bfe_u32 s44, s2, 0x20001\n"
    "s_bfe_u32 s45, s2, 0x20003\ns_lshl2_add_u32 s37, s44, s45\ns_and_b32 s46, s2, 1\ns_lshr_b32 s47, s2, 5\ns_lshl3_add_u32 s38, s46, s47\ns_bfe_u32 s48, s2, 0x30003\n"
    "s_lshl3_add_u32 s39, s35, s48\ns_lshr_b32 s40, s2, 6\ns_lshl_b32 s49, s35, 5\ns_add_u32 s41, s49, s36\ns_lshl1_add_u32 s50, s45, s47\ns_and_b32 s42, s50, 7\n"
    "s_lshl_b32 s86, s41, 2\ns_load_dword s96, s[26:27], s86\ns_load_dword s95, s[28:29], s86\nv_mov_b32 v96, 0\nv_mov_b32 v97, 0xf149f2ca\nv_bfe_u32 v100, v0, 4, 2\n"
    "v_lshlrev_b32 v99, 2, v100\nv_and_b32 v102, 7, v0\nv_add_u32 v101, 1, v102\nv_sub_u32 v98, v99, v101\nv_add_u32 v104, 8, v102\nv_sub_u32 v103, v104, v101\n"
    "v_cmp_le_u32 vcc, v98, v103\ns_nop 1\nv_cndmask_b32 v128, v97, v96, vcc\nv_add_u32 v106, 1, v99\nv_sub_u32 v105, v106, v101\nv_cmp_le_u32 vcc, v105, v103\ns_nop 1\n"
    "v_cndmask_b32 v129, v97, v96, vcc\nv_add_u32 v108, 2, v99\nv_sub_u32 v107, v108, v101\nv_cmp_le_u32 vcc, v107, v103\ns_nop 1\nv_cndmask_b32 v130, v97, v96, vcc\n"
    "v_add_u32 v110, 3, v99\nv_sub_u32 v109, v110, v101\nv_cmp_le_u32 vcc, v109, v103\ns_nop 1\nv_cndmask_b32 v131, v97, v96, vcc\ns_add_u32 s46, s3, 4\n"
    "s_mul_i32 s45, s46, 0xd00\ns_add_u32 s44, s45, 0x18040\nv_lshl_add_u32 v252, v100, 4, s44\nv_and_b32 v112, 15, v0\nv_max_u32 v111, 1, v112\ns_mov_b32 s47, 0x1a0\n"
    "v_mad_u32_u24 v252, v111, s47, v252\ns_add_u32 s48, s45, 0x18d00\nv_lshl_add_u32 v253, v100, 4, s48\nv_mad_u32_u24 v253, v102, s47, v253\nv_and_b32 v113, 3, v0\n"
    "s_add_u32 s49, s45, 0x18080\nv_lshl_add_u32 v254, v113, 3, s49\nv_bfe_u32 v116, v0, 2, 2\nv_add_u32 v115, v99, v116\nv_max_u32 v114, 1, v115\n"
    "v_mad_u32_u24 v254, v114, s47, v254\nv_bfe_u32 v117, v0, 3, 1\ns_lshr_b32 s59, s46, 1\ns_lshl2_add_u32 s57, s37, s59\ns_lshl_b32 s56, s57, 15\ns_lshl_b32 s65, s38, 11\n"
    "s_add_u32 s51, s56, s65\ns_and_b32 s72, s46, 1\ns_lshl_b32 s66, s72, 8\ns_add_u32 s50, s51, s66\nv_lshl_add_u32 v1, v117, 10, s50\nv_bfe_u32 v118, v0, 5, 1\n"
    "v_lshl_add_u32 v1, v118, 9, v1\nv_bfe_u32 v119, v0, 4, 1\nv_lshl_add_u32 v1, v119, 4, v1\nv_lshl_add_u32 v1, v102, 5, v1\nv_mov_b64 v[10:11], 0\nv_mov_b64 v[12:13], 0\n"
    "v_mov_b64 v[14:15], 0\nv_mov_b64 v[16:17], 0\nv_mov_b64 v[18:19], 0\nv_mov_b64 v[20:21], 0\nv_mov_b64 v[22:23], 0\nv_mov_b64 v[24:25], 0\nv_mov_b32 v96, 0x27f70\n"
    "s_lshr_b32 s45, s3, 1\ns_lshl_b32 s44, s45, 6\nv_and_b32 v99, 15, v0\ns_mov_b32 s46, 0x23c00\nv_lshl_add_u32 v98, v99, 10, s46\nv_bfe_u32 v101, v0, 4, 2\n"
    "v_xor_b32 v100, v101, v99\nv_lshl_add_u32 v98, v100, 4, v98\nv_xor_b32 v97, s44, v98\nv_cmp_gt_u32 vcc, 2, v99\ns_nop 1\nv_cndmask_b32 v26, v97, v96, vcc\n"
    "v_mov_b32 v102, 0\nv_cmp_gt_u32 vcc, 2, v99\ns_nop 1\nv_cndmask_b32 v27, -1, v102, vcc\nv_bfe_u32 v104, v0, 2, 2\nv_lshl_add_u32 v103, v101, 3, v104\n"
    "s_lshl_b32 s47, s45, 11\nv_lshl_add_u32 v29, v103, 6, s47\nv_and_b32 v105, 3, v0\nv_lshl_add_u32 v29, v105, 3, v29\nv_xor_b32 v107, s3, v101\nv_and_b32 v106, 1, v107\n"
    "v_lshl_add_u32 v29, v106, 5, v29\ns_add_u32 s49, s45, 1\ns_lshl_b32 s48, s49, 15\nv_add_u32 v28, s48, v29\ns_lshl_b32 s51, s3, 6\ns_add_u32 s50, s51, 0x27c80\n"
    "v_lshl_add_u32 v30, v101, 4, s50\ns_and_b32 s59, s3, 1\ns_lshl_b32 s57, s59, 6\ns_add_u32 s56, s57, 0x27c00\nv_lshl_add_u32 v31, v101, 4, s56\ns_lshl_b32 s66, s3, 7\n"
    "s_add_u32 s65, s66, 0x24000\nv_lshl_add_u32 v32, v99, 3, s65\ns_mov_b32 s72, 0x24000\nv_lshl_add_u32 v33, v99, 3, s72\ns_lshl_b32 s74, s3, 5\ns_add_u32 s73, s74, 0x18040\n"
    "v_lshl_add_u32 v34, v101, 3, s73\ns_mov_b32 s75, 0xd0\nv_mad_u32_u24 v34, v99, s75, v34\nv_and_b32 v108, 0xff, v0\nv_lshlrev_b32 v35, 4, v108\n"
    "s_lshl2_add_u32 s78, s40, s3\ns_lshl_b32 s77, s78, 9\ns_add_u32 s76, s77, 0x1002000\nv_lshl_add_u32 v36, v105, 7, s76\nv_and_b32 v109, 4, v0\n"
    "v_lshl_add_u32 v36, v109, 2, v36\nv_and_b32 v110, 63, v0\ns_lshl2_add_u32 s81, s37, s3\ns_lshl_b32 s80, s81, 15\ns_lshl_b32 s82, s38, 11\ns_add_u32 s79, s80, s82\n"
    "v_lshl_add_u32 v37, v110, 4, s79\ns_lshl_b32 s84, s78, 15\ns_add_u32 s83, s84, 0xe01000\nv_lshl_add_u32 v58, v110, 4, s83\ns_and_b32 s45, s38, 3\n"
    "s_lshl2_add_u32 s49, s37, s3\ns_lshl_b32 s48, s49, 9\ns_add_u32 s47, s48, 0x1008000\ns_lshr_b32 s51, s38, 2\ns_lshl_b32 s50, s51, 7\ns_add_u32 s46, s47, s50\n"
    "s_lshl2_add_u32 s44, s45, s46\nv_mov_b32 v60, s44\nv_mov_b32 v96, 0x27f70\ns_lshl_b32 s56, s3, 8\nv_and_b32 v99, 15, v0\ns_mov_b32 s57, 0x23c00\n"
    "v_lshl_add_u32 v98, v99, 10, s57\nv_bfe_u32 v101, v0, 4, 2\nv_xor_b32 v100, v101, v99\nv_lshl_add_u32 v98, v100, 4, v98\nv_xor_b32 v97, s56, v98\nv_cmp_gt_u32 vcc, 2, v99\n"
    "s_nop 1\nv_cndmask_b32 v40, v97, v96, vcc\nv_and_b32 v102, 63, v0\ns_lshl_b32 s59, s41, 9\nv_lshl_add_u32 v41, v102, 4, s59\ns_lshl_b32 s72, s41, 13\n"
    "s_lshl_b32 s73, s3, 11\ns_add_u32 s66, s72, s73\ns_add_u32 s65, s66, 0x200000\nv_lshl_add_u32 v42, v102, 4, s65\nv_and_b32 v103, 1, v0\ns_lshl_b32 s76, s2, 7\n"
    "s_lshl_b32 s77, s3, 5\ns_add_u32 s75, s76, s77\ns_add_u32 s74, s75, 0x400000\nv_lshl_add_u32 v43, v103, 4, s74\ns_and_b32 s80, s2, 3\ns_lshr_b32 s83, s2, 2\n"
    "s_lshl_b32 s82, s83, 7\ns_add_u32 s81, s82, 0x1004000\ns_lshl4_add_u32 s79, s80, s81\ns_lshl2_add_u32 s78, s3, s79\nv_mov_b32 v2, s78\ns_mul_i32 s85, s3, 0x1760\n"
    "s_add_u32 s84, s85, 0x18000\nv_lshl_add_u32 v3, v102, 3, s84\nv_and_b32 v96, 63, v0\ns_lshl2_add_u32 s46, s40, s3\ns_lshl_b32 s45, s46, 15\ns_add_u32 s44, s45, 0xe05000\n"
    "v_lshl_add_u32 v4, v96, 4, s44\ns_add_u32 s47, s45, 0xe07000\nv_lshl_add_u32 v5, v96, 4, s47\nv_and_b32 v97, 31, v0\ns_lshl_b32 s50, s39, 15\ns_lshl_b32 s51, s40, 8\n"
    "s_add_u32 s49, s50, s51\ns_lshl_b32 s56, s3, 6\ns_add_u32 s48, s49, s56\nv_lshl_add_u32 v6, v97, 10, s48\nv_bfe_u32 v98, v0, 5, 1\nv_lshl_add_u32 v6, v98, 4, v6\n"
    "s_add_u32 s57, s45, 0xe03000\nv_lshl_add_u32 v59, v96, 4, s57\ns_lshr_b32 s66, s3, 1\ns_lshl_b32 s65, s66, 8\ns_add_u32 s59, s65, 0x20000\nv_lshl_add_u32 v62, v96, 2, s59\n"
    "s_lshl2_add_u32 s72, s66, 0x27d90\nv_mov_b32 v7, s72\ns_mul_i32 s74, s66, 0x600\ns_add_u32 s73, s74, 0x1ed78\nv_mov_b32 v8, s73\ns_lshl_b32 s74, s3, 9\n"
    "s_and_b32 s75, s74, 512\ns_add_u32 s75, s75, 133120\ns_add_u32 s74, s74, 131072\ns_bitcmp1_b32 s3, 0\ns_cselect_b64 s[76:77], s[16:17], s[14:15]\n"
    "s_cselect_b64 s[50:51], s[20:21], s[18:19]\ns_cselect_b64 s[78:79], s[24:25], s[22:23]\ns_bitcmp1_b32 s3, 1\ns_cselect_b64 s[76:77], s[50:51], s[76:77]\n"
    "v_lshl_add_u32 v44, v77, 4, s74\nv_lshl_add_u32 v45, v77, 4, s75\ns_lshl_b32 s86, s63, 17\ns_sub_u32 s86, s86, 0x4000\ns_max_i32 s86, s86, 0\ns_lshl_b32 s73, s3, 2\n"
    "s_max_u32 s87, s73, 2\ns_add_u32 s73, s73, 4\ns_lshl_b32 s72, s87, 10\ns_add_u32 s86, s86, s72\ns_add_u32 s52, s8, s86\ns_addc_u32 s53, s9, 0\ns_add_u32 s72, s72, 146432\n"
    ".LL5:\ns_mov_b32 m0, s72\nv_xor_b32 v82, s87, v77\nv_lshlrev_b32 v82, 4, v82\nglobal_load_lds_dwordx4 v82, s[52:53]\ns_add_u32 s52, s52, 1024\ns_addc_u32 s53, s53, 0\n"
    "s_add_u32 s72, s72, 1024\ns_add_u32 s87, s87, 1\ns_cmp_lg_u32 s87, s73\ns_cbranch_scc1 .LL5\ns_mul_i32 s86, s60, 196608\ns_add_u32 s54, s10, s86\ns_addc_u32 s55, s11, 0\n"
    "s_lshl_b32 s86, s60, 12\ns_add_u32 m0, s69, s86\ns_add_u32 s60, s60, 1\nglobal_load_lds_dwordx4 v76, s[54:55]\ns_add_u32 m0, m0, 31744\ns_and_b32 s60, s60, 7\n"
    "global_load_lds_dwordx4 v76, s[54:55] offset:1024\ns_add_u32 m0, m0, 31744\ns_nop 0\nglobal_load_lds_dwordx4 v76, s[54:55] offset:2048\ns_add_u32 m0, s64, 4096\n"
    "s_lshl_b32 s86, s58, 7\ns_add_u32 s52, s70, s86\ns_addc_u32 s53, s71, 0\nglobal_load_lds_dwordx4 v72, s[52:53]\ns_add_u32 m0, s64, 5120\ns_add_u32 s58, s58, 1\n"
    "global_load_lds_dwordx4 v73, s[52:53]\ns_add_u32 m0, s64, 6144\ns_and_b32 s58, s58, 7\nglobal_load_lds_dwordx4 v74, s[52:53]\ns_add_u32 m0, s64, 7168\ns_nop 0\n"
    "global_load_lds_dwordx4 v75, s[52:53]\nv_mov_b32 v232, -1\nv_mov_b32 v233, -1\nv_mov_b32 v234, -1\nv_mov_b32 v235, -1\nv_bitop3_b32 v84, 64, v27, v26 bitop3:0x6a\n"
    "s_mov_b32 s61, s42\ns_mov_b32 s62, s42\ns_movk_i32 s86, 128\nv_bitop3_b32 v85, 64, v27, v40 bitop3:0x6a\nv_bitop3_b32 v86, s86, v27, v40 bitop3:0x6a\n"
    "v_bitop3_b32 v87, s86, v27, v85 bitop3:0x6a\ns_waitcnt lgkmcnt(0)\ns_lshl_b32 s97, s96, 16\ns_and_b32 s96, s96, 0xffff0000\ns_cmp_lt_u32 s3, 2\n"
    "s_cselect_b32 s96, s97, s96\ns_mov_b32 s97, 0x3fb8aa3b\nv_mov_b32 v108, s96\nv_mul_f32 v109, s97, v108\nv_fma_f32 v110, s97, v108, -v109\nv_exp_f32 v109, v109\n"
    "v_fmac_f32 v110, 0x32a57060, v108\nv_mul_f32 v110, 0x3f317218, v110\nv_fmac_f32 v109, v109, v110\nv_cvt_pk_bf16_f32 v109, 0, v109\nv_mul_f32 v108, s97, v109\n"
    "v_mul_f32 v110, 0xbe22f983, v109\nv_readfirstlane_b32 s92, v109\nv_readfirstlane_b32 s93, v108\nv_readfirstlane_b32 s94, v110\ns_and_b32 s97, s95, 0xffff0000\n"
    "s_lshl_b32 s96, s95, 16\ns_lshl_b32 s87, s61, 12\ns_lshl_b32 s86, s61, 7\nv_add_u32 v120, s87, v28\nv_add_u32 v122, s87, v29\ns_add_u32 s61, s61, 1\n"
    "v_xor_b32 v121, 0x800, v120\ns_and_b32 s61, s61, 7\nv_bitop3_b32 v88, s86, v27, v26 bitop3:0x6a\nv_bitop3_b32 v89, s86, v27, v84 bitop3:0x6a\n"
    "s_waitcnt vmcnt(7) lgkmcnt(0)\ns_barrier\ns_setprio 0\nds_read_b128 v[132:135], v88\nds_read_b128 v[136:139], v89\nds_read_b64_tr_b16 v[96:97], v120\n"
    "ds_read_b64_tr_b16 v[98:99], v120 offset:256\nds_read_b64_tr_b16 v[100:101], v121\nds_read_b64_tr_b16 v[102:103], v121 offset:256\nds_read_b64_tr_b16 v[104:105], v122\n"
    "ds_read_b64_tr_b16 v[106:107], v122 offset:256\ns_setprio 0\ns_mul_i32 s86, s60, 196608\ns_add_u32 s54, s10, s86\ns_addc_u32 s55, s11, 0\ns_lshl_b32 s86, s60, 12\n"
    "s_add_u32 m0, s69, s86\ns_add_u32 s60, s60, 1\nglobal_load_lds_dwordx4 v76, s[54:55]\ns_add_u32 m0, m0, 31744\ns_and_b32 s60, s60, 7\n"
    "global_load_lds_dwordx4 v76, s[54:55] offset:1024\ns_add_u32 m0, m0, 31744\ns_nop 0\nglobal_load_lds_dwordx4 v76, s[54:55] offset:2048\ns_add_u32 m0, s64, 8192\n"
    "s_lshl_b32 s86, s58, 7\ns_add_u32 s52, s70, s86\ns_addc_u32 s53, s71, 0\nglobal_load_lds_dwordx4 v72, s[52:53]\ns_add_u32 m0, s64, 9216\ns_add_u32 s58, s58, 1\n"
    "global_load_lds_dwordx4 v73, s[52:53]\ns_add_u32 m0, s64, 10240\ns_and_b32 s58, s58, 7\nglobal_load_lds_dwordx4 v74, s[52:53]\ns_add_u32 m0, s64, 11264\ns_nop 0\n"
    "global_load_lds_dwordx4 v75, s[52:53]\ns_setprio 0\ns_lshl_b32 s87, s61, 12\ns_lshl_b32 s86, s61, 7\nv_add_u32 v120, s87, v28\nv_add_u32 v122, s87, v29\n"
    "s_add_u32 s61, s61, 1\nv_xor_b32 v121, 0x800, v120\ns_and_b32 s61, s61, 7\nv_bitop3_b32 v88, s86, v27, v26 bitop3:0x6a\nv_bitop3_b32 v89, s86, v27, v84 bitop3:0x6a\n"
    "s_waitcnt vmcnt(7) lgkmcnt(0)\ns_barrier\ns_setprio 2\nv_mfma_f32_16x16x32_bf16 v[64:67], v[96:99], v[132:135], 0\n"
    "v_mfma_f32_16x16x32_bf16 v[68:71], v[104:107], v[132:135], 0\nv_mfma_f32_16x16x32_bf16 v[64:67], v[100:103], v[136:139], v[64:67]\ns_setprio 0\n"
    "ds_read_b128 v[132:135], v88\nds_read_b128 v[136:139], v89\nds_read_b64_tr_b16 v[96:97], v120\nds_read_b64_tr_b16 v[98:99], v120 offset:256\n"
    "ds_read_b64_tr_b16 v[100:101], v121\nds_read_b64_tr_b16 v[102:103], v121 offset:256\nds_read_b64_tr_b16 v[104:105], v122\nds_read_b64_tr_b16 v[106:107], v122 offset:256\n"
    "s_setprio 0\ns_mul_i32 s86, s60, 196608\ns_add_u32 s54, s10, s86\ns_addc_u32 s55, s11, 0\ns_lshl_b32 s86, s60, 12\ns_add_u32 m0, s69, s86\ns_add_u32 s60, s60, 1\n"
    "global_load_lds_dwordx4 v76, s[54:55]\ns_add_u32 m0, m0, 31744\ns_and_b32 s60, s60, 7\nglobal_load_lds_dwordx4 v76, s[54:55] offset:1024\ns_add_u32 m0, m0, 31744\ns_nop 0\n"
    "global_load_lds_dwordx4 v76, s[54:55] offset:2048\ns_add_u32 m0, s64, 0\ns_lshl_b32 s86, s58, 7\ns_add_u32 s52, s70, s86\ns_addc_u32 s53, s71, 0\n"
    "global_load_lds_dwordx4 v72, s[52:53]\ns_add_u32 m0, s64, 1024\ns_add_u32 s58, s58, 1\nglobal_load_lds_dwordx4 v73, s[52:53]\ns_add_u32 m0, s64, 2048\n"
    "s_and_b32 s58, s58, 7\nglobal_load_lds_dwordx4 v74, s[52:53]\ns_add_u32 m0, s64, 3072\ns_nop 0\nglobal_load_lds_dwordx4 v75, s[52:53]\ns_setprio 0\n"
    "ds_read_b128 v[196:199], v40\nds_read_b128 v[200:203], v85\nds_read_b128 v[204:207], v86\nds_read_b128 v[208:211], v87\ns_lshl_b32 s87, s61, 12\ns_lshl_b32 s86, s61, 7\n"
    "v_add_u32 v120, s87, v28\nv_add_u32 v122, s87, v29\ns_add_u32 s61, s61, 1\nv_xor_b32 v121, 0x800, v120\ns_and_b32 s61, s61, 7\nv_bitop3_b32 v88, s86, v27, v26 bitop3:0x6a\n"
    "v_bitop3_b32 v89, s86, v27, v84 bitop3:0x6a\ns_waitcnt vmcnt(7) lgkmcnt(0)\ns_barrier\ns_setprio 2\nv_mfma_f32_16x16x32_bf16 v[64:67], v[96:99], v[132:135], v[64:67]\n"
    "v_mfma_f32_16x16x32_bf16 v[68:71], v[104:107], v[132:135], v[68:71]\nv_mfma_f32_16x16x32_bf16 v[64:67], v[100:103], v[136:139], v[64:67]\n"
    "v_mfma_f32_16x16x32_bf16 v[92:95], v[196:199], v[196:199], 0\nv_mfma_f32_16x16x32_bf16 v[92:95], v[200:203], v[200:203], v[92:95]\n"
    "v_mfma_f32_16x16x32_bf16 v[92:95], v[204:207], v[204:207], v[92:95]\nv_mfma_f32_16x16x32_bf16 v[92:95], v[208:211], v[208:211], v[92:95]\ns_setprio 0\n"
    "ds_read_b128 v[132:135], v88\nds_read_b128 v[136:139], v89\nds_read_b64_tr_b16 v[96:97], v120\nds_read_b64_tr_b16 v[98:99], v120 offset:256\n"
    "ds_read_b64_tr_b16 v[100:101], v121\nds_read_b64_tr_b16 v[102:103], v121 offset:256\nds_read_b64_tr_b16 v[104:105], v122\nds_read_b64_tr_b16 v[106:107], v122 offset:256\n"
    "s_setprio 0\ns_mul_i32 s86, s60, 196608\ns_add_u32 s54, s10, s86\ns_addc_u32 s55, s11, 0\ns_lshl_b32 s86, s60, 12\ns_add_u32 m0, s69, s86\ns_add_u32 s60, s60, 1\n"
    "global_load_lds_dwordx4 v76, s[54:55]\ns_add_u32 m0, m0, 31744\ns_and_b32 s60, s60, 7\nglobal_load_lds_dwordx4 v76, s[54:55] offset:1024\ns_add_u32 m0, m0, 31744\ns_nop 0\n"
    "global_load_lds_dwordx4 v76, s[54:55] offset:2048\ns_add_u32 m0, s64, 4096\ns_lshl_b32 s86, s58, 7\ns_add_u32 s52, s70, s86\ns_addc_u32 s53, s71, 0\n"
    "global_load_lds_dwordx4 v72, s[52:53]\ns_add_u32 m0, s64, 5120\ns_add_u32 s58, s58, 1\nglobal_load_lds_dwordx4 v73, s[52:53]\ns_add_u32 m0, s64, 6144\n"
    "s_and_b32 s58, s58, 7\nglobal_load_lds_dwordx4 v74, s[52:53]\ns_add_u32 m0, s64, 7168\ns_nop 0\nglobal_load_lds_dwordx4 v75, s[52:53]\ns_setprio 0\n"
    "s_lshl_b32 s87, s61, 12\ns_lshl_b32 s86, s61, 7\nv_add_u32 v120, s87, v28\nv_add_u32 v122, s87, v29\ns_add_u32 s61, s61, 1\nv_xor_b32 v121, 0x800, v120\n"
    "s_and_b32 s61, s61, 7\nv_bitop3_b32 v88, s86, v27, v26 bitop3:0x6a\nv_bitop3_b32 v89, s86, v27, v84 bitop3:0x6a\ns_waitcnt vmcnt(7) lgkmcnt(0)\ns_barrier\ns_setprio 2\n"
    "v_mfma_f32_16x16x32_bf16 v[64:67], v[96:99], v[132:135], v[64:67]\nv_mfma_f32_16x16x32_bf16 v[68:71], v[104:107], v[132:135], v[68:71]\n"
    "v_mfma_f32_16x16x32_bf16 v[64:67], v[100:103], v[136:139], v[64:67]\ns_setprio 0\nds_read_b128 v[132:135], v88\nds_read_b128 v[136:139], v89\n"
    "ds_read_b64_tr_b16 v[96:97], v120\nds_read_b64_tr_b16 v[98:99], v120 offset:256\nds_read_b64_tr_b16 v[100:101], v121\nds_read_b64_tr_b16 v[102:103], v121 offset:256\n"
    "ds_read_b64_tr_b16 v[104:105], v122\nds_read_b64_tr_b16 v[106:107], v122 offset:256\ns_setprio 0\ns_mul_i32 s86, s60, 196608\ns_add_u32 s54, s10, s86\n"
    "s_addc_u32 s55, s11, 0\ns_lshl_b32 s86, s60, 12\ns_add_u32 m0, s69, s86\ns_add_u32 s60, s60, 1\nglobal_load_lds_dwordx4 v76, s[54:55]\ns_add_u32 m0, m0, 31744\n"
    "s_and_b32 s60, s60, 7\nglobal_load_lds_dwordx4 v76, s[54:55] offset:1024\ns_add_u32 m0, m0, 31744\ns_nop 0\nglobal_load_lds_dwordx4 v76, s[54:55] offset:2048\n"
    "s_add_u32 m0, s64, 8192\ns_lshl_b32 s86, s58, 7\ns_add_u32 s52, s70, s86\ns_addc_u32 s53, s71, 0\nglobal_load_lds_dwordx4 v72, s[52:53]\ns_add_u32 m0, s64, 9216\n"
    "s_add_u32 s58, s58, 1\nglobal_load_lds_dwordx4 v73, s[52:53]\ns_add_u32 m0, s64, 10240\ns_and_b32 s58, s58, 7\nglobal_load_lds_dwordx4 v74, s[52:53]\n"
    "s_add_u32 m0, s64, 11264\ns_nop 0\nglobal_load_lds_dwordx4 v75, s[52:53]\ns_setprio 0\ns_lshl_b32 s87, s61, 12\ns_lshl_b32 s86, s61, 7\nv_add_u32 v120, s87, v28\n"
    "v_add_u32 v122, s87, v29\ns_add_u32 s61, s61, 1\nv_xor_b32 v121, 0x800, v120\ns_and_b32 s61, s61, 7\nv_bitop3_b32 v88, s86, v27, v26 bitop3:0x6a\n"
    "v_bitop3_b32 v89, s86, v27, v84 bitop3:0x6a\ns_waitcnt vmcnt(7) lgkmcnt(0)\ns_barrier\ns_setprio 2\nv_mfma_f32_16x16x32_bf16 v[64:67], v[96:99], v[132:135], v[64:67]\n"
    "v_mfma_f32_16x16x32_bf16 v[68:71], v[104:107], v[132:135], v[68:71]\nv_mfma_f32_16x16x32_bf16 v[64:67], v[100:103], v[136:139], v[64:67]\ns_setprio 0\n"
    "ds_read_b128 v[132:135], v88\nds_read_b128 v[136:139], v89\nds_read_b64_tr_b16 v[96:97], v120\nds_read_b64_tr_b16 v[98:99], v120 offset:256\n"
    "ds_read_b64_tr_b16 v[100:101], v121\nds_read_b64_tr_b16 v[102:103], v121 offset:256\nds_read_b64_tr_b16 v[104:105], v122\nds_read_b64_tr_b16 v[106:107], v122 offset:256\n"
    "s_setprio 0\ns_mul_i32 s86, s60, 196608\ns_add_u32 s54, s10, s86\ns_addc_u32 s55, s11, 0\ns_lshl_b32 s86, s60, 12\ns_add_u32 m0, s69, s86\ns_add_u32 s60, s60, 1\n"
    "global_load_lds_dwordx4 v76, s[54:55]\ns_add_u32 m0, m0, 31744\ns_and_b32 s60, s60, 7\nglobal_load_lds_dwordx4 v76, s[54:55] offset:1024\ns_add_u32 m0, m0, 31744\ns_nop 0\n"
    "global_load_lds_dwordx4 v76, s[54:55] offset:2048\ns_add_u32 m0, s64, 0\ns_lshl_b32 s86, s58, 7\ns_add_u32 s52, s70, s86\ns_addc_u32 s53, s71, 0\n"
    "global_load_lds_dwordx4 v72, s[52:53]\ns_add_u32 m0, s64, 1024\ns_add_u32 s58, s58, 1\nglobal_load_lds_dwordx4 v73, s[52:53]\ns_add_u32 m0, s64, 2048\n"
    "s_and_b32 s58, s58, 7\nglobal_load_lds_dwordx4 v74, s[52:53]\ns_add_u32 m0, s64, 3072\ns_nop 0\nglobal_load_lds_dwordx4 v75, s[52:53]\ns_setprio 0\n"
    "s_lshl_b32 s87, s61, 12\ns_lshl_b32 s86, s61, 7\nv_add_u32 v120, s87, v28\nv_add_u32 v122, s87, v29\ns_add_u32 s61, s61, 1\nv_xor_b32 v121, 0x800, v120\n"
    "s_and_b32 s61, s61, 7\nv_bitop3_b32 v88, s86, v27, v26 bitop3:0x6a\nv_bitop3_b32 v89, s86, v27, v84 bitop3:0x6a\ns_waitcnt vmcnt(7) lgkmcnt(0)\ns_barrier\ns_setprio 2\n"
    "v_mfma_f32_16x16x32_bf16 v[64:67], v[96:99], v[132:135], v[64:67]\nv_mfma_f32_16x16x32_bf16 v[68:71], v[104:107], v[132:135], v[68:71]\n"
    "v_mfma_f32_16x16x32_bf16 v[64:67], v[100:103], v[136:139], v[64:67]\ns_setprio 0\nds_read_b128 v[132:135], v88\nds_read_b128 v[136:139], v89\n"
    "ds_read_b64_tr_b16 v[96:97], v120\nds_read_b64_tr_b16 v[98:99], v120 offset:256\nds_read_b64_tr_b16 v[100:101], v121\nds_read_b64_tr_b16 v[102:103], v121 offset:256\n"
    "ds_read_b64_tr_b16 v[104:105], v122\nds_read_b64_tr_b16 v[106:107], v122 offset:256\ns_setprio 0\ns_mul_i32 s86, s60, 196608\ns_add_u32 s54, s10, s86\n"
    "s_addc_u32 s55, s11, 0\ns_lshl_b32 s86, s60, 12\ns_add_u32 m0, s69, s86\ns_add_u32 s60, s60, 1\nglobal_load_lds_dwordx4 v76, s[54:55]\ns_add_u32 m0, m0, 31744\n"
    "s_and_b32 s60, s60, 7\nglobal_load_lds_dwordx4 v76, s[54:55] offset:1024\ns_add_u32 m0, m0, 31744\ns_nop 0\nglobal_load_lds_dwordx4 v76, s[54:55] offset:2048\n"
    "s_add_u32 m0, s64, 4096\ns_lshl_b32 s86, s58, 7\ns_add_u32 s52, s70, s86\ns_addc_u32 s53, s71, 0\nglobal_load_lds_dwordx4 v72, s[52:53]\ns_add_u32 m0, s64, 5120\n"
    "s_add_u32 s58, s58, 1\nglobal_load_lds_dwordx4 v73, s[52:53]\ns_add_u32 m0, s64, 6144\ns_and_b32 s58, s58, 7\nglobal_load_lds_dwordx4 v74, s[52:53]\n"
    "s_add_u32 m0, s64, 7168\ns_nop 0\nglobal_load_lds_dwordx4 v75, s[52:53]\ns_setprio 0\ns_lshl_b32 s87, s61, 12\ns_lshl_b32 s86, s61, 7\nv_add_u32 v120, s87, v28\n"
    "v_add_u32 v122, s87, v29\ns_add_u32 s61, s61, 1\nv_xor_b32 v121, 0x800, v120\ns_and_b32 s61, s61, 7\nv_bitop3_b32 v88, s86, v27, v26 bitop3:0x6a\n"
    "v_bitop3_b32 v89, s86, v27, v84 bitop3:0x6a\ns_waitcnt vmcnt(7) lgkmcnt(0)\ns_barrier\ns_setprio 2\nv_mfma_f32_16x16x32_bf16 v[64:67], v[96:99], v[132:135], v[64:67]\n"
    "v_mfma_f32_16x16x32_bf16 v[68:71], v[104:107], v[132:135], v[68:71]\nv_mfma_f32_16x16x32_bf16 v[64:67], v[100:103], v[136:139], v[64:67]\ns_setprio 0\n"
    "ds_read_b128 v[132:135], v88\nds_read_b128 v[136:139], v89\nds_read_b64_tr_b16 v[96:97], v120\nds_read_b64_tr_b16 v[98:99], v120 offset:256\n"
    "ds_read_b64_tr_b16 v[100:101], v121\nds_read_b64_tr_b16 v[102:103], v121 offset:256\nds_read_b64_tr_b16 v[104:105], v122\nds_read_b64_tr_b16 v[106:107], v122 offset:256\n"
    "s_setprio 0\nglobal_store_dwordx4 v37, v[232:235], s[32:33] offset:0 sc1\nglobal_store_dwordx4 v37, v[232:235], s[32:33] offset:1024 sc1\n"
    "global_load_dwordx4 v[140:143], v36, s[32:33] sc1\ns_mov_b32 m0, s75\ns_mov_b32 exec_hi, 0\nglobal_load_lds_dwordx4 v41, s[78:79]\ns_mov_b32 m0, s74\ns_nop 0\n"
    "global_load_lds_dwordx4 v41, s[76:77]\ns_mov_b64 exec, -1\ns_add_u32 m0, s69, 143360\ns_nop 0\nglobal_load_lds_dwordx4 v35, s[4:5]\n"
    "global_store_dwordx4 v42, v[232:235], s[32:33] offset:0 sc1\nglobal_store_dwordx4 v42, v[232:235], s[32:33] offset:1024 sc1\ns_mov_b64 exec, 3\n"
    "global_store_dwordx4 v43, v[232:235], s[32:33] sc1\ns_mov_b64 exec, -1\ns_setprio 0\ns_lshl_b32 s87, s61, 12\ns_lshl_b32 s86, s61, 7\nv_add_u32 v120, s87, v28\n"
    "v_add_u32 v122, s87, v29\ns_add_u32 s61, s61, 1\nv_xor_b32 v121, 0x800, v120\ns_and_b32 s61, s61, 7\nv_bitop3_b32 v88, s86, v27, v26 bitop3:0x6a\n"
    "v_bitop3_b32 v89, s86, v27, v84 bitop3:0x6a\ns_waitcnt vmcnt(9) lgkmcnt(0)\ns_barrier\ns_setprio 0\nv_mfma_f32_16x16x32_bf16 v[64:67], v[96:99], v[132:135], v[64:67]\n"
    "v_mfma_f32_16x16x32_bf16 v[68:71], v[104:107], v[132:135], v[68:71]\nv_mfma_f32_16x16x32_bf16 v[64:67], v[100:103], v[136:139], v[64:67]\ns_setprio 0\n"
    "ds_read_b128 v[132:135], v88\nds_read_b128 v[136:139], v89\nds_read_b64_tr_b16 v[96:97], v120\nds_read_b64_tr_b16 v[98:99], v120 offset:256\n"
    "ds_read_b64_tr_b16 v[100:101], v121\nds_read_b64_tr_b16 v[102:103], v121 offset:256\nds_read_b64_tr_b16 v[104:105], v122\nds_read_b64_tr_b16 v[106:107], v122 offset:256\n"
    "s_setprio 0\ns_setprio 0\ns_mov_b64 exec, 0xffff\nds_write_b32 v32, v93\ns_mov_b32 exec_lo, 0x100001\ns_mov_b32 exec_hi, 0x10000100\nds_write_b32 v32, v92 offset:4\n"
    "s_mov_b32 exec_lo, 0x200002\ns_mov_b32 exec_hi, 0x20000200\nds_write_b32 v32, v93 offset:4\ns_mov_b32 exec_lo, 0x400004\ns_mov_b32 exec_hi, 0x40000400\n"
    "ds_write_b32 v32, v94 offset:4\ns_mov_b32 exec_lo, 0x800008\ns_mov_b32 exec_hi, 0x80000800\nds_write_b32 v32, v95 offset:4\ns_mov_b64 exec, -1\ns_waitcnt lgkmcnt(5)\n"
    "s_setprio 0\nv_mfma_f32_16x16x32_bf16 v[64:67], v[96:99], v[132:135], v[64:67]\nv_mfma_f32_16x16x32_bf16 v[68:71], v[104:107], v[132:135], v[68:71]\n"
    "v_mfma_f32_16x16x32_bf16 v[64:67], v[100:103], v[136:139], v[64:67]\ns_add_u32 s34, s34, 1\ns_mov_b32 exec_lo, 0x20002\ns_mov_b32 exec_hi, 0x20002\ns_nop 3\n"
    "ds_add_f32 v31, v68 offset:0\nds_add_f32 v31, v69 offset:4\nds_add_f32 v31, v70 offset:8\nds_add_f32 v31, v71 offset:12\nds_write_b128 v30, v[64:67]\ns_mov_b64 exec, -1\n"
    "s_waitcnt lgkmcnt(0)\ns_barrier\ns_setprio 0\ns_setprio 1\nds_read_b64 v[108:109], v33 offset:0\nds_read_b64 v[110:111], v33 offset:128\n"
    "ds_read_b64 v[112:113], v33 offset:256\nds_read_b64 v[114:115], v33 offset:384\ns_mov_b32 s86, 0x3b000000\ns_waitcnt lgkmcnt(0)\n"
    "v_pk_add_f32 v[108:109], v[108:109], v[110:111]\nv_pk_add_f32 v[112:113], v[112:113], v[114:115]\nv_pk_add_f32 v[108:109], v[108:109], v[112:113]\n"
    "v_mul_f32 v108, s86, v108\nv_mul_f32 v110, v108, v108\nv_fma_f32 v109, v109, s86, -v110\nv_max_f32 v109, 0, v109\nv_cvt_pk_bf16_f32 v109, 0, v109\n"
    "v_add_f32 v109, 0x3727c5ac, v109\nv_cvt_pk_bf16_f32 v109, 0, v109\nv_sqrt_f32 v109, v109\ns_nop 0\nv_cvt_pk_bf16_f32 v109, 0, v109\nv_rcp_f32 v109, v109\n"
    "v_cvt_pk_bf16_f32 v90, 0, -v108\ns_nop 1\nv_fmac_f32_dpp v64, v64, v90 row_newbcast:1 row_mask:0xf bank_mask:0xf\n"
    "v_fmac_f32_dpp v65, v65, v90 row_newbcast:1 row_mask:0xf bank_mask:0xf\nv_fmac_f32_dpp v66, v66, v90 row_newbcast:1 row_mask:0xf bank_mask:0xf\n"
    "v_fmac_f32_dpp v67, v67, v90 row_newbcast:1 row_mask:0xf bank_mask:0xf\nv_mul_f32 v64, v64, v109\nv_mul_f32 v65, v65, v109\nv_mul_f32 v66, v66, v109\n"
    "v_mul_f32 v67, v67, v109\nv_cvt_pk_bf16_f32 v64, v64, v65\nv_cvt_pk_bf16_f32 v65, v66, v67\nds_write_b64 v34, v[64:65]\nv_mov_b32 v9, s34\ns_waitcnt vmcnt(7)\n"
    "s_mov_b64 exec, 1\nglobal_store_dword v60, v9, s[32:33] sc1\ns_mov_b64 exec, -1\ns_setprio 0\ns_waitcnt vmcnt(7)\nv_xor_b32 v140, s34, v140\nv_xor_b32 v141, s34, v141\n"
    "v_xor_b32 v142, s34, v142\nv_xor_b32 v143, s34, v143\nv_or3_b32 v140, v140, v141, v142\nv_or_b32 v140, v140, v143\nv_cmp_ne_u32 vcc, 0, v140\ns_nop 0\ns_cmp_eq_u64 vcc, 0\n"
    "s_cselect_b32 s43, 1, 0\ns_cbranch_scc1 .LL7\ns_waitcnt vmcnt(0)\ns_branch .LL8\n.LL7:\nglobal_load_dwordx4 v[132:135], v58, s[32:33] offset:-4096 sc1\n"
    "global_load_dwordx4 v[136:139], v58, s[32:33] offset:-3072 sc1\nglobal_load_dwordx4 v[140:143], v58, s[32:33] offset:-2048 sc1\n"
    "global_load_dwordx4 v[144:147], v58, s[32:33] offset:-1024 sc1\nglobal_load_dwordx4 v[148:151], v58, s[32:33] offset:0 sc1\n"
    "global_load_dwordx4 v[152:155], v58, s[32:33] offset:1024 sc1\nglobal_load_dwordx4 v[156:159], v58, s[32:33] offset:2048 sc1\n"
    "global_load_dwordx4 v[160:163], v58, s[32:33] offset:3072 sc1\nglobal_load_dwordx4 v[164:167], v59, s[32:33] offset:-4096 sc1\n"
    "global_load_dwordx4 v[168:171], v59, s[32:33] offset:-3072 sc1\nglobal_load_dwordx4 v[172:175], v59, s[32:33] offset:-2048 sc1\n"
    "global_load_dwordx4 v[176:179], v59, s[32:33] offset:-1024 sc1\n.LL8:\n.LHL12:\ns_setprio 1\ns_waitcnt lgkmcnt(0)\ns_barrier\nds_read_b128 v[200:203], v252\n"
    "ds_read_b128 v[204:207], v252 offset:208\ns_mov_b32 exec_lo, 0x00ff00ff\ns_mov_b32 exec_hi, 0xff00ff00\nds_read_b128 v[10:13], v253\n"
    "ds_read_b128 v[14:17], v253 offset:208\ns_mov_b64 exec, -1\nds_read_b64_tr_b16 v[208:209], v254 offset:0\nds_read_b64_tr_b16 v[210:211], v254 offset:32\n"
    "ds_read_b64_tr_b16 v[212:213], v254 offset:208\nds_read_b64_tr_b16 v[214:215], v254 offset:240\ns_cmp_eq_u32 s43, 0\ns_cbranch_scc1 .LL14\n"
    "global_load_dwordx4 v[112:115], v5, s[32:33] offset:0 sc1\nglobal_load_dwordx4 v[116:119], v5, s[32:33] offset:1024 sc1\n"
    "global_load_dwordx4 v[120:123], v5, s[32:33] offset:2048 sc1\nglobal_load_dwordx4 v[124:127], v5, s[32:33] offset:3072 sc1\n.LL14:\nv_mov_b32 v228, 0x3f803f80\n"
    "v_mov_b32 v229, 0x3f803f80\nv_mov_b32 v230, 0x3f803f80\nv_mov_b32 v231, 0x3f803f80\ns_mov_b32 s82, 0x00ff00ff\ns_mov_b32 s83, s82\ns_mov_b32 s81, 0x3eb8aa3b\n"
    "s_waitcnt lgkmcnt(5)\nv_mfma_f32_16x16x32_bf16 v[216:219], v[200:203], v[10:13], v[128:131]\ns_waitcnt lgkmcnt(4)\n"
    "v_mfma_f32_16x16x32_bf16 v[220:223], v[204:207], v[14:17], v[128:131]\ns_nop 5\nv_max_f32 v224, v216, v217\nv_max3_f32 v224, v224, v218, v219\nv_max_f32 v225, v220, v221\n"
    "v_max3_f32 v225, v225, v222, v223\ns_nop 1\nv_permlane16_swap_b32 v224, v225\nv_max_f32 v226, v224, v225\nv_max_f32 v225, v224, v225\ns_nop 1\n"
    "v_permlane32_swap_b32 v226, v225\nv_max_f32 v226, v226, v225\nv_mul_f32 v224, 0xbeb8aa3b, v226\nv_mul_f32 v225, 0xbeb8aa3b, v226\ns_nop 1\n"
    "v_permlane16_swap_b32 v224, v225\nv_pk_fma_f32 v[216:217], v[216:217], s[80:81], v[224:225] op_sel:[0,1,0] op_sel_hi:[1,1,0]\n"
    "v_pk_fma_f32 v[218:219], v[218:219], s[80:81], v[224:225] op_sel:[0,1,0] op_sel_hi:[1,1,0]\n"
    "v_pk_fma_f32 v[220:221], v[220:221], s[80:81], v[224:225] op_sel:[0,1,1] op_sel_hi:[1,1,1]\n"
    "v_pk_fma_f32 v[222:223], v[222:223], s[80:81], v[224:225] op_sel:[0,1,1] op_sel_hi:[1,1,1]\nv_exp_f32 v216, v216\nv_exp_f32 v217, v217\nv_exp_f32 v218, v218\n"
    "v_exp_f32 v219, v219\nv_exp_f32 v220, v220\nv_exp_f32 v221, v221\nv_exp_f32 v222, v222\nv_exp_f32 v223, v223\ns_mov_b64 exec, s[82:83]\nv_cvt_pk_bf16_f32 v18, v216, v217\n"
    "v_cvt_pk_bf16_f32 v19, v218, v219\nv_cvt_pk_bf16_f32 v22, v220, v221\nv_cvt_pk_bf16_f32 v23, v222, v223\ns_not_b64 exec, exec\nv_cvt_pk_bf16_f32 v20, v216, v217\n"
    "v_cvt_pk_bf16_f32 v21, v218, v219\nv_cvt_pk_bf16_f32 v24, v220, v221\nv_cvt_pk_bf16_f32 v25, v222, v223\ns_mov_b64 exec, -1\ns_waitcnt lgkmcnt(0)\n"
    "v_mfma_f32_16x16x32_bf16 v[216:219], v[228:231], v[18:21], 0\nv_mfma_f32_16x16x32_bf16 v[220:223], v[228:231], v[22:25], 0\n"
    "v_mfma_f32_16x16x32_bf16 v[200:203], v[208:211], v[18:21], 0\nv_mfma_f32_16x16x32_bf16 v[204:207], v[212:215], v[22:25], 0\ns_nop 4\nv_rcp_f32 v224, v216\n"
    "v_rcp_f32 v225, v220\ns_nop 0\nv_pk_mul_f32 v[200:201], v[200:201], v[224:225] op_sel:[0,0] op_sel_hi:[1,0]\n"
    "v_pk_mul_f32 v[202:203], v[202:203], v[224:225] op_sel:[0,0] op_sel_hi:[1,0]\nv_pk_mul_f32 v[204:205], v[204:205], v[224:225] op_sel:[0,1] op_sel_hi:[1,1]\n"
    "v_pk_mul_f32 v[206:207], v[206:207], v[224:225] op_sel:[0,1] op_sel_hi:[1,1]\nv_cvt_pk_bf16_f32 v216, v200, v201\nv_cvt_pk_bf16_f32 v218, v204, v205\n"
    "v_cvt_pk_bf16_f32 v217, v202, v203\nv_cvt_pk_bf16_f32 v219, v206, v207\nv_permlane16_swap_b32 v216, v218\ns_nop 0\nv_permlane16_swap_b32 v217, v219\n"
    "global_store_dwordx4 v1, v[216:219], s[32:33] sc1\ns_setprio 0\nv_bfe_u32 v30, v0, 5, 1\ns_lshl_b32 s15, s3, 11\ns_add_u32 s14, s15, 0x25600\n"
    "v_lshl_add_u32 v48, v30, 8, s14\nv_and_b32 v31, 31, v0\nv_lshl_add_u32 v48, v31, 2, v48\ns_add_u32 s16, s15, 0x25a00\nv_lshl_add_u32 v49, v30, 8, s16\n"
    "v_lshl_add_u32 v49, v31, 2, v49\nv_and_b32 v32, 63, v0\nv_lshl_add_u32 v50, v32, 4, s14\ns_mov_b32 s17, 0x1f600\nv_lshl_add_u32 v51, v32, 4, s17\nv_bfe_u32 v33, v0, 3, 3\n"
    "s_lshl_b32 s23, s40, 6\ns_lshl4_add_u32 s22, s3, s23\ns_lshl_b32 s21, s22, 13\ns_add_u32 s20, s7, s21\ns_lshl_b32 s24, s39, 7\ns_add_u32 s19, s20, s24\n"
    "s_add_u32 s18, s19, 0x200000\nv_lshl_add_u32 v52, v33, 13, s18\nv_and_b32 v34, 7, v0\nv_lshl_add_u32 v52, v34, 4, v52\ns_add_u32 s25, s19, 0x210000\n"
    "v_lshl_add_u32 v53, v33, 13, s25\nv_lshl_add_u32 v53, v34, 4, v53\ns_lshl_b32 s45, s3, 8\ns_add_u32 s44, s45, 0x27600\nv_lshl_add_u32 v54, v32, 2, s44\n"
    "s_add_u32 s14, s7, 0x400000\nv_lshl_add_u32 v20, v0, 4, s14\ns_lshl_b32 s17, s41, 13\ns_add_u32 s16, s7, s17\ns_add_u32 s15, s16, 0x200000\nv_lshl_add_u32 v21, v0, 4, s15\n"
    "v_add_u32 v22, 8192, v20\nv_add_u32 v23, 16384, v20\nv_add_u32 v24, 24576, v20\ns_cmp_eq_u32 s6, 0\ns_cbranch_scc0 .LL18\ns_cmp_eq_u32 s43, 1\ns_cselect_b64 vcc, -1, 0\n"
    "s_cbranch_scc1 .LL19\ns_waitcnt vmcnt(1)\n.LL19:\ns_waitcnt vmcnt(18)\ns_mov_b64 exec, 1\nglobal_store_dword v2, v9, s[32:33] sc1\ns_mov_b64 exec, -1\n"
    "s_waitcnt lgkmcnt(0)\ns_cbranch_vccz .Lcn20\nglobal_load_dwordx4 v[180:183], v59, s[32:33] offset:0 sc1\nglobal_load_dwordx4 v[184:187], v59, s[32:33] offset:1024 sc1\n"
    "global_load_dwordx4 v[188:191], v59, s[32:33] offset:2048 sc1\nglobal_load_dwordx4 v[192:195], v59, s[32:33] offset:3072 sc1\n.Lcn20:\ns_barrier\ns_setprio 0\n"
    "ds_read_b32 v244, v62 offset:2048\nds_read_b32 v245, v62 offset:2560\nds_read_b32 v242, v62 offset:1024\nds_read_b32 v243, v62 offset:1536\nds_read_b32 v240, v62 offset:0\n"
    "ds_read_b32 v241, v62 offset:512\ns_waitcnt lgkmcnt(0)\nv_lshlrev_b32 v220, 16, v244\nv_and_b32 v221, 0xffff0000, v244\nv_lshlrev_b32 v222, 16, v245\n"
    "v_and_b32 v223, 0xffff0000, v245\nv_lshlrev_b32 v224, 16, v242\nv_and_b32 v225, 0xffff0000, v242\nv_lshlrev_b32 v226, 16, v243\nv_and_b32 v227, 0xffff0000, v243\n"
    "v_mul_f32 v212, v220, v224\nv_mul_f32 v214, v221, v225\nv_mul_f32 v213, v220, v226\nv_mul_f32 v215, v221, v227\nv_fma_f32 v212, -v222, v226, v212\n"
    "v_fma_f32 v214, -v223, v227, v214\nv_fma_f32 v213, -v222, v224, -v213\nv_fma_f32 v215, -v223, v225, -v215\ns_bitcmp1_b32 s3, 0\ns_cselect_b32 s86, 0x43800000, 1.0\n"
    "s_mov_b32 s87, 0x43810000\ns_cselect_b32 s87, 0x44010000, s87\nv_max3_f32 v236, |v212|, |v213|, |v214|\ns_cbranch_vccz .Lcn21\n"
    "global_load_dwordx4 v[64:67], v4, s[32:33] offset:-4096 sc1\nglobal_load_dwordx4 v[68:71], v4, s[32:33] offset:-3072 sc1\n"
    "global_load_dwordx4 v[72:75], v4, s[32:33] offset:-2048 sc1\nglobal_load_dwordx4 v[76:79], v4, s[32:33] offset:-1024 sc1\n.Lcn21:\nv_max_f32_e64 v236, v236, |v215|\n"
    "v_lshlrev_b32 v196, 16, v240\nv_and_b32 v197, 0xffff0000, v240\nv_lshlrev_b32 v198, 16, v241\nv_and_b32 v199, 0xffff0000, v241\nv_mul_f32 v196, s93, v196\n"
    "v_mul_f32 v197, s93, v197\nv_mul_f32 v198, s94, v198\nv_mul_f32 v199, s94, v199\nv_mul_f32 v208, s86, v196\nv_mul_f32 v209, s86, v197\nv_mul_f32 v210, s87, v196\n"
    "v_mul_f32 v211, s87, v197\nv_max_f32_dpp v236, v236, v236 row_shr:1 row_mask:0xf bank_mask:0xf bound_ctrl:0\nv_mul_f32 v229, s86, v198\nv_mul_f32 v231, s86, v199\n"
    "v_mul_f32 v217, s87, v198\nv_mul_f32 v219, s87, v199\nv_max_f32_dpp v236, v236, v236 row_shr:2 row_mask:0xf bank_mask:0xf bound_ctrl:0\nv_exp_f32 v208, v208\n"
    "v_exp_f32 v209, v209\nv_exp_f32 v210, v210\nv_exp_f32 v211, v211\nv_max_f32_dpp v236, v236, v236 row_shr:4 row_mask:0xf bank_mask:0xf bound_ctrl:0\nv_fract_f32 v229, v229\n"
    "v_fract_f32 v231, v231\nv_fract_f32 v217, v217\nv_fract_f32 v219, v219\nv_max_f32_dpp v236, v236, v236 row_shr:8 row_mask:0xf bank_mask:0xf bound_ctrl:0\n"
    "v_cos_f32 v228, v229\ns_cbranch_vccz .Lcn22\nglobal_load_dwordx4 v[80:83], v4, s[32:33] offset:0 sc1\nglobal_load_dwordx4 v[84:87], v4, s[32:33] offset:1024 sc1\n"
    "global_load_dwordx4 v[88:91], v4, s[32:33] offset:2048 sc1\nglobal_load_dwordx4 v[92:95], v4, s[32:33] offset:3072 sc1\n.Lcn22:\nv_cos_f32 v230, v231\n"
    "v_cos_f32 v216, v217\nv_cos_f32 v218, v219\nv_max_f32_dpp v236, v236, v236 row_bcast:15 row_mask:0xf bank_mask:0xf bound_ctrl:0\nv_sin_f32 v229, v229\n"
    "v_sin_f32 v231, v231\nv_sin_f32 v217, v217\nv_sin_f32 v219, v219\nv_max_f32_dpp v236, v236, v236 row_bcast:31 row_mask:0xf bank_mask:0xf bound_ctrl:0\n"
    "v_mul_f32 v228, v228, v208\nv_mul_f32 v230, v230, v209\nv_mul_f32 v216, v216, v210\nv_mul_f32 v218, v218, v211\nv_mul_f32 v229, v229, v208\nv_mul_f32 v231, v231, v209\n"
    "v_mul_f32 v217, v217, v210\nv_mul_f32 v219, v219, v211\nv_readlane_b32 s86, v236, 63\ns_nop 0\ns_lshr_b32 s86, s86, 23\ns_max_u32 s86, s86, 9\ns_min_u32 s86, s86, 254\n"
    "s_sub_u32 s87, 261, s86\ns_lshl_b32 s87, s87, 23\ns_sub_u32 s86, s86, 7\ns_lshl_b32 s86, s86, 23\nv_mul_f32 v212, s87, v212\nv_mul_f32 v213, s87, v213\n"
    "v_mul_f32 v214, s87, v214\nv_mul_f32 v215, s87, v215\nv_mov_b32 v236, s86\ns_cbranch_vccz .Lcn23\nglobal_load_dwordx4 v[96:99], v5, s[32:33] offset:-4096 sc1\n"
    "global_load_dwordx4 v[100:103], v5, s[32:33] offset:-3072 sc1\nglobal_load_dwordx4 v[104:107], v5, s[32:33] offset:-2048 sc1\n"
    "global_load_dwordx4 v[108:111], v5, s[32:33] offset:-1024 sc1\n.Lcn23:\nv_mul_f32 v236, s92, v236\nv_mov_b32 v29, 0\n"
    "v_pk_mul_f32 v[200:201], v[216:217], v[212:213] op_sel_hi:[0,1]\nv_pk_mul_f32 v[202:203], v[218:219], v[214:215] op_sel_hi:[0,1]\n"
    "v_pk_fma_f32 v[216:217], v[216:217], v[212:213], v[200:201] op_sel:[1,1,0] op_sel_hi:[1,0,1] neg_lo:[0,1,0]\n"
    "v_pk_fma_f32 v[218:219], v[218:219], v[214:215], v[202:203] op_sel:[1,1,0] op_sel_hi:[1,0,1] neg_lo:[0,1,0]\ns_bitcmp1_b32 s3, 0\ns_cbranch_scc0 .LL9\n"
    "v_pk_mul_f32 v[200:201], v[212:213], v[228:229] op_sel_hi:[0,1]\nv_pk_mul_f32 v[202:203], v[214:215], v[230:231] op_sel_hi:[0,1]\n"
    "v_pk_fma_f32 v[212:213], v[212:213], v[228:229], v[200:201] op_sel:[1,1,0] op_sel_hi:[1,0,1] neg_lo:[0,1,0]\n"
    "v_pk_fma_f32 v[214:215], v[214:215], v[230:231], v[202:203] op_sel:[1,1,0] op_sel_hi:[1,0,1] neg_lo:[0,1,0]\n.LL9:\ns_mov_b64 exec, 1\nds_write_b32 v7, v236\n"
    "ds_write_b16 v8, v29\ns_mov_b64 exec, -1\nv_cvt_pk_f16_f32 v204, v212, v213\nv_cvt_pk_f16_f32 v205, v214, v215\n"
    "v_pk_mul_f32 v[200:201], v[212:213], v[228:229] op_sel_hi:[0,1]\nv_pk_mul_f32 v[202:203], v[214:215], v[230:231] op_sel_hi:[0,1]\nds_write_b64 v3, v[204:205] offset:0\n"
    "v_pk_fma_f32 v[212:213], v[212:213], v[228:229], v[200:201] op_sel:[1,1,0] op_sel_hi:[1,0,1] neg_lo:[0,1,0]\n"
    "v_pk_fma_f32 v[214:215], v[214:215], v[230:231], v[202:203] op_sel:[1,1,0] op_sel_hi:[1,0,1] neg_lo:[0,1,0]\nv_cvt_pk_f16_f32 v206, v212, v213\n"
    "v_cvt_pk_f16_f32 v207, v214, v215\nv_pk_mul_f32 v[200:201], v[212:213], v[228:229] op_sel_hi:[0,1]\nv_pk_mul_f32 v[202:203], v[214:215], v[230:231] op_sel_hi:[0,1]\n"
    "ds_write_b64 v3, v[206:207] offset:544\nv_pk_fma_f32 v[212:213], v[212:213], v[228:229], v[200:201] op_sel:[1,1,0] op_sel_hi:[1,0,1] neg_lo:[0,1,0]\n"
    "v_pk_fma_f32 v[214:215], v[214:215], v[230:231], v[202:203] op_sel:[1,1,0] op_sel_hi:[1,0,1] neg_lo:[0,1,0]\nv_cvt_pk_f16_f32 v204, v212, v213\n"
    "v_cvt_pk_f16_f32 v205, v214, v215\nv_pk_mul_f32 v[200:201], v[212:213], v[228:229] op_sel_hi:[0,1]\nv_pk_mul_f32 v[202:203], v[214:215], v[230:231] op_sel_hi:[0,1]\n"
    "ds_write_b64 v3, v[204:205] offset:1088\nv_pk_fma_f32 v[212:213], v[212:213], v[228:229], v[200:201] op_sel:[1,1,0] op_sel_hi:[1,0,1] neg_lo:[0,1,0]\n"
    "v_pk_fma_f32 v[214:215], v[214:215], v[230:231], v[202:203] op_sel:[1,1,0] op_sel_hi:[1,0,1] neg_lo:[0,1,0]\nv_cvt_pk_f16_f32 v206, v212, v213\n"
    "v_cvt_pk_f16_f32 v207, v214, v215\nv_pk_mul_f32 v[200:201], v[212:213], v[228:229] op_sel_hi:[0,1]\nv_pk_mul_f32 v[202:203], v[214:215], v[230:231] op_sel_hi:[0,1]\n"
    "ds_write_b64 v3, v[206:207] offset:1632\nv_pk_fma_f32 v[212:213], v[212:213], v[228:229], v[200:201] op_sel:[1,1,0] op_sel_hi:[1,0,1] neg_lo:[0,1,0]\n"
    "v_pk_fma_f32 v[214:215], v[214:215], v[230:231], v[202:203] op_sel:[1,1,0] op_sel_hi:[1,0,1] neg_lo:[0,1,0]\nv_cvt_pk_f16_f32 v204, v212, v213\n"
    "v_cvt_pk_f16_f32 v205, v214, v215\nv_pk_mul_f32 v[200:201], v[212:213], v[228:229] op_sel_hi:[0,1]\nv_pk_mul_f32 v[202:203], v[214:215], v[230:231] op_sel_hi:[0,1]\n"
    "ds_write_b64 v3, v[204:205] offset:2176\nv_pk_fma_f32 v[212:213], v[212:213], v[228:229], v[200:201] op_sel:[1,1,0] op_sel_hi:[1,0,1] neg_lo:[0,1,0]\n"
    "v_pk_fma_f32 v[214:215], v[214:215], v[230:231], v[202:203] op_sel:[1,1,0] op_sel_hi:[1,0,1] neg_lo:[0,1,0]\nv_cvt_pk_f16_f32 v206, v212, v213\n"
    "v_cvt_pk_f16_f32 v207, v214, v215\nv_pk_mul_f32 v[200:201], v[212:213], v[228:229] op_sel_hi:[0,1]\nv_pk_mul_f32 v[202:203], v[214:215], v[230:231] op_sel_hi:[0,1]\n"
    "ds_write_b64 v3, v[206:207] offset:2720\nv_pk_fma_f32 v[212:213], v[212:213], v[228:229], v[200:201] op_sel:[1,1,0] op_sel_hi:[1,0,1] neg_lo:[0,1,0]\n"
    "v_pk_fma_f32 v[214:215], v[214:215], v[230:231], v[202:203] op_sel:[1,1,0] op_sel_hi:[1,0,1] neg_lo:[0,1,0]\nv_cvt_pk_f16_f32 v204, v212, v213\n"
    "v_cvt_pk_f16_f32 v205, v214, v215\nv_pk_mul_f32 v[200:201], v[212:213], v[228:229] op_sel_hi:[0,1]\nv_pk_mul_f32 v[202:203], v[214:215], v[230:231] op_sel_hi:[0,1]\n"
    "ds_write_b64 v3, v[204:205] offset:3264\nv_pk_fma_f32 v[212:213], v[212:213], v[228:229], v[200:201] op_sel:[1,1,0] op_sel_hi:[1,0,1] neg_lo:[0,1,0]\n"
    "v_pk_fma_f32 v[214:215], v[214:215], v[230:231], v[202:203] op_sel:[1,1,0] op_sel_hi:[1,0,1] neg_lo:[0,1,0]\nv_cvt_pk_f16_f32 v206, v212, v213\n"
    "v_cvt_pk_f16_f32 v207, v214, v215\nds_write_b64 v3, v[206:207] offset:3808\ns_bitcmp1_b32 s3, 0\ns_cbranch_scc1 .LL10\n"
    "v_pk_mul_f32 v[200:201], v[228:229], v[228:229] op_sel_hi:[0,1]\nv_pk_mul_f32 v[202:203], v[230:231], v[230:231] op_sel_hi:[0,1]\n"
    "v_pk_fma_f32 v[228:229], v[228:229], v[228:229], v[200:201] op_sel:[1,1,0] op_sel_hi:[1,0,1] neg_lo:[0,1,0]\n"
    "v_pk_fma_f32 v[230:231], v[230:231], v[230:231], v[202:203] op_sel:[1,1,0] op_sel_hi:[1,0,1] neg_lo:[0,1,0]\n.LL10:\nv_cvt_pk_f16_f32 v204, v216, v217\n"
    "v_cvt_pk_f16_f32 v205, v218, v219\nv_pk_mul_f32 v[200:201], v[216:217], v[228:229] op_sel_hi:[0,1]\nv_pk_mul_f32 v[202:203], v[218:219], v[230:231] op_sel_hi:[0,1]\n"
    "ds_write_b64 v3, v[204:205] offset:4352\nv_pk_fma_f32 v[216:217], v[216:217], v[228:229], v[200:201] op_sel:[1,1,0] op_sel_hi:[1,0,1] neg_lo:[0,1,0]\n"
    "v_pk_fma_f32 v[218:219], v[218:219], v[230:231], v[202:203] op_sel:[1,1,0] op_sel_hi:[1,0,1] neg_lo:[0,1,0]\nv_cvt_pk_f16_f32 v206, v216, v217\n"
    "v_cvt_pk_f16_f32 v207, v218, v219\nv_pk_mul_f32 v[200:201], v[216:217], v[228:229] op_sel_hi:[0,1]\nv_pk_mul_f32 v[202:203], v[218:219], v[230:231] op_sel_hi:[0,1]\n"
    "ds_write_b64 v3, v[206:207] offset:4896\nv_pk_fma_f32 v[216:217], v[216:217], v[228:229], v[200:201] op_sel:[1,1,0] op_sel_hi:[1,0,1] neg_lo:[0,1,0]\n"
    "v_pk_fma_f32 v[218:219], v[218:219], v[230:231], v[202:203] op_sel:[1,1,0] op_sel_hi:[1,0,1] neg_lo:[0,1,0]\nv_cvt_pk_f16_f32 v204, v216, v217\n"
    "v_cvt_pk_f16_f32 v205, v218, v219\nds_write_b64 v3, v[204:205] offset:5440\ns_setprio 0\nglobal_load_dwordx4 v[240:243], v6, s[8:9]\n"
    "global_load_dwordx4 v[244:247], v6, s[8:9] offset:32\ns_cmp_eq_u32 s43, 1\ns_cbranch_scc1 .LL17\nv_and_b32 v10, 3, v0\ns_lshl2_add_u32 s16, s40, s3\n"
    "s_lshl_b32 s15, s16, 9\ns_add_u32 s14, s15, 0x1002000\nv_lshl_add_u32 v3, v10, 7, s14\nv_and_b32 v11, 4, v0\nv_lshl_add_u32 v3, v11, 2, v3\ns_mov_b32 s80, 0x10000\n"
    "global_load_dwordx4 v[196:199], v3, s[32:33] sc1\ns_waitcnt vmcnt(0)\n.Ltp24:\nv_xor_b32 v196, s34, v196\nv_xor_b32 v197, s34, v197\nv_xor_b32 v198, s34, v198\n"
    "v_xor_b32 v199, s34, v199\nv_or3_b32 v196, v196, v197, v198\nv_or_b32 v196, v196, v199\nv_cmp_ne_u32 vcc, 0, v196\ns_cbranch_vccz .Ltq25\ns_sub_u32 s80, s80, 1\n"
    "s_cbranch_scc1 .Ltq25\ns_sleep 2\nglobal_load_dwordx4 v[196:199], v3, s[32:33] sc1\ns_waitcnt vmcnt(0)\ns_branch .Ltp24\n.Ltq25:\n"
    "global_load_dwordx4 v[132:135], v58, s[32:33] offset:-4096 sc1\nglobal_load_dwordx4 v[136:139], v58, s[32:33] offset:-3072 sc1\n"
    "global_load_dwordx4 v[140:143], v58, s[32:33] offset:-2048 sc1\nglobal_load_dwordx4 v[144:147], v58, s[32:33] offset:-1024 sc1\n"
    "global_load_dwordx4 v[148:151], v58, s[32:33] offset:0 sc1\nglobal_load_dwordx4 v[152:155], v58, s[32:33] offset:1024 sc1\n"
    "global_load_dwordx4 v[156:159], v58, s[32:33] offset:2048 sc1\nglobal_load_dwordx4 v[160:163], v58, s[32:33] offset:3072 sc1\n"
    "global_load_dwordx4 v[164:167], v59, s[32:33] offset:-4096 sc1\nglobal_load_dwordx4 v[168:171], v59, s[32:33] offset:-3072 sc1\n"
    "global_load_dwordx4 v[172:175], v59, s[32:33] offset:-2048 sc1\nglobal_load_dwordx4 v[176:179], v59, s[32:33] offset:-1024 sc1\n"
    "global_load_dwordx4 v[180:183], v59, s[32:33] offset:0 sc1\nglobal_load_dwordx4 v[184:187], v59, s[32:33] offset:1024 sc1\n"
    "global_load_dwordx4 v[188:191], v59, s[32:33] offset:2048 sc1\nglobal_load_dwordx4 v[192:195], v59, s[32:33] offset:3072 sc1\n"
    "global_load_dwordx4 v[64:67], v4, s[32:33] offset:-4096 sc1\nglobal_load_dwordx4 v[68:71], v4, s[32:33] offset:-3072 sc1\n"
    "global_load_dwordx4 v[72:75], v4, s[32:33] offset:-2048 sc1\nglobal_load_dwordx4 v[76:79], v4, s[32:33] offset:-1024 sc1\n"
    "global_load_dwordx4 v[80:83], v4, s[32:33] offset:0 sc1\nglobal_load_dwordx4 v[84:87], v4, s[32:33] offset:1024 sc1\n"
    "global_load_dwordx4 v[88:91], v4, s[32:33] offset:2048 sc1\nglobal_load_dwordx4 v[92:95], v4, s[32:33] offset:3072 sc1\n"
    "global_load_dwordx4 v[96:99], v5, s[32:33] offset:-4096 sc1\nglobal_load_dwordx4 v[100:103], v5, s[32:33] offset:-3072 sc1\n"
    "global_load_dwordx4 v[104:107], v5, s[32:33] offset:-2048 sc1\nglobal_load_dwordx4 v[108:111], v5, s[32:33] offset:-1024 sc1\n"
    "global_load_dwordx4 v[112:115], v5, s[32:33] offset:0 sc1\nglobal_load_dwordx4 v[116:119], v5, s[32:33] offset:1024 sc1\n"
    "global_load_dwordx4 v[120:123], v5, s[32:33] offset:2048 sc1\nglobal_load_dwordx4 v[124:127], v5, s[32:33] offset:3072 sc1\ns_waitcnt vmcnt(0)\ns_mov_b32 s43, 1\n.LL17:\n"
    "s_branch .LL16\n.LL18:\ns_waitcnt lgkmcnt(0)\ns_barrier\nv_and_b32 v10, 63, v0\ns_lshl_b32 s16, s39, 15\ns_lshl2_add_u32 s18, s40, s3\ns_lshl_b32 s17, s18, 11\n"
    "s_add_u32 s15, s16, s17\ns_add_u32 s14, s15, 0x600000\nv_lshl_add_u32 v2, v10, 4, s14\ns_lshl_b32 s20, s3, 11\ns_add_u32 s19, s20, 0x1c200\nv_lshl_add_u32 v3, v10, 4, s19\n"
    "v_bfe_u32 v12, v0, 5, 1\nv_bfe_u32 v13, v0, 2, 2\nv_lshl_add_u32 v11, v12, 2, v13\nv_lshl_add_u32 v4, v11, 6, s19\nv_bfe_u32 v14, v0, 4, 1\nv_lshl_add_u32 v4, v14, 5, v4\n"
    "v_and_b32 v15, 3, v0\nv_lshl_add_u32 v4, v15, 3, v4\nglobal_load_dwordx4 v[240:243], v2, s[32:33] offset:0 sc1\n"
    "global_load_dwordx4 v[244:247], v2, s[32:33] offset:1024 sc1\ns_mov_b32 s80, 0x10000\n.LL26:\ns_waitcnt vmcnt(0)\ns_mov_b64 s[82:83], 0\nv_max3_u32 v5, v240, v241, v242\n"
    "v_max_u32 v5, v5, v243\nv_cmp_eq_u32 vcc, -1, v5\ns_nop 0\ns_or_b64 s[82:83], s[82:83], vcc\ns_cbranch_vccz .Lag28\ns_mov_b64 exec, vcc\n"
    "global_load_dwordx4 v[240:243], v2, s[32:33] offset:0 sc1\ns_mov_b64 exec, -1\n.Lag28:\nv_max3_u32 v5, v244, v245, v246\nv_max_u32 v5, v5, v247\nv_cmp_eq_u32 vcc, -1, v5\n"
    "s_nop 0\ns_or_b64 s[82:83], s[82:83], vcc\ns_cbranch_vccz .Lag29\ns_mov_b64 exec, vcc\nglobal_load_dwordx4 v[244:247], v2, s[32:33] offset:1024 sc1\ns_mov_b64 exec, -1\n"
    ".Lag29:\ns_cmp_eq_u64 s[82:83], 0\ns_cbranch_scc1 .LL27\ns_sub_u32 s80, s80, 1\ns_cbranch_scc0 .LL26\ns_waitcnt vmcnt(0)\n.LL27:\nds_write_b128 v3, v[240:243]\n"
    "ds_write_b128 v3, v[244:247] offset:1024\nds_read_b64_tr_b16 v[240:241], v4 offset:0\nds_read_b64_tr_b16 v[242:243], v4 offset:512\n"
    "ds_read_b64_tr_b16 v[244:245], v4 offset:1024\nds_read_b64_tr_b16 v[246:247], v4 offset:1536\ns_waitcnt lgkmcnt(0)\n.LL16:\nv_mov_b32 v36, 0x3f803f80\n"
    "s_waitcnt vmcnt(2) lgkmcnt(0)\ns_setprio 2\ns_barrier\nds_read_b128 v[196:199], v51 offset:24576\nds_read_b128 v[200:203], v51 offset:25600\n"
    "ds_read_b128 v[204:207], v51 offset:26624\nds_read_b128 v[208:211], v51 offset:27648\nds_read_b128 v[212:215], v51 offset:28672\nds_read_b128 v[216:219], v51 offset:29696\n"
    "s_waitcnt lgkmcnt(5)\nv_mfma_f32_32x32x16_bf16 v[2:17], v[132:135], v[196:199], 0\nds_read_b128 v[196:199], v51 offset:30720\ns_waitcnt lgkmcnt(5)\n"
    "v_mfma_f32_32x32x16_bf16 v[2:17], v[136:139], v[200:203], v[2:17]\nds_read_b128 v[200:203], v51 offset:31744\ns_waitcnt lgkmcnt(5)\n"
    "v_mfma_f32_32x32x16_bf16 v[2:17], v[140:143], v[204:207], v[2:17]\ns_barrier\nds_read_b128 v[204:207], v51 offset:0\ns_waitcnt lgkmcnt(5)\n"
    "v_mfma_f32_32x32x16_bf16 v[2:17], v[144:147], v[208:211], v[2:17]\nds_read_b128 v[208:211], v51 offset:1024\ns_waitcnt lgkmcnt(5)\n"
    "v_mfma_f32_32x32x16_bf16 v[2:17], v[148:151], v[212:215], v[2:17]\nds_read_b128 v[212:215], v51 offset:2048\ns_waitcnt lgkmcnt(5)\n"
    "v_mfma_f32_32x32x16_bf16 v[2:17], v[152:155], v[216:219], v[2:17]\nds_read_b128 v[216:219], v51 offset:3072\ns_waitcnt lgkmcnt(5)\n"
    "v_mfma_f32_32x32x16_bf16 v[2:17], v[156:159], v[196:199], v[2:17]\nds_read_b128 v[196:199], v51 offset:4096\ns_waitcnt lgkmcnt(5)\n"
    "v_mfma_f32_32x32x16_bf16 v[2:17], v[160:163], v[200:203], v[2:17]\nds_read_b128 v[200:203], v51 offset:5120\ns_waitcnt lgkmcnt(5)\n"
    "v_mfma_f32_32x32x16_bf16 v[2:17], v[164:167], v[204:207], v[2:17]\nds_read_b128 v[204:207], v51 offset:6144\ns_waitcnt lgkmcnt(5)\n"
    "v_mfma_f32_32x32x16_bf16 v[2:17], v[168:171], v[208:211], v[2:17]\nds_read_b128 v[208:211], v51 offset:7168\ns_waitcnt lgkmcnt(5)\n"
    "v_mfma_f32_32x32x16_bf16 v[2:17], v[172:175], v[212:215], v[2:17]\ns_barrier\nds_read_b128 v[212:215], v51 offset:8192\ns_waitcnt lgkmcnt(5)\n"
    "v_mfma_f32_32x32x16_bf16 v[2:17], v[176:179], v[216:219], v[2:17]\nds_read_b128 v[216:219], v51 offset:9216\ns_waitcnt lgkmcnt(5)\n"
    "v_mfma_f32_32x32x16_bf16 v[2:17], v[180:183], v[196:199], v[2:17]\nds_read_b128 v[196:199], v51 offset:10240\ns_waitcnt lgkmcnt(5)\n"
    "v_mfma_f32_32x32x16_bf16 v[2:17], v[184:187], v[200:203], v[2:17]\nds_read_b128 v[200:203], v51 offset:11264\ns_waitcnt lgkmcnt(5)\n"
    "v_mfma_f32_32x32x16_bf16 v[2:17], v[188:191], v[204:207], v[2:17]\nds_read_b128 v[204:207], v51 offset:12288\ns_waitcnt lgkmcnt(5)\n"
    "v_mfma_f32_32x32x16_bf16 v[2:17], v[192:195], v[208:211], v[2:17]\nds_read_b128 v[208:211], v51 offset:13312\ns_waitcnt lgkmcnt(5)\n"
    "v_mfma_f32_32x32x16_bf16 v[2:17], v[64:67], v[212:215], v[2:17]\nds_read_b128 v[212:215], v51 offset:14336\ns_waitcnt lgkmcnt(5)\n"
    "v_mfma_f32_32x32x16_bf16 v[2:17], v[68:71], v[216:219], v[2:17]\nds_read_b128 v[216:219], v51 offset:15360\ns_waitcnt lgkmcnt(5)\n"
    "v_mfma_f32_32x32x16_bf16 v[2:17], v[72:75], v[196:199], v[2:17]\ns_barrier\nds_read_b128 v[196:199], v51 offset:16384\ns_waitcnt lgkmcnt(5)\n"
    "v_mfma_f32_32x32x16_bf16 v[2:17], v[76:79], v[200:203], v[2:17]\nds_read_b128 v[200:203], v51 offset:17408\ns_waitcnt lgkmcnt(5)\n"
    "v_mfma_f32_32x32x16_bf16 v[2:17], v[80:83], v[204:207], v[2:17]\nds_read_b128 v[204:207], v51 offset:18432\ns_waitcnt lgkmcnt(5)\n"
    "v_mfma_f32_32x32x16_bf16 v[2:17], v[84:87], v[208:211], v[2:17]\nds_read_b128 v[208:211], v51 offset:19456\ns_waitcnt lgkmcnt(5)\n"
    "v_mfma_f32_32x32x16_bf16 v[2:17], v[88:91], v[212:215], v[2:17]\nds_read_b128 v[212:215], v51 offset:20480\ns_waitcnt lgkmcnt(5)\n"
    "v_mfma_f32_32x32x16_bf16 v[2:17], v[92:95], v[216:219], v[2:17]\nds_read_b128 v[216:219], v51 offset:21504\ns_waitcnt lgkmcnt(5)\n"
    "v_mfma_f32_32x32x16_bf16 v[2:17], v[96:99], v[196:199], v[2:17]\nds_read_b128 v[196:199], v51 offset:22528\ns_waitcnt lgkmcnt(5)\n"
    "v_mfma_f32_32x32x16_bf16 v[2:17], v[100:103], v[200:203], v[2:17]\nds_read_b128 v[200:203], v51 offset:23552\ns_cmp_eq_u32 s6, 0\ns_cbranch_scc0 .LL30\ns_waitcnt vmcnt(0)\n"
    "v_permlane32_swap_b32 v240, v242\nv_permlane32_swap_b32 v241, v243\nv_permlane32_swap_b32 v244, v246\nv_permlane32_swap_b32 v245, v247\n.LL30:\ns_waitcnt lgkmcnt(5)\n"
    "v_mfma_f32_32x32x16_bf16 v[2:17], v[104:107], v[204:207], v[2:17]\ns_waitcnt lgkmcnt(4)\nv_mfma_f32_32x32x16_bf16 v[2:17], v[108:111], v[208:211], v[2:17]\n"
    "v_lshlrev_b32 v220, 16, v240\nv_and_b32 v221, 0xffff0000, v240\nv_lshlrev_b32 v222, 16, v241\nv_and_b32 v223, 0xffff0000, v241\ns_waitcnt lgkmcnt(3)\n"
    "v_mfma_f32_32x32x16_bf16 v[2:17], v[112:115], v[212:215], v[2:17]\nv_lshlrev_b32 v224, 16, v242\nv_and_b32 v225, 0xffff0000, v242\nv_lshlrev_b32 v226, 16, v243\n"
    "v_and_b32 v227, 0xffff0000, v243\ns_waitcnt lgkmcnt(2)\nv_mfma_f32_32x32x16_bf16 v[2:17], v[116:119], v[216:219], v[2:17]\nv_lshlrev_b32 v228, 16, v244\n"
    "v_and_b32 v229, 0xffff0000, v244\nv_lshlrev_b32 v230, 16, v245\nv_and_b32 v231, 0xffff0000, v245\ns_waitcnt lgkmcnt(1)\n"
    "v_mfma_f32_32x32x16_bf16 v[2:17], v[120:123], v[196:199], v[2:17]\nv_lshlrev_b32 v232, 16, v246\nv_and_b32 v233, 0xffff0000, v246\nv_lshlrev_b32 v234, 16, v247\n"
    "v_and_b32 v235, 0xffff0000, v247\ns_waitcnt lgkmcnt(0)\nv_mfma_f32_32x32x16_bf16 v[2:17], v[124:127], v[200:203], v[2:17]\ns_nop 11\n"
    "v_pk_add_f32 v[2:3], v[2:3], v[220:221]\nv_pk_add_f32 v[4:5], v[4:5], v[222:223]\nv_pk_add_f32 v[6:7], v[6:7], v[224:225]\nv_pk_add_f32 v[8:9], v[8:9], v[226:227]\n"
    "v_pk_add_f32 v[10:11], v[10:11], v[228:229]\nv_pk_add_f32 v[12:13], v[12:13], v[230:231]\nv_pk_add_f32 v[14:15], v[14:15], v[232:233]\n"
    "v_pk_add_f32 v[16:17], v[16:17], v[234:235]\nv_cvt_pk_bf16_f32 v40, v2, v3\nv_cvt_pk_bf16_f32 v41, v4, v5\nds_write2_b32 v48, v40, v41 offset0:0 offset1:32\n"
    "v_cvt_pk_bf16_f32 v42, v6, v7\nv_cvt_pk_bf16_f32 v43, v8, v9\nds_write2_b32 v48, v42, v43 offset0:128 offset1:160\nv_cvt_pk_bf16_f32 v44, v10, v11\n"
    "v_cvt_pk_bf16_f32 v45, v12, v13\nds_write2_b32 v49, v44, v45 offset0:0 offset1:32\nv_cvt_pk_bf16_f32 v46, v14, v15\nv_cvt_pk_bf16_f32 v47, v16, v17\n"
    "ds_write2_b32 v49, v46, v47 offset0:128 offset1:160\nds_read_b128 v[2:5], v50\nds_read_b128 v[6:9], v50 offset:1024\nv_dot2_f32_bf16 v32, v40, v36, 0\n"
    "v_dot2_f32_bf16 v33, v40, v40, 0\nv_dot2_f32_bf16 v34, v41, v36, 0\nv_dot2_f32_bf16 v35, v41, v41, 0\nv_dot2c_f32_bf16 v32, v36, v42\nv_dot2c_f32_bf16 v33, v42, v42\n"
    "v_dot2c_f32_bf16 v34, v36, v43\nv_dot2c_f32_bf16 v35, v43, v43\nv_dot2c_f32_bf16 v32, v36, v44\nv_dot2c_f32_bf16 v33, v44, v44\nv_dot2c_f32_bf16 v34, v36, v45\n"
    "v_dot2c_f32_bf16 v35, v45, v45\nv_dot2c_f32_bf16 v32, v36, v46\nv_dot2c_f32_bf16 v33, v46, v46\nv_dot2c_f32_bf16 v34, v36, v47\nv_dot2c_f32_bf16 v35, v47, v47\ns_nop 1\n"
    "v_add_f32 v32, v32, v34\nv_add_f32 v33, v33, v35\ns_nop 1\nv_permlane32_swap_b32 v32, v33\nv_add_f32 v32, v32, v33\nds_write_b32 v54, v32\ns_waitcnt lgkmcnt(0)\ns_barrier\n"
    "global_store_dwordx4 v52, v[2:5], s[32:33] sc1\nglobal_store_dwordx4 v53, v[6:9], s[32:33] sc1\ns_setprio 0\ns_mov_b32 s14, 0x1d550\nv_lshl_add_u32 v14, v0, 3, s14\n"
    "v_lshrrev_b32 v48, 3, v0\nv_lshl_add_u32 v14, v48, 4, v14\ns_mov_b32 s15, 0x18000\nv_lshl_add_u32 v15, v48, 7, s15\nv_xor_b32 v50, v0, v48\nv_and_b32 v49, 7, v50\n"
    "v_lshl_add_u32 v15, v49, 4, v15\nv_and_b32 v51, 0x7f, v0\ns_lshr_b32 s18, s3, 1\ns_mul_i32 s17, s18, 0xfc0\ns_add_u32 s16, s17, 0x209c0\nv_lshl_add_u32 v16, v51, 4, s16\n"
    "s_lshl_b32 s20, s41, 7\ns_add_u32 s19, s20, 0x600000\nv_lshl_add_u32 v52, v48, 15, s19\nv_and_b32 v53, 1, v0\nv_lshl_add_u32 v52, v53, 6, v52\nv_bfe_u32 v54, v0, 1, 2\n"
    "v_lshl_add_u32 v52, v54, 4, v52\nv_lshrrev_b32 v56, 4, v0\ns_lshl_b32 s23, s35, 18\ns_lshl_b32 s24, s36, 8\ns_add_u32 s22, s23, s24\ns_add_u32 s21, s22, 0x1200000\n"
    "v_lshl_add_u32 v55, v56, 13, s21\nv_and_b32 v57, 15, v0\nv_lshl_add_u32 v55, v57, 4, v55\ns_cmp_eq_u32 s6, 0\ns_cselect_b64 vcc, -1, 0\nv_cndmask_b32 v17, v55, v52, vcc\n"
    "v_bfe_u32 v60, v0, 5, 1\nv_and_b32 v62, 31, v0\nv_sub_u32 v61, 0x3ff, v62\nv_lshl_add_u32 v59, v60, 3, v61\nv_lshrrev_b32 v58, 2, v59\ns_mul_i32 s44, s18, 0x2100\n"
    "s_add_u32 s25, s44, 0x17800\nv_lshl_add_u32 v19, v58, 3, s25\nv_and_b32 v63, 3, v59\ns_mov_b32 s45, 0x840\nv_mad_u32_u24 v19, v63, s45, v19\n"
    "s_lshl2_add_u32 s46, s3, 0x27f80\nv_mov_b32 v12, s46\ns_add_u32 s47, s6, 12\nv_mov_b32 v13, s47\ns_mov_b32 s48, 0x1cc34\nv_lshl_add_u32 v5, v0, 3, s48\n"
    "v_add_u32 v65, 0xffffff86, v0\nv_lshrrev_b32 v64, 3, v65\nv_lshl_add_u32 v5, v64, 4, v5\ns_mov_b32 s49, 0x1cc38\nv_lshl_add_u32 v6, v0, 3, s49\n"
    "v_add_u32 v67, 0xffffff87, v0\nv_lshrrev_b32 v66, 3, v67\nv_lshl_add_u32 v6, v66, 4, v6\nv_bfe_u32 v69, v0, 2, 2\nv_lshl_add_u32 v68, v60, 3, v69\n"
    "s_mul_i32 s51, s18, 0x3180\ns_cmp_eq_u32 s6, 0\ns_cbranch_scc0 .LL32\ns_barrier\ns_sleep 0\n.LL32:\ns_add_u32 s50, s51, 0x20300\nv_lshl_add_u32 v2, v68, 4, s50\n"
    "v_lshl_add_u32 v2, v53, 3, v2\ns_and_b32 s53, s3, 1\ns_xor_b32 s52, s53, 1\nv_and_b32 v71, 3, v56\nv_and_b32 v70, s52, v71\ns_mov_b32 s54, 0x480\n"
    "v_mad_u32_u24 v2, v70, s54, v2\nv_bfe_u32 v72, v0, 1, 1\ns_mov_b32 s55, 0x240\nv_mad_u32_u24 v2, v72, s55, v2\ns_add_u32 s56, s50, 0x1f80\nv_lshl_add_u32 v4, v68, 5, s56\n"
    "v_and_b32 v73, 3, v0\nv_lshl_add_u32 v4, v73, 3, v4\nv_and_b32 v74, 1, v56\nv_mad_u32_u24 v3, v74, s54, v4\ns_add_u32 s58, s50, 0x6c0\ns_lshl_b32 s60, s18, 11\n"
    "s_add_u32 s59, s60, 0x26600\ns_cmp_eq_u32 s53, 0\ns_cselect_b32 s57, s58, s59\nv_lshl_add_u32 v7, v62, 5, s57\nv_lshl_add_u32 v7, v60, 4, v7\n"
    "v_lshl_add_u32 v8, v62, 6, s56\nv_lshrrev_b32 v77, 1, v0\nv_and_b32 v76, 2, v77\nv_xor_b32 v75, v60, v76\nv_lshl_add_u32 v8, v75, 4, v8\nv_lshlrev_b32 v79, 4, v0\n"
    "v_and_b32 v81, 16, v0\nv_lshlrev_b32 v80, 1, v81\nv_xor_b32 v78, v79, v80\nv_add_u32 v9, 0x21280, v78\ns_lshl_b32 s61, s52, 8\nv_add_u32 v10, s61, v19\n"
    "v_and_b32 v82, 63, v0\ns_mul_i32 s64, s18, 0x1950\ns_mul_i32 s65, s53, 0x500\ns_add_u32 s63, s64, s65\ns_add_u32 s62, s63, 0x1d530\nv_lshl_add_u32 v40, v82, 4, s62\n"
    "v_add_u32 v84, 3, v82\nv_lshrrev_b32 v83, 2, v84\nv_lshl_add_u32 v40, v83, 4, v40\ns_add_u32 s66, s63, 0x1d528\nv_lshl_add_u32 v41, v82, 4, s66\nv_add_u32 v86, 2, v82\n"
    "v_lshrrev_b32 v85, 2, v86\nv_lshl_add_u32 v41, v85, 4, v41\ns_add_u32 s67, s63, 0x1d550\nv_lshl_add_u32 v42, v82, 4, s67\nv_bfe_u32 v87, v0, 2, 4\n"
    "v_lshl_add_u32 v42, v87, 4, v42\ns_mul_i32 s70, s53, 0x480\ns_add_u32 s69, s50, s70\ns_add_u32 s68, s69, 0x250\nv_lshl_add_u32 v43, v82, 4, s68\nv_add_u32 v89, 1, v82\n"
    "v_lshrrev_b32 v88, 5, v89\nv_lshl_add_u32 v43, v88, 6, v43\ns_add_u32 s71, s69, 0x260\nv_lshl_add_u32 v44, v82, 4, s71\nv_lshrrev_b32 v90, 5, v86\n"
    "v_lshl_add_u32 v44, v90, 6, v44\ns_add_u32 s72, s69, 0x1f90\nv_lshl_add_u32 v91, v82, 4, s72\nv_lshrrev_b32 v92, 6, v89\nv_lshl_add_u32 v91, v92, 7, v91\n"
    "s_add_u32 s75, s69, 0x1f80\ns_add_u32 s74, s75, 0x400\ns_add_u32 s73, s74, 0xfffffb80\nv_mov_b32 v93, s73\nv_add_u32 v94, s53, v89\nv_cmp_gt_u32 vcc, 0x41, v94\ns_nop 1\n"
    "v_cndmask_b32 v45, v93, v91, vcc\ns_add_u32 s76, s75, 0x900\nv_lshl_add_u32 v29, v82, 4, s76\ns_add_u32 s78, s69, 0x440\ns_add_u32 s77, s78, 0xfffff700\n"
    "v_mov_b32 v95, s77\nv_cmp_gt_u32 vcc, 64, v89\ns_nop 1\nv_cndmask_b32 v30, v95, v43, vcc\nv_cmp_gt_u32 vcc, 64, v86\ns_nop 1\nv_cndmask_b32 v31, v95, v44, vcc\n"
    "s_lshl3_add_u32 s79, s18, 0x27fd0\nv_lshl_add_u32 v11, v53, 2, s79\ns_cmp_eq_u32 s6, 0\ns_cbranch_scc1 .LL34\nds_read_b64 v[64:65], v19 offset:896\n"
    "ds_read_b64 v[66:67], v19 offset:904\nds_read_b64 v[68:69], v19 offset:864\nds_read_b64 v[70:71], v19 offset:872\nds_read_b64 v[72:73], v19 offset:832\n"
    "ds_read_b64 v[74:75], v19 offset:840\nds_read_b64 v[76:77], v19 offset:800\nds_read_b64 v[78:79], v19 offset:808\nds_read_b64 v[80:81], v19 offset:704\n"
    "ds_read_b64 v[82:83], v19 offset:712\nds_read_b64 v[84:85], v19 offset:672\nds_read_b64 v[86:87], v19 offset:680\nds_read_b64 v[88:89], v19 offset:640\n"
    "ds_read_b64 v[90:91], v19 offset:648\nds_read_b64 v[92:93], v19 offset:608\nds_read_b64 v[94:95], v19 offset:616\nds_read_b64 v[96:97], v10 offset:512\n"
    "ds_read_b64 v[98:99], v10 offset:520\nds_read_b64 v[100:101], v10 offset:480\nds_read_b64 v[102:103], v10 offset:488\nds_read_b64 v[104:105], v10 offset:448\n"
    "ds_read_b64 v[106:107], v10 offset:456\nds_read_b64 v[108:109], v10 offset:416\nds_read_b64 v[110:111], v10 offset:424\nds_read_b64 v[112:113], v10 offset:320\n"
    "ds_read_b64 v[114:115], v10 offset:328\nds_read_b64 v[116:117], v10 offset:288\nds_read_b64 v[118:119], v10 offset:296\nds_read_b64 v[120:121], v10 offset:256\n"
    "ds_read_b64 v[122:123], v10 offset:264\nds_read_b64 v[124:125], v10 offset:224\nds_read_b64 v[126:127], v10 offset:232\ns_sleep 5\n.LL34:\nv_mov_b32 v34, 125952\n"
    "v_mov_b32 v32, 163216\ns_barrier\nglobal_load_dwordx4 v[240:243], v21, s[32:33] sc1\nglobal_load_dwordx4 v[208:211], v20, s[32:33] sc1\n"
    "global_load_dwordx4 v[212:215], v22, s[32:33] sc1\nglobal_load_dwordx4 v[216:219], v23, s[32:33] sc1\nglobal_load_dwordx4 v[220:223], v24, s[32:33] sc1\n"
    "s_cmp_eq_u32 s6, 0\ns_cbranch_scc0 .LL33\nds_read_b64 v[32:33], v32\nds_read_u16 v35, v34 offset:1536\nds_read_u16 v34, v34\ns_waitcnt lgkmcnt(0)\ns_barrier\n"
    "v_cvt_f32_f16 v34, v34\nv_cvt_f32_f16 v35, v35\nv_mul_f32_e64 v34, -v34, v32\nv_mul_f32_e64 v35, -v35, v33\nv_readfirstlane_b32 s88, v32\nv_readfirstlane_b32 s90, v33\n"
    "v_readfirstlane_b32 s92, v34\nv_readfirstlane_b32 s94, v35\n.LL33:\ns_setprio 0\ns_mov_b32 s84, 0x3e800000\ns_mov_b32 s86, 0x3727c5ac\ns_mov_b32 s80, 0x10000\n.Lln37:\n"
    "s_waitcnt vmcnt(4)\nv_max3_u32 v60, v240, v241, v242\nv_max_u32 v60, v60, v243\nv_cmp_eq_u32 s[82:83], -1, v60\nv_lshlrev_b32 v32, 16, v240\n"
    "v_and_b32 v36, 0xffff0000, v240\nv_lshlrev_b32 v33, 16, v241\nv_and_b32 v37, 0xffff0000, v241\nv_lshlrev_b32 v34, 16, v242\nv_and_b32 v38, 0xffff0000, v242\n"
    "v_lshlrev_b32 v35, 16, v243\nv_and_b32 v39, 0xffff0000, v243\ns_waitcnt vmcnt(3)\nv_lshlrev_b32 v48, 16, v208\nv_lshlrev_b32 v49, 16, v209\nv_lshlrev_b32 v50, 16, v210\n"
    "v_lshlrev_b32 v51, 16, v211\nv_pk_fma_f32 v[52:53], v[48:49], v[48:49], v[208:209]\nv_pk_fma_f32 v[54:55], v[50:51], v[50:51], v[210:211]\ns_waitcnt vmcnt(2)\n"
    "v_lshlrev_b32 v56, 16, v212\nv_lshlrev_b32 v57, 16, v213\nv_lshlrev_b32 v58, 16, v214\nv_lshlrev_b32 v59, 16, v215\nv_pk_add_f32 v[52:53], v[52:53], v[212:213]\n"
    "v_pk_add_f32 v[54:55], v[54:55], v[214:215]\nv_pk_add_f32 v[48:49], v[48:49], v[56:57]\nv_pk_add_f32 v[50:51], v[50:51], v[58:59]\n"
    "v_pk_fma_f32 v[52:53], v[56:57], v[56:57], v[52:53]\nv_pk_fma_f32 v[54:55], v[58:59], v[58:59], v[54:55]\ns_waitcnt vmcnt(1)\nv_lshlrev_b32 v56, 16, v216\n"
    "v_lshlrev_b32 v57, 16, v217\nv_lshlrev_b32 v58, 16, v218\nv_lshlrev_b32 v59, 16, v219\nv_pk_add_f32 v[52:53], v[52:53], v[216:217]\n"
    "v_pk_add_f32 v[54:55], v[54:55], v[218:219]\nv_pk_add_f32 v[48:49], v[48:49], v[56:57]\nv_pk_add_f32 v[50:51], v[50:51], v[58:59]\n"
    "v_pk_fma_f32 v[52:53], v[56:57], v[56:57], v[52:53]\nv_pk_fma_f32 v[54:55], v[58:59], v[58:59], v[54:55]\ns_waitcnt vmcnt(0)\nv_lshlrev_b32 v56, 16, v220\n"
    "v_lshlrev_b32 v57, 16, v221\nv_lshlrev_b32 v58, 16, v222\nv_lshlrev_b32 v59, 16, v223\nv_pk_add_f32 v[52:53], v[52:53], v[220:221]\n"
    "v_pk_add_f32 v[54:55], v[54:55], v[222:223]\nv_pk_add_f32 v[48:49], v[48:49], v[56:57]\nv_pk_add_f32 v[50:51], v[50:51], v[58:59]\n"
    "v_pk_fma_f32 v[52:53], v[56:57], v[56:57], v[52:53]\nv_pk_fma_f32 v[54:55], v[58:59], v[58:59], v[54:55]\nv_pk_mul_f32 v[48:49], v[48:49], s[84:85] op_sel_hi:[1,0]\n"
    "v_pk_mul_f32 v[50:51], v[50:51], s[84:85] op_sel_hi:[1,0]\nv_pk_fma_f32 v[60:61], v[48:49], v[48:49], s[86:87] op_sel_hi:[1,1,0] neg_lo:[0,0,1] neg_hi:[0,0,1]\n"
    "v_pk_fma_f32 v[62:63], v[50:51], v[50:51], s[86:87] op_sel_hi:[1,1,0] neg_lo:[0,0,1] neg_hi:[0,0,1]\n"
    "v_pk_fma_f32 v[52:53], v[52:53], s[84:85], v[60:61] op_sel_hi:[1,0,1] neg_lo:[0,0,1] neg_hi:[0,0,1]\n"
    "v_pk_fma_f32 v[54:55], v[54:55], s[84:85], v[62:63] op_sel_hi:[1,0,1] neg_lo:[0,0,1] neg_hi:[0,0,1]\nv_pk_add_f32 v[56:57], v[52:53], v[54:55]\nv_max_f32 v52, s86, v52\n"
    "v_max_f32 v53, s86, v53\nv_max_f32 v54, s86, v54\nv_max_f32 v55, s86, v55\nv_cmp_u_f32 vcc, v56, v57\nv_rsq_f32 v52, v52\nv_rsq_f32 v53, v53\nv_rsq_f32 v54, v54\n"
    "v_rsq_f32 v55, v55\ns_or_b64 vcc, vcc, s[82:83]\ns_cbranch_vccnz .Lls39\n.Llo38:\nv_pk_mul_f32 v[48:49], v[48:49], v[52:53]\nv_pk_mul_f32 v[50:51], v[50:51], v[54:55]\n"
    "v_pk_fma_f32 v[244:245], v[32:33], v[52:53], v[48:49] neg_lo:[0,0,1] neg_hi:[0,0,1]\nv_pk_fma_f32 v[246:247], v[34:35], v[54:55], v[50:51] neg_lo:[0,0,1] neg_hi:[0,0,1]\n"
    "v_pk_fma_f32 v[248:249], v[36:37], v[52:53], v[48:49] neg_lo:[0,0,1] neg_hi:[0,0,1]\nv_pk_fma_f32 v[250:251], v[38:39], v[54:55], v[50:51] neg_lo:[0,0,1] neg_hi:[0,0,1]\n"
    "v_cvt_pk_f16_f32 v56, v244, v245\nv_cvt_pk_f16_f32 v57, v246, v247\nds_write_b64 v14, v[56:57] offset:0\nv_cvt_pk_f16_f32 v58, v248, v249\n"
    "v_cvt_pk_f16_f32 v59, v250, v251\nds_write_b64 v14, v[58:59] offset:6480\ns_cmp_eq_u32 s6, 0\ns_cbranch_scc0 .LL35\nds_read_b64 v[64:65], v19 offset:896\n"
    "ds_read_b64 v[66:67], v19 offset:904\nds_read_b64 v[68:69], v19 offset:864\nds_read_b64 v[70:71], v19 offset:872\nds_read_b64 v[72:73], v19 offset:832\n"
    "ds_read_b64 v[74:75], v19 offset:840\nds_read_b64 v[76:77], v19 offset:800\nds_read_b64 v[78:79], v19 offset:808\nds_read_b64 v[80:81], v19 offset:704\n"
    "ds_read_b64 v[82:83], v19 offset:712\nds_read_b64 v[84:85], v19 offset:672\nds_read_b64 v[86:87], v19 offset:680\nds_read_b64 v[88:89], v19 offset:640\n"
    "ds_read_b64 v[90:91], v19 offset:648\nds_read_b64 v[92:93], v19 offset:608\nds_read_b64 v[94:95], v19 offset:616\nds_read_b64 v[96:97], v10 offset:512\n"
    "ds_read_b64 v[98:99], v10 offset:520\nds_read_b64 v[100:101], v10 offset:480\nds_read_b64 v[102:103], v10 offset:488\nds_read_b64 v[104:105], v10 offset:448\n"
    "ds_read_b64 v[106:107], v10 offset:456\nds_read_b64 v[108:109], v10 offset:416\nds_read_b64 v[110:111], v10 offset:424\nds_read_b64 v[112:113], v10 offset:320\n"
    "ds_read_b64 v[114:115], v10 offset:328\nds_read_b64 v[116:117], v10 offset:288\nds_read_b64 v[118:119], v10 offset:296\nds_read_b64 v[120:121], v10 offset:256\n"
    "ds_read_b64 v[122:123], v10 offset:264\nds_read_b64 v[124:125], v10 offset:224\nds_read_b64 v[126:127], v10 offset:232\n.LL35:\ns_waitcnt lgkmcnt(0)\ns_barrier\n"
    "s_bitcmp1_b32 s3, 0\ns_cbranch_scc1 .LL47\ns_setprio 0\nds_read_b64 v[196:197], v41\nds_read_b128 v[198:201], v40\nds_read_b64 v[202:203], v42\n"
    "ds_read_b64 v[48:49], v41 offset:2560\nds_read_b128 v[50:53], v40 offset:2560\nds_read_b64 v[54:55], v42 offset:2560\ns_waitcnt lgkmcnt(3)\n"
    "v_alignbit_b32 v204, v197, v196, 16\nv_alignbit_b32 v205, v198, v197, 16\nv_alignbit_b32 v206, v199, v198, 16\nv_alignbit_b32 v207, v200, v199, 16\n"
    "v_alignbit_b32 v208, v201, v200, 16\nv_alignbit_b32 v209, v202, v201, 16\nv_alignbit_b32 v210, v203, v202, 16\nv_pk_add_f16 v212, v196, v204\n"
    "v_pk_add_f16 v213, v197, v205\nv_pk_add_f16 v214, v198, v206\nv_pk_add_f16 v215, v199, v207\nv_pk_add_f16 v216, v200, v208\nv_pk_add_f16 v217, v201, v209\n"
    "v_pk_add_f16 v218, v202, v210\nv_pk_add_f16 v211, v214, v215\nv_pk_add_f16 v219, v216, v217\nv_pk_add_f16 v232, v205, v214\nv_pk_add_f16 v233, v206, v215\n"
    "v_pk_add_f16 v234, v207, v216\nv_pk_add_f16 v235, v208, v217\nds_write_b64 v43, v[232:233] offset:4040\nv_pk_add_f16 v228, v213, v214\nv_pk_add_f16 v230, v215, v216\n"
    "ds_write_b64 v44, v[234:235] offset:4032\nv_pk_add_f16 v229, v211, v200\nv_pk_add_f16 v231, v219, v202\nv_pk_add_f16 v228, v228, v199\nv_pk_add_f16 v230, v230, v201\n"
    "v_pk_add_f16 v236, v213, v211\nv_pk_add_f16 v237, v211, v216\nds_write_b128 v43, v[228:231]\nv_pk_add_f16 v238, v215, v219\nv_pk_add_f16 v239, v219, v218\n"
    "v_pk_add_f16 v220, v212, v200\nv_pk_add_f16 v221, v213, v201\nv_pk_add_f16 v222, v214, v202\nv_pk_add_f16 v223, v215, v203\nv_pk_add_f16 v224, v236, v204\n"
    "v_pk_add_f16 v225, v237, v205\nv_pk_add_f16 v226, v238, v206\nv_pk_add_f16 v227, v239, v207\nds_write_b128 v29, v[224:227]\nv_pk_add_f16 v220, v220, v236\n"
    "v_pk_add_f16 v221, v221, v237\nv_pk_add_f16 v222, v222, v238\nv_pk_add_f16 v223, v223, v239\nds_write_b128 v45, v[220:223]\ns_mov_b64 exec, 1\n"
    "ds_write_b32 v12, v13 offset:80\ns_mov_b64 exec, -1\ns_waitcnt lgkmcnt(6)\nv_alignbit_b32 v204, v50, v49, 16\nv_alignbit_b32 v205, v51, v50, 16\n"
    "v_alignbit_b32 v206, v52, v51, 16\nv_alignbit_b32 v207, v53, v52, 16\nv_alignbit_b32 v208, v54, v53, 16\nv_pk_add_f16 v212, v49, v204\nv_pk_add_f16 v213, v50, v205\n"
    "v_pk_add_f16 v214, v51, v206\nv_pk_add_f16 v215, v52, v207\nv_pk_add_f16 v216, v53, v208\nv_pk_add_f16 v60, v204, v213\nv_pk_add_f16 v61, v205, v214\n"
    "v_pk_add_f16 v62, v206, v215\nv_pk_add_f16 v63, v207, v216\nds_write_b64 v30, v[60:61] offset:6344\nv_pk_add_f16 v56, v212, v51\nv_pk_add_f16 v57, v213, v52\n"
    "v_pk_add_f16 v58, v214, v53\nv_pk_add_f16 v59, v215, v54\nds_write_b64 v31, v[62:63] offset:6336\nv_pk_add_f16 v56, v56, v213\nv_pk_add_f16 v57, v57, v214\n"
    "v_pk_add_f16 v58, v58, v215\nv_pk_add_f16 v59, v59, v216\nds_write_b128 v30, v[56:59] offset:2304\nds_read_b32 v22, v5\nds_read_b32 v23, v5 offset:6480\n"
    "ds_read_b64 v[46:47], v6\nds_read_b64 v[20:21], v6 offset:6480\ns_mov_b32 s80, 0x100000\n.Lsl50:\nds_read_b32 v28, v11\ns_waitcnt lgkmcnt(0)\nv_cmp_gt_i32 vcc, v13, v28\n"
    "s_cbranch_vccz .Lsr49\ns_sleep 0\ns_sub_u32 s80, s80, 1\ns_cbranch_scc0 .Lsl50\n.Lsr49:\ns_setprio 2\nds_read_b64_tr_b16 v[48:49], v2 offset:1984\n"
    "ds_read_b64_tr_b16 v[50:51], v2 offset:2048\nds_read_b64_tr_b16 v[52:53], v2 offset:1728\nds_read_b64_tr_b16 v[54:55], v2 offset:1792\n"
    "ds_read_b64_tr_b16 v[56:57], v2 offset:1408\nds_read_b64_tr_b16 v[58:59], v2 offset:1472\nds_read_b64_tr_b16 v[60:61], v2 offset:1152\n"
    "ds_read_b64_tr_b16 v[62:63], v2 offset:1216\nds_read_b64_tr_b16 v[212:213], v2 offset:832\nds_read_b64_tr_b16 v[214:215], v2 offset:896\n"
    "ds_read_b64_tr_b16 v[216:217], v2 offset:576\nds_read_b64_tr_b16 v[218:219], v2 offset:640\ns_waitcnt lgkmcnt(10)\n"
    "v_mfma_f32_32x32x16_f16 v[196:211], v[48:51], v[64:67], 0\nds_read_b64_tr_b16 v[220:221], v2 offset:6016\nds_read_b64_tr_b16 v[222:223], v2 offset:6080\n"
    "v_fmac_f32 v32, s96, v244\nv_fmac_f32 v33, s96, v245\nv_fmac_f32 v34, s96, v246\ns_waitcnt lgkmcnt(10)\nv_mfma_f32_32x32x16_f16 v[196:211], v[52:55], v[68:71], v[196:211]\n"
    "ds_read_b64_tr_b16 v[224:225], v2 offset:5760\nds_read_b64_tr_b16 v[226:227], v2 offset:5824\nv_fmac_f32 v35, s96, v247\nv_fmac_f32 v36, s97, v248\n"
    "v_fmac_f32 v37, s97, v249\ns_waitcnt lgkmcnt(10)\nv_mfma_f32_32x32x16_f16 v[196:211], v[56:59], v[72:75], v[196:211]\nds_read_b64_tr_b16 v[48:49], v2 offset:5440\n"
    "ds_read_b64_tr_b16 v[50:51], v2 offset:5504\nv_fmac_f32 v38, s97, v250\nv_fmac_f32 v39, s97, v251\n"
    "v_cvt_f32_f16_sdwa v24, v22 dst_sel:DWORD dst_unused:UNUSED_PAD src0_sel:WORD_1\ns_waitcnt lgkmcnt(10)\nv_mfma_f32_32x32x16_f16 v[196:211], v[60:63], v[76:79], v[196:211]\n"
    "ds_read_b64_tr_b16 v[64:65], v2 offset:5184\nds_read_b64_tr_b16 v[66:67], v2 offset:5248\nv_cvt_f32_f16_sdwa v26, v46 dst_sel:DWORD dst_unused:UNUSED_PAD src0_sel:WORD_1\n"
    "v_cvt_f32_f16 v25, v46\nv_cvt_f32_f16 v27, v47\ns_waitcnt lgkmcnt(10)\nv_mfma_f32_32x32x16_f16 v[196:211], v[212:215], v[96:99], v[196:211]\n"
    "ds_read_b64_tr_b16 v[52:53], v2 offset:4864\nds_read_b64_tr_b16 v[54:55], v2 offset:4928\nv_cvt_f32_f16_sdwa v28, v23 dst_sel:DWORD dst_unused:UNUSED_PAD src0_sel:WORD_1\n"
    "v_cvt_f32_f16_sdwa v30, v20 dst_sel:DWORD dst_unused:UNUSED_PAD src0_sel:WORD_1\nv_cvt_f32_f16 v29, v20\ns_waitcnt lgkmcnt(10)\n"
    "v_mfma_f32_32x32x16_f16 v[196:211], v[216:219], v[100:103], v[196:211]\nds_read_b64_tr_b16 v[68:69], v2 offset:4608\nds_read_b64_tr_b16 v[70:71], v2 offset:4672\n"
    "v_cvt_f32_f16 v31, v21\nv_fmac_f32 v32, s92, v24\nv_fmac_f32 v33, s92, v25\ns_waitcnt lgkmcnt(10)\nv_mfma_f32_32x32x16_f16 v[196:211], v[220:223], v[80:83], v[196:211]\n"
    "v_fmac_f32 v34, s92, v26\nv_fmac_f32 v35, s92, v27\nv_fmac_f32 v36, s94, v28\ns_waitcnt lgkmcnt(8)\nv_mfma_f32_32x32x16_f16 v[196:211], v[224:227], v[84:87], v[196:211]\n"
    "v_fmac_f32 v37, s94, v29\nv_fmac_f32 v38, s94, v30\nv_fmac_f32 v39, s94, v31\ns_waitcnt lgkmcnt(6)\nv_mfma_f32_32x32x16_f16 v[196:211], v[48:51], v[88:91], v[196:211]\n"
    "s_waitcnt lgkmcnt(4)\nv_mfma_f32_32x32x16_f16 v[196:211], v[64:67], v[92:95], v[196:211]\ns_waitcnt lgkmcnt(2)\n"
    "v_mfma_f32_32x32x16_f16 v[196:211], v[52:55], v[112:115], v[196:211]\ns_waitcnt lgkmcnt(0)\nv_mfma_f32_32x32x16_f16 v[196:211], v[68:71], v[116:119], v[196:211]\ns_nop 11\n"
    "ds_write_b128 v7, v[196:199] offset:0\nds_write_b128 v7, v[200:203] offset:1024\nds_write_b128 v7, v[204:207] offset:4032\nds_write_b128 v7, v[208:211] offset:5056\n"
    "s_setprio 0\ns_branch .LL48\n.LL47:\ns_setprio 0\nds_read_b64 v[196:197], v41\nds_read_b128 v[198:201], v40\nds_read_b64 v[202:203], v42\ns_waitcnt lgkmcnt(0)\n"
    "v_alignbit_b32 v204, v197, v196, 16\nv_alignbit_b32 v205, v198, v197, 16\nv_alignbit_b32 v206, v199, v198, 16\nv_alignbit_b32 v207, v200, v199, 16\n"
    "v_alignbit_b32 v208, v201, v200, 16\nv_alignbit_b32 v209, v202, v201, 16\nv_alignbit_b32 v210, v203, v202, 16\nv_pk_add_f16 v212, v196, v204\n"
    "v_pk_add_f16 v213, v197, v205\nv_pk_add_f16 v214, v198, v206\nv_pk_add_f16 v215, v199, v207\nv_pk_add_f16 v216, v200, v208\nv_pk_add_f16 v217, v201, v209\n"
    "v_pk_add_f16 v218, v202, v210\nv_pk_add_f16 v211, v214, v215\nv_pk_add_f16 v219, v216, v217\nv_pk_add_f16 v232, v205, v214\nv_pk_add_f16 v233, v206, v215\n"
    "v_pk_add_f16 v234, v207, v216\nv_pk_add_f16 v235, v208, v217\nds_write_b64 v43, v[232:233] offset:4040\nv_pk_add_f16 v228, v213, v214\nv_pk_add_f16 v230, v215, v216\n"
    "ds_write_b64 v44, v[234:235] offset:4032\nv_pk_add_f16 v229, v211, v200\nv_pk_add_f16 v231, v219, v202\nv_pk_add_f16 v228, v228, v199\nv_pk_add_f16 v230, v230, v201\n"
    "v_pk_add_f16 v236, v213, v211\nv_pk_add_f16 v237, v211, v216\nds_write_b128 v43, v[228:231]\nv_pk_add_f16 v238, v215, v219\nv_pk_add_f16 v239, v219, v218\n"
    "v_pk_add_f16 v220, v212, v200\nv_pk_add_f16 v221, v213, v201\nv_pk_add_f16 v222, v214, v202\nv_pk_add_f16 v223, v215, v203\nv_pk_add_f16 v224, v236, v204\n"
    "v_pk_add_f16 v225, v237, v205\nv_pk_add_f16 v226, v238, v206\nv_pk_add_f16 v227, v239, v207\nds_write_b128 v29, v[224:227]\nv_pk_add_f16 v220, v220, v236\n"
    "v_pk_add_f16 v221, v221, v237\nv_pk_add_f16 v222, v222, v238\nv_pk_add_f16 v223, v223, v239\nds_write_b128 v45, v[220:223]\ns_mov_b64 exec, 1\n"
    "ds_write_b32 v12, v13 offset:80\ns_mov_b64 exec, -1\nds_read_b32 v22, v5\nds_read_b32 v23, v5 offset:6480\nds_read_b64 v[46:47], v6\nds_read_b64 v[20:21], v6 offset:6480\n"
    "s_mov_b32 s80, 0x100000\n.Lsl53:\nds_read_b32 v28, v11\ns_waitcnt lgkmcnt(0)\nv_cmp_gt_i32 vcc, v13, v28\ns_cbranch_vccz .Lsr52\ns_sleep 0\ns_sub_u32 s80, s80, 1\n"
    "s_cbranch_scc0 .Lsl53\n.Lsr52:\ns_setprio 2\nds_read_b64_tr_b16 v[48:49], v3 offset:512\nds_read_b64_tr_b16 v[50:51], v3 offset:640\n"
    "ds_read_b64_tr_b16 v[52:53], v3 offset:0\nds_read_b64_tr_b16 v[54:55], v3 offset:128\nds_read_b64_tr_b16 v[56:57], v4 offset:512\n"
    "ds_read_b64_tr_b16 v[58:59], v4 offset:640\nds_read_b64_tr_b16 v[60:61], v4 offset:0\nds_read_b64_tr_b16 v[62:63], v4 offset:128\ns_waitcnt lgkmcnt(6)\n"
    "v_mfma_f32_32x32x16_f16 v[228:243], v[48:51], v[96:99], 0\nds_read_b64_tr_b16 v[48:49], v3 offset:2816\nds_read_b64_tr_b16 v[50:51], v3 offset:2944\n"
    "ds_read_b64_tr_b16 v[96:97], v3 offset:2304\nds_read_b64_tr_b16 v[98:99], v3 offset:2432\nv_fmac_f32 v32, s96, v244\nv_fmac_f32 v33, s96, v245\nv_fmac_f32 v34, s96, v246\n"
    "s_waitcnt lgkmcnt(8)\nv_mfma_f32_32x32x16_f16 v[228:243], v[52:55], v[100:103], v[228:243]\nds_read_b64_tr_b16 v[52:53], v4 offset:2816\n"
    "ds_read_b64_tr_b16 v[54:55], v4 offset:2944\nds_read_b64_tr_b16 v[100:101], v4 offset:2304\nds_read_b64_tr_b16 v[102:103], v4 offset:2432\nv_fmac_f32 v35, s96, v247\n"
    "v_fmac_f32 v36, s97, v248\nv_fmac_f32 v37, s97, v249\ns_waitcnt lgkmcnt(10)\nv_mfma_f32_32x32x16_f16 v[212:227], v[56:59], v[104:107], 0\n"
    "ds_read_b64_tr_b16 v[56:57], v2 offset:832\nds_read_b64_tr_b16 v[58:59], v2 offset:896\nv_fmac_f32 v38, s97, v250\nv_fmac_f32 v39, s97, v251\n"
    "v_cvt_f32_f16_sdwa v24, v22 dst_sel:DWORD dst_unused:UNUSED_PAD src0_sel:WORD_1\ns_waitcnt lgkmcnt(10)\n"
    "v_mfma_f32_32x32x16_f16 v[212:227], v[60:63], v[108:111], v[212:227]\nds_read_b64_tr_b16 v[104:105], v2 offset:576\nds_read_b64_tr_b16 v[106:107], v2 offset:640\n"
    "v_cvt_f32_f16_sdwa v26, v46 dst_sel:DWORD dst_unused:UNUSED_PAD src0_sel:WORD_1\nv_cvt_f32_f16 v25, v46\nv_cvt_f32_f16 v27, v47\ns_waitcnt lgkmcnt(10)\n"
    "v_mfma_f32_32x32x16_f16 v[228:243], v[48:51], v[112:115], v[228:243]\nds_read_b64_tr_b16 v[60:61], v2 offset:256\nds_read_b64_tr_b16 v[62:63], v2 offset:320\n"
    "v_cvt_f32_f16_sdwa v28, v23 dst_sel:DWORD dst_unused:UNUSED_PAD src0_sel:WORD_1\nv_cvt_f32_f16_sdwa v30, v20 dst_sel:DWORD dst_unused:UNUSED_PAD src0_sel:WORD_1\n"
    "v_cvt_f32_f16 v29, v20\ns_waitcnt lgkmcnt(10)\nv_mfma_f32_32x32x16_f16 v[228:243], v[96:99], v[116:119], v[228:243]\nds_read_b64_tr_b16 v[108:109], v2 offset:0\n"
    "ds_read_b64_tr_b16 v[110:111], v2 offset:64\nv_cvt_f32_f16 v31, v21\nv_fmac_f32 v32, s92, v24\nv_fmac_f32 v33, s92, v25\ns_waitcnt lgkmcnt(10)\n"
    "v_mfma_f32_32x32x16_f16 v[212:227], v[52:55], v[120:123], v[212:227]\nds_read_b64_tr_b16 v[48:49], v2 offset:4864\nds_read_b64_tr_b16 v[50:51], v2 offset:4928\n"
    "v_fmac_f32 v34, s92, v26\nv_fmac_f32 v35, s92, v27\nv_fmac_f32 v36, s94, v28\ns_waitcnt lgkmcnt(10)\n"
    "v_mfma_f32_32x32x16_f16 v[212:227], v[100:103], v[124:127], v[212:227]\nds_read_b64_tr_b16 v[112:113], v2 offset:4608\nds_read_b64_tr_b16 v[114:115], v2 offset:4672\n"
    "v_fmac_f32 v37, s94, v29\nv_fmac_f32 v38, s94, v30\nv_fmac_f32 v39, s94, v31\ns_waitcnt lgkmcnt(10)\nv_mfma_f32_32x32x16_f16 v[196:211], v[56:59], v[64:67], 0\n"
    "ds_read_b64_tr_b16 v[96:97], v2 offset:4288\nds_read_b64_tr_b16 v[98:99], v2 offset:4352\ns_waitcnt lgkmcnt(10)\n"
    "v_mfma_f32_32x32x16_f16 v[196:211], v[104:107], v[68:71], v[196:211]\nds_read_b64_tr_b16 v[116:117], v2 offset:4032\nds_read_b64_tr_b16 v[118:119], v2 offset:4096\n"
    "s_waitcnt lgkmcnt(10)\nv_mfma_f32_32x32x16_f16 v[196:211], v[60:63], v[72:75], v[196:211]\ns_waitcnt lgkmcnt(8)\n"
    "v_mfma_f32_32x32x16_f16 v[196:211], v[108:111], v[76:79], v[196:211]\ns_waitcnt lgkmcnt(6)\nv_mfma_f32_32x32x16_f16 v[196:211], v[48:51], v[80:83], v[196:211]\n"
    "v_add_f32 v236, v236, v220\nv_add_f32 v237, v237, v221\nv_add_f32 v238, v238, v222\nv_add_f32 v239, v239, v223\ns_waitcnt lgkmcnt(4)\n"
    "v_mfma_f32_32x32x16_f16 v[196:211], v[112:115], v[84:87], v[196:211]\nv_add_f32 v240, v240, v224\nv_add_f32 v241, v241, v225\nv_add_f32 v242, v242, v226\n"
    "v_add_f32 v243, v243, v227\ns_waitcnt lgkmcnt(2)\nv_mfma_f32_32x32x16_f16 v[196:211], v[96:99], v[88:91], v[196:211]\nv_xor_b32 v45, 32, v8\ns_waitcnt lgkmcnt(0)\n"
    "v_mfma_f32_32x32x16_f16 v[196:211], v[116:119], v[92:95], v[196:211]\nds_write_b128 v8, v[228:231] offset:0\nds_write_b128 v45, v[232:235] offset:0\n"
    "ds_write_b128 v8, v[236:239] offset:2048\nds_write_b128 v45, v[240:243] offset:2048\ns_nop 7\nds_write_b128 v7, v[196:199]\nds_write_b128 v7, v[200:203] offset:1024\n"
    "s_setprio 0\n.LL48:\ns_waitcnt lgkmcnt(0)\ns_barrier\nds_read_b128 v[48:51], v15\nds_read_b128 v[52:55], v15 offset:8192\nds_read_b128 v[56:59], v16\n"
    "ds_read_b128 v[60:63], v16 offset:12672\nds_read_b128 v[196:199], v9\nds_read_b128 v[200:203], v9 offset:12672\ns_mov_b32 vcc_lo, 0x55555555\ns_mov_b32 vcc_hi, 0x55555555\n"
    "s_waitcnt lgkmcnt(4)\nv_pk_fma_f32 v[32:33], v[48:49], s[88:89], v[32:33] op_sel_hi:[1,0,1]\nv_pk_fma_f32 v[34:35], v[50:51], s[88:89], v[34:35] op_sel_hi:[1,0,1]\n"
    "v_pk_fma_f32 v[36:37], v[52:53], s[90:91], v[36:37] op_sel_hi:[1,0,1]\nv_pk_fma_f32 v[38:39], v[54:55], s[90:91], v[38:39] op_sel_hi:[1,0,1]\ns_waitcnt lgkmcnt(2)\n"
    "v_pk_fma_f32 v[32:33], v[56:57], s[88:89], v[32:33] op_sel_hi:[1,0,1]\nv_pk_fma_f32 v[34:35], v[58:59], s[88:89], v[34:35] op_sel_hi:[1,0,1]\n"
    "v_pk_fma_f32 v[36:37], v[60:61], s[90:91], v[36:37] op_sel_hi:[1,0,1]\nv_pk_fma_f32 v[38:39], v[62:63], s[90:91], v[38:39] op_sel_hi:[1,0,1]\ns_waitcnt lgkmcnt(0)\n"
    "v_pk_fma_f32 v[32:33], v[196:197], s[88:89], v[32:33] op_sel_hi:[1,0,1]\nv_pk_fma_f32 v[34:35], v[198:199], s[88:89], v[34:35] op_sel_hi:[1,0,1]\n"
    "v_pk_fma_f32 v[36:37], v[200:201], s[90:91], v[36:37] op_sel_hi:[1,0,1]\nv_pk_fma_f32 v[38:39], v[202:203], s[90:91], v[38:39] op_sel_hi:[1,0,1]\ns_cmp_eq_u32 s6, 0\n"
    "s_cbranch_scc0 .LL55\nv_cvt_pk_bf16_f32 v52, v32, v33\nv_cvt_pk_bf16_f32 v53, v34, v35\nv_cvt_pk_bf16_f32 v54, v36, v37\nv_cvt_pk_bf16_f32 v55, v38, v39\ns_nop 0\n"
    "v_cndmask_b32_dpp v48, v54, v52, vcc quad_perm:[1,0,3,2] row_mask:0xf bank_mask:0xf\nv_cndmask_b32_dpp v49, v55, v53, vcc quad_perm:[1,0,3,2] row_mask:0xf bank_mask:0xf\n"
    "s_not_b64 vcc, vcc\nv_cndmask_b32_dpp v50, v52, v54, vcc quad_perm:[1,0,3,2] row_mask:0xf bank_mask:0xf\n"
    "v_cndmask_b32_dpp v51, v53, v55, vcc quad_perm:[1,0,3,2] row_mask:0xf bank_mask:0xf\nglobal_store_dwordx4 v17, v[48:51], s[32:33] sc1\ns_branch .LH213\n.LL55:\n"
    "v_cvt_pk_bf16_f32 v48, v32, v36\nv_cvt_pk_bf16_f32 v49, v33, v37\nv_cvt_pk_bf16_f32 v50, v34, v38\nv_cvt_pk_bf16_f32 v51, v35, v39\n"
    "global_store_dwordx4 v17, v[48:51], s[32:33] sc1\ns_lshl_b32 s15, s41, 13\ns_add_u32 s14, s15, 0x1200000\nv_lshl_add_u32 v2, v0, 4, s14\nv_lshrrev_b32 v7, 3, v0\n"
    "s_lshl_b32 s17, s36, 16\ns_lshl_b32 s18, s35, 7\ns_add_u32 s16, s17, s18\nv_lshl_add_u32 v3, v7, 10, s16\nv_and_b32 v8, 7, v0\nv_lshl_add_u32 v3, v8, 4, v3\n"
    "v_lshrrev_b32 v9, 6, v0\nv_lshlrev_b32 v10, 4, v0\nv_lshl_add_u32 v5, v9, 4, v10\ns_mov_b32 s19, 0x410\nv_lshlrev_b32 v11, 2, v7\nv_mad_u32_u24 v6, v8, s19, v11\n"
    "s_mov_b32 s80, 0x10000\ns_sleep 14\n.LL56:\nglobal_load_dwordx4 v[52:55], v2, s[32:33] sc1\ns_waitcnt vmcnt(0)\nds_write_b128 v5, v[52:55]\nv_max3_u32 v4, v52, v53, v54\n"
    "v_max_u32 v4, v4, v55\nv_cmp_eq_u32 vcc, -1, v4\ns_cbranch_vccz .LL57\ns_waitcnt lgkmcnt(0)\ns_sub_u32 s80, s80, 1\ns_cbranch_scc0 .LL56\n.LL57:\ns_waitcnt lgkmcnt(0)\n"
    "s_barrier\nds_read2st64_b32 v[56:57], v6 offset1:1\nds_read2st64_b32 v[58:59], v6 offset0:2 offset1:3\ns_waitcnt lgkmcnt(0)\n"
    "global_store_dwordx4 v3, v[56:59], s[30:31] sc1\ns_endpgm\n.LH213:\ns_mov_b32 s6, 1\ns_mov_b32 s7, 8388608\nv_and_b32 v196, 63, v0\nv_lshlrev_b32 v26, 4, v196\n"
    "v_bfe_u32 v197, v0, 1, 5\nv_lshl_add_u32 v27, v197, 6, 32\nv_and_b32 v198, 1, v0\nv_lshl_add_u32 v27, v198, 4, v27\ns_mov_b32 s14, 0x1020\nv_lshl_add_u32 v9, v197, 6, s14\n"
    "v_lshl_add_u32 v9, v198, 4, v9\nv_bfe_u32 v199, v0, 4, 2\ns_lshl_b32 s16, s3, 6\ns_add_u32 s15, s16, 0x27c80\nv_lshl_add_u32 v39, v199, 4, s15\nv_bfe_u32 v201, v0, 2, 2\n"
    "v_lshl_add_u32 v200, v199, 3, v201\ns_lshr_b32 s20, s3, 1\ns_add_u32 s19, s20, 1\ns_lshl_b32 s18, s19, 15\ns_and_b32 s22, s3, 3\ns_lshl_b32 s21, s22, 13\n"
    "s_add_u32 s17, s18, s21\nv_lshl_add_u32 v32, v200, 6, s17\nv_and_b32 v202, 3, v0\nv_lshl_add_u32 v32, v202, 3, v32\nv_xor_b32 v204, s3, v199\nv_and_b32 v203, 1, v204\n"
    "v_lshl_add_u32 v32, v203, 5, v32\ns_add_u32 s44, s3, 1\ns_and_b32 s25, s44, 3\ns_lshl_b32 s24, s25, 13\ns_add_u32 s23, s18, s24\nv_lshl_add_u32 v33, v200, 6, s23\n"
    "v_lshl_add_u32 v33, v202, 3, v33\nv_lshl_add_u32 v33, v203, 5, v33\ns_add_u32 s48, s3, 2\ns_and_b32 s47, s48, 3\ns_lshl_b32 s46, s47, 13\ns_add_u32 s45, s18, s46\n"
    "v_lshl_add_u32 v34, v200, 6, s45\nv_lshl_add_u32 v34, v202, 3, v34\nv_lshl_add_u32 v34, v203, 5, v34\ns_add_u32 s52, s3, 3\ns_and_b32 s51, s52, 3\ns_lshl_b32 s50, s51, 13\n"
    "s_add_u32 s49, s18, s50\nv_lshl_add_u32 v35, v200, 6, s49\nv_lshl_add_u32 v35, v202, 3, v35\nv_lshl_add_u32 v35, v203, 5, v35\nds_read_b128 v[244:247], v39\n"
    "s_lshl2_add_u32 s15, s37, s3\ns_lshl_b32 s14, s15, 15\ns_add_u32 s62, s14, 0x600000\ns_mul_i32 s16, s3, 0x3000\ns_add_u32 s64, s16, 0x18000\ns_lshl_b32 s21, s37, 2\n"
    "s_max_u32 s20, s21, 1\ns_add_u32 s19, s20, -1\ns_lshl_b32 s18, s19, 15\ns_add_u32 s17, s18, 0x600000\ns_lshl_b32 s22, s3, 13\ns_add_u32 s60, s17, s22\n"
    "s_lshl_b32 s24, s3, 12\ns_add_u32 s23, s24, 0x24000\ns_add_u32 s65, s23, 0xfffffc00\ns_and_b32 s66, s42, 7\ns_lshl_b32 s25, s66, 12\ns_add_u32 s44, s62, s25\n"
    "s_add_u32 s69, s66, 1\ns_and_b32 s68, s69, 7\ns_lshl_b32 s67, s68, 12\ns_add_u32 s45, s62, s67\ns_and_b32 s17, s42, 7\ns_add_u32 s16, s17, 2\ns_and_b32 s15, s16, 7\n"
    "s_lshl_b32 s14, s15, 12\ns_add_u32 s46, s62, s14\ns_add_u32 s20, s17, 3\ns_and_b32 s19, s20, 7\ns_lshl_b32 s18, s19, 12\ns_add_u32 s47, s62, s18\ns_add_u32 s23, s17, 4\n"
    "s_and_b32 s22, s23, 7\ns_lshl_b32 s21, s22, 12\ns_add_u32 s48, s62, s21\ns_add_u32 s66, s17, 5\ns_and_b32 s25, s66, 7\ns_lshl_b32 s24, s25, 12\ns_add_u32 s49, s62, s24\n"
    "s_add_u32 s69, s17, 6\ns_and_b32 s68, s69, 7\ns_lshl_b32 s67, s68, 12\ns_add_u32 s50, s62, s67\ns_and_b32 s17, s42, 7\ns_add_u32 s16, s17, 7\ns_and_b32 s15, s16, 7\n"
    "s_lshl_b32 s14, s15, 12\ns_add_u32 s51, s62, s14\ns_add_u32 s60, s60, s32\ns_addc_u32 s61, s33, 0\nds_read_b64_tr_b16 v[64:65], v32 offset:0\n"
    "ds_read_b64_tr_b16 v[66:67], v32 offset:256\nds_read_b64_tr_b16 v[68:69], v32 offset:2048\nds_read_b64_tr_b16 v[70:71], v32 offset:2304\n"
    "ds_read_b64_tr_b16 v[72:73], v32 offset:4096\nds_read_b64_tr_b16 v[74:75], v32 offset:4352\nds_read_b64_tr_b16 v[76:77], v32 offset:6144\n"
    "ds_read_b64_tr_b16 v[78:79], v32 offset:6400\nds_read_b64_tr_b16 v[80:81], v33 offset:0\nds_read_b64_tr_b16 v[82:83], v33 offset:256\n"
    "ds_read_b64_tr_b16 v[84:85], v33 offset:2048\nds_read_b64_tr_b16 v[86:87], v33 offset:2304\nds_read_b64_tr_b16 v[88:89], v33 offset:4096\n"
    "ds_read_b64_tr_b16 v[90:91], v33 offset:4352\nds_read_b64_tr_b16 v[92:93], v33 offset:6144\nds_read_b64_tr_b16 v[94:95], v33 offset:6400\n"
    "ds_read_b64_tr_b16 v[96:97], v34 offset:0\nds_read_b64_tr_b16 v[98:99], v34 offset:256\nds_read_b64_tr_b16 v[100:101], v34 offset:2048\n"
    "ds_read_b64_tr_b16 v[102:103], v34 offset:2304\nds_read_b64_tr_b16 v[104:105], v34 offset:4096\nds_read_b64_tr_b16 v[106:107], v34 offset:4352\n"
    "ds_read_b64_tr_b16 v[108:109], v34 offset:6144\nds_read_b64_tr_b16 v[110:111], v34 offset:6400\nds_read_b64_tr_b16 v[112:113], v35 offset:0\n"
    "ds_read_b64_tr_b16 v[114:115], v35 offset:256\nds_read_b64_tr_b16 v[116:117], v35 offset:2048\nds_read_b64_tr_b16 v[118:119], v35 offset:2304\n"
    "ds_read_b64_tr_b16 v[120:121], v35 offset:4096\nds_read_b64_tr_b16 v[122:123], v35 offset:4352\nds_read_b64_tr_b16 v[124:125], v35 offset:6144\n"
    "ds_read_b64_tr_b16 v[126:127], v35 offset:6400\ns_waitcnt lgkmcnt(0)\ns_sleep 11\ns_setprio 3\ns_barrier\ns_add_u32 m0, s64, 0\ns_add_u32 s52, s32, s44\n"
    "s_addc_u32 s53, s33, 0\nglobal_load_lds_dwordx4 v26, s[52:53] offset:0 sc1\nglobal_load_lds_dwordx4 v26, s[52:53] offset:1024 sc1\n"
    "global_load_lds_dwordx4 v26, s[52:53] offset:2048 sc1\nglobal_load_lds_dwordx4 v26, s[52:53] offset:3072 sc1\nv_mov_b64 v[10:11], 0\nv_mov_b64 v[12:13], 0\n"
    "v_mov_b64 v[14:15], 0\nv_mov_b64 v[16:17], 0\nv_mov_b64 v[18:19], 0\nv_mov_b64 v[20:21], 0\nv_mov_b64 v[22:23], 0\nv_mov_b64 v[24:25], 0\nv_add_u32 v1, 8388608, v1\n"
    "v_bfe_u32 v197, v0, 4, 2\nv_bfe_u32 v198, v0, 2, 2\nv_lshl_add_u32 v196, v197, 3, v198\ns_add_u32 m0, s64, 4096\ns_add_u32 s52, s32, s45\ns_addc_u32 s53, s33, 0\n"
    "global_load_lds_dwordx4 v26, s[52:53] offset:0 sc1\nglobal_load_lds_dwordx4 v26, s[52:53] offset:1024 sc1\nglobal_load_lds_dwordx4 v26, s[52:53] offset:2048 sc1\n"
    "global_load_lds_dwordx4 v26, s[52:53] offset:3072 sc1\ns_and_b32 s16, s3, 3\ns_lshl_b32 s15, s16, 12\ns_add_u32 s14, s15, 0x24000\nv_lshl_add_u32 v28, v196, 5, s14\n"
    "v_and_b32 v199, 3, v0\nv_lshl_add_u32 v28, v199, 3, v28\ns_add_u32 s20, s3, 1\ns_and_b32 s19, s20, 3\ns_lshl_b32 s18, s19, 12\ns_add_u32 s17, s18, 0x24000\n"
    "v_lshl_add_u32 v29, v196, 5, s17\nv_lshl_add_u32 v29, v199, 3, v29\ns_add_u32 s24, s3, 2\ns_and_b32 s23, s24, 3\ns_lshl_b32 s22, s23, 12\ns_add_u32 s21, s22, 0x24000\n"
    "v_lshl_add_u32 v30, v196, 5, s21\nv_lshl_add_u32 v30, v199, 3, v30\ns_add_u32 s68, s3, 3\ns_and_b32 s67, s68, 3\ns_lshl_b32 s66, s67, 12\ns_add_u32 s25, s66, 0x24000\n"
    "v_lshl_add_u32 v31, v196, 5, s25\nv_lshl_add_u32 v31, v199, 3, v31\nv_and_b32 v200, 63, v0\ns_lshl_b32 s72, s35, 15\ns_and_b32 s74, s36, 3\ns_lshl_b32 s73, s74, 13\n"
    "s_add_u32 s71, s72, s73\ns_lshl_b32 s75, s3, 11\ns_add_u32 s70, s71, s75\ns_add_u32 s69, s70, 0xd00000\nv_lshl_add_u32 v36, v200, 4, s69\nv_and_b32 v196, 63, v0\n"
    "s_max_u32 s20, s37, 1\ns_add_u32 s19, s20, -1\ns_lshr_b32 s18, s19, 2\ns_and_b32 s21, s35, 1\ns_lshl1_add_u32 s17, s18, s21\ns_lshl_b32 s16, s17, 15\n"
    "s_and_b32 s23, s19, 3\ns_lshl_b32 s22, s23, 13\ns_add_u32 s15, s16, s22\ns_add_u32 s14, s15, 0xd01800\nv_lshl_add_u32 v37, v196, 4, s14\nv_and_b32 v198, 7, v0\n"
    "v_bfe_u32 v199, v0, 4, 2\nv_lshl_add_u32 v197, v198, 2, v199\ns_lshl_b32 s25, s3, 9\ns_add_u32 s24, s25, 0x21000\nv_lshl_add_u32 v38, v197, 3, s24\nv_and_b32 v200, 8, v0\n"
    "v_lshl_add_u32 v38, v200, 5, v38\ns_lshl_b32 s67, s3, 12\ns_add_u32 s66, s67, 0x24000\nv_lshl_add_u32 v43, v197, 3, s66\nv_lshl_add_u32 v43, v200, 5, v43\n"
    "v_and_b32 v201, 15, v0\ns_add_u32 s68, s66, 0x80\nv_lshl_add_u32 v44, v201, 3, s68\ns_lshl_b32 s70, s3, 5\ns_add_u32 s69, s70, 0x18040\nv_lshl_add_u32 v40, v199, 3, s69\n"
    "s_mov_b32 s71, 0xd0\nv_mad_u32_u24 v40, v201, s71, v40\nv_and_b32 v202, 31, v0\ns_mov_b32 s72, 0x27f80\nv_lshl_add_u32 v41, v202, 2, s72\nv_mov_b32 v42, 2\n"
    "s_lshl2_add_u32 s75, s40, s3\ns_lshl_b32 s74, s75, 15\ns_add_u32 s73, s74, 0xe05000\nv_lshl_add_u32 v4, v196, 4, s73\ns_add_u32 s76, s74, 0xe07000\n"
    "v_lshl_add_u32 v5, v196, 4, s76\ns_mov_b32 s86, 0x3b000000\ns_add_u32 m0, s64, 8192\ns_add_u32 s52, s32, s46\ns_addc_u32 s53, s33, 0\n"
    "global_load_lds_dwordx4 v26, s[52:53] offset:0 sc1\nglobal_load_lds_dwordx4 v26, s[52:53] offset:1024 sc1\nglobal_load_lds_dwordx4 v26, s[52:53] offset:2048 sc1\n"
    "global_load_lds_dwordx4 v26, s[52:53] offset:3072 sc1\ns_setprio 0\ns_waitcnt vmcnt(8)\ns_barrier\ns_setprio 2\ns_add_u32 m0, s65, 1024\ns_nop 0\n"
    "global_load_lds_dwordx4 v27, s[60:61] offset:0 sc1\ns_add_u32 m0, s65, 0\ns_nop 0\nglobal_load_lds_dwordx4 v27, s[60:61] offset:2048 sc1\ns_add_u32 m0, s65, 3072\ns_nop 0\n"
    "global_load_lds_dwordx4 v9, s[60:61] offset:0 sc1\ns_add_u32 m0, s65, 2048\ns_nop 0\nglobal_load_lds_dwordx4 v9, s[60:61] offset:2048 sc1\ns_setprio 0\ns_waitcnt vmcnt(8)\n"
    "s_barrier\ns_setprio 2\ns_add_u32 m0, s64, 0\ns_add_u32 s52, s32, s47\ns_addc_u32 s53, s33, 0\nglobal_load_lds_dwordx4 v26, s[52:53] offset:0 sc1\n"
    "global_load_lds_dwordx4 v26, s[52:53] offset:1024 sc1\nglobal_load_lds_dwordx4 v26, s[52:53] offset:2048 sc1\nglobal_load_lds_dwordx4 v26, s[52:53] offset:3072 sc1\n"
    "s_setprio 0\ns_waitcnt vmcnt(8)\ns_barrier\ns_setprio 2\ns_add_u32 m0, s64, 4096\ns_add_u32 s52, s32, s48\ns_addc_u32 s53, s33, 0\n"
    "global_load_lds_dwordx4 v26, s[52:53] offset:0 sc1\nglobal_load_lds_dwordx4 v26, s[52:53] offset:1024 sc1\nglobal_load_lds_dwordx4 v26, s[52:53] offset:2048 sc1\n"
    "global_load_lds_dwordx4 v26, s[52:53] offset:3072 sc1\ns_setprio 0\ns_waitcnt vmcnt(8)\nds_read_b64_tr_b16 v[196:197], v28 offset:0\n"
    "ds_read_b64_tr_b16 v[198:199], v28 offset:128\nds_read_b64_tr_b16 v[200:201], v28 offset:1024\nds_read_b64_tr_b16 v[202:203], v28 offset:1152\n"
    "ds_read_b64_tr_b16 v[204:205], v28 offset:2048\nds_read_b64_tr_b16 v[206:207], v28 offset:2176\nds_read_b64_tr_b16 v[208:209], v28 offset:3072\n"
    "ds_read_b64_tr_b16 v[210:211], v28 offset:3200\ns_waitcnt vmcnt(4)\ns_barrier\ns_setprio 2\ns_add_u32 m0, s64, 8192\ns_add_u32 s52, s32, s49\ns_addc_u32 s53, s33, 0\n"
    "global_load_lds_dwordx4 v26, s[52:53] offset:0 sc1\nglobal_load_lds_dwordx4 v26, s[52:53] offset:1024 sc1\nglobal_load_lds_dwordx4 v26, s[52:53] offset:2048 sc1\n"
    "global_load_lds_dwordx4 v26, s[52:53] offset:3072 sc1\ns_setprio 0\ns_waitcnt lgkmcnt(0)\nv_pk_maximum3_f16 v62, v196, v197, v198\nv_pk_maximum3_f16 v63, v199, v200, v201\n"
    "v_pk_maximum3_f16 v62, v62, v202, v203\nv_pk_maximum3_f16 v63, v63, v204, v205\nv_pk_maximum3_f16 v62, v62, v206, v207\nv_pk_maximum3_f16 v63, v63, v208, v209\n"
    "v_pk_maximum3_f16 v62, v62, v210, v211\nv_pk_maximum3_f16 v62, v62, v63, v63\nv_cmp_u_f16_sdwa vcc, v62, v62 src0_sel:WORD_0 src1_sel:WORD_1\ns_cbranch_vccnz .Lxs59\n"
    ".Lxr60:\ns_waitcnt vmcnt(4)\ns_barrier\ns_waitcnt lgkmcnt(0)\ns_setprio 2\nv_mfma_f32_16x16x32_bf16 v[46:49], v[64:67], v[196:199], 0\n"
    "v_mfma_f32_16x16x32_bf16 v[46:49], v[68:71], v[200:203], v[46:49]\nv_mfma_f32_16x16x32_bf16 v[46:49], v[72:75], v[204:207], v[46:49]\n"
    "v_mfma_f32_16x16x32_bf16 v[46:49], v[76:79], v[208:211], v[46:49]\ns_setprio 2\ns_add_u32 m0, s64, 0\ns_add_u32 s52, s32, s50\ns_addc_u32 s53, s33, 0\n"
    "global_load_lds_dwordx4 v26, s[52:53] offset:0 sc1\nglobal_load_lds_dwordx4 v26, s[52:53] offset:1024 sc1\nglobal_load_lds_dwordx4 v26, s[52:53] offset:2048 sc1\n"
    "global_load_lds_dwordx4 v26, s[52:53] offset:3072 sc1\ns_setprio 0\nds_read_b64_tr_b16 v[196:197], v29 offset:0\nds_read_b64_tr_b16 v[198:199], v29 offset:128\n"
    "ds_read_b64_tr_b16 v[200:201], v29 offset:1024\nds_read_b64_tr_b16 v[202:203], v29 offset:1152\nds_read_b64_tr_b16 v[204:205], v29 offset:2048\n"
    "ds_read_b64_tr_b16 v[206:207], v29 offset:2176\nds_read_b64_tr_b16 v[208:209], v29 offset:3072\nds_read_b64_tr_b16 v[210:211], v29 offset:3200\ns_waitcnt vmcnt(4)\n"
    "s_barrier\ns_waitcnt lgkmcnt(0)\ns_setprio 2\nv_mfma_f32_16x16x32_bf16 v[46:49], v[80:83], v[196:199], v[46:49]\n"
    "v_mfma_f32_16x16x32_bf16 v[46:49], v[84:87], v[200:203], v[46:49]\nv_mfma_f32_16x16x32_bf16 v[46:49], v[88:91], v[204:207], v[46:49]\n"
    "v_mfma_f32_16x16x32_bf16 v[46:49], v[92:95], v[208:211], v[46:49]\ns_setprio 2\ns_add_u32 m0, s64, 4096\ns_add_u32 s52, s32, s51\ns_addc_u32 s53, s33, 0\n"
    "global_load_lds_dwordx4 v26, s[52:53] offset:0 sc1\nglobal_load_lds_dwordx4 v26, s[52:53] offset:1024 sc1\nglobal_load_lds_dwordx4 v26, s[52:53] offset:2048 sc1\n"
    "global_load_lds_dwordx4 v26, s[52:53] offset:3072 sc1\nglobal_load_dwordx4 v[212:215], v36, s[32:33] offset:0 sc1\n"
    "global_load_dwordx4 v[216:219], v36, s[32:33] offset:1024 sc1\nglobal_load_dwordx4 v[220:223], v37, s[32:33] offset:0 sc1\n"
    "global_load_dwordx4 v[224:227], v37, s[32:33] offset:1024 sc1\ns_setprio 0\nds_read_b64_tr_b16 v[196:197], v30 offset:0\nds_read_b64_tr_b16 v[198:199], v30 offset:128\n"
    "ds_read_b64_tr_b16 v[200:201], v30 offset:1024\nds_read_b64_tr_b16 v[202:203], v30 offset:1152\nds_read_b64_tr_b16 v[204:205], v30 offset:2048\n"
    "ds_read_b64_tr_b16 v[206:207], v30 offset:2176\nds_read_b64_tr_b16 v[208:209], v30 offset:3072\nds_read_b64_tr_b16 v[210:211], v30 offset:3200\ns_waitcnt vmcnt(8)\n"
    "s_barrier\ns_waitcnt lgkmcnt(0)\ns_setprio 2\nv_mfma_f32_16x16x32_bf16 v[46:49], v[96:99], v[196:199], v[46:49]\n"
    "v_mfma_f32_16x16x32_bf16 v[46:49], v[100:103], v[200:203], v[46:49]\nv_mfma_f32_16x16x32_bf16 v[46:49], v[104:107], v[204:207], v[46:49]\n"
    "v_mfma_f32_16x16x32_bf16 v[46:49], v[108:111], v[208:211], v[46:49]\ns_setprio 2\nglobal_load_dwordx4 v[64:67], v4, s[32:33] offset:-4096 sc1\n"
    "global_load_dwordx4 v[68:71], v4, s[32:33] offset:-3072 sc1\nglobal_load_dwordx4 v[72:75], v4, s[32:33] offset:-2048 sc1\n"
    "global_load_dwordx4 v[76:79], v4, s[32:33] offset:-1024 sc1\ns_setprio 0\nds_read_b64_tr_b16 v[196:197], v31 offset:0\nds_read_b64_tr_b16 v[198:199], v31 offset:128\n"
    "ds_read_b64_tr_b16 v[200:201], v31 offset:1024\nds_read_b64_tr_b16 v[202:203], v31 offset:1152\nds_read_b64_tr_b16 v[204:205], v31 offset:2048\n"
    "ds_read_b64_tr_b16 v[206:207], v31 offset:2176\nds_read_b64_tr_b16 v[208:209], v31 offset:3072\nds_read_b64_tr_b16 v[210:211], v31 offset:3200\ns_waitcnt vmcnt(8)\n"
    "s_barrier\ns_waitcnt lgkmcnt(0)\ns_setprio 2\nv_mfma_f32_16x16x32_bf16 v[46:49], v[112:115], v[196:199], v[46:49]\n"
    "v_mfma_f32_16x16x32_bf16 v[46:49], v[116:119], v[200:203], v[46:49]\nv_mfma_f32_16x16x32_bf16 v[46:49], v[120:123], v[204:207], v[46:49]\n"
    "v_mfma_f32_16x16x32_bf16 v[46:49], v[124:127], v[208:211], v[46:49]\ns_setprio 2\nglobal_load_dwordx4 v[80:83], v4, s[32:33] offset:0 sc1\n"
    "global_load_dwordx4 v[84:87], v4, s[32:33] offset:1024 sc1\nglobal_load_dwordx4 v[88:91], v4, s[32:33] offset:2048 sc1\n"
    "global_load_dwordx4 v[92:95], v4, s[32:33] offset:3072 sc1\ns_setprio 0\ns_waitcnt vmcnt(8)\ns_setprio 2\ns_mov_b32 s80, 0x10000\n.Lpl64:\nv_add_f32 v228, v212, v216\n"
    "v_add_f32 v229, v213, v217\nv_add_f32 v230, v214, v218\nv_add_f32 v231, v215, v219\nv_permlane16_swap_b32 v228, v229\ns_nop 0\nv_permlane16_swap_b32 v230, v231\n"
    "v_add_f32 v228, v228, v229\nv_add_f32 v230, v230, v231\ns_nop 1\nv_permlane32_swap_b32 v228, v230\nv_add_f32 v228, v228, v230\nv_cmp_u_f32 vcc, v228, v228\n"
    "s_cbranch_vccnz .Lps62\n.Lpr63:\nv_mov_b32 v229, v228\ns_nop 1\nv_mov_b32_dpp v229, v228 row_ror:8 row_mask:0xf bank_mask:0x3\n"
    "v_mov_b32_dpp v228, v228 row_ror:8 row_mask:0xf bank_mask:0xc\nv_mul_f32 v228, s86, v228\nv_mul_f32 v230, v228, v228\nv_fma_f32 v229, v229, s86, -v230\n"
    "v_max_f32 v229, 0, v229\nv_cvt_pk_bf16_f32 v229, 0, v229\nv_add_f32 v229, 0x3727c5ac, v229\nv_cvt_pk_bf16_f32 v229, 0, v229\nv_sqrt_f32 v229, v229\ns_nop 0\n"
    "v_cvt_pk_bf16_f32 v229, 0, v229\nv_rcp_f32 v229, v229\nv_cvt_pk_bf16_f32 v228, -v228, -v228\ns_mov_b32 exec_lo, 0xff00ff00\ns_mov_b32 exec_hi, 0xff00ff00\n"
    "v_mov_b32 v228, 0\ns_mov_b64 exec, -1\nds_write_b64 v38, v[228:229]\ns_waitcnt lgkmcnt(0)\ns_barrier\ns_setprio 2\n"
    "global_load_dwordx4 v[96:99], v5, s[32:33] offset:-4096 sc1\nglobal_load_dwordx4 v[100:103], v5, s[32:33] offset:-3072 sc1\n"
    "global_load_dwordx4 v[104:107], v5, s[32:33] offset:-2048 sc1\nglobal_load_dwordx4 v[108:111], v5, s[32:33] offset:-1024 sc1\ns_setprio 0\ns_setprio 0\n"
    "ds_write_b32 v41, v42\ns_mov_b32 s80, 0x10000\n.Lpl69:\nv_add_f32 v232, v220, v224\nv_add_f32 v233, v221, v225\nv_add_f32 v234, v222, v226\nv_add_f32 v235, v223, v227\n"
    "v_permlane16_swap_b32 v232, v233\ns_nop 0\nv_permlane16_swap_b32 v234, v235\nv_add_f32 v232, v232, v233\nv_add_f32 v234, v234, v235\ns_nop 1\n"
    "v_permlane32_swap_b32 v232, v234\nv_add_f32 v232, v232, v234\nv_cmp_u_f32 vcc, v232, v232\ns_cbranch_vccnz .Lps67\n.Lpr68:\nv_mov_b32 v233, v232\ns_nop 1\n"
    "v_mov_b32_dpp v233, v232 row_ror:8 row_mask:0xf bank_mask:0x3\nv_mov_b32_dpp v232, v232 row_ror:8 row_mask:0xf bank_mask:0xc\nv_mul_f32 v232, s86, v232\n"
    "v_mul_f32 v234, v232, v232\nv_fma_f32 v233, v233, s86, -v234\nv_max_f32 v233, 0, v233\nv_cvt_pk_bf16_f32 v233, 0, v233\nv_add_f32 v233, 0x3727c5ac, v233\n"
    "v_cvt_pk_bf16_f32 v233, 0, v233\nv_sqrt_f32 v233, v233\ns_nop 0\nv_cvt_pk_bf16_f32 v233, 0, v233\nv_rcp_f32 v233, v233\nv_cvt_pk_bf16_f32 v232, 0, v232\n"
    "ds_write_b64 v43, v[232:233]\nds_read_b64 v[232:233], v44\ns_waitcnt lgkmcnt(0)\nv_fma_f32 v46, -v232, v244, v46\nv_fma_f32 v47, -v232, v245, v47\n"
    "v_fma_f32 v48, -v232, v246, v48\nv_fma_f32 v49, -v232, v247, v49\nv_mul_f32 v46, v46, v233\nv_mul_f32 v47, v47, v233\nv_mul_f32 v48, v48, v233\nv_mul_f32 v49, v49, v233\n"
    "v_cvt_pk_bf16_f32 v46, v46, v47\nv_cvt_pk_bf16_f32 v47, v48, v49\nds_write_b64 v40, v[46:47]\nv_mov_b32 v62, 0\ns_branch .LHL12\n.LG1:\ns_waitcnt lgkmcnt(0)\n"
    "s_mov_b32 s86, 16777216\ns_load_dword s34, s[32:33], s86\ns_mov_b32 s6, 0\ns_mov_b32 s7, 0\ns_and_b32 s35, s2, 7\ns_lshr_b32 s36, s2, 3\ns_bfe_u32 s44, s2, 0x20001\n"
    "s_bfe_u32 s45, s2, 0x20003\ns_lshl2_add_u32 s37, s44, s45\ns_and_b32 s46, s2, 1\ns_lshr_b32 s47, s2, 5\ns_lshl3_add_u32 s38, s46, s47\ns_bfe_u32 s48, s2, 0x30003\n"
    "s_lshl3_add_u32 s39, s35, s48\ns_lshr_b32 s40, s2, 6\ns_lshl_b32 s49, s35, 5\ns_add_u32 s41, s49, s36\ns_lshl1_add_u32 s50, s45, s47\ns_and_b32 s42, s50, 7\n"
    "s_lshl_b32 s86, s41, 2\ns_load_dword s96, s[26:27], s86\ns_load_dword s95, s[28:29], s86\ns_mov_b32 s44, 0x1d550\nv_lshl_add_u32 v14, v0, 3, s44\nv_lshrrev_b32 v48, 3, v0\n"
    "v_lshl_add_u32 v14, v48, 4, v14\ns_mov_b32 s45, 0x18000\nv_lshl_add_u32 v15, v48, 7, s45\nv_xor_b32 v50, v0, v48\nv_and_b32 v49, 7, v50\nv_lshl_add_u32 v15, v49, 4, v15\n"
    "s_mov_b32 s46, 0x25e00\nv_lshl_add_u32 v16, v0, 4, s46\ns_lshl_b32 s48, s41, 7\ns_add_u32 s47, s48, 0x600000\nv_lshl_add_u32 v51, v48, 15, s47\nv_and_b32 v52, 1, v0\n"
    "v_lshl_add_u32 v51, v52, 6, v51\nv_bfe_u32 v53, v0, 1, 2\nv_lshl_add_u32 v51, v53, 4, v51\nv_lshrrev_b32 v55, 4, v0\ns_lshl_b32 s51, s35, 18\ns_lshl_b32 s52, s36, 8\n"
    "s_add_u32 s50, s51, s52\ns_add_u32 s49, s50, 0x1200000\nv_lshl_add_u32 v54, v55, 13, s49\nv_and_b32 v56, 15, v0\nv_lshl_add_u32 v54, v56, 4, v54\ns_cmp_eq_u32 s6, 0\n"
    "s_cselect_b64 vcc, -1, 0\nv_cndmask_b32 v17, v54, v51, vcc\nv_bfe_u32 v59, v0, 5, 1\nv_and_b32 v61, 31, v0\nv_sub_u32 v60, 0x3ff, v61\nv_lshl_add_u32 v58, v59, 3, v60\n"
    "v_lshrrev_b32 v57, 2, v58\ns_lshr_b32 s55, s3, 1\ns_mul_i32 s54, s55, 0x2100\ns_add_u32 s53, s54, 0x17800\nv_lshl_add_u32 v19, v57, 3, s53\nv_and_b32 v62, 3, v58\n"
    "s_mov_b32 s56, 0x840\nv_mad_u32_u24 v19, v62, s56, v19\ns_lshl2_add_u32 s57, s3, 0x27f80\nv_mov_b32 v12, s57\ns_add_u32 s58, s6, 12\nv_mov_b32 v13, s58\n"
    "v_and_b32 v63, 0xff, v0\nv_mov_b32 v65, 0x1d000\nv_mov_b32 v67, 0x1fdb0\nv_mov_b32 v69, 0x20b10\nv_mov_b32 v70, 0x21870\nv_cmp_gt_u32 vcc, 0xa1, v63\ns_nop 1\n"
    "v_cndmask_b32 v68, v70, v69, vcc\nv_cmp_gt_u32 vcc, 0x7b, v63\ns_nop 1\nv_cndmask_b32 v66, v68, v67, vcc\nv_cmp_gt_u32 vcc, 0x55, v63\ns_nop 1\n"
    "v_cndmask_b32 v64, v66, v65, vcc\nv_lshl_add_u32 v18, v63, 4, v64\nv_mov_b32 v72, 0x1950\nv_mov_b32 v73, 0x3180\nv_cmp_gt_u32 vcc, 0x55, v63\ns_nop 1\n"
    "v_cndmask_b32 v71, v73, v72, vcc\nv_add_u32 v1, v18, v71\ns_mul_i32 s61, s55, 0x1950\ns_and_b32 s63, s3, 1\ns_mul_i32 s62, s63, 0xa00\ns_add_u32 s60, s61, s62\n"
    "s_add_u32 s59, s60, 0x1d000\nv_lshl_add_u32 v2, v59, 4, s59\ns_mov_b32 s64, 0x50\nv_mad_u32_u24 v2, v61, s64, v2\ns_lshl_b32 s66, s3, 12\ns_add_u32 s65, s66, 0x18000\n"
    "v_lshl_add_u32 v3, v61, 7, s65\nv_and_b32 v75, 7, v0\nv_xor_b32 v74, v59, v75\nv_lshl_add_u32 v3, v74, 4, v3\ns_add_u32 s44, s7, 0x400000\nv_lshl_add_u32 v20, v0, 4, s44\n"
    "s_lshl_b32 s47, s41, 13\ns_add_u32 s46, s7, s47\ns_add_u32 s45, s46, 0x200000\nv_lshl_add_u32 v21, v0, 4, s45\nv_add_u32 v22, 8192, v20\nv_add_u32 v23, 16384, v20\n"
    "v_add_u32 v24, 24576, v20\nv_and_b32 v56, 63, v0\ns_lshl_b32 s46, s41, 13\ns_lshl_b32 s47, s3, 11\ns_add_u32 s45, s46, s47\ns_add_u32 s44, s45, 0x800000\n"
    "v_lshl_add_u32 v4, v56, 4, s44\ns_add_u32 s48, s45, 0xa00000\nv_lshl_add_u32 v5, v56, 4, s48\nv_and_b32 v57, 1, v0\ns_lshl_b32 s51, s2, 7\ns_lshl_b32 s52, s3, 5\n"
    "s_add_u32 s50, s51, s52\ns_add_u32 s49, s50, 0xc00000\nv_lshl_add_u32 v6, v57, 4, s49\nv_and_b32 v58, 0x7f, v0\ns_lshr_b32 s55, s3, 1\ns_mul_i32 s54, s55, 0x2100\n"
    "s_add_u32 s53, s54, 0x18000\nv_lshl_add_u32 v47, v58, 4, s53\nv_and_b32 v59, 31, v0\ns_mov_b32 s56, 0x27600\nv_lshl_add_u32 v29, v59, 2, s56\ns_lshl_b32 s60, s40, 13\n"
    "s_add_u32 s59, s7, s60\ns_lshl_b32 s61, s39, 7\ns_add_u32 s58, s59, s61\ns_add_u32 s57, s58, 0x400000\nv_lshl_add_u32 v30, v59, 2, s57\nv_mov_b32 v25, 0\nv_mov_b32 v26, 0\n"
    "v_mov_b32 v27, 0\nv_mov_b32 v28, 0\nv_mov_b32 v8, -1\nv_mov_b32 v9, -1\nv_mov_b32 v10, -1\nv_mov_b32 v11, -1\nv_mov_b32 v31, v47\nv_mov_b32 v206, v1\nv_mov_b32 v7, 0\n"
    "v_mov_b64 v[208:209], v[2:3]\nv_mov_b64 v[210:211], v[4:5]\nv_mov_b64 v[212:213], v[6:7]\nv_mov_b64 v[214:215], v[8:9]\nv_mov_b64 v[216:217], v[10:11]\n"
    "v_mov_b64 v[218:219], v[12:13]\nv_mov_b64 v[220:221], v[14:15]\nv_mov_b64 v[222:223], v[16:17]\nv_mov_b64 v[224:225], v[18:19]\nv_mov_b64 v[226:227], v[20:21]\n"
    "v_mov_b64 v[228:229], v[22:23]\nv_mov_b64 v[230:231], v[24:25]\nv_mov_b64 v[232:233], v[26:27]\nv_mov_b64 v[234:235], v[28:29]\nv_mov_b64 v[236:237], v[30:31]\n"
    "v_mov_b32 v64, 0\nv_mov_b32 v65, 0xf149f2ca\nv_bfe_u32 v68, v0, 4, 2\nv_lshlrev_b32 v67, 2, v68\ns_add_u32 s45, s37, s3\ns_cmp_eq_u32 s45, 0\ns_cselect_b32 s44, 8, 0\n"
    "v_and_b32 v71, 7, v0\nv_add_u32 v70, 1, v71\nv_max_u32 v69, s44, v70\nv_sub_u32 v66, v67, v69\nv_add_u32 v73, 8, v71\nv_sub_u32 v72, v73, v69\nv_cmp_le_u32 vcc, v66, v72\n"
    "s_nop 1\nv_cndmask_b32 v2, v65, v64, vcc\nv_add_u32 v75, 1, v67\nv_sub_u32 v74, v75, v69\nv_cmp_le_u32 vcc, v74, v72\ns_nop 1\nv_cndmask_b32 v3, v65, v64, vcc\n"
    "v_add_u32 v77, 2, v67\nv_sub_u32 v76, v77, v69\nv_cmp_le_u32 vcc, v76, v72\ns_nop 1\nv_cndmask_b32 v4, v65, v64, vcc\nv_add_u32 v79, 3, v67\nv_sub_u32 v78, v79, v69\n"
    "v_cmp_le_u32 vcc, v78, v72\ns_nop 1\nv_cndmask_b32 v5, v65, v64, vcc\ns_mul_i32 s47, s3, 0xd00\ns_add_u32 s46, s47, 0x18040\nv_lshl_add_u32 v6, v68, 4, s46\n"
    "v_and_b32 v81, 15, v0\nv_max_u32 v80, 1, v81\ns_mov_b32 s48, 0x1a0\nv_mad_u32_u24 v6, v80, s48, v6\ns_add_u32 s49, s47, 0x18d00\nv_lshl_add_u32 v7, v68, 4, s49\n"
    "v_mad_u32_u24 v7, v71, s48, v7\nv_and_b32 v82, 3, v0\ns_add_u32 s50, s47, 0x18080\nv_lshl_add_u32 v8, v82, 3, s50\nv_bfe_u32 v85, v0, 2, 2\nv_add_u32 v84, v67, v85\n"
    "v_max_u32 v83, 1, v84\nv_mad_u32_u24 v8, v83, s48, v8\nv_bfe_u32 v86, v0, 3, 1\ns_lshr_b32 s55, s3, 1\ns_lshl2_add_u32 s54, s37, s55\ns_lshl_b32 s53, s54, 15\n"
    "s_lshl_b32 s56, s38, 11\ns_add_u32 s52, s53, s56\ns_and_b32 s58, s3, 1\ns_lshl_b32 s57, s58, 8\ns_add_u32 s51, s52, s57\nv_lshl_add_u32 v9, v86, 10, s51\n"
    "v_bfe_u32 v87, v0, 5, 1\nv_lshl_add_u32 v9, v87, 9, v9\nv_bfe_u32 v88, v0, 4, 1\nv_lshl_add_u32 v9, v88, 4, v9\nv_lshl_add_u32 v9, v71, 5, v9\nv_mov_b64 v[10:11], 0\n"
    "v_mov_b64 v[12:13], 0\nv_mov_b64 v[14:15], 0\nv_mov_b64 v[16:17], 0\nv_mov_b64 v[18:19], 0\nv_mov_b64 v[20:21], 0\nv_mov_b64 v[22:23], 0\nv_mov_b64 v[24:25], 0\n"
    "v_bfe_u32 v65, v0, 4, 1\nv_bfe_u32 v66, v0, 5, 1\nv_xor_b32 v64, v65, v66\nv_bfe_u32 v69, v0, 2, 2\nv_lshl_add_u32 v68, v66, 3, v69\nv_lshlrev_b32 v67, 6, v68\n"
    "v_lshl_add_u32 v26, v64, 5, v67\nv_and_b32 v70, 1, v0\nv_lshl_add_u32 v26, v70, 4, v26\nv_and_b32 v71, 2, v0\nv_lshl_add_u32 v26, v71, 2, v26\ns_mul_i32 s45, s3, 0x1a00\n"
    "s_add_u32 s44, s45, 0x18d00\nv_lshl_add_u32 v27, v66, 4, s44\nv_and_b32 v72, 31, v0\ns_mov_b32 s46, 0xd0\nv_mad_u32_u24 v27, v72, s46, v27\nv_and_b32 v73, 19, v0\n"
    "s_mov_b32 s47, 0x27c00\nv_lshl_add_u32 v28, v73, 2, s47\nv_and_b32 v74, 4, v0\nv_lshl_add_u32 v28, v74, 3, v28\nv_and_b32 v75, 8, v0\nv_lshl_add_u32 v28, v75, 1, v28\n"
    "s_mul_i32 s49, s3, 0x3000\ns_add_u32 s48, s49, 0x18000\nv_lshl_add_u32 v29, v72, 7, s48\nv_bfe_u32 v77, v0, 1, 3\nv_xor_b32 v76, v66, v77\nv_lshl_add_u32 v29, v76, 4, v29\n"
    "s_and_b32 s52, s41, 3\ns_lshr_b32 s55, s41, 2\ns_lshl_b32 s54, s55, 7\ns_add_u32 s53, s54, 0x1002000\ns_lshl3_add_u32 s51, s52, s53\ns_lshl2_add_u32 s50, s3, s51\n"
    "v_mov_b32 v30, s50\nv_and_b32 v78, 0x7f, v0\ns_mov_b32 s56, 0x23800\nv_lshl_add_u32 v31, v78, 3, s56\ns_mov_b32 s57, 0x23000\nv_lshl_add_u32 v32, v78, 4, s57\n"
    "v_and_b32 v79, 63, v0\ns_lshr_b32 s60, s3, 1\ns_lshl_b32 s59, s60, 8\ns_add_u32 s58, s59, 0x20000\nv_lshl_add_u32 v33, v79, 2, s58\ns_lshl_b32 s62, s41, 11\n"
    "s_add_u32 s61, s62, 0xe00000\nv_lshl_add_u32 v38, v0, 4, s61\ns_add_u32 s63, s49, 0x1a000\nv_add_u32 v39, s63, v67\nv_lshl_add_u32 v39, v64, 5, v39\nv_and_b32 v80, 3, v0\n"
    "v_lshl_add_u32 v39, v80, 3, v39\ns_mul_i32 s65, s3, 0x2200\ns_add_u32 s64, s65, 0x1f400\nv_lshl_add_u32 v188, v79, 3, s64\ns_lshl_b32 s67, s39, 9\n"
    "s_add_u32 s66, s67, 0x1008000\nv_lshl_add_u32 v187, v80, 7, s66\nv_mov_b32 v191, 0x10000\ns_lshl_b32 s70, s41, 13\ns_lshl_b32 s71, s3, 11\ns_add_u32 s69, s70, s71\n"
    "s_add_u32 s68, s69, 0x200000\nv_lshl_add_u32 v192, v79, 4, s68\ns_lshl_b32 s74, s2, 7\ns_lshl_b32 s75, s3, 5\ns_add_u32 s73, s74, s75\ns_add_u32 s72, s73, 0x400000\n"
    "v_lshl_add_u32 v193, v70, 4, s72\ns_and_b32 s46, s2, 3\ns_lshr_b32 s49, s2, 2\ns_lshl_b32 s48, s49, 7\ns_add_u32 s47, s48, 0x1004000\ns_lshl4_add_u32 s45, s46, s47\n"
    "s_lshl2_add_u32 s44, s3, s45\nv_mov_b32 v194, s44\nv_and_b32 v64, 63, v0\ns_lshl_b32 s52, s41, 13\ns_lshl_b32 s53, s3, 11\ns_add_u32 s51, s52, s53\n"
    "s_add_u32 s50, s51, 0x600000\nv_lshl_add_u32 v200, v64, 4, s50\ns_lshl_b32 s56, s41, 10\ns_lshl_b32 s57, s3, 8\ns_add_u32 s55, s56, s57\ns_add_u32 s54, s55, 0xd00000\n"
    "v_lshl_add_u32 v201, v64, 4, s54\nv_and_b32 v65, 15, v0\ns_add_u32 s58, s53, 0x1004000\nv_lshl_add_u32 v202, v65, 7, s58\nv_bfe_u32 v66, v0, 4, 2\n"
    "v_lshl_add_u32 v202, v66, 4, v202\nv_mov_b32 v196, -1\nv_mov_b32 v197, -1\nv_mov_b32 v198, -1\nv_mov_b32 v199, -1\ns_lshl_b32 s60, s39, 15\ns_add_u32 s59, s60, 0x1000\n"
    "v_lshl_add_u32 v182, v0, 4, s59\ns_add_u32 s61, s59, 0x2000\nv_lshl_add_u32 v183, v0, 4, s61\ns_add_u32 s62, s59, 0x4000\nv_lshl_add_u32 v184, v0, 4, s62\n"
    "s_add_u32 s63, s59, 0x6000\nv_lshl_add_u32 v185, v0, 4, s63\ns_mov_b32 s64, 0x1f600\nv_lshl_add_u32 v186, v0, 4, s64\ns_mul_i32 s66, s3, 0x1760\n"
    "s_add_u32 s65, s66, 0x18000\nv_lshl_add_u32 v189, v66, 4, s65\ns_mov_b32 s67, 0x220\nv_mad_u32_u24 v189, v65, s67, v189\ns_lshr_b32 s70, s3, 1\ns_mul_i32 s69, s70, 0x4400\n"
    "s_add_u32 s68, s69, 0x1f400\nv_lshl_add_u32 v190, v66, 4, s68\nv_mad_u32_u24 v190, v65, s67, v190\ns_cmp_eq_u32 s3, 3\ns_cbranch_scc0 .LL74\nv_mov_b32 v64, 0\n"
    "v_lshlrev_b32 v65, 2, v0\nv_add_u32 v65, 162048, v65\ns_mov_b32 exec_hi, 0\nds_write_b32 v65, v64\ns_mov_b64 exec, 15\nv_mov_b32 v64, 0x3f803f80\ns_mov_b32 exec_lo, -1\n"
    "s_mov_b32 exec_hi, 15\nds_write_b32 v65, v64 offset:880\ns_mov_b64 exec, -1\n.LL74:\ns_and_b32 s54, s42, 7\ns_lshl_b32 s44, s54, 12\ns_add_u32 s56, s54, 1\n"
    "s_and_b32 s55, s56, 7\ns_lshl_b32 s45, s55, 12\ns_add_u32 s58, s54, 2\ns_and_b32 s57, s58, 7\ns_lshl_b32 s46, s57, 12\ns_add_u32 s60, s54, 3\ns_and_b32 s59, s60, 7\n"
    "s_lshl_b32 s47, s59, 12\ns_add_u32 s62, s54, 4\ns_and_b32 s61, s62, 7\ns_lshl_b32 s48, s61, 12\ns_add_u32 s64, s54, 5\ns_and_b32 s63, s64, 7\ns_lshl_b32 s49, s63, 12\n"
    "s_add_u32 s66, s54, 6\ns_and_b32 s65, s66, 7\ns_lshl_b32 s50, s65, 12\ns_add_u32 s68, s54, 7\ns_and_b32 s67, s68, 7\ns_lshl_b32 s51, s67, 12\ns_mov_b32 s81, 0x3b000000\n"
    "s_mov_b32 s84, -1\ns_mov_b32 s85, 0\nv_mov_b32 v143, 0x3f803f80\nv_add_u32 v116, s44, v26\nv_add_u32 v118, 0x10000, v116\nv_mov_b32 v112, 0\nv_mov_b32 v113, 0\n"
    "v_mov_b32 v114, 0\nv_mov_b32 v115, 0\nv_mov_b32 v121, 0\nv_mov_b32 v122, 0\nv_mov_b32 v123, 0\nv_mov_b32 v125, 0\nv_mov_b32 v126, 0\nv_mov_b32 v127, 0\nv_mov_b32 v129, 0\n"
    "v_mov_b32 v130, 0\nv_mov_b32 v131, 0\nv_mov_b32 v133, 0\nv_mov_b32 v134, 0\nv_mov_b32 v135, 0\nv_xor_b32 v176, 32, v29\nv_xor_b32 v177, 64, v29\nv_xor_b32 v178, 0x60, v29\n"
    "s_waitcnt lgkmcnt(0)\ns_lshl_b32 s97, s96, 16\ns_and_b32 s96, s96, 0xffff0000\ns_cmp_lt_u32 s3, 2\ns_cselect_b32 s96, s97, s96\ns_mov_b32 s97, 0x3fb8aa3b\n"
    "v_mov_b32 v66, s96\nv_mul_f32 v67, s97, v66\nv_fma_f32 v68, s97, v66, -v67\nv_exp_f32 v67, v67\nv_fmac_f32 v68, 0x32a57060, v66\nv_mul_f32 v68, 0x3f317218, v68\n"
    "v_fmac_f32 v67, v67, v68\nv_cvt_pk_bf16_f32 v67, 0, v67\nv_mul_f32 v66, s97, v67\nv_mul_f32 v68, 0x3e22f983, v67\nv_readfirstlane_b32 s92, v67\n"
    "v_readfirstlane_b32 s93, v66\nv_readfirstlane_b32 s94, v68\ns_and_b32 s97, s95, 0xffff0000\ns_lshl_b32 s96, s95, 16\ns_add_u32 s34, s34, 1\nv_mov_b32 v180, s34\n"
    "s_cmp_lt_u32 s3, 2\ns_cbranch_scc0 .LL73\ns_waitcnt vmcnt(0)\nds_read_b64_tr_b16 v[244:245], v39\nds_read_b64_tr_b16 v[246:247], v39 offset:256\ns_waitcnt lgkmcnt(0)\n"
    "global_store_dwordx4 v38, v[244:247], s[32:33] sc1\n.LL73:\n.Lwret:\ns_mov_b64 exec, -1\ns_waitcnt lgkmcnt(0)\ns_barrier\nds_read_b128 v[144:147], v29 offset:0\n"
    "ds_read_b128 v[148:151], v176 offset:0\nds_read_b128 v[152:155], v177 offset:0\nds_read_b128 v[156:159], v178 offset:0\nds_read_b64_tr_b16 v[44:45], v116 offset:0\n"
    "ds_read_b64_tr_b16 v[46:47], v116 offset:256\nds_read_b64_tr_b16 v[48:49], v116 offset:1024\nds_read_b64_tr_b16 v[50:51], v116 offset:1280\n"
    "ds_read_b64_tr_b16 v[52:53], v116 offset:2048\nds_read_b64_tr_b16 v[54:55], v116 offset:2304\nds_read_b64_tr_b16 v[56:57], v116 offset:3072\n"
    "ds_read_b64_tr_b16 v[58:59], v116 offset:3328\nds_read_b64_tr_b16 v[60:61], v116 offset:32768\nds_read_b64_tr_b16 v[62:63], v116 offset:33024\ns_waitcnt lgkmcnt(13)\n"
    "v_dot2c_f32_bf16 v112, v143, v144\nv_dot2c_f32_bf16 v113, v144, v144\nv_dot2c_f32_bf16 v114, v143, v145\nv_dot2c_f32_bf16 v115, v145, v145\n"
    "v_dot2c_f32_bf16 v112, v143, v146\nv_dot2c_f32_bf16 v113, v146, v146\nv_dot2c_f32_bf16 v114, v143, v147\nv_dot2c_f32_bf16 v115, v147, v147\ns_waitcnt lgkmcnt(12)\n"
    "v_dot2c_f32_bf16 v112, v143, v148\nv_dot2c_f32_bf16 v113, v148, v148\nv_dot2c_f32_bf16 v114, v143, v149\nv_dot2c_f32_bf16 v115, v149, v149\n"
    "v_dot2c_f32_bf16 v112, v143, v150\nv_dot2c_f32_bf16 v113, v150, v150\nv_dot2c_f32_bf16 v114, v143, v151\nv_dot2c_f32_bf16 v115, v151, v151\ns_waitcnt lgkmcnt(11)\n"
    "v_dot2c_f32_bf16 v112, v143, v152\nv_dot2c_f32_bf16 v113, v152, v152\nv_dot2c_f32_bf16 v114, v143, v153\nv_dot2c_f32_bf16 v115, v153, v153\n"
    "v_dot2c_f32_bf16 v112, v143, v154\nv_dot2c_f32_bf16 v113, v154, v154\nv_dot2c_f32_bf16 v114, v143, v155\nv_dot2c_f32_bf16 v115, v155, v155\ns_waitcnt lgkmcnt(10)\n"
    "v_dot2c_f32_bf16 v112, v143, v156\nv_dot2c_f32_bf16 v113, v156, v156\nv_dot2c_f32_bf16 v114, v143, v157\nv_dot2c_f32_bf16 v115, v157, v157\n"
    "v_dot2c_f32_bf16 v112, v143, v158\nv_dot2c_f32_bf16 v113, v158, v158\nv_dot2c_f32_bf16 v114, v143, v159\nv_dot2c_f32_bf16 v115, v159, v159\ns_waitcnt lgkmcnt(8)\n"
    "v_mfma_f32_32x32x16_bf16 v[64:79], v[44:47], v[144:147], 0\nds_read_b64_tr_b16 v[44:45], v116 offset:33792\nds_read_b64_tr_b16 v[46:47], v116 offset:34048\n"
    "v_add_u32 v117, s45, v26\nv_add_u32 v119, 0x10000, v117\ns_waitcnt lgkmcnt(8)\nv_mfma_f32_32x32x16_bf16 v[64:79], v[48:51], v[148:151], v[64:79]\n"
    "ds_read_b64_tr_b16 v[48:49], v116 offset:34816\nds_read_b64_tr_b16 v[50:51], v116 offset:35072\ns_waitcnt lgkmcnt(8)\n"
    "v_mfma_f32_32x32x16_bf16 v[64:79], v[52:55], v[152:155], v[64:79]\nds_read_b64_tr_b16 v[52:53], v116 offset:35840\nds_read_b64_tr_b16 v[54:55], v116 offset:36096\n"
    "s_waitcnt lgkmcnt(8)\nv_mfma_f32_32x32x16_bf16 v[64:79], v[56:59], v[156:159], v[64:79]\nds_read_b64_tr_b16 v[56:57], v118 offset:0\n"
    "ds_read_b64_tr_b16 v[58:59], v118 offset:256\ns_waitcnt lgkmcnt(8)\nv_mfma_f32_32x32x16_bf16 v[80:95], v[60:63], v[144:147], 0\n"
    "ds_read_b64_tr_b16 v[60:61], v118 offset:1024\nds_read_b64_tr_b16 v[62:63], v118 offset:1280\ns_waitcnt lgkmcnt(8)\n"
    "v_mfma_f32_32x32x16_bf16 v[80:95], v[44:47], v[148:151], v[80:95]\nds_read_b64_tr_b16 v[44:45], v118 offset:2048\nds_read_b64_tr_b16 v[46:47], v118 offset:2304\n"
    "s_waitcnt lgkmcnt(8)\nv_mfma_f32_32x32x16_bf16 v[80:95], v[48:51], v[152:155], v[80:95]\nds_read_b64_tr_b16 v[48:49], v118 offset:3072\n"
    "ds_read_b64_tr_b16 v[50:51], v118 offset:3328\ns_barrier\ns_waitcnt lgkmcnt(8)\nv_mfma_f32_32x32x16_bf16 v[80:95], v[52:55], v[156:159], v[80:95]\n"
    "ds_read_b128 v[160:163], v29 offset:4096\nds_read_b64_tr_b16 v[52:53], v117 offset:0\nds_read_b64_tr_b16 v[54:55], v117 offset:256\ns_waitcnt lgkmcnt(9)\n"
    "v_mfma_f32_32x32x16_bf16 v[96:111], v[56:59], v[144:147], 0\nds_read_b128 v[164:167], v176 offset:4096\nds_read_b64_tr_b16 v[56:57], v117 offset:1024\n"
    "ds_read_b64_tr_b16 v[58:59], v117 offset:1280\ns_waitcnt lgkmcnt(10)\nv_mfma_f32_32x32x16_bf16 v[96:111], v[60:63], v[148:151], v[96:111]\n"
    "ds_read_b128 v[168:171], v177 offset:4096\nds_read_b64_tr_b16 v[60:61], v117 offset:2048\nds_read_b64_tr_b16 v[62:63], v117 offset:2304\ns_waitcnt lgkmcnt(11)\n"
    "v_mfma_f32_32x32x16_bf16 v[96:111], v[44:47], v[152:155], v[96:111]\nds_read_b128 v[172:175], v178 offset:4096\nds_read_b64_tr_b16 v[44:45], v117 offset:3072\n"
    "ds_read_b64_tr_b16 v[46:47], v117 offset:3328\ns_waitcnt lgkmcnt(12)\nv_mfma_f32_32x32x16_bf16 v[96:111], v[48:51], v[156:159], v[96:111]\n"
    "ds_read_b64_tr_b16 v[48:49], v117 offset:32768\nds_read_b64_tr_b16 v[50:51], v117 offset:33024\ns_waitcnt lgkmcnt(13)\nv_dot2c_f32_bf16 v112, v143, v160\n"
    "v_dot2c_f32_bf16 v113, v160, v160\nv_dot2c_f32_bf16 v114, v143, v161\nv_dot2c_f32_bf16 v115, v161, v161\nv_dot2c_f32_bf16 v112, v143, v162\n"
    "v_dot2c_f32_bf16 v113, v162, v162\nv_dot2c_f32_bf16 v114, v143, v163\nv_dot2c_f32_bf16 v115, v163, v163\ns_waitcnt lgkmcnt(10)\nv_dot2c_f32_bf16 v112, v143, v164\n"
    "v_dot2c_f32_bf16 v113, v164, v164\nv_dot2c_f32_bf16 v114, v143, v165\nv_dot2c_f32_bf16 v115, v165, v165\nv_dot2c_f32_bf16 v112, v143, v166\n"
    "v_dot2c_f32_bf16 v113, v166, v166\nv_dot2c_f32_bf16 v114, v143, v167\nv_dot2c_f32_bf16 v115, v167, v167\ns_waitcnt lgkmcnt(7)\nv_dot2c_f32_bf16 v112, v143, v168\n"
    "v_dot2c_f32_bf16 v113, v168, v168\nv_dot2c_f32_bf16 v114, v143, v169\nv_dot2c_f32_bf16 v115, v169, v169\nv_dot2c_f32_bf16 v112, v143, v170\n"
    "v_dot2c_f32_bf16 v113, v170, v170\nv_dot2c_f32_bf16 v114, v143, v171\nv_dot2c_f32_bf16 v115, v171, v171\ns_waitcnt lgkmcnt(4)\nv_dot2c_f32_bf16 v112, v143, v172\n"
    "v_dot2c_f32_bf16 v113, v172, v172\nv_dot2c_f32_bf16 v114, v143, v173\nv_dot2c_f32_bf16 v115, v173, v173\nv_dot2c_f32_bf16 v112, v143, v174\n"
    "v_dot2c_f32_bf16 v113, v174, v174\nv_dot2c_f32_bf16 v114, v143, v175\nv_dot2c_f32_bf16 v115, v175, v175\nv_mfma_f32_32x32x16_bf16 v[64:79], v[52:55], v[160:163], v[64:79]\n"
    "ds_read_b64_tr_b16 v[52:53], v117 offset:33792\nds_read_b64_tr_b16 v[54:55], v117 offset:34048\nv_add_u32 v116, s46, v26\nv_add_u32 v118, 0x10000, v116\n"
    "v_mfma_f32_32x32x16_bf16 v[64:79], v[56:59], v[164:167], v[64:79]\nds_read_b64_tr_b16 v[56:57], v117 offset:34816\nds_read_b64_tr_b16 v[58:59], v117 offset:35072\n"
    "s_cmp_lt_u32 s3, 2\ns_cbranch_scc0 .LL72\ns_waitcnt vmcnt(0)\ns_mov_b64 exec, 1\nglobal_store_dword v30, v180, s[32:33] sc1\ns_mov_b64 exec, -1\n.LL72:\n"
    "v_mfma_f32_32x32x16_bf16 v[64:79], v[60:63], v[168:171], v[64:79]\nds_read_b64_tr_b16 v[60:61], v117 offset:35840\nds_read_b64_tr_b16 v[62:63], v117 offset:36096\n"
    "s_waitcnt lgkmcnt(8)\nv_mfma_f32_32x32x16_bf16 v[64:79], v[44:47], v[172:175], v[64:79]\nds_read_b64_tr_b16 v[44:45], v119 offset:0\n"
    "ds_read_b64_tr_b16 v[46:47], v119 offset:256\ns_waitcnt lgkmcnt(8)\nv_mfma_f32_32x32x16_bf16 v[80:95], v[48:51], v[160:163], v[80:95]\n"
    "ds_read_b64_tr_b16 v[48:49], v119 offset:1024\nds_read_b64_tr_b16 v[50:51], v119 offset:1280\ns_waitcnt lgkmcnt(8)\n"
    "v_mfma_f32_32x32x16_bf16 v[80:95], v[52:55], v[164:167], v[80:95]\nds_read_b64_tr_b16 v[52:53], v119 offset:2048\nds_read_b64_tr_b16 v[54:55], v119 offset:2304\n"
    "s_waitcnt lgkmcnt(8)\nv_mfma_f32_32x32x16_bf16 v[80:95], v[56:59], v[168:171], v[80:95]\nds_read_b64_tr_b16 v[56:57], v119 offset:3072\n"
    "ds_read_b64_tr_b16 v[58:59], v119 offset:3328\ns_barrier\ns_waitcnt lgkmcnt(8)\nv_mfma_f32_32x32x16_bf16 v[80:95], v[60:63], v[172:175], v[80:95]\n"
    "ds_read_b128 v[144:147], v29 offset:8192\nds_read_b64_tr_b16 v[60:61], v116 offset:0\nds_read_b64_tr_b16 v[62:63], v116 offset:256\ns_waitcnt lgkmcnt(9)\n"
    "v_mfma_f32_32x32x16_bf16 v[96:111], v[44:47], v[160:163], v[96:111]\nds_read_b128 v[148:151], v176 offset:8192\nds_read_b64_tr_b16 v[44:45], v116 offset:1024\n"
    "ds_read_b64_tr_b16 v[46:47], v116 offset:1280\ns_waitcnt lgkmcnt(10)\nv_mfma_f32_32x32x16_bf16 v[96:111], v[48:51], v[164:167], v[96:111]\n"
    "ds_read_b128 v[152:155], v177 offset:8192\nds_read_b64_tr_b16 v[48:49], v116 offset:2048\nds_read_b64_tr_b16 v[50:51], v116 offset:2304\ns_waitcnt lgkmcnt(11)\n"
    "v_mfma_f32_32x32x16_bf16 v[96:111], v[52:55], v[168:171], v[96:111]\nds_read_b128 v[156:159], v178 offset:8192\nds_read_b64_tr_b16 v[52:53], v116 offset:3072\n"
    "ds_read_b64_tr_b16 v[54:55], v116 offset:3328\ns_waitcnt lgkmcnt(12)\nv_mfma_f32_32x32x16_bf16 v[96:111], v[56:59], v[172:175], v[96:111]\n"
    "ds_read_b64_tr_b16 v[56:57], v116 offset:32768\nds_read_b64_tr_b16 v[58:59], v116 offset:33024\ns_waitcnt lgkmcnt(13)\nv_dot2c_f32_bf16 v112, v143, v144\n"
    "v_dot2c_f32_bf16 v113, v144, v144\nv_dot2c_f32_bf16 v114, v143, v145\nv_dot2c_f32_bf16 v115, v145, v145\nv_dot2c_f32_bf16 v112, v143, v146\n"
    "v_dot2c_f32_bf16 v113, v146, v146\nv_dot2c_f32_bf16 v114, v143, v147\nv_dot2c_f32_bf16 v115, v147, v147\ns_waitcnt lgkmcnt(10)\nv_dot2c_f32_bf16 v112, v143, v148\n"
    "v_dot2c_f32_bf16 v113, v148, v148\nv_dot2c_f32_bf16 v114, v143, v149\nv_dot2c_f32_bf16 v115, v149, v149\nv_dot2c_f32_bf16 v112, v143, v150\n"
    "v_dot2c_f32_bf16 v113, v150, v150\nv_dot2c_f32_bf16 v114, v143, v151\nv_dot2c_f32_bf16 v115, v151, v151\ns_waitcnt lgkmcnt(7)\nv_dot2c_f32_bf16 v112, v143, v152\n"
    "v_dot2c_f32_bf16 v113, v152, v152\nv_dot2c_f32_bf16 v114, v143, v153\nv_dot2c_f32_bf16 v115, v153, v153\nv_dot2c_f32_bf16 v112, v143, v154\n"
    "v_dot2c_f32_bf16 v113, v154, v154\nv_dot2c_f32_bf16 v114, v143, v155\nv_dot2c_f32_bf16 v115, v155, v155\ns_waitcnt lgkmcnt(4)\nv_dot2c_f32_bf16 v112, v143, v156\n"
    "v_dot2c_f32_bf16 v113, v156, v156\nv_dot2c_f32_bf16 v114, v143, v157\nv_dot2c_f32_bf16 v115, v157, v157\nv_dot2c_f32_bf16 v112, v143, v158\n"
    "v_dot2c_f32_bf16 v113, v158, v158\nv_dot2c_f32_bf16 v114, v143, v159\nv_dot2c_f32_bf16 v115, v159, v159\nv_mfma_f32_32x32x16_bf16 v[64:79], v[60:63], v[144:147], v[64:79]\n"
    "ds_read_b64_tr_b16 v[60:61], v116 offset:33792\nds_read_b64_tr_b16 v[62:63], v116 offset:34048\nv_add_u32 v117, s47, v26\nv_add_u32 v119, 0x10000, v117\n"
    "v_mfma_f32_32x32x16_bf16 v[64:79], v[44:47], v[148:151], v[64:79]\nds_read_b64_tr_b16 v[44:45], v116 offset:34816\nds_read_b64_tr_b16 v[46:47], v116 offset:35072\n"
    "v_mfma_f32_32x32x16_bf16 v[64:79], v[48:51], v[152:155], v[64:79]\nds_read_b64_tr_b16 v[48:49], v116 offset:35840\nds_read_b64_tr_b16 v[50:51], v116 offset:36096\n"
    "s_waitcnt lgkmcnt(8)\nv_mfma_f32_32x32x16_bf16 v[64:79], v[52:55], v[156:159], v[64:79]\nds_read_b64_tr_b16 v[52:53], v118 offset:0\n"
    "ds_read_b64_tr_b16 v[54:55], v118 offset:256\ns_waitcnt lgkmcnt(8)\nv_mfma_f32_32x32x16_bf16 v[80:95], v[56:59], v[144:147], v[80:95]\n"
    "ds_read_b64_tr_b16 v[56:57], v118 offset:1024\nds_read_b64_tr_b16 v[58:59], v118 offset:1280\ns_waitcnt lgkmcnt(8)\n"
    "v_mfma_f32_32x32x16_bf16 v[80:95], v[60:63], v[148:151], v[80:95]\nds_read_b64_tr_b16 v[60:61], v118 offset:2048\nds_read_b64_tr_b16 v[62:63], v118 offset:2304\n"
    "s_waitcnt lgkmcnt(8)\nv_mfma_f32_32x32x16_bf16 v[80:95], v[44:47], v[152:155], v[80:95]\nds_read_b64_tr_b16 v[44:45], v118 offset:3072\n"
    "ds_read_b64_tr_b16 v[46:47], v118 offset:3328\ns_barrier\ns_waitcnt lgkmcnt(8)\nv_mfma_f32_32x32x16_bf16 v[80:95], v[48:51], v[156:159], v[80:95]\n"
    "ds_read_b128 v[160:163], v29 offset:0\nds_read_b64_tr_b16 v[48:49], v117 offset:0\nds_read_b64_tr_b16 v[50:51], v117 offset:256\ns_waitcnt lgkmcnt(9)\n"
    "v_mfma_f32_32x32x16_bf16 v[96:111], v[52:55], v[144:147], v[96:111]\nds_read_b128 v[164:167], v176 offset:0\nds_read_b64_tr_b16 v[52:53], v117 offset:1024\n"
    "ds_read_b64_tr_b16 v[54:55], v117 offset:1280\ns_waitcnt lgkmcnt(10)\nv_mfma_f32_32x32x16_bf16 v[96:111], v[56:59], v[148:151], v[96:111]\n"
    "ds_read_b128 v[168:171], v177 offset:0\nds_read_b64_tr_b16 v[56:57], v117 offset:2048\nds_read_b64_tr_b16 v[58:59], v117 offset:2304\ns_waitcnt lgkmcnt(11)\n"
    "v_mfma_f32_32x32x16_bf16 v[96:111], v[60:63], v[152:155], v[96:111]\nds_read_b128 v[172:175], v178 offset:0\nds_read_b64_tr_b16 v[60:61], v117 offset:3072\n"
    "ds_read_b64_tr_b16 v[62:63], v117 offset:3328\ns_waitcnt lgkmcnt(12)\nv_mfma_f32_32x32x16_bf16 v[96:111], v[44:47], v[156:159], v[96:111]\n"
    "ds_read_b64_tr_b16 v[44:45], v117 offset:32768\nds_read_b64_tr_b16 v[46:47], v117 offset:33024\ns_waitcnt lgkmcnt(13)\nv_dot2c_f32_bf16 v112, v143, v160\n"
    "v_dot2c_f32_bf16 v113, v160, v160\nv_dot2c_f32_bf16 v114, v143, v161\nv_dot2c_f32_bf16 v115, v161, v161\nv_dot2c_f32_bf16 v112, v143, v162\n"
    "v_dot2c_f32_bf16 v113, v162, v162\nv_dot2c_f32_bf16 v114, v143, v163\nv_dot2c_f32_bf16 v115, v163, v163\ns_waitcnt lgkmcnt(10)\nv_dot2c_f32_bf16 v112, v143, v164\n"
    "v_dot2c_f32_bf16 v113, v164, v164\nv_dot2c_f32_bf16 v114, v143, v165\nv_dot2c_f32_bf16 v115, v165, v165\nv_dot2c_f32_bf16 v112, v143, v166\n"
    "v_dot2c_f32_bf16 v113, v166, v166\nv_dot2c_f32_bf16 v114, v143, v167\nv_dot2c_f32_bf16 v115, v167, v167\ns_waitcnt lgkmcnt(7)\nv_dot2c_f32_bf16 v112, v143, v168\n"
    "v_dot2c_f32_bf16 v113, v168, v168\nv_dot2c_f32_bf16 v114, v143, v169\nv_dot2c_f32_bf16 v115, v169, v169\nv_dot2c_f32_bf16 v112, v143, v170\n"
    "v_dot2c_f32_bf16 v113, v170, v170\nv_dot2c_f32_bf16 v114, v143, v171\nv_dot2c_f32_bf16 v115, v171, v171\ns_waitcnt lgkmcnt(4)\nv_dot2c_f32_bf16 v112, v143, v172\n"
    "v_dot2c_f32_bf16 v113, v172, v172\nv_dot2c_f32_bf16 v114, v143, v173\nv_dot2c_f32_bf16 v115, v173, v173\nv_dot2c_f32_bf16 v112, v143, v174\n"
    "v_dot2c_f32_bf16 v113, v174, v174\nv_dot2c_f32_bf16 v114, v143, v175\nv_dot2c_f32_bf16 v115, v175, v175\nv_mfma_f32_32x32x16_bf16 v[64:79], v[48:51], v[160:163], v[64:79]\n"
    "ds_read_b64_tr_b16 v[48:49], v117 offset:33792\nds_read_b64_tr_b16 v[50:51], v117 offset:34048\nv_add_u32 v116, s48, v26\nv_add_u32 v118, 0x10000, v116\n"
    "v_mfma_f32_32x32x16_bf16 v[64:79], v[52:55], v[164:167], v[64:79]\nds_read_b64_tr_b16 v[52:53], v117 offset:34816\nds_read_b64_tr_b16 v[54:55], v117 offset:35072\n"
    "v_mfma_f32_32x32x16_bf16 v[64:79], v[56:59], v[168:171], v[64:79]\nds_read_b64_tr_b16 v[56:57], v117 offset:35840\nds_read_b64_tr_b16 v[58:59], v117 offset:36096\n"
    "s_waitcnt lgkmcnt(8)\nv_mfma_f32_32x32x16_bf16 v[64:79], v[60:63], v[172:175], v[64:79]\nds_read_b64_tr_b16 v[60:61], v119 offset:0\n"
    "ds_read_b64_tr_b16 v[62:63], v119 offset:256\ns_waitcnt lgkmcnt(8)\nv_mfma_f32_32x32x16_bf16 v[80:95], v[44:47], v[160:163], v[80:95]\n"
    "ds_read_b64_tr_b16 v[44:45], v119 offset:1024\nds_read_b64_tr_b16 v[46:47], v119 offset:1280\ns_waitcnt lgkmcnt(8)\n"
    "v_mfma_f32_32x32x16_bf16 v[80:95], v[48:51], v[164:167], v[80:95]\nds_read_b64_tr_b16 v[48:49], v119 offset:2048\nds_read_b64_tr_b16 v[50:51], v119 offset:2304\n"
    "s_waitcnt lgkmcnt(8)\nv_mfma_f32_32x32x16_bf16 v[80:95], v[52:55], v[168:171], v[80:95]\nds_read_b64_tr_b16 v[52:53], v119 offset:3072\n"
    "ds_read_b64_tr_b16 v[54:55], v119 offset:3328\ns_barrier\ns_waitcnt lgkmcnt(8)\nv_mfma_f32_32x32x16_bf16 v[80:95], v[56:59], v[172:175], v[80:95]\n"
    "ds_read_b128 v[144:147], v29 offset:4096\nds_read_b64_tr_b16 v[56:57], v116 offset:0\nds_read_b64_tr_b16 v[58:59], v116 offset:256\ns_waitcnt lgkmcnt(9)\n"
    "v_mfma_f32_32x32x16_bf16 v[96:111], v[60:63], v[160:163], v[96:111]\nds_read_b128 v[148:151], v176 offset:4096\nds_read_b64_tr_b16 v[60:61], v116 offset:1024\n"
    "ds_read_b64_tr_b16 v[62:63], v116 offset:1280\ns_waitcnt lgkmcnt(10)\nv_mfma_f32_32x32x16_bf16 v[96:111], v[44:47], v[164:167], v[96:111]\n"
    "ds_read_b128 v[152:155], v177 offset:4096\nds_read_b64_tr_b16 v[44:45], v116 offset:2048\nds_read_b64_tr_b16 v[46:47], v116 offset:2304\ns_waitcnt lgkmcnt(11)\n"
    "v_mfma_f32_32x32x16_bf16 v[96:111], v[48:51], v[168:171], v[96:111]\nds_read_b128 v[156:159], v178 offset:4096\nds_read_b64_tr_b16 v[48:49], v116 offset:3072\n"
    "ds_read_b64_tr_b16 v[50:51], v116 offset:3328\ns_waitcnt lgkmcnt(12)\nv_mfma_f32_32x32x16_bf16 v[96:111], v[52:55], v[172:175], v[96:111]\n"
    "ds_read_b64_tr_b16 v[52:53], v116 offset:32768\nds_read_b64_tr_b16 v[54:55], v116 offset:33024\ns_waitcnt lgkmcnt(13)\nv_dot2c_f32_bf16 v112, v143, v144\n"
    "v_dot2c_f32_bf16 v113, v144, v144\nv_dot2c_f32_bf16 v114, v143, v145\nv_dot2c_f32_bf16 v115, v145, v145\nv_dot2c_f32_bf16 v112, v143, v146\n"
    "v_dot2c_f32_bf16 v113, v146, v146\nv_dot2c_f32_bf16 v114, v143, v147\nv_dot2c_f32_bf16 v115, v147, v147\ns_waitcnt lgkmcnt(10)\nv_dot2c_f32_bf16 v112, v143, v148\n"
    "v_dot2c_f32_bf16 v113, v148, v148\nv_dot2c_f32_bf16 v114, v143, v149\nv_dot2c_f32_bf16 v115, v149, v149\nv_dot2c_f32_bf16 v112, v143, v150\n"
    "v_dot2c_f32_bf16 v113, v150, v150\nv_dot2c_f32_bf16 v114, v143, v151\nv_dot2c_f32_bf16 v115, v151, v151\ns_waitcnt lgkmcnt(7)\nv_dot2c_f32_bf16 v112, v143, v152\n"
    "v_dot2c_f32_bf16 v113, v152, v152\nv_dot2c_f32_bf16 v114, v143, v153\nv_dot2c_f32_bf16 v115, v153, v153\nv_dot2c_f32_bf16 v112, v143, v154\n"
    "v_dot2c_f32_bf16 v113, v154, v154\nv_dot2c_f32_bf16 v114, v143, v155\nv_dot2c_f32_bf16 v115, v155, v155\ns_waitcnt lgkmcnt(4)\nv_dot2c_f32_bf16 v112, v143, v156\n"
    "v_dot2c_f32_bf16 v113, v156, v156\nv_dot2c_f32_bf16 v114, v143, v157\nv_dot2c_f32_bf16 v115, v157, v157\nv_dot2c_f32_bf16 v112, v143, v158\n"
    "v_dot2c_f32_bf16 v113, v158, v158\nv_dot2c_f32_bf16 v114, v143, v159\nv_dot2c_f32_bf16 v115, v159, v159\nv_mfma_f32_32x32x16_bf16 v[64:79], v[56:59], v[144:147], v[64:79]\n"
    "ds_read_b64_tr_b16 v[56:57], v116 offset:33792\nds_read_b64_tr_b16 v[58:59], v116 offset:34048\nv_add_u32 v117, s49, v26\nv_add_u32 v119, 0x10000, v117\n"
    "v_mfma_f32_32x32x16_bf16 v[64:79], v[60:63], v[148:151], v[64:79]\nds_read_b64_tr_b16 v[60:61], v116 offset:34816\nds_read_b64_tr_b16 v[62:63], v116 offset:35072\n"
    "v_mfma_f32_32x32x16_bf16 v[64:79], v[44:47], v[152:155], v[64:79]\nds_read_b64_tr_b16 v[44:45], v116 offset:35840\nds_read_b64_tr_b16 v[46:47], v116 offset:36096\n"
    "s_waitcnt lgkmcnt(8)\nv_mfma_f32_32x32x16_bf16 v[64:79], v[48:51], v[156:159], v[64:79]\nds_read_b64_tr_b16 v[48:49], v118 offset:0\n"
    "ds_read_b64_tr_b16 v[50:51], v118 offset:256\ns_waitcnt lgkmcnt(8)\nv_mfma_f32_32x32x16_bf16 v[80:95], v[52:55], v[144:147], v[80:95]\n"
    "ds_read_b64_tr_b16 v[52:53], v118 offset:1024\nds_read_b64_tr_b16 v[54:55], v118 offset:1280\ns_waitcnt lgkmcnt(8)\n"
    "v_mfma_f32_32x32x16_bf16 v[80:95], v[56:59], v[148:151], v[80:95]\nds_read_b64_tr_b16 v[56:57], v118 offset:2048\nds_read_b64_tr_b16 v[58:59], v118 offset:2304\n"
    "s_waitcnt lgkmcnt(8)\nv_mfma_f32_32x32x16_bf16 v[80:95], v[60:63], v[152:155], v[80:95]\nds_read_b64_tr_b16 v[60:61], v118 offset:3072\n"
    "ds_read_b64_tr_b16 v[62:63], v118 offset:3328\ns_barrier\ns_waitcnt lgkmcnt(8)\nv_mfma_f32_32x32x16_bf16 v[80:95], v[44:47], v[156:159], v[80:95]\n"
    "ds_read_b128 v[160:163], v29 offset:8192\nds_read_b64_tr_b16 v[44:45], v117 offset:0\nds_read_b64_tr_b16 v[46:47], v117 offset:256\ns_waitcnt lgkmcnt(9)\n"
    "v_mfma_f32_32x32x16_bf16 v[96:111], v[48:51], v[144:147], v[96:111]\nds_read_b128 v[164:167], v176 offset:8192\nds_read_b64_tr_b16 v[48:49], v117 offset:1024\n"
    "ds_read_b64_tr_b16 v[50:51], v117 offset:1280\ns_waitcnt lgkmcnt(10)\nv_mfma_f32_32x32x16_bf16 v[96:111], v[52:55], v[148:151], v[96:111]\n"
    "ds_read_b128 v[168:171], v177 offset:8192\nds_read_b64_tr_b16 v[52:53], v117 offset:2048\nds_read_b64_tr_b16 v[54:55], v117 offset:2304\ns_waitcnt lgkmcnt(11)\n"
    "v_mfma_f32_32x32x16_bf16 v[96:111], v[56:59], v[152:155], v[96:111]\nds_read_b128 v[172:175], v178 offset:8192\nds_read_b64_tr_b16 v[56:57], v117 offset:3072\n"
    "ds_read_b64_tr_b16 v[58:59], v117 offset:3328\ns_waitcnt lgkmcnt(12)\nv_mfma_f32_32x32x16_bf16 v[96:111], v[60:63], v[156:159], v[96:111]\n"
    "ds_read_b64_tr_b16 v[60:61], v117 offset:32768\nds_read_b64_tr_b16 v[62:63], v117 offset:33024\ns_waitcnt lgkmcnt(13)\nv_dot2c_f32_bf16 v112, v143, v160\n"
    "v_dot2c_f32_bf16 v113, v160, v160\nv_dot2c_f32_bf16 v114, v143, v161\nv_dot2c_f32_bf16 v115, v161, v161\nv_dot2c_f32_bf16 v112, v143, v162\n"
    "v_dot2c_f32_bf16 v113, v162, v162\nv_dot2c_f32_bf16 v114, v143, v163\nv_dot2c_f32_bf16 v115, v163, v163\ns_waitcnt lgkmcnt(10)\nv_dot2c_f32_bf16 v112, v143, v164\n"
    "v_dot2c_f32_bf16 v113, v164, v164\nv_dot2c_f32_bf16 v114, v143, v165\nv_dot2c_f32_bf16 v115, v165, v165\nv_dot2c_f32_bf16 v112, v143, v166\n"
    "v_dot2c_f32_bf16 v113, v166, v166\nv_dot2c_f32_bf16 v114, v143, v167\nv_dot2c_f32_bf16 v115, v167, v167\ns_waitcnt lgkmcnt(7)\nv_dot2c_f32_bf16 v112, v143, v168\n"
    "v_dot2c_f32_bf16 v113, v168, v168\nv_dot2c_f32_bf16 v114, v143, v169\nv_dot2c_f32_bf16 v115, v169, v169\nv_dot2c_f32_bf16 v112, v143, v170\n"
    "v_dot2c_f32_bf16 v113, v170, v170\nv_dot2c_f32_bf16 v114, v143, v171\nv_dot2c_f32_bf16 v115, v171, v171\ns_waitcnt lgkmcnt(4)\nv_dot2c_f32_bf16 v112, v143, v172\n"
    "v_dot2c_f32_bf16 v113, v172, v172\nv_dot2c_f32_bf16 v114, v143, v173\nv_dot2c_f32_bf16 v115, v173, v173\nv_dot2c_f32_bf16 v112, v143, v174\n"
    "v_dot2c_f32_bf16 v113, v174, v174\nv_dot2c_f32_bf16 v114, v143, v175\nv_dot2c_f32_bf16 v115, v175, v175\nv_mfma_f32_32x32x16_bf16 v[64:79], v[44:47], v[160:163], v[64:79]\n"
    "ds_read_b64_tr_b16 v[44:45], v117 offset:33792\nds_read_b64_tr_b16 v[46:47], v117 offset:34048\nv_add_u32 v116, s50, v26\nv_add_u32 v118, 0x10000, v116\n"
    "v_mfma_f32_32x32x16_bf16 v[64:79], v[48:51], v[164:167], v[64:79]\nds_read_b64_tr_b16 v[48:49], v117 offset:34816\nds_read_b64_tr_b16 v[50:51], v117 offset:35072\n"
    "v_mfma_f32_32x32x16_bf16 v[64:79], v[52:55], v[168:171], v[64:79]\nds_read_b64_tr_b16 v[52:53], v117 offset:35840\nds_read_b64_tr_b16 v[54:55], v117 offset:36096\n"
    "s_waitcnt lgkmcnt(8)\nv_mfma_f32_32x32x16_bf16 v[64:79], v[56:59], v[172:175], v[64:79]\nds_read_b64_tr_b16 v[56:57], v119 offset:0\n"
    "ds_read_b64_tr_b16 v[58:59], v119 offset:256\ns_waitcnt lgkmcnt(8)\nv_mfma_f32_32x32x16_bf16 v[80:95], v[60:63], v[160:163], v[80:95]\n"
    "ds_read_b64_tr_b16 v[60:61], v119 offset:1024\nds_read_b64_tr_b16 v[62:63], v119 offset:1280\ns_waitcnt lgkmcnt(8)\n"
    "v_mfma_f32_32x32x16_bf16 v[80:95], v[44:47], v[164:167], v[80:95]\nds_read_b64_tr_b16 v[44:45], v119 offset:2048\nds_read_b64_tr_b16 v[46:47], v119 offset:2304\n"
    "s_waitcnt lgkmcnt(8)\nv_mfma_f32_32x32x16_bf16 v[80:95], v[48:51], v[168:171], v[80:95]\nds_read_b64_tr_b16 v[48:49], v119 offset:3072\n"
    "ds_read_b64_tr_b16 v[50:51], v119 offset:3328\ns_barrier\ns_waitcnt lgkmcnt(8)\nv_mfma_f32_32x32x16_bf16 v[80:95], v[52:55], v[172:175], v[80:95]\n"
    "ds_read_b128 v[144:147], v29 offset:0\nds_read_b64_tr_b16 v[52:53], v116 offset:0\nds_read_b64_tr_b16 v[54:55], v116 offset:256\ns_waitcnt lgkmcnt(9)\n"
    "v_mfma_f32_32x32x16_bf16 v[96:111], v[56:59], v[160:163], v[96:111]\nds_read_b128 v[148:151], v176 offset:0\nds_read_b64_tr_b16 v[56:57], v116 offset:1024\n"
    "ds_read_b64_tr_b16 v[58:59], v116 offset:1280\ns_waitcnt lgkmcnt(10)\nv_mfma_f32_32x32x16_bf16 v[96:111], v[60:63], v[164:167], v[96:111]\n"
    "ds_read_b128 v[152:155], v177 offset:0\nds_read_b64_tr_b16 v[60:61], v116 offset:2048\nds_read_b64_tr_b16 v[62:63], v116 offset:2304\ns_waitcnt lgkmcnt(11)\n"
    "v_mfma_f32_32x32x16_bf16 v[96:111], v[44:47], v[168:171], v[96:111]\nds_read_b128 v[156:159], v178 offset:0\nds_read_b64_tr_b16 v[44:45], v116 offset:3072\n"
    "ds_read_b64_tr_b16 v[46:47], v116 offset:3328\ns_waitcnt lgkmcnt(12)\nv_mfma_f32_32x32x16_bf16 v[96:111], v[48:51], v[172:175], v[96:111]\n"
    "ds_read_b64_tr_b16 v[48:49], v116 offset:32768\nds_read_b64_tr_b16 v[50:51], v116 offset:33024\ns_waitcnt lgkmcnt(13)\nv_dot2c_f32_bf16 v112, v143, v144\n"
    "v_dot2c_f32_bf16 v113, v144, v144\nv_dot2c_f32_bf16 v114, v143, v145\nv_dot2c_f32_bf16 v115, v145, v145\nv_dot2c_f32_bf16 v112, v143, v146\n"
    "v_dot2c_f32_bf16 v113, v146, v146\nv_dot2c_f32_bf16 v114, v143, v147\nv_dot2c_f32_bf16 v115, v147, v147\ns_waitcnt lgkmcnt(10)\nv_dot2c_f32_bf16 v112, v143, v148\n"
    "v_dot2c_f32_bf16 v113, v148, v148\nv_dot2c_f32_bf16 v114, v143, v149\nv_dot2c_f32_bf16 v115, v149, v149\nv_dot2c_f32_bf16 v112, v143, v150\n"
    "v_dot2c_f32_bf16 v113, v150, v150\nv_dot2c_f32_bf16 v114, v143, v151\nv_dot2c_f32_bf16 v115, v151, v151\ns_waitcnt lgkmcnt(7)\nv_dot2c_f32_bf16 v112, v143, v152\n"
    "v_dot2c_f32_bf16 v113, v152, v152\nv_dot2c_f32_bf16 v114, v143, v153\nv_dot2c_f32_bf16 v115, v153, v153\nv_dot2c_f32_bf16 v112, v143, v154\n"
    "v_dot2c_f32_bf16 v113, v154, v154\nv_dot2c_f32_bf16 v114, v143, v155\nv_dot2c_f32_bf16 v115, v155, v155\ns_waitcnt lgkmcnt(4)\nv_dot2c_f32_bf16 v112, v143, v156\n"
    "v_dot2c_f32_bf16 v113, v156, v156\nv_dot2c_f32_bf16 v114, v143, v157\nv_dot2c_f32_bf16 v115, v157, v157\nv_dot2c_f32_bf16 v112, v143, v158\n"
    "v_dot2c_f32_bf16 v113, v158, v158\nv_dot2c_f32_bf16 v114, v143, v159\nv_dot2c_f32_bf16 v115, v159, v159\nv_mfma_f32_32x32x16_bf16 v[64:79], v[52:55], v[144:147], v[64:79]\n"
    "ds_read_b64_tr_b16 v[52:53], v116 offset:33792\nds_read_b64_tr_b16 v[54:55], v116 offset:34048\nv_add_u32 v117, s51, v26\nv_add_u32 v119, 0x10000, v117\n"
    "v_mfma_f32_32x32x16_bf16 v[64:79], v[56:59], v[148:151], v[64:79]\nds_read_b64_tr_b16 v[56:57], v116 offset:34816\nds_read_b64_tr_b16 v[58:59], v116 offset:35072\n"
    "v_mfma_f32_32x32x16_bf16 v[64:79], v[60:63], v[152:155], v[64:79]\nds_read_b64_tr_b16 v[60:61], v116 offset:35840\nds_read_b64_tr_b16 v[62:63], v116 offset:36096\n"
    "s_waitcnt lgkmcnt(8)\nv_mfma_f32_32x32x16_bf16 v[64:79], v[44:47], v[156:159], v[64:79]\nds_read_b64_tr_b16 v[44:45], v118 offset:0\n"
    "ds_read_b64_tr_b16 v[46:47], v118 offset:256\ns_waitcnt lgkmcnt(8)\nv_mfma_f32_32x32x16_bf16 v[80:95], v[48:51], v[144:147], v[80:95]\n"
    "ds_read_b64_tr_b16 v[48:49], v118 offset:1024\nds_read_b64_tr_b16 v[50:51], v118 offset:1280\ns_waitcnt lgkmcnt(8)\n"
    "v_mfma_f32_32x32x16_bf16 v[80:95], v[52:55], v[148:151], v[80:95]\nds_read_b64_tr_b16 v[52:53], v118 offset:2048\nds_read_b64_tr_b16 v[54:55], v118 offset:2304\n"
    "s_waitcnt lgkmcnt(8)\nv_mfma_f32_32x32x16_bf16 v[80:95], v[56:59], v[152:155], v[80:95]\nds_read_b64_tr_b16 v[56:57], v118 offset:3072\n"
    "ds_read_b64_tr_b16 v[58:59], v118 offset:3328\ns_waitcnt lgkmcnt(8)\nv_mfma_f32_32x32x16_bf16 v[80:95], v[60:63], v[156:159], v[80:95]\ns_waitcnt lgkmcnt(6)\n"
    "v_mfma_f32_32x32x16_bf16 v[96:111], v[44:47], v[144:147], v[96:111]\ns_waitcnt lgkmcnt(4)\nv_mfma_f32_32x32x16_bf16 v[96:111], v[48:51], v[148:151], v[96:111]\n"
    "s_waitcnt lgkmcnt(2)\nv_mfma_f32_32x32x16_bf16 v[96:111], v[52:55], v[152:155], v[96:111]\ns_waitcnt lgkmcnt(0)\n"
    "v_mfma_f32_32x32x16_bf16 v[96:111], v[56:59], v[156:159], v[96:111]\ns_barrier\nds_read_b128 v[160:163], v29 offset:4096\nds_read_b128 v[164:167], v176 offset:4096\n"
    "ds_read_b128 v[168:171], v177 offset:4096\nds_read_b128 v[172:175], v178 offset:4096\nds_read_b64_tr_b16 v[60:61], v117 offset:0\n"
    "ds_read_b64_tr_b16 v[62:63], v117 offset:256\nds_read_b64_tr_b16 v[44:45], v117 offset:1024\nds_read_b64_tr_b16 v[46:47], v117 offset:1280\n"
    "ds_read_b64_tr_b16 v[48:49], v117 offset:2048\nds_read_b64_tr_b16 v[50:51], v117 offset:2304\nds_read_b64_tr_b16 v[52:53], v117 offset:3072\n"
    "ds_read_b64_tr_b16 v[54:55], v117 offset:3328\nds_read_b64_tr_b16 v[56:57], v117 offset:32768\nds_read_b64_tr_b16 v[58:59], v117 offset:33024\ns_waitcnt lgkmcnt(13)\n"
    "v_dot2c_f32_bf16 v112, v143, v160\nv_dot2c_f32_bf16 v113, v160, v160\nv_dot2c_f32_bf16 v114, v143, v161\nv_dot2c_f32_bf16 v115, v161, v161\n"
    "v_dot2c_f32_bf16 v112, v143, v162\nv_dot2c_f32_bf16 v113, v162, v162\nv_dot2c_f32_bf16 v114, v143, v163\nv_dot2c_f32_bf16 v115, v163, v163\ns_waitcnt lgkmcnt(12)\n"
    "v_dot2c_f32_bf16 v112, v143, v164\nv_dot2c_f32_bf16 v113, v164, v164\nv_dot2c_f32_bf16 v114, v143, v165\nv_dot2c_f32_bf16 v115, v165, v165\n"
    "v_dot2c_f32_bf16 v112, v143, v166\nv_dot2c_f32_bf16 v113, v166, v166\nv_dot2c_f32_bf16 v114, v143, v167\nv_dot2c_f32_bf16 v115, v167, v167\ns_waitcnt lgkmcnt(11)\n"
    "v_dot2c_f32_bf16 v112, v143, v168\nv_dot2c_f32_bf16 v113, v168, v168\nv_dot2c_f32_bf16 v114, v143, v169\nv_dot2c_f32_bf16 v115, v169, v169\n"
    "v_dot2c_f32_bf16 v112, v143, v170\nv_dot2c_f32_bf16 v113, v170, v170\nv_dot2c_f32_bf16 v114, v143, v171\nv_dot2c_f32_bf16 v115, v171, v171\ns_waitcnt lgkmcnt(10)\n"
    "v_dot2c_f32_bf16 v112, v143, v172\nv_dot2c_f32_bf16 v113, v172, v172\nv_dot2c_f32_bf16 v114, v143, v173\nv_dot2c_f32_bf16 v115, v173, v173\n"
    "v_dot2c_f32_bf16 v112, v143, v174\nv_dot2c_f32_bf16 v113, v174, v174\nv_dot2c_f32_bf16 v114, v143, v175\nv_dot2c_f32_bf16 v115, v175, v175\ns_waitcnt lgkmcnt(8)\n"
    "v_mfma_f32_32x32x16_bf16 v[64:79], v[60:63], v[160:163], v[64:79]\nds_read_b64_tr_b16 v[60:61], v117 offset:33792\nds_read_b64_tr_b16 v[62:63], v117 offset:34048\n"
    "v_add_f32 v112, v112, v114\nv_add_f32 v113, v113, v115\ns_waitcnt lgkmcnt(8)\nv_mfma_f32_32x32x16_bf16 v[64:79], v[44:47], v[164:167], v[64:79]\n"
    "ds_read_b64_tr_b16 v[44:45], v117 offset:34816\nds_read_b64_tr_b16 v[46:47], v117 offset:35072\nv_permlane32_swap_b32 v112, v113\nv_add_f32 v138, v112, v113\n"
    "v_add_f32 v137, v112, v113\ns_waitcnt lgkmcnt(8)\nv_mfma_f32_32x32x16_bf16 v[64:79], v[48:51], v[168:171], v[64:79]\nds_read_b64_tr_b16 v[48:49], v117 offset:35840\n"
    "ds_read_b64_tr_b16 v[50:51], v117 offset:36096\nv_permlane32_swap_b32 v138, v137\nv_mul_f32 v138, s81, v138\nv_mul_f32 v112, v138, v138\ns_waitcnt lgkmcnt(8)\n"
    "v_mfma_f32_32x32x16_bf16 v[64:79], v[52:55], v[172:175], v[64:79]\nds_read_b64_tr_b16 v[52:53], v119 offset:0\nds_read_b64_tr_b16 v[54:55], v119 offset:256\n"
    "v_fma_f32 v137, v137, s81, -v112\nv_max_f32 v137, 0, v137\nv_cvt_pk_bf16_f32 v137, 0, v137\ns_waitcnt lgkmcnt(8)\n"
    "v_mfma_f32_32x32x16_bf16 v[80:95], v[56:59], v[160:163], v[80:95]\nds_read_b64_tr_b16 v[56:57], v119 offset:1024\nds_read_b64_tr_b16 v[58:59], v119 offset:1280\n"
    "v_add_f32 v137, 0x3727c5ac, v137\nv_cvt_pk_bf16_f32 v137, 0, v137\ns_waitcnt lgkmcnt(8)\nv_mfma_f32_32x32x16_bf16 v[80:95], v[60:63], v[164:167], v[80:95]\n"
    "ds_read_b64_tr_b16 v[60:61], v119 offset:2048\nds_read_b64_tr_b16 v[62:63], v119 offset:2304\nv_sqrt_f32 v137, v137\nv_cvt_pk_bf16_f32 v138, -v138, -v138\ns_barrier\n"
    "ds_read_b32 v124, v28 offset:0\nds_read_b32 v128, v28 offset:128\nds_read_b32 v132, v28 offset:256\ns_waitcnt lgkmcnt(11)\n"
    "v_mfma_f32_32x32x16_bf16 v[80:95], v[44:47], v[168:171], v[80:95]\nds_read_b64_tr_b16 v[44:45], v119 offset:3072\nds_read_b64_tr_b16 v[46:47], v119 offset:3328\n"
    "v_cndmask_b32 v120, 0, v138, s[84:85]\nv_cvt_pk_bf16_f32 v137, 0, v137\nv_rcp_f32 v137, v137\ns_waitcnt lgkmcnt(11)\n"
    "v_mfma_f32_32x32x16_bf16 v[80:95], v[48:51], v[172:175], v[80:95]\ns_waitcnt lgkmcnt(0)\nv_cvt_pk_bf16_f32 v140, 0, v124\nv_cvt_pk_bf16_f32 v141, 0, v128\n"
    "v_cvt_pk_bf16_f32 v142, 0, v132\nv_mfma_f32_32x32x16_bf16 v[96:111], v[52:55], v[160:163], v[96:111]\nv_sub_f32 v124, v124, v140\nv_sub_f32 v128, v128, v141\n"
    "v_sub_f32 v132, v132, v142\nv_cvt_pk_bf16_f32 v124, v140, v124\nv_cvt_pk_bf16_f32 v128, v141, v128\nv_cvt_pk_bf16_f32 v132, v142, v132\n"
    "v_mfma_f32_32x32x16_bf16 v[64:79], v[124:127], v[120:123], v[64:79]\nv_mfma_f32_32x32x16_bf16 v[80:95], v[128:131], v[120:123], v[80:95]\n"
    "v_mfma_f32_32x32x16_bf16 v[96:111], v[56:59], v[164:167], v[96:111]\ns_nop 9\nv_mul_f32 v64, v64, v137\nv_mul_f32 v65, v65, v137\nv_mul_f32 v66, v66, v137\n"
    "v_mul_f32 v67, v67, v137\nv_mul_f32 v68, v68, v137\nv_mul_f32 v69, v69, v137\nv_mfma_f32_32x32x16_bf16 v[96:111], v[60:63], v[168:171], v[96:111]\n"
    "v_mul_f32 v70, v70, v137\nv_mul_f32 v71, v71, v137\nv_cvt_pk_bf16_f32 v64, v64, v65\nv_cvt_pk_bf16_f32 v65, v66, v67\nv_cvt_pk_bf16_f32 v66, v68, v69\n"
    "v_cvt_pk_bf16_f32 v67, v70, v71\nds_write_b128 v27, v[64:67] offset:0\nv_mfma_f32_32x32x16_bf16 v[96:111], v[44:47], v[172:175], v[96:111]\nv_mul_f32 v72, v72, v137\n"
    "v_mul_f32 v73, v73, v137\nv_mul_f32 v74, v74, v137\nv_mul_f32 v75, v75, v137\nv_mul_f32 v76, v76, v137\nv_mul_f32 v77, v77, v137\n"
    "v_mfma_f32_32x32x16_bf16 v[96:111], v[132:135], v[120:123], v[96:111]\nv_mul_f32 v78, v78, v137\nv_mul_f32 v79, v79, v137\nv_cvt_pk_bf16_f32 v72, v72, v73\n"
    "v_cvt_pk_bf16_f32 v73, v74, v75\nv_cvt_pk_bf16_f32 v74, v76, v77\nv_cvt_pk_bf16_f32 v75, v78, v79\nds_write_b128 v27, v[72:75] offset:32\n"
    "v_pk_mul_f32 v[80:81], v[80:81], v[136:137] op_sel:[0,1]\nv_pk_mul_f32 v[82:83], v[82:83], v[136:137] op_sel:[0,1]\n"
    "v_pk_mul_f32 v[84:85], v[84:85], v[136:137] op_sel:[0,1]\nv_pk_mul_f32 v[86:87], v[86:87], v[136:137] op_sel:[0,1]\nv_cvt_pk_bf16_f32 v80, v80, v81\n"
    "v_cvt_pk_bf16_f32 v81, v82, v83\nv_cvt_pk_bf16_f32 v82, v84, v85\nv_cvt_pk_bf16_f32 v83, v86, v87\nds_write_b128 v27, v[80:83] offset:64\n"
    "v_pk_mul_f32 v[88:89], v[88:89], v[136:137] op_sel:[0,1]\nv_pk_mul_f32 v[90:91], v[90:91], v[136:137] op_sel:[0,1]\n"
    "v_pk_mul_f32 v[92:93], v[92:93], v[136:137] op_sel:[0,1]\nv_pk_mul_f32 v[94:95], v[94:95], v[136:137] op_sel:[0,1]\nv_cvt_pk_bf16_f32 v88, v88, v89\n"
    "v_cvt_pk_bf16_f32 v89, v90, v91\nv_cvt_pk_bf16_f32 v90, v92, v93\nv_cvt_pk_bf16_f32 v91, v94, v95\nds_write_b128 v27, v[88:91] offset:96\n"
    "v_pk_mul_f32 v[96:97], v[96:97], v[136:137] op_sel:[0,1]\nv_pk_mul_f32 v[98:99], v[98:99], v[136:137] op_sel:[0,1]\n"
    "v_pk_mul_f32 v[100:101], v[100:101], v[136:137] op_sel:[0,1]\nv_pk_mul_f32 v[102:103], v[102:103], v[136:137] op_sel:[0,1]\nv_cvt_pk_bf16_f32 v96, v96, v97\n"
    "v_cvt_pk_bf16_f32 v97, v98, v99\nv_cvt_pk_bf16_f32 v98, v100, v101\nv_cvt_pk_bf16_f32 v99, v102, v103\nds_write_b128 v27, v[96:99] offset:128\n"
    "v_pk_mul_f32 v[104:105], v[104:105], v[136:137] op_sel:[0,1]\nv_pk_mul_f32 v[106:107], v[106:107], v[136:137] op_sel:[0,1]\n"
    "v_pk_mul_f32 v[108:109], v[108:109], v[136:137] op_sel:[0,1]\nv_pk_mul_f32 v[110:111], v[110:111], v[136:137] op_sel:[0,1]\nv_cvt_pk_bf16_f32 v104, v104, v105\n"
    "v_cvt_pk_bf16_f32 v105, v106, v107\nv_cvt_pk_bf16_f32 v106, v108, v109\nv_cvt_pk_bf16_f32 v107, v110, v111\nds_write_b128 v27, v[104:107] offset:160\n.LGL75:\n"
    "s_waitcnt lgkmcnt(0)\ns_barrier\nds_read_b128 v[64:67], v6\nds_read_b128 v[68:71], v6 offset:208\ns_mov_b32 exec_lo, 0x00ff00ff\ns_mov_b32 exec_hi, 0xff00ff00\n"
    "ds_read_b128 v[10:13], v7\nds_read_b128 v[14:17], v7 offset:208\ns_mov_b64 exec, -1\nds_read_b64_tr_b16 v[72:73], v8 offset:0\nds_read_b64_tr_b16 v[74:75], v8 offset:32\n"
    "ds_read_b64_tr_b16 v[76:77], v8 offset:208\nds_read_b64_tr_b16 v[78:79], v8 offset:240\nv_mov_b32 v92, 0x3f803f80\nv_mov_b32 v93, 0x3f803f80\nv_mov_b32 v94, 0x3f803f80\n"
    "v_mov_b32 v95, 0x3f803f80\ns_mov_b32 s82, 0x00ff00ff\ns_mov_b32 s83, s82\ns_mov_b32 s81, 0x3eb8aa3b\ns_waitcnt lgkmcnt(5)\n"
    "v_mfma_f32_16x16x32_bf16 v[80:83], v[64:67], v[10:13], v[2:5]\ns_waitcnt lgkmcnt(4)\nv_mfma_f32_16x16x32_bf16 v[84:87], v[68:71], v[14:17], v[2:5]\ns_nop 5\n"
    "v_max_f32 v88, v80, v81\nv_max3_f32 v88, v88, v82, v83\nv_max_f32 v89, v84, v85\nv_max3_f32 v89, v89, v86, v87\ns_nop 1\nv_permlane16_swap_b32 v88, v89\n"
    "v_max_f32 v90, v88, v89\nv_max_f32 v89, v88, v89\ns_nop 1\nv_permlane32_swap_b32 v90, v89\nv_max_f32 v90, v90, v89\nv_mul_f32 v88, 0xbeb8aa3b, v90\n"
    "v_mul_f32 v89, 0xbeb8aa3b, v90\ns_nop 1\nv_permlane16_swap_b32 v88, v89\nv_pk_fma_f32 v[80:81], v[80:81], s[80:81], v[88:89] op_sel:[0,1,0] op_sel_hi:[1,1,0]\n"
    "v_pk_fma_f32 v[82:83], v[82:83], s[80:81], v[88:89] op_sel:[0,1,0] op_sel_hi:[1,1,0]\nv_pk_fma_f32 v[84:85], v[84:85], s[80:81], v[88:89] op_sel:[0,1,1] op_sel_hi:[1,1,1]\n"
    "v_pk_fma_f32 v[86:87], v[86:87], s[80:81], v[88:89] op_sel:[0,1,1] op_sel_hi:[1,1,1]\nv_exp_f32 v80, v80\nv_exp_f32 v81, v81\nv_exp_f32 v82, v82\nv_exp_f32 v83, v83\n"
    "v_exp_f32 v84, v84\nv_exp_f32 v85, v85\nv_exp_f32 v86, v86\nv_exp_f32 v87, v87\ns_mov_b64 exec, s[82:83]\nv_cvt_pk_bf16_f32 v18, v80, v81\nv_cvt_pk_bf16_f32 v19, v82, v83\n"
    "v_cvt_pk_bf16_f32 v22, v84, v85\nv_cvt_pk_bf16_f32 v23, v86, v87\ns_not_b64 exec, exec\nv_cvt_pk_bf16_f32 v20, v80, v81\nv_cvt_pk_bf16_f32 v21, v82, v83\n"
    "v_cvt_pk_bf16_f32 v24, v84, v85\nv_cvt_pk_bf16_f32 v25, v86, v87\ns_mov_b64 exec, -1\ns_waitcnt lgkmcnt(0)\nv_mfma_f32_16x16x32_bf16 v[80:83], v[92:95], v[18:21], 0\n"
    "v_mfma_f32_16x16x32_bf16 v[84:87], v[92:95], v[22:25], 0\nv_mfma_f32_16x16x32_bf16 v[64:67], v[72:75], v[18:21], 0\n"
    "v_mfma_f32_16x16x32_bf16 v[68:71], v[76:79], v[22:25], 0\ns_nop 4\nv_rcp_f32 v88, v80\nv_rcp_f32 v89, v84\ns_nop 0\n"
    "v_pk_mul_f32 v[64:65], v[64:65], v[88:89] op_sel:[0,0] op_sel_hi:[1,0]\nv_pk_mul_f32 v[66:67], v[66:67], v[88:89] op_sel:[0,0] op_sel_hi:[1,0]\n"
    "v_pk_mul_f32 v[68:69], v[68:69], v[88:89] op_sel:[0,1] op_sel_hi:[1,1]\nv_pk_mul_f32 v[70:71], v[70:71], v[88:89] op_sel:[0,1] op_sel_hi:[1,1]\n"
    "v_cvt_pk_bf16_f32 v80, v64, v65\nv_cvt_pk_bf16_f32 v82, v68, v69\nv_cvt_pk_bf16_f32 v81, v66, v67\nv_cvt_pk_bf16_f32 v83, v70, v71\nv_permlane16_swap_b32 v80, v82\n"
    "s_nop 0\nv_permlane16_swap_b32 v81, v83\nglobal_store_dwordx4 v9, v[80:83], s[32:33] sc1\ns_cmp_eq_u32 s6, 0\ns_cbranch_scc1 .LL77\ns_lshl_b32 s15, s39, 15\n"
    "s_add_u32 s14, s15, 0x801000\nv_lshl_add_u32 v182, v0, 4, s14\ns_add_u32 s16, s15, 0x803000\nv_lshl_add_u32 v183, v0, 4, s16\ns_add_u32 s17, s15, 0x805000\n"
    "v_lshl_add_u32 v184, v0, 4, s17\ns_add_u32 s18, s15, 0x807000\nv_lshl_add_u32 v185, v0, 4, s18\ns_mov_b32 s19, 0x1f600\nv_lshl_add_u32 v186, v0, 4, s19\n"
    "v_and_b32 v100, 63, v0\ns_lshl_b32 s22, s41, 13\ns_lshl_b32 s23, s3, 11\ns_add_u32 s21, s22, s23\ns_add_u32 s20, s21, 0x1200000\nv_lshl_add_u32 v200, v100, 4, s20\n"
    "v_mov_b32 v201, v200\ns_mov_b32 s24, 0x1004000\nv_lshl_add_u32 v202, v100, 4, s24\nv_mov_b32 v196, -1\nv_mov_b32 v197, -1\nv_mov_b32 v198, -1\nv_mov_b32 v199, -1\n"
    "s_barrier\ns_mov_b32 s14, 0x1d550\nv_lshl_add_u32 v14, v0, 3, s14\nv_lshrrev_b32 v48, 3, v0\nv_lshl_add_u32 v14, v48, 4, v14\ns_mov_b32 s15, 0x18000\n"
    "v_lshl_add_u32 v15, v48, 7, s15\nv_xor_b32 v50, v0, v48\nv_and_b32 v49, 7, v50\nv_lshl_add_u32 v15, v49, 4, v15\ns_mov_b32 s16, 0x25e00\nv_lshl_add_u32 v16, v0, 4, s16\n"
    "s_lshl_b32 s18, s41, 7\ns_add_u32 s17, s18, 0x600000\nv_lshl_add_u32 v51, v48, 15, s17\nv_and_b32 v52, 1, v0\nv_lshl_add_u32 v51, v52, 6, v51\nv_bfe_u32 v53, v0, 1, 2\n"
    "v_lshl_add_u32 v51, v53, 4, v51\nv_lshrrev_b32 v55, 4, v0\ns_lshl_b32 s21, s35, 18\ns_lshl_b32 s22, s36, 8\ns_add_u32 s20, s21, s22\ns_add_u32 s19, s20, 0x1200000\n"
    "v_lshl_add_u32 v54, v55, 13, s19\nv_and_b32 v56, 15, v0\nv_lshl_add_u32 v54, v56, 4, v54\ns_cmp_eq_u32 s6, 0\ns_cselect_b64 vcc, -1, 0\nv_cndmask_b32 v17, v54, v51, vcc\n"
    "v_bfe_u32 v59, v0, 5, 1\nv_and_b32 v61, 31, v0\nv_sub_u32 v60, 0x3ff, v61\nv_lshl_add_u32 v58, v59, 3, v60\nv_lshrrev_b32 v57, 2, v58\ns_lshr_b32 s25, s3, 1\n"
    "s_mul_i32 s24, s25, 0x2100\ns_add_u32 s23, s24, 0x17800\nv_lshl_add_u32 v19, v57, 3, s23\nv_and_b32 v62, 3, v58\ns_mov_b32 s44, 0x840\nv_mad_u32_u24 v19, v62, s44, v19\n"
    "s_lshl2_add_u32 s45, s3, 0x27f80\nv_mov_b32 v12, s45\ns_add_u32 s46, s6, 12\nv_mov_b32 v13, s46\nv_and_b32 v63, 0xff, v0\nv_mov_b32 v65, 0x1d000\nv_mov_b32 v67, 0x1fdb0\n"
    "v_mov_b32 v69, 0x20b10\nv_mov_b32 v70, 0x21870\nv_cmp_gt_u32 vcc, 0xa1, v63\ns_nop 1\nv_cndmask_b32 v68, v70, v69, vcc\nv_cmp_gt_u32 vcc, 0x7b, v63\ns_nop 1\n"
    "v_cndmask_b32 v66, v68, v67, vcc\nv_cmp_gt_u32 vcc, 0x55, v63\ns_nop 1\nv_cndmask_b32 v64, v66, v65, vcc\nv_lshl_add_u32 v18, v63, 4, v64\nv_mov_b32 v72, 0x1950\n"
    "v_mov_b32 v73, 0x3180\nv_cmp_gt_u32 vcc, 0x55, v63\ns_nop 1\nv_cndmask_b32 v71, v73, v72, vcc\nv_add_u32 v1, v18, v71\ns_mul_i32 s49, s25, 0x1950\ns_and_b32 s51, s3, 1\n"
    "s_mul_i32 s50, s51, 0xa00\ns_add_u32 s48, s49, s50\ns_add_u32 s47, s48, 0x1d000\nv_lshl_add_u32 v2, v59, 4, s47\ns_mov_b32 s52, 0x50\nv_mad_u32_u24 v2, v61, s52, v2\n"
    "s_lshl_b32 s54, s3, 12\ns_add_u32 s53, s54, 0x18000\nv_lshl_add_u32 v3, v61, 7, s53\nv_and_b32 v75, 7, v0\nv_xor_b32 v74, v59, v75\nv_lshl_add_u32 v3, v74, 4, v3\n"
    "s_add_u32 s14, s7, 0x400000\nv_lshl_add_u32 v20, v0, 4, s14\ns_lshl_b32 s17, s41, 13\ns_add_u32 s16, s7, s17\ns_add_u32 s15, s16, 0x200000\nv_lshl_add_u32 v21, v0, 4, s15\n"
    "v_add_u32 v22, 8192, v20\nv_add_u32 v23, 16384, v20\nv_add_u32 v24, 24576, v20\nv_and_b32 v56, 63, v0\ns_lshl_b32 s16, s41, 13\ns_lshl_b32 s17, s3, 11\n"
    "s_add_u32 s15, s16, s17\ns_add_u32 s14, s15, 0x800000\nv_lshl_add_u32 v4, v56, 4, s14\ns_add_u32 s18, s15, 0xa00000\nv_lshl_add_u32 v5, v56, 4, s18\nv_and_b32 v57, 1, v0\n"
    "s_lshl_b32 s21, s2, 7\ns_lshl_b32 s22, s3, 5\ns_add_u32 s20, s21, s22\ns_add_u32 s19, s20, 0xc00000\nv_lshl_add_u32 v6, v57, 4, s19\nv_and_b32 v58, 0x7f, v0\n"
    "s_lshr_b32 s25, s3, 1\ns_mul_i32 s24, s25, 0x2100\ns_add_u32 s23, s24, 0x18000\nv_lshl_add_u32 v47, v58, 4, s23\nv_and_b32 v59, 31, v0\ns_mov_b32 s44, 0x27600\n"
    "v_lshl_add_u32 v29, v59, 2, s44\ns_lshl_b32 s48, s40, 13\ns_add_u32 s47, s7, s48\ns_lshl_b32 s49, s39, 7\ns_add_u32 s46, s47, s49\ns_add_u32 s45, s46, 0x400000\n"
    "v_lshl_add_u32 v30, v59, 2, s45\nv_mov_b32 v25, 0\nv_mov_b32 v26, 0\nv_mov_b32 v27, 0\nv_mov_b32 v28, 0\nv_mov_b32 v8, -1\nv_mov_b32 v9, -1\nv_mov_b32 v10, -1\n"
    "v_mov_b32 v11, -1\ns_sleep 6\nglobal_load_dwordx4 v[208:211], v182, s[32:33] offset:-4096 sc1\nglobal_load_dwordx4 v[212:215], v182, s[32:33] offset:0 sc1\n"
    "global_load_dwordx4 v[216:219], v183, s[32:33] offset:-4096 sc1\nglobal_load_dwordx4 v[220:223], v183, s[32:33] offset:0 sc1\n"
    "global_load_dwordx4 v[224:227], v184, s[32:33] offset:-4096 sc1\nglobal_load_dwordx4 v[228:231], v184, s[32:33] offset:0 sc1\n"
    "global_load_dwordx4 v[232:235], v185, s[32:33] offset:-4096 sc1\nglobal_load_dwordx4 v[236:239], v185, s[32:33] offset:0 sc1\ns_branch .LL78\n.LL77:\ns_barrier\n"
    "ds_read_b32 v242, v33\nds_read_b32 v243, v33 offset:512\nds_read_b64 v[240:241], v31\nds_read_b64 v[244:245], v31 offset:1024\nds_read_b128 v[246:249], v32\n"
    "ds_read_b64 v[250:251], v32 offset:16\ns_bitcmp1_b32 s3, 0\ns_cselect_b32 s82, 0x43000000, 0\ns_mov_b32 s86, 0x41000000\ns_waitcnt lgkmcnt(0)\n"
    "v_lshlrev_b32 v112, 16, v242\nv_and_b32 v113, 0xffff0000, v242\nv_lshlrev_b32 v114, 16, v243\nv_and_b32 v115, 0xffff0000, v243\nv_mul_f32 v112, s93, v112\n"
    "v_mul_f32 v113, s93, v113\nv_mul_f32 v114, s94, v114\nv_mul_f32 v115, s94, v115\nv_mul_f32 v132, s82, v112\nv_mul_f32 v133, s82, v113\nv_mul_f32 v134, s86, v112\n"
    "v_mul_f32 v135, s86, v113\nv_mul_f32 v117, s82, v114\nv_mul_f32 v119, s82, v115\nv_mul_f32 v121, s86, v114\nv_mul_f32 v123, s86, v115\nv_exp_f32 v132, v132\n"
    "v_exp_f32 v133, v133\nv_exp_f32 v134, v134\nv_exp_f32 v135, v135\nv_fract_f32 v117, v117\nv_fract_f32 v119, v119\nv_fract_f32 v121, v121\nv_fract_f32 v123, v123\n"
    "v_cos_f32 v116, v117\nv_cos_f32 v118, v119\nv_cos_f32 v120, v121\nv_cos_f32 v122, v123\nv_sin_f32 v117, v117\nv_sin_f32 v119, v119\nv_sin_f32 v121, v121\n"
    "v_sin_f32 v123, v123\nv_mul_f32 v116, v116, v132\nv_mul_f32 v118, v118, v133\nv_mul_f32 v120, v120, v134\nv_mul_f32 v122, v122, v135\n"
    "global_load_dwordx4 v[36:39], v187, s[32:33] sc1\nv_mul_f32 v117, v117, v132\nv_mul_f32 v119, v119, v133\nv_mul_f32 v121, v121, v134\nv_mul_f32 v123, v123, v135\n"
    "s_lshr_b32 s83, s3, 1\ns_mul_i32 s83, s83, 0x6000600\ns_mov_b32 s84, 0xffff\nv_add_u32 v240, s83, v240\nv_add_u32 v241, s83, v241\nv_add_u32 v244, s83, v244\n"
    "v_add_u32 v245, s83, v245\nv_add_u32 v246, s83, v246\nv_add_u32 v247, s83, v247\nv_add_u32 v248, s83, v248\nv_add_u32 v249, s83, v249\nv_add_u32 v250, s83, v250\n"
    "v_add_u32 v251, s83, v251\nv_cvt_pk_f16_f32 v144, v116, v117\nv_cvt_pk_f16_f32 v145, v118, v119\nv_pk_mul_f32 v[124:125], v[116:117], v[120:121] op_sel_hi:[0,1]\n"
    "v_pk_mul_f32 v[126:127], v[118:119], v[122:123] op_sel_hi:[0,1]\n"
    "v_pk_fma_f32 v[116:117], v[116:117], v[120:121], v[124:125] op_sel:[1,1,0] op_sel_hi:[1,0,1] neg_lo:[0,1,0]\n"
    "v_pk_fma_f32 v[118:119], v[118:119], v[122:123], v[126:127] op_sel:[1,1,0] op_sel_hi:[1,0,1] neg_lo:[0,1,0]\nv_cvt_pk_f16_f32 v146, v116, v117\n"
    "v_cvt_pk_f16_f32 v147, v118, v119\nv_pk_mul_f32 v[124:125], v[116:117], v[120:121] op_sel_hi:[0,1]\nv_pk_mul_f32 v[126:127], v[118:119], v[122:123] op_sel_hi:[0,1]\n"
    "v_pk_fma_f32 v[116:117], v[116:117], v[120:121], v[124:125] op_sel:[1,1,0] op_sel_hi:[1,0,1] neg_lo:[0,1,0]\n"
    "v_pk_fma_f32 v[118:119], v[118:119], v[122:123], v[126:127] op_sel:[1,1,0] op_sel_hi:[1,0,1] neg_lo:[0,1,0]\nv_cvt_pk_f16_f32 v148, v116, v117\n"
    "v_cvt_pk_f16_f32 v149, v118, v119\nv_pk_mul_f32 v[124:125], v[116:117], v[120:121] op_sel_hi:[0,1]\nv_pk_mul_f32 v[126:127], v[118:119], v[122:123] op_sel_hi:[0,1]\n"
    "v_pk_fma_f32 v[116:117], v[116:117], v[120:121], v[124:125] op_sel:[1,1,0] op_sel_hi:[1,0,1] neg_lo:[0,1,0]\n"
    "v_pk_fma_f32 v[118:119], v[118:119], v[122:123], v[126:127] op_sel:[1,1,0] op_sel_hi:[1,0,1] neg_lo:[0,1,0]\nv_cvt_pk_f16_f32 v150, v116, v117\n"
    "v_cvt_pk_f16_f32 v151, v118, v119\nv_pk_mul_f32 v[124:125], v[116:117], v[120:121] op_sel_hi:[0,1]\nv_pk_mul_f32 v[126:127], v[118:119], v[122:123] op_sel_hi:[0,1]\n"
    "v_pk_fma_f32 v[116:117], v[116:117], v[120:121], v[124:125] op_sel:[1,1,0] op_sel_hi:[1,0,1] neg_lo:[0,1,0]\n"
    "v_pk_fma_f32 v[118:119], v[118:119], v[122:123], v[126:127] op_sel:[1,1,0] op_sel_hi:[1,0,1] neg_lo:[0,1,0]\nv_cvt_pk_f16_f32 v152, v116, v117\n"
    "v_cvt_pk_f16_f32 v153, v118, v119\nv_pk_mul_f32 v[124:125], v[116:117], v[120:121] op_sel_hi:[0,1]\nv_pk_mul_f32 v[126:127], v[118:119], v[122:123] op_sel_hi:[0,1]\n"
    "v_pk_fma_f32 v[116:117], v[116:117], v[120:121], v[124:125] op_sel:[1,1,0] op_sel_hi:[1,0,1] neg_lo:[0,1,0]\n"
    "v_pk_fma_f32 v[118:119], v[118:119], v[122:123], v[126:127] op_sel:[1,1,0] op_sel_hi:[1,0,1] neg_lo:[0,1,0]\nv_cvt_pk_f16_f32 v154, v116, v117\n"
    "v_cvt_pk_f16_f32 v155, v118, v119\nv_pk_mul_f32 v[124:125], v[116:117], v[120:121] op_sel_hi:[0,1]\nv_pk_mul_f32 v[126:127], v[118:119], v[122:123] op_sel_hi:[0,1]\n"
    "v_pk_fma_f32 v[116:117], v[116:117], v[120:121], v[124:125] op_sel:[1,1,0] op_sel_hi:[1,0,1] neg_lo:[0,1,0]\n"
    "v_pk_fma_f32 v[118:119], v[118:119], v[122:123], v[126:127] op_sel:[1,1,0] op_sel_hi:[1,0,1] neg_lo:[0,1,0]\nv_cvt_pk_f16_f32 v156, v116, v117\n"
    "v_cvt_pk_f16_f32 v157, v118, v119\nv_pk_mul_f32 v[124:125], v[116:117], v[120:121] op_sel_hi:[0,1]\nv_pk_mul_f32 v[126:127], v[118:119], v[122:123] op_sel_hi:[0,1]\n"
    "v_pk_fma_f32 v[116:117], v[116:117], v[120:121], v[124:125] op_sel:[1,1,0] op_sel_hi:[1,0,1] neg_lo:[0,1,0]\n"
    "v_pk_fma_f32 v[118:119], v[118:119], v[122:123], v[126:127] op_sel:[1,1,0] op_sel_hi:[1,0,1] neg_lo:[0,1,0]\nv_cvt_pk_f16_f32 v158, v116, v117\n"
    "v_cvt_pk_f16_f32 v159, v118, v119\nv_pk_mul_f32 v[124:125], v[116:117], v[120:121] op_sel_hi:[0,1]\nv_pk_mul_f32 v[126:127], v[118:119], v[122:123] op_sel_hi:[0,1]\n"
    "v_pk_fma_f32 v[116:117], v[116:117], v[120:121], v[124:125] op_sel:[1,1,0] op_sel_hi:[1,0,1] neg_lo:[0,1,0]\n"
    "v_pk_fma_f32 v[118:119], v[118:119], v[122:123], v[126:127] op_sel:[1,1,0] op_sel_hi:[1,0,1] neg_lo:[0,1,0]\nv_cvt_pk_f16_f32 v160, v116, v117\n"
    "v_cvt_pk_f16_f32 v161, v118, v119\nv_pk_mul_f32 v[124:125], v[116:117], v[120:121] op_sel_hi:[0,1]\nv_pk_mul_f32 v[126:127], v[118:119], v[122:123] op_sel_hi:[0,1]\n"
    "v_pk_fma_f32 v[116:117], v[116:117], v[120:121], v[124:125] op_sel:[1,1,0] op_sel_hi:[1,0,1] neg_lo:[0,1,0]\n"
    "v_pk_fma_f32 v[118:119], v[118:119], v[122:123], v[126:127] op_sel:[1,1,0] op_sel_hi:[1,0,1] neg_lo:[0,1,0]\nv_cvt_pk_f16_f32 v162, v116, v117\n"
    "v_cvt_pk_f16_f32 v163, v118, v119\nv_pk_mul_f32 v[124:125], v[116:117], v[120:121] op_sel_hi:[0,1]\nv_pk_mul_f32 v[126:127], v[118:119], v[122:123] op_sel_hi:[0,1]\n"
    "v_pk_fma_f32 v[116:117], v[116:117], v[120:121], v[124:125] op_sel:[1,1,0] op_sel_hi:[1,0,1] neg_lo:[0,1,0]\n"
    "v_pk_fma_f32 v[118:119], v[118:119], v[122:123], v[126:127] op_sel:[1,1,0] op_sel_hi:[1,0,1] neg_lo:[0,1,0]\nv_cvt_pk_f16_f32 v164, v116, v117\n"
    "v_cvt_pk_f16_f32 v165, v118, v119\nv_pk_mul_f32 v[124:125], v[116:117], v[120:121] op_sel_hi:[0,1]\nv_pk_mul_f32 v[126:127], v[118:119], v[122:123] op_sel_hi:[0,1]\n"
    "v_pk_fma_f32 v[116:117], v[116:117], v[120:121], v[124:125] op_sel:[1,1,0] op_sel_hi:[1,0,1] neg_lo:[0,1,0]\n"
    "v_pk_fma_f32 v[118:119], v[118:119], v[122:123], v[126:127] op_sel:[1,1,0] op_sel_hi:[1,0,1] neg_lo:[0,1,0]\nv_cvt_pk_f16_f32 v166, v116, v117\n"
    "v_cvt_pk_f16_f32 v167, v118, v119\nv_pk_mul_f32 v[124:125], v[116:117], v[120:121] op_sel_hi:[0,1]\nv_pk_mul_f32 v[126:127], v[118:119], v[122:123] op_sel_hi:[0,1]\n"
    "v_pk_fma_f32 v[116:117], v[116:117], v[120:121], v[124:125] op_sel:[1,1,0] op_sel_hi:[1,0,1] neg_lo:[0,1,0]\n"
    "v_pk_fma_f32 v[118:119], v[118:119], v[122:123], v[126:127] op_sel:[1,1,0] op_sel_hi:[1,0,1] neg_lo:[0,1,0]\nv_cvt_pk_f16_f32 v168, v116, v117\n"
    "v_cvt_pk_f16_f32 v169, v118, v119\nv_pk_mul_f32 v[124:125], v[116:117], v[120:121] op_sel_hi:[0,1]\nv_pk_mul_f32 v[126:127], v[118:119], v[122:123] op_sel_hi:[0,1]\n"
    "v_pk_fma_f32 v[116:117], v[116:117], v[120:121], v[124:125] op_sel:[1,1,0] op_sel_hi:[1,0,1] neg_lo:[0,1,0]\n"
    "v_pk_fma_f32 v[118:119], v[118:119], v[122:123], v[126:127] op_sel:[1,1,0] op_sel_hi:[1,0,1] neg_lo:[0,1,0]\nv_cvt_pk_f16_f32 v170, v116, v117\n"
    "v_cvt_pk_f16_f32 v171, v118, v119\nv_pk_mul_f32 v[124:125], v[116:117], v[120:121] op_sel_hi:[0,1]\nv_pk_mul_f32 v[126:127], v[118:119], v[122:123] op_sel_hi:[0,1]\n"
    "v_pk_fma_f32 v[116:117], v[116:117], v[120:121], v[124:125] op_sel:[1,1,0] op_sel_hi:[1,0,1] neg_lo:[0,1,0]\n"
    "v_pk_fma_f32 v[118:119], v[118:119], v[122:123], v[126:127] op_sel:[1,1,0] op_sel_hi:[1,0,1] neg_lo:[0,1,0]\nv_cvt_pk_f16_f32 v172, v116, v117\n"
    "v_cvt_pk_f16_f32 v173, v118, v119\nv_pk_mul_f32 v[124:125], v[116:117], v[120:121] op_sel_hi:[0,1]\nv_pk_mul_f32 v[126:127], v[118:119], v[122:123] op_sel_hi:[0,1]\n"
    "v_pk_fma_f32 v[116:117], v[116:117], v[120:121], v[124:125] op_sel:[1,1,0] op_sel_hi:[1,0,1] neg_lo:[0,1,0]\n"
    "v_pk_fma_f32 v[118:119], v[118:119], v[122:123], v[126:127] op_sel:[1,1,0] op_sel_hi:[1,0,1] neg_lo:[0,1,0]\nv_cvt_pk_f16_f32 v174, v116, v117\n"
    "v_cvt_pk_f16_f32 v175, v118, v119\nv_mov_b64 v[2:3], v[208:209]\nv_mov_b64 v[4:5], v[210:211]\nv_mov_b64 v[6:7], v[212:213]\nv_mov_b64 v[8:9], v[214:215]\n"
    "v_mov_b64 v[10:11], v[216:217]\nv_mov_b64 v[12:13], v[218:219]\nv_mov_b64 v[14:15], v[220:221]\nv_mov_b64 v[16:17], v[222:223]\nv_mov_b64 v[18:19], v[224:225]\n"
    "v_mov_b64 v[20:21], v[226:227]\nv_mov_b64 v[22:23], v[228:229]\nv_mov_b64 v[24:25], v[230:231]\nv_mov_b64 v[26:27], v[232:233]\nv_mov_b64 v[28:29], v[234:235]\n"
    "v_mov_b64 v[30:31], v[236:237]\nv_mov_b32 v47, v31\nv_mov_b32 v1, v206\ns_waitcnt vmcnt(0)\ns_mov_b32 s80, 0x10000\n.Ltp79:\nv_xor_b32 v36, s34, v36\n"
    "v_xor_b32 v37, s34, v37\nv_xor_b32 v38, s34, v38\nv_xor_b32 v39, s34, v39\nv_or3_b32 v36, v36, v37, v38\nv_or_b32 v36, v36, v39\nv_cmp_ne_u32 vcc, 0, v36\n"
    "s_cbranch_vccz .Ltq80\ns_sub_u32 s80, s80, 1\ns_cbranch_scc1 .Ltq80\ns_sleep 2\nglobal_load_dwordx4 v[36:39], v187, s[32:33] sc1\ns_waitcnt vmcnt(0)\ns_branch .Ltp79\n"
    ".Ltq80:\ns_sleep 1\nglobal_load_dwordx4 v[208:211], v182, s[32:33] offset:-4096 sc1\nglobal_load_dwordx4 v[212:215], v182, s[32:33] offset:0 sc1\n"
    "global_load_dwordx4 v[216:219], v183, s[32:33] offset:-4096 sc1\nglobal_load_dwordx4 v[220:223], v183, s[32:33] offset:0 sc1\n"
    "global_load_dwordx4 v[224:227], v184, s[32:33] offset:-4096 sc1\nglobal_load_dwordx4 v[228:231], v184, s[32:33] offset:0 sc1\n"
    "global_load_dwordx4 v[232:235], v185, s[32:33] offset:-4096 sc1\nglobal_load_dwordx4 v[236:239], v185, s[32:33] offset:0 sc1\n.LL78:\ns_mov_b32 s80, 0x10000\ns_setprio 0\n"
    ".LL83:\ns_waitcnt vmcnt(7)\nds_write_b128 v186, v[208:211] offset:24576\nv_max3_u32 v31, v208, v209, 0\nv_max3_u32 v31, v210, v211, v31\ns_waitcnt vmcnt(6)\n"
    "ds_write_b128 v186, v[212:215] offset:28672\nv_max3_u32 v31, v212, v213, v31\nv_max3_u32 v31, v214, v215, v31\nv_cmp_eq_u32 vcc, -1, v31\ns_cbranch_vccnz .LL85\n.LL84:\n"
    "s_waitcnt lgkmcnt(0)\ns_barrier\nglobal_load_dwordx4 v[112:115], v202, s[32:33] sc1\nglobal_store_dwordx4 v200, v[196:199], s[32:33] offset:0 sc1\n"
    "global_store_dwordx4 v200, v[196:199], s[32:33] offset:1024 sc1\ns_mov_b64 exec, 0xffff\nglobal_store_dwordx4 v201, v[196:199], s[32:33] sc1\ns_mov_b64 exec, -1\n.LL94:\n"
    "s_waitcnt vmcnt(9)\nds_write_b128 v186, v[216:219] offset:0\nv_max3_u32 v31, v216, v217, 0\nv_max3_u32 v31, v218, v219, v31\ns_waitcnt vmcnt(8)\n"
    "ds_write_b128 v186, v[220:223] offset:4096\nv_max3_u32 v31, v220, v221, v31\nv_max3_u32 v31, v222, v223, v31\nv_cmp_eq_u32 vcc, -1, v31\ns_cbranch_vccnz .LL96\n.LL95:\n"
    "s_waitcnt lgkmcnt(0)\ns_barrier\n.LL103:\ns_waitcnt vmcnt(7)\nds_write_b128 v186, v[224:227] offset:8192\nv_max3_u32 v31, v224, v225, 0\nv_max3_u32 v31, v226, v227, v31\n"
    "s_waitcnt vmcnt(6)\nds_write_b128 v186, v[228:231] offset:12288\nv_max3_u32 v31, v228, v229, v31\nv_max3_u32 v31, v230, v231, v31\nv_cmp_eq_u32 vcc, -1, v31\n"
    "s_cbranch_vccnz .LL105\n.LL104:\ns_waitcnt lgkmcnt(0)\ns_barrier\n.LL110:\ns_waitcnt vmcnt(5)\nds_write_b128 v186, v[232:235] offset:16384\nv_max3_u32 v31, v232, v233, 0\n"
    "v_max3_u32 v31, v234, v235, v31\ns_waitcnt vmcnt(4)\nds_write_b128 v186, v[236:239] offset:20480\nv_max3_u32 v31, v236, v237, v31\nv_max3_u32 v31, v238, v239, v31\n"
    "v_cmp_eq_u32 vcc, -1, v31\ns_cbranch_vccnz .LL112\n.LL111:\ns_waitcnt lgkmcnt(0)\ns_barrier\ns_setprio 0\ns_cmp_eq_u32 s6, 0\ns_cbranch_scc1 .LL115\n"
    "ds_write_b128 v47, v[40:43]\nv_alignbit_b32 v48, v41, v40, 16\nv_alignbit_b32 v49, v42, v41, 16\nv_alignbit_b32 v50, v43, v42, 16\nv_alignbit_b32 v51, v44, v43, 16\n"
    "ds_write_b128 v47, v[48:51] offset:2112\nv_mov_b32 v52, v41\nv_mov_b32 v53, v42\nv_mov_b32 v54, v43\nv_mov_b32 v55, v44\nds_write_b128 v47, v[52:55] offset:4224\n"
    "v_alignbit_b32 v56, v42, v41, 16\nv_alignbit_b32 v57, v43, v42, 16\nv_alignbit_b32 v58, v44, v43, 16\nv_alignbit_b32 v59, v45, v44, 16\n"
    "ds_write_b128 v47, v[56:59] offset:6336\ns_waitcnt vmcnt(0)\ns_branch .LL116\n.LL115:\nv_and_or_b32 v124, v240, s84, v191\nv_alignbit_b32 v125, 1, v240, 16\n"
    "v_and_or_b32 v126, v241, s84, v191\nv_alignbit_b32 v127, 1, v241, 16\nv_and_or_b32 v128, v244, s84, v191\nv_alignbit_b32 v129, 1, v244, 16\n"
    "v_and_or_b32 v130, v245, s84, v191\nv_alignbit_b32 v131, 1, v245, 16\nv_and_or_b32 v132, v246, s84, v191\nv_alignbit_b32 v133, 1, v246, 16\n"
    "v_and_or_b32 v134, v247, s84, v191\nv_alignbit_b32 v135, 1, v247, 16\nv_and_or_b32 v136, v248, s84, v191\nv_alignbit_b32 v137, 1, v248, 16\n"
    "v_and_or_b32 v138, v249, s84, v191\nv_alignbit_b32 v139, 1, v249, 16\nv_and_or_b32 v140, v250, s84, v191\nv_alignbit_b32 v141, 1, v250, 16\n"
    "v_and_or_b32 v142, v251, s84, v191\nv_alignbit_b32 v143, 1, v251, 16\ns_waitcnt vmcnt(0)\ns_mov_b32 s80, 0x10000\n.Ltp134:\nv_xor_b32 v112, s34, v112\n"
    "v_xor_b32 v113, s34, v113\nv_xor_b32 v114, s34, v114\nv_xor_b32 v115, s34, v115\nv_or3_b32 v112, v112, v113, v114\nv_or_b32 v112, v112, v115\nv_cmp_ne_u32 vcc, 0, v112\n"
    "s_cbranch_vccz .Ltq135\ns_sub_u32 s80, s80, 1\ns_cbranch_scc1 .Ltq135\ns_sleep 2\nglobal_load_dwordx4 v[112:115], v202, s[32:33] sc1\ns_waitcnt vmcnt(0)\ns_branch .Ltp134\n"
    ".Ltq135:\n.LL116:\ns_waitcnt lgkmcnt(0)\ns_barrier\ns_cmp_eq_u32 s3, 3\ns_cbranch_scc0 .LL131\nds_read2_b32 v[208:209], v29 offset0:0 offset1:32\n"
    "ds_read2_b32 v[210:211], v29 offset0:64 offset1:96\nds_read2_b32 v[212:213], v29 offset0:128 offset1:160\nds_read2_b32 v[214:215], v29 offset0:192 offset1:224\n"
    "s_mov_b32 s86, 0x3c000000\ns_mov_b32 s82, 0xffff0000\ns_waitcnt lgkmcnt(2)\nv_pk_add_f32 v[208:209], v[208:209], v[210:211]\ns_waitcnt lgkmcnt(0)\n"
    "v_pk_add_f32 v[212:213], v[212:213], v[214:215]\nv_pk_add_f32 v[208:209], v[208:209], v[212:213]\nv_pk_mul_f32 v[208:209], v[208:209], s[86:87] op_sel_hi:[1,0]\n"
    "v_fma_f32 v209, -v208, v208, v209\nv_cvt_pk_bf16_f32 v208, v208, 0\nv_max_f32 v209, 0x8000000, v209\nv_sub_u32 v209, v209, v208\nv_add_u32 v209, 0x8000, v209\n"
    "v_and_or_b32 v209, v209, s82, v208\ns_mov_b32 exec_hi, 0\nglobal_store_dword v30, v209, s[32:33] sc1\ns_mov_b64 exec, -1\n.LL131:\ns_cmp_eq_u32 s6, 0\n"
    "s_cbranch_scc0 .LL132\nds_write_b64 v188, v[144:145] offset:0\nds_write_b64 v188, v[146:147] offset:544\nds_write_b64 v188, v[148:149] offset:1088\n"
    "ds_write_b64 v188, v[150:151] offset:1632\nds_write_b64 v188, v[152:153] offset:2176\nds_write_b64 v188, v[154:155] offset:2720\nds_write_b64 v188, v[156:157] offset:3264\n"
    "ds_write_b64 v188, v[158:159] offset:3808\nds_write_b64 v188, v[160:161] offset:4352\nds_write_b64 v188, v[162:163] offset:4896\nds_write_b64 v188, v[164:165] offset:5440\n"
    "ds_write_b64 v188, v[166:167] offset:5984\nds_write_b64 v188, v[168:169] offset:6528\nds_write_b64 v188, v[170:171] offset:7072\nds_write_b64 v188, v[172:173] offset:7616\n"
    "ds_write_b64 v188, v[174:175] offset:8160\ns_waitcnt lgkmcnt(0)\ns_barrier\nds_read_b128 v[48:51], v189 offset:0\nds_read_b128 v[52:55], v190 offset:0\n"
    "ds_read_b128 v[56:59], v190 offset:8704\nds_read_b128 v[60:63], v189 offset:64\nds_read_b128 v[64:67], v190 offset:64\nds_read_b128 v[68:71], v190 offset:8768\n"
    "ds_read_b128 v[72:75], v189 offset:128\nds_read_b128 v[76:79], v190 offset:128\nds_read_b128 v[80:83], v190 offset:8832\nds_read_b128 v[84:87], v189 offset:192\n"
    "ds_read_b128 v[88:91], v190 offset:192\nds_read_b128 v[92:95], v190 offset:8896\nds_read_b128 v[96:99], v189 offset:256\nds_read_b128 v[100:103], v190 offset:256\n"
    "ds_read_b128 v[104:107], v190 offset:8960\ns_waitcnt lgkmcnt(12)\nv_mfma_f32_16x16x32_f16 v[108:111], v[48:51], v[52:55], 0\n"
    "v_mfma_f32_16x16x32_f16 v[112:115], v[48:51], v[56:59], 0\nds_read_b128 v[48:51], v189 offset:320\nds_read_b128 v[52:55], v190 offset:320\n"
    "ds_read_b128 v[56:59], v190 offset:9024\ns_waitcnt lgkmcnt(12)\nv_mfma_f32_16x16x32_f16 v[108:111], v[60:63], v[64:67], v[108:111]\n"
    "v_mfma_f32_16x16x32_f16 v[112:115], v[60:63], v[68:71], v[112:115]\nds_read_b128 v[60:63], v189 offset:384\nds_read_b128 v[64:67], v190 offset:384\n"
    "ds_read_b128 v[68:71], v190 offset:9088\ns_waitcnt lgkmcnt(12)\nv_mfma_f32_16x16x32_f16 v[108:111], v[72:75], v[76:79], v[108:111]\n"
    "v_mfma_f32_16x16x32_f16 v[112:115], v[72:75], v[80:83], v[112:115]\nds_read_b128 v[72:75], v189 offset:448\nds_read_b128 v[76:79], v190 offset:448\n"
    "ds_read_b128 v[80:83], v190 offset:9152\ns_waitcnt lgkmcnt(12)\nv_mfma_f32_16x16x32_f16 v[108:111], v[84:87], v[88:91], v[108:111]\n"
    "v_mfma_f32_16x16x32_f16 v[112:115], v[84:87], v[92:95], v[112:115]\ns_waitcnt lgkmcnt(9)\nv_mfma_f32_16x16x32_f16 v[108:111], v[96:99], v[100:103], v[108:111]\n"
    "v_mfma_f32_16x16x32_f16 v[112:115], v[96:99], v[104:107], v[112:115]\ns_waitcnt lgkmcnt(6)\nv_mfma_f32_16x16x32_f16 v[108:111], v[48:51], v[52:55], v[108:111]\n"
    "v_mfma_f32_16x16x32_f16 v[112:115], v[48:51], v[56:59], v[112:115]\ns_waitcnt lgkmcnt(3)\nv_mfma_f32_16x16x32_f16 v[108:111], v[60:63], v[64:67], v[108:111]\n"
    "v_mfma_f32_16x16x32_f16 v[112:115], v[60:63], v[68:71], v[112:115]\ns_waitcnt lgkmcnt(0)\nv_mfma_f32_16x16x32_f16 v[108:111], v[72:75], v[76:79], v[108:111]\n"
    "v_mfma_f32_16x16x32_f16 v[112:115], v[72:75], v[80:83], v[112:115]\ns_nop 6\nv_cvt_pk_f16_f32 v48, v108, v109\nds_write_b16 v124, v48 offset:59392\n"
    "ds_write_b16_d16_hi v125, v48 offset:59392\nv_cvt_pk_f16_f32 v49, v110, v111\nds_write_b16 v126, v49 offset:59392\nds_write_b16_d16_hi v127, v49 offset:59392\n"
    "v_cvt_pk_f16_f32 v50, v112, v113\nds_write_b16 v128, v50 offset:59392\nds_write_b16_d16_hi v129, v50 offset:59392\nv_cvt_pk_f16_f32 v51, v114, v115\n"
    "ds_write_b16 v130, v51 offset:59392\nds_write_b16_d16_hi v131, v51 offset:59392\ns_branch .LL133\n.LL132:\nds_read_b64 v[64:65], v19 offset:2016\n"
    "ds_read_b64 v[66:67], v19 offset:2024\nds_read_b64 v[68:69], v19 offset:1984\nds_read_b64 v[70:71], v19 offset:1992\nds_read_b64 v[72:73], v19 offset:1952\n"
    "ds_read_b64 v[74:75], v19 offset:1960\nds_read_b64 v[76:77], v19 offset:1920\nds_read_b64 v[78:79], v19 offset:1928\nds_read_b64 v[80:81], v19 offset:1888\n"
    "ds_read_b64 v[82:83], v19 offset:1896\nds_read_b64 v[84:85], v19 offset:1856\nds_read_b64 v[86:87], v19 offset:1864\nds_read_b64 v[88:89], v19 offset:1824\n"
    "ds_read_b64 v[90:91], v19 offset:1832\nds_read_b64 v[92:93], v19 offset:1792\nds_read_b64 v[94:95], v19 offset:1800\nds_read_b64 v[96:97], v19 offset:1760\n"
    "ds_read_b64 v[98:99], v19 offset:1768\nds_read_b64 v[100:101], v19 offset:1728\nds_read_b64 v[102:103], v19 offset:1736\nds_read_b64 v[104:105], v19 offset:1696\n"
    "ds_read_b64 v[106:107], v19 offset:1704\nds_read_b64 v[108:109], v19 offset:1664\nds_read_b64 v[110:111], v19 offset:1672\nds_read_b64 v[112:113], v19 offset:1632\n"
    "ds_read_b64 v[114:115], v19 offset:1640\nds_read_b64 v[116:117], v19 offset:1600\nds_read_b64 v[118:119], v19 offset:1608\nds_read_b64 v[120:121], v19 offset:1568\n"
    "ds_read_b64 v[122:123], v19 offset:1576\nds_read_b64 v[124:125], v19 offset:1536\nds_read_b64 v[126:127], v19 offset:1544\nds_read_b64 v[128:129], v19 offset:1504\n"
    "ds_read_b64 v[130:131], v19 offset:1512\nds_read_b64 v[132:133], v19 offset:1472\nds_read_b64 v[134:135], v19 offset:1480\nds_read_b64 v[136:137], v19 offset:1440\n"
    "ds_read_b64 v[138:139], v19 offset:1448\nds_read_b64 v[140:141], v19 offset:1408\nds_read_b64 v[142:143], v19 offset:1416\nds_read_b64 v[144:145], v19 offset:1376\n"
    "ds_read_b64 v[146:147], v19 offset:1384\nds_read_b64 v[148:149], v19 offset:1344\nds_read_b64 v[150:151], v19 offset:1352\nds_read_b64 v[152:153], v19 offset:1312\n"
    "ds_read_b64 v[154:155], v19 offset:1320\nds_read_b64 v[156:157], v19 offset:1280\nds_read_b64 v[158:159], v19 offset:1288\nds_read_b64 v[160:161], v19 offset:1248\n"
    "ds_read_b64 v[162:163], v19 offset:1256\nds_read_b64 v[164:165], v19 offset:1216\nds_read_b64 v[166:167], v19 offset:1224\nds_read_b64 v[168:169], v19 offset:1184\n"
    "ds_read_b64 v[170:171], v19 offset:1192\nds_read_b64 v[172:173], v19 offset:1152\nds_read_b64 v[174:175], v19 offset:1160\nds_read_b64 v[176:177], v19 offset:1120\n"
    "ds_read_b64 v[178:179], v19 offset:1128\nds_read_b64 v[180:181], v19 offset:1088\nds_read_b64 v[182:183], v19 offset:1096\nds_read_b64 v[184:185], v19 offset:1056\n"
    "ds_read_b64 v[186:187], v19 offset:1064\nds_read_b64 v[188:189], v19 offset:1024\nds_read_b64 v[190:191], v19 offset:1032\nds_read_b64 v[192:193], v19 offset:992\n"
    "ds_read_b64 v[194:195], v19 offset:1000\nds_read_b64 v[196:197], v19 offset:960\nds_read_b64 v[198:199], v19 offset:968\nds_read_b64 v[200:201], v19 offset:928\n"
    "ds_read_b64 v[202:203], v19 offset:936\n.LL133:\ns_waitcnt lgkmcnt(0)\ns_barrier\ns_or_b32 s81, s2, s3\ns_or_b32 s81, s81, s6\ns_cbranch_scc1 .LL129\n"
    "v_mov_b32 v60, 16777216\nv_mov_b32 v61, s34\ns_mov_b64 exec, 1\nglobal_store_dword v60, v61, s[32:33] sc1\ns_mov_b64 exec, -1\n.LL129:\n"
    "global_load_dwordx4 v[240:243], v21, s[32:33] sc1\nglobal_load_dwordx4 v[208:211], v20, s[32:33] sc1\nglobal_load_dwordx4 v[212:215], v22, s[32:33] sc1\n"
    "global_load_dwordx4 v[216:219], v23, s[32:33] sc1\nglobal_load_dwordx4 v[220:223], v24, s[32:33] sc1\ns_cmp_eq_u32 s6, 0\ns_cbranch_scc0 .LL117\nv_mov_b32 v60, 163216\n"
    "ds_read_b64 v[60:61], v60\nds_read_u16 v64, v132 offset:59392\nds_read_u16 v65, v133 offset:59392\nds_read_u16 v66, v134 offset:59392\nds_read_u16 v67, v135 offset:59392\n"
    "ds_read_u16 v68, v136 offset:59392\nds_read_u16 v69, v137 offset:59392\nds_read_u16 v70, v138 offset:59392\nds_read_u16 v71, v139 offset:59392\n"
    "ds_read_u16 v72, v140 offset:59392\nds_read_u16 v73, v141 offset:59392\nds_read_u16 v74, v142 offset:59392\nds_read_u16 v75, v143 offset:59392\ns_waitcnt lgkmcnt(0)\n"
    "v_lshl_or_b32 v40, v65, 16, v64\nv_lshl_or_b32 v41, v67, 16, v66\nv_lshl_or_b32 v42, v69, 16, v68\nv_lshl_or_b32 v43, v71, 16, v70\nv_lshl_or_b32 v44, v73, 16, v72\n"
    "v_lshl_or_b32 v45, v75, 16, v74\nv_readfirstlane_b32 s88, v60\nv_readfirstlane_b32 s90, v61\nds_write_b128 v47, v[40:43]\nv_alignbit_b32 v48, v41, v40, 16\n"
    "v_alignbit_b32 v49, v42, v41, 16\nv_alignbit_b32 v50, v43, v42, 16\nv_alignbit_b32 v51, v44, v43, 16\nds_write_b128 v47, v[48:51] offset:2112\nv_mov_b32 v52, v41\n"
    "v_mov_b32 v53, v42\nv_mov_b32 v54, v43\nv_mov_b32 v55, v44\nds_write_b128 v47, v[52:55] offset:4224\nv_alignbit_b32 v56, v42, v41, 16\nv_alignbit_b32 v57, v43, v42, 16\n"
    "v_alignbit_b32 v58, v44, v43, 16\nv_alignbit_b32 v59, v45, v44, 16\nds_write_b128 v47, v[56:59] offset:6336\ns_waitcnt lgkmcnt(0)\ns_barrier\n.LL117:\n"
    "v_mov_b64 v[60:61], 0\nv_mov_b64 v[62:63], 0\ns_cmp_eq_u32 s3, 3\ns_cbranch_scc1 .LL119\ns_cmp_eq_u32 s3, 2\ns_cselect_b32 exec_hi, 3, -1\nds_write_b128 v18, v[60:63]\n"
    "ds_write_b128 v1, v[60:63]\ns_mov_b64 exec, -1\n.LL119:\ns_cmp_eq_u32 s6, 0\ns_cbranch_scc0 .LL118\nds_read_b64 v[64:65], v19 offset:2016\n"
    "ds_read_b64 v[66:67], v19 offset:2024\nds_read_b64 v[68:69], v19 offset:1984\nds_read_b64 v[70:71], v19 offset:1992\nds_read_b64 v[72:73], v19 offset:1952\n"
    "ds_read_b64 v[74:75], v19 offset:1960\nds_read_b64 v[76:77], v19 offset:1920\nds_read_b64 v[78:79], v19 offset:1928\nds_read_b64 v[80:81], v19 offset:1888\n"
    "ds_read_b64 v[82:83], v19 offset:1896\nds_read_b64 v[84:85], v19 offset:1856\nds_read_b64 v[86:87], v19 offset:1864\nds_read_b64 v[88:89], v19 offset:1824\n"
    "ds_read_b64 v[90:91], v19 offset:1832\nds_read_b64 v[92:93], v19 offset:1792\nds_read_b64 v[94:95], v19 offset:1800\nds_read_b64 v[96:97], v19 offset:1760\n"
    "ds_read_b64 v[98:99], v19 offset:1768\nds_read_b64 v[100:101], v19 offset:1728\nds_read_b64 v[102:103], v19 offset:1736\nds_read_b64 v[104:105], v19 offset:1696\n"
    "ds_read_b64 v[106:107], v19 offset:1704\nds_read_b64 v[108:109], v19 offset:1664\nds_read_b64 v[110:111], v19 offset:1672\nds_read_b64 v[112:113], v19 offset:1632\n"
    "ds_read_b64 v[114:115], v19 offset:1640\nds_read_b64 v[116:117], v19 offset:1600\nds_read_b64 v[118:119], v19 offset:1608\nds_read_b64 v[120:121], v19 offset:1568\n"
    "ds_read_b64 v[122:123], v19 offset:1576\nds_read_b64 v[124:125], v19 offset:1536\nds_read_b64 v[126:127], v19 offset:1544\nds_read_b64 v[128:129], v19 offset:1504\n"
    "ds_read_b64 v[130:131], v19 offset:1512\nds_read_b64 v[132:133], v19 offset:1472\nds_read_b64 v[134:135], v19 offset:1480\nds_read_b64 v[136:137], v19 offset:1440\n"
    "ds_read_b64 v[138:139], v19 offset:1448\nds_read_b64 v[140:141], v19 offset:1408\nds_read_b64 v[142:143], v19 offset:1416\nds_read_b64 v[144:145], v19 offset:1376\n"
    "ds_read_b64 v[146:147], v19 offset:1384\nds_read_b64 v[148:149], v19 offset:1344\nds_read_b64 v[150:151], v19 offset:1352\nds_read_b64 v[152:153], v19 offset:1312\n"
    "ds_read_b64 v[154:155], v19 offset:1320\nds_read_b64 v[156:157], v19 offset:1280\nds_read_b64 v[158:159], v19 offset:1288\nds_read_b64 v[160:161], v19 offset:1248\n"
    "ds_read_b64 v[162:163], v19 offset:1256\nds_read_b64 v[164:165], v19 offset:1216\nds_read_b64 v[166:167], v19 offset:1224\nds_read_b64 v[168:169], v19 offset:1184\n"
    "ds_read_b64 v[170:171], v19 offset:1192\nds_read_b64 v[172:173], v19 offset:1152\nds_read_b64 v[174:175], v19 offset:1160\nds_read_b64 v[176:177], v19 offset:1120\n"
    "ds_read_b64 v[178:179], v19 offset:1128\nds_read_b64 v[180:181], v19 offset:1088\nds_read_b64 v[182:183], v19 offset:1096\nds_read_b64 v[184:185], v19 offset:1056\n"
    "ds_read_b64 v[186:187], v19 offset:1064\nds_read_b64 v[188:189], v19 offset:1024\nds_read_b64 v[190:191], v19 offset:1032\nds_read_b64 v[192:193], v19 offset:992\n"
    "ds_read_b64 v[194:195], v19 offset:1000\nds_read_b64 v[196:197], v19 offset:960\nds_read_b64 v[198:199], v19 offset:968\nds_read_b64 v[200:201], v19 offset:928\n"
    "ds_read_b64 v[202:203], v19 offset:936\n.LL118:\ns_mov_b32 s84, 0x3e800000\ns_mov_b32 s86, 0x3727c5ac\ns_mov_b32 s80, 0x10000\n.Lln120:\ns_waitcnt vmcnt(4)\n"
    "v_max3_u32 v60, v240, v241, v242\nv_max_u32 v60, v60, v243\nv_cmp_eq_u32 s[82:83], -1, v60\nv_lshlrev_b32 v32, 16, v240\nv_and_b32 v36, 0xffff0000, v240\n"
    "v_lshlrev_b32 v33, 16, v241\nv_and_b32 v37, 0xffff0000, v241\nv_lshlrev_b32 v34, 16, v242\nv_and_b32 v38, 0xffff0000, v242\nv_lshlrev_b32 v35, 16, v243\n"
    "v_and_b32 v39, 0xffff0000, v243\ns_waitcnt vmcnt(3)\nv_lshlrev_b32 v48, 16, v208\nv_lshlrev_b32 v49, 16, v209\nv_lshlrev_b32 v50, 16, v210\nv_lshlrev_b32 v51, 16, v211\n"
    "v_pk_fma_f32 v[52:53], v[48:49], v[48:49], v[208:209]\nv_pk_fma_f32 v[54:55], v[50:51], v[50:51], v[210:211]\ns_waitcnt vmcnt(2)\nv_lshlrev_b32 v56, 16, v212\n"
    "v_lshlrev_b32 v57, 16, v213\nv_lshlrev_b32 v58, 16, v214\nv_lshlrev_b32 v59, 16, v215\nv_pk_add_f32 v[52:53], v[52:53], v[212:213]\n"
    "v_pk_add_f32 v[54:55], v[54:55], v[214:215]\nv_pk_add_f32 v[48:49], v[48:49], v[56:57]\nv_pk_add_f32 v[50:51], v[50:51], v[58:59]\n"
    "v_pk_fma_f32 v[52:53], v[56:57], v[56:57], v[52:53]\nv_pk_fma_f32 v[54:55], v[58:59], v[58:59], v[54:55]\ns_waitcnt vmcnt(1)\nv_lshlrev_b32 v56, 16, v216\n"
    "v_lshlrev_b32 v57, 16, v217\nv_lshlrev_b32 v58, 16, v218\nv_lshlrev_b32 v59, 16, v219\nv_pk_add_f32 v[52:53], v[52:53], v[216:217]\n"
    "v_pk_add_f32 v[54:55], v[54:55], v[218:219]\nv_pk_add_f32 v[48:49], v[48:49], v[56:57]\nv_pk_add_f32 v[50:51], v[50:51], v[58:59]\n"
    "v_pk_fma_f32 v[52:53], v[56:57], v[56:57], v[52:53]\nv_pk_fma_f32 v[54:55], v[58:59], v[58:59], v[54:55]\ns_waitcnt vmcnt(0)\nv_lshlrev_b32 v56, 16, v220\n"
    "v_lshlrev_b32 v57, 16, v221\nv_lshlrev_b32 v58, 16, v222\nv_lshlrev_b32 v59, 16, v223\nv_pk_add_f32 v[52:53], v[52:53], v[220:221]\n"
    "v_pk_add_f32 v[54:55], v[54:55], v[222:223]\nv_pk_add_f32 v[48:49], v[48:49], v[56:57]\nv_pk_add_f32 v[50:51], v[50:51], v[58:59]\n"
    "v_pk_fma_f32 v[52:53], v[56:57], v[56:57], v[52:53]\nv_pk_fma_f32 v[54:55], v[58:59], v[58:59], v[54:55]\nv_pk_mul_f32 v[48:49], v[48:49], s[84:85] op_sel_hi:[1,0]\n"
    "v_pk_mul_f32 v[50:51], v[50:51], s[84:85] op_sel_hi:[1,0]\nv_pk_fma_f32 v[60:61], v[48:49], v[48:49], s[86:87] op_sel_hi:[1,1,0] neg_lo:[0,0,1] neg_hi:[0,0,1]\n"
    "v_pk_fma_f32 v[62:63], v[50:51], v[50:51], s[86:87] op_sel_hi:[1,1,0] neg_lo:[0,0,1] neg_hi:[0,0,1]\n"
    "v_pk_fma_f32 v[52:53], v[52:53], s[84:85], v[60:61] op_sel_hi:[1,0,1] neg_lo:[0,0,1] neg_hi:[0,0,1]\n"
    "v_pk_fma_f32 v[54:55], v[54:55], s[84:85], v[62:63] op_sel_hi:[1,0,1] neg_lo:[0,0,1] neg_hi:[0,0,1]\nv_pk_add_f32 v[56:57], v[52:53], v[54:55]\nv_max_f32 v52, s86, v52\n"
    "v_max_f32 v53, s86, v53\nv_max_f32 v54, s86, v54\nv_max_f32 v55, s86, v55\nv_cmp_u_f32 vcc, v56, v57\nv_rsq_f32 v52, v52\nv_rsq_f32 v53, v53\nv_rsq_f32 v54, v54\n"
    "v_rsq_f32 v55, v55\ns_or_b64 vcc, vcc, s[82:83]\ns_cbranch_vccnz .Lls122\n.Llo121:\nv_pk_mul_f32 v[48:49], v[48:49], v[52:53]\nv_pk_mul_f32 v[50:51], v[50:51], v[54:55]\n"
    "v_pk_fma_f32 v[244:245], v[32:33], v[52:53], v[48:49] neg_lo:[0,0,1] neg_hi:[0,0,1]\nv_pk_fma_f32 v[246:247], v[34:35], v[54:55], v[50:51] neg_lo:[0,0,1] neg_hi:[0,0,1]\n"
    "v_pk_fma_f32 v[248:249], v[36:37], v[52:53], v[48:49] neg_lo:[0,0,1] neg_hi:[0,0,1]\nv_pk_fma_f32 v[250:251], v[38:39], v[54:55], v[50:51] neg_lo:[0,0,1] neg_hi:[0,0,1]\n"
    "v_cvt_pk_f16_f32 v56, v244, v245\nv_cvt_pk_f16_f32 v57, v246, v247\nds_write_b64 v14, v[56:57] offset:0\nv_cvt_pk_f16_f32 v58, v248, v249\n"
    "v_cvt_pk_f16_f32 v59, v250, v251\nds_write_b64 v14, v[58:59] offset:6480\ns_waitcnt lgkmcnt(0)\ns_barrier\nds_read_b128 v[220:223], v2 offset:1392\n"
    "ds_read_b128 v[224:227], v2 offset:1360\nds_read_b128 v[228:231], v2 offset:1312\nds_read_b128 v[232:235], v2 offset:1280\ns_cmp_eq_u32 s6, 0\ns_waitcnt lgkmcnt(3)\n"
    "v_mfma_f32_32x32x16_f16 v[204:219], v[64:67], v[220:223], 0\nds_read_b128 v[220:223], v2 offset:1232\ns_waitcnt lgkmcnt(3)\n"
    "v_mfma_f32_32x32x16_f16 v[204:219], v[68:71], v[224:227], v[204:219]\nds_read_b128 v[224:227], v2 offset:1200\nds_read_b128 v[68:71], v2 offset:1152\n"
    "s_cbranch_scc0 .LL138\nglobal_store_dwordx4 v4, v[8:11], s[32:33] offset:0 sc1\n.LL138:\ns_waitcnt lgkmcnt(4)\n"
    "v_mfma_f32_32x32x16_f16 v[204:219], v[72:75], v[228:231], v[204:219]\nds_read_b128 v[228:231], v2 offset:1120\ns_waitcnt lgkmcnt(4)\n"
    "v_mfma_f32_32x32x16_f16 v[204:219], v[76:79], v[232:235], v[204:219]\nds_read_b128 v[72:75], v2 offset:1072\ns_waitcnt lgkmcnt(4)\n"
    "v_mfma_f32_32x32x16_f16 v[204:219], v[80:83], v[220:223], v[204:219]\nds_read_b128 v[232:235], v2 offset:1040\ns_cbranch_scc0 .LL139\n"
    "global_store_dwordx4 v4, v[8:11], s[32:33] offset:1024 sc1\n.LL139:\ns_waitcnt lgkmcnt(4)\nv_mfma_f32_32x32x16_f16 v[204:219], v[84:87], v[224:227], v[204:219]\n"
    "ds_read_b128 v[76:79], v2 offset:992\ns_waitcnt lgkmcnt(4)\nv_mfma_f32_32x32x16_f16 v[204:219], v[88:91], v[68:71], v[204:219]\nds_read_b128 v[220:223], v2 offset:960\n"
    "s_waitcnt lgkmcnt(4)\nv_mfma_f32_32x32x16_f16 v[204:219], v[92:95], v[228:231], v[204:219]\nds_read_b128 v[80:83], v2 offset:912\ns_cbranch_scc0 .LL140\n"
    "global_store_dwordx4 v5, v[8:11], s[32:33] offset:0 sc1\n.LL140:\ns_waitcnt lgkmcnt(4)\nv_mfma_f32_32x32x16_f16 v[204:219], v[96:99], v[72:75], v[204:219]\n"
    "ds_read_b128 v[224:227], v2 offset:880\ns_waitcnt lgkmcnt(4)\nv_mfma_f32_32x32x16_f16 v[204:219], v[100:103], v[232:235], v[204:219]\nds_read_b128 v[84:87], v2 offset:832\n"
    "s_waitcnt lgkmcnt(4)\nv_mfma_f32_32x32x16_f16 v[204:219], v[104:107], v[76:79], v[204:219]\nds_read_b128 v[68:71], v2 offset:800\ns_cbranch_scc0 .LL141\n"
    "global_store_dwordx4 v5, v[8:11], s[32:33] offset:1024 sc1\n.LL141:\ns_waitcnt lgkmcnt(4)\nv_mfma_f32_32x32x16_f16 v[204:219], v[108:111], v[220:223], v[204:219]\n"
    "ds_read_b128 v[88:91], v2 offset:752\ns_waitcnt lgkmcnt(4)\nv_mfma_f32_32x32x16_f16 v[204:219], v[112:115], v[80:83], v[204:219]\nds_read_b128 v[228:231], v2 offset:720\n"
    "s_waitcnt lgkmcnt(4)\nv_mfma_f32_32x32x16_f16 v[204:219], v[116:119], v[224:227], v[204:219]\nds_read_b128 v[92:95], v2 offset:672\ns_cbranch_scc0 .LL142\n"
    "s_mov_b64 exec, 3\nglobal_store_dwordx4 v6, v[8:11], s[32:33] sc1\ns_mov_b64 exec, -1\n.LL142:\ns_waitcnt lgkmcnt(4)\n"
    "v_mfma_f32_32x32x16_f16 v[204:219], v[120:123], v[84:87], v[204:219]\nds_read_b128 v[72:75], v2 offset:640\ns_waitcnt lgkmcnt(4)\n"
    "v_mfma_f32_32x32x16_f16 v[204:219], v[124:127], v[68:71], v[204:219]\nds_read_b128 v[96:99], v2 offset:592\ns_waitcnt lgkmcnt(4)\n"
    "v_mfma_f32_32x32x16_f16 v[204:219], v[128:131], v[88:91], v[204:219]\nds_read_b128 v[232:235], v2 offset:560\ns_waitcnt lgkmcnt(4)\n"
    "v_mfma_f32_32x32x16_f16 v[204:219], v[132:135], v[228:231], v[204:219]\nds_read_b128 v[100:103], v2 offset:512\ns_waitcnt lgkmcnt(4)\n"
    "v_mfma_f32_32x32x16_f16 v[204:219], v[136:139], v[92:95], v[204:219]\nds_read_b128 v[76:79], v2 offset:480\ns_waitcnt lgkmcnt(4)\n"
    "v_mfma_f32_32x32x16_f16 v[204:219], v[140:143], v[72:75], v[204:219]\nds_read_b128 v[104:107], v2 offset:432\ns_waitcnt lgkmcnt(4)\n"
    "v_mfma_f32_32x32x16_f16 v[204:219], v[144:147], v[96:99], v[204:219]\nds_read_b128 v[220:223], v2 offset:400\ns_waitcnt lgkmcnt(4)\n"
    "v_mfma_f32_32x32x16_f16 v[204:219], v[148:151], v[232:235], v[204:219]\nds_read_b128 v[108:111], v2 offset:352\ns_waitcnt lgkmcnt(4)\n"
    "v_mfma_f32_32x32x16_f16 v[204:219], v[152:155], v[100:103], v[204:219]\nds_read_b128 v[80:83], v2 offset:320\nv_fmac_f32 v32, s96, v244\nv_fmac_f32 v33, s96, v245\n"
    "v_fmac_f32 v34, s96, v246\ns_waitcnt lgkmcnt(4)\nv_mfma_f32_32x32x16_f16 v[204:219], v[156:159], v[76:79], v[204:219]\nds_read_b128 v[112:115], v2 offset:272\n"
    "v_fmac_f32 v35, s96, v247\nv_fmac_f32 v36, s97, v248\nv_fmac_f32 v37, s97, v249\ns_waitcnt lgkmcnt(4)\n"
    "v_mfma_f32_32x32x16_f16 v[204:219], v[160:163], v[104:107], v[204:219]\nds_read_b128 v[224:227], v2 offset:240\nv_fmac_f32 v38, s97, v250\nv_fmac_f32 v39, s97, v251\n"
    "v_xor_b32 v238, 32, v3\ns_waitcnt lgkmcnt(4)\nv_mfma_f32_32x32x16_f16 v[204:219], v[164:167], v[220:223], v[204:219]\nds_read_b128 v[116:119], v2 offset:192\n"
    "v_xor_b32 v239, 64, v3\nv_xor_b32 v240, 0x60, v3\ns_waitcnt lgkmcnt(4)\nv_mfma_f32_32x32x16_f16 v[204:219], v[168:171], v[108:111], v[204:219]\n"
    "ds_read_b128 v[84:87], v2 offset:160\ns_waitcnt lgkmcnt(4)\nv_mfma_f32_32x32x16_f16 v[204:219], v[172:175], v[80:83], v[204:219]\nds_read_b128 v[120:123], v2 offset:112\n"
    "s_waitcnt lgkmcnt(4)\nv_mfma_f32_32x32x16_f16 v[204:219], v[176:179], v[112:115], v[204:219]\nds_read_b128 v[68:71], v2 offset:80\ns_waitcnt lgkmcnt(4)\n"
    "v_mfma_f32_32x32x16_f16 v[204:219], v[180:183], v[224:227], v[204:219]\nds_read_b128 v[124:127], v2 offset:32\ns_waitcnt lgkmcnt(4)\n"
    "v_mfma_f32_32x32x16_f16 v[204:219], v[184:187], v[116:119], v[204:219]\ns_waitcnt lgkmcnt(3)\nv_mfma_f32_32x32x16_f16 v[204:219], v[188:191], v[84:87], v[204:219]\n"
    "s_waitcnt lgkmcnt(2)\nv_mfma_f32_32x32x16_f16 v[204:219], v[192:195], v[120:123], v[204:219]\ns_waitcnt lgkmcnt(1)\n"
    "v_mfma_f32_32x32x16_f16 v[204:219], v[196:199], v[68:71], v[204:219]\ns_waitcnt lgkmcnt(0)\nv_mfma_f32_32x32x16_f16 v[204:219], v[200:203], v[124:127], v[204:219]\n"
    "s_nop 11\nds_write_b128 v3, v[204:207]\nds_write_b128 v238, v[208:211]\nds_write_b128 v239, v[212:215]\nds_write_b128 v240, v[216:219]\ns_waitcnt vmcnt(0)\n"
    "s_waitcnt lgkmcnt(0)\ns_barrier\nds_read_b128 v[48:51], v15\nds_read_b128 v[52:55], v15 offset:8192\ns_cmp_lt_u32 s3, 2\ns_cbranch_scc1 .LL143\nds_read_b128 v[56:59], v16\n"
    "ds_read_b128 v[60:63], v16 offset:2048\ns_waitcnt lgkmcnt(0)\nv_pk_fma_f32 v[32:33], v[56:57], s[88:89], v[32:33] op_sel_hi:[1,0,1]\n"
    "v_pk_fma_f32 v[34:35], v[58:59], s[88:89], v[34:35] op_sel_hi:[1,0,1]\nv_pk_fma_f32 v[36:37], v[60:61], s[90:91], v[36:37] op_sel_hi:[1,0,1]\n"
    "v_pk_fma_f32 v[38:39], v[62:63], s[90:91], v[38:39] op_sel_hi:[1,0,1]\n.LL143:\ns_mov_b32 vcc_lo, 0x55555555\ns_mov_b32 vcc_hi, 0x55555555\ns_waitcnt lgkmcnt(0)\n"
    "v_pk_fma_f32 v[32:33], v[48:49], s[88:89], v[32:33] op_sel_hi:[1,0,1]\nv_pk_fma_f32 v[34:35], v[50:51], s[88:89], v[34:35] op_sel_hi:[1,0,1]\n"
    "v_pk_fma_f32 v[36:37], v[52:53], s[90:91], v[36:37] op_sel_hi:[1,0,1]\nv_pk_fma_f32 v[38:39], v[54:55], s[90:91], v[38:39] op_sel_hi:[1,0,1]\ns_cmp_eq_u32 s6, 0\n"
    "s_cbranch_scc0 .LL144\nv_cvt_pk_bf16_f32 v52, v32, v33\nv_cvt_pk_bf16_f32 v53, v34, v35\nv_cvt_pk_bf16_f32 v54, v36, v37\nv_cvt_pk_bf16_f32 v55, v38, v39\ns_nop 0\n"
    "v_cndmask_b32_dpp v48, v54, v52, vcc quad_perm:[1,0,3,2] row_mask:0xf bank_mask:0xf\nv_cndmask_b32_dpp v49, v55, v53, vcc quad_perm:[1,0,3,2] row_mask:0xf bank_mask:0xf\n"
    "s_not_b64 vcc, vcc\nv_cndmask_b32_dpp v50, v52, v54, vcc quad_perm:[1,0,3,2] row_mask:0xf bank_mask:0xf\n"
    "v_cndmask_b32_dpp v51, v53, v55, vcc quad_perm:[1,0,3,2] row_mask:0xf bank_mask:0xf\nglobal_store_dwordx4 v17, v[48:51], s[32:33] sc1\ns_branch .LG276\n.LL144:\n"
    "v_cvt_pk_bf16_f32 v48, v32, v36\nv_cvt_pk_bf16_f32 v49, v33, v37\nv_cvt_pk_bf16_f32 v50, v34, v38\nv_cvt_pk_bf16_f32 v51, v35, v39\n"
    "global_store_dwordx4 v17, v[48:51], s[32:33] sc1\ns_lshl_b32 s15, s41, 13\ns_add_u32 s14, s15, 0x1200000\nv_lshl_add_u32 v2, v0, 4, s14\nv_lshrrev_b32 v7, 3, v0\n"
    "s_lshl_b32 s17, s36, 16\ns_lshl_b32 s18, s35, 7\ns_add_u32 s16, s17, s18\nv_lshl_add_u32 v3, v7, 10, s16\nv_and_b32 v8, 7, v0\nv_lshl_add_u32 v3, v8, 4, v3\n"
    "v_lshrrev_b32 v9, 6, v0\nv_lshlrev_b32 v10, 4, v0\nv_lshl_add_u32 v5, v9, 4, v10\ns_mov_b32 s19, 0x410\nv_lshlrev_b32 v11, 2, v7\nv_mad_u32_u24 v6, v8, s19, v11\n"
    "s_mov_b32 s80, 0x10000\ns_sleep 16\n.LL145:\nglobal_load_dwordx4 v[52:55], v2, s[32:33] sc1\ns_waitcnt vmcnt(0)\nds_write_b128 v5, v[52:55]\nv_max3_u32 v4, v52, v53, v54\n"
    "v_max_u32 v4, v4, v55\nv_cmp_eq_u32 vcc, -1, v4\ns_cbranch_vccz .LL146\ns_waitcnt lgkmcnt(0)\ns_sub_u32 s80, s80, 1\ns_cbranch_scc0 .LL145\n.LL146:\ns_waitcnt lgkmcnt(0)\n"
    "s_barrier\nds_read2st64_b32 v[56:57], v6 offset1:1\nds_read2st64_b32 v[58:59], v6 offset0:2 offset1:3\ns_waitcnt lgkmcnt(0)\n"
    "global_store_dwordx4 v3, v[56:59], s[30:31] sc1\ns_endpgm\n.LG276:\ns_mov_b32 s6, 1\ns_mov_b32 s7, 8388608\nv_bfe_u32 v65, v0, 4, 1\nv_bfe_u32 v66, v0, 5, 1\n"
    "v_xor_b32 v64, v65, v66\nv_bfe_u32 v69, v0, 2, 2\nv_lshl_add_u32 v68, v66, 3, v69\nv_lshlrev_b32 v67, 6, v68\nv_lshl_add_u32 v26, v64, 5, v67\nv_and_b32 v70, 1, v0\n"
    "v_lshl_add_u32 v26, v70, 4, v26\nv_and_b32 v71, 2, v0\nv_lshl_add_u32 v26, v71, 2, v26\ns_mul_i32 s15, s3, 0x1a00\ns_add_u32 s14, s15, 0x18d00\n"
    "v_lshl_add_u32 v27, v66, 4, s14\nv_and_b32 v72, 31, v0\ns_mov_b32 s16, 0xd0\nv_mad_u32_u24 v27, v72, s16, v27\nv_and_b32 v73, 19, v0\ns_mov_b32 s17, 0x27c00\n"
    "v_lshl_add_u32 v28, v73, 2, s17\nv_and_b32 v74, 4, v0\nv_lshl_add_u32 v28, v74, 3, v28\nv_and_b32 v75, 8, v0\nv_lshl_add_u32 v28, v75, 1, v28\ns_mul_i32 s19, s3, 0x3000\n"
    "s_add_u32 s18, s19, 0x18000\nv_add_u32 v29, s18, v67\nv_lshl_add_u32 v29, v65, 5, v29\nv_and_b32 v76, 3, v0\nv_lshl_add_u32 v29, v76, 3, v29\nv_and_b32 v77, 63, v0\n"
    "s_lshl_b32 s23, s35, 15\ns_and_b32 s25, s36, 3\ns_lshl_b32 s24, s25, 13\ns_add_u32 s22, s23, s24\ns_lshl_b32 s44, s3, 11\ns_add_u32 s21, s22, s44\n"
    "s_add_u32 s20, s21, 0xd00000\nv_lshl_add_u32 v34, v77, 2, s20\nv_lshlrev_b32 v35, 4, v77\ns_mov_b32 s45, 0x21000\nv_lshl_add_u32 v36, v0, 3, s45\ns_and_b32 s54, s42, 7\n"
    "s_lshl_b32 s44, s54, 12\ns_add_u32 s56, s54, 1\ns_and_b32 s55, s56, 7\ns_lshl_b32 s45, s55, 12\ns_add_u32 s58, s54, 2\ns_and_b32 s57, s58, 7\ns_lshl_b32 s46, s57, 12\n"
    "s_add_u32 s60, s54, 3\ns_and_b32 s59, s60, 7\ns_lshl_b32 s47, s59, 12\ns_add_u32 s62, s54, 4\ns_and_b32 s61, s62, 7\ns_lshl_b32 s48, s61, 12\ns_add_u32 s64, s54, 5\n"
    "s_and_b32 s63, s64, 7\ns_lshl_b32 s49, s63, 12\ns_add_u32 s66, s54, 6\ns_and_b32 s65, s66, 7\ns_lshl_b32 s50, s65, 12\ns_add_u32 s68, s54, 7\ns_and_b32 s67, s68, 7\n"
    "s_lshl_b32 s51, s67, 12\ns_mov_b32 s81, 0x3b000000\ns_mov_b32 s84, -1\ns_mov_b32 s85, 0\nv_mov_b32 v143, 0x3f803f80\nv_add_u32 v116, s44, v26\n"
    "v_add_u32 v118, 0x10000, v116\nv_mov_b32 v112, 0\nv_mov_b32 v113, 0\nv_mov_b32 v114, 0\nv_mov_b32 v115, 0\nv_mov_b32 v121, 0\nv_mov_b32 v122, 0\nv_mov_b32 v123, 0\n"
    "v_mov_b32 v125, 0\nv_mov_b32 v126, 0\nv_mov_b32 v127, 0\nv_mov_b32 v129, 0\nv_mov_b32 v130, 0\nv_mov_b32 v131, 0\nv_mov_b32 v133, 0\nv_mov_b32 v134, 0\nv_mov_b32 v135, 0\n"
    "ds_read_b32 v124, v28 offset:0\nds_read_b32 v128, v28 offset:128\nds_read_b32 v132, v28 offset:256\ns_waitcnt lgkmcnt(0)\nv_cvt_pk_bf16_f32 v140, 0, v124\n"
    "v_cvt_pk_bf16_f32 v141, 0, v128\nv_cvt_pk_bf16_f32 v142, 0, v132\nv_sub_f32 v124, v124, v140\nv_sub_f32 v128, v128, v141\nv_sub_f32 v132, v132, v142\n"
    "v_cvt_pk_bf16_f32 v124, v140, v124\nv_cvt_pk_bf16_f32 v128, v141, v128\nv_cvt_pk_bf16_f32 v132, v142, v132\ns_lshl_b32 s86, s42, 8\nv_add_u32 v34, s86, v34\n"
    "s_and_b32 s86, s42, 0\ns_lshl2_add_u32 s15, s37, s3\ns_lshl_b32 s14, s15, 15\ns_add_u32 s82, s14, 0x600000\ns_mul_i32 s16, s3, 0x3000\ns_add_u32 s87, s16, 0x18000\n"
    "s_add_u32 s82, s82, s32\ns_addc_u32 s83, s33, 0\nds_read_b64_tr_b16 v[192:193], v116 offset:0\nds_read_b64_tr_b16 v[194:195], v116 offset:256\n"
    "ds_read_b64_tr_b16 v[196:197], v116 offset:1024\nds_read_b64_tr_b16 v[198:199], v116 offset:1280\nds_read_b64_tr_b16 v[200:201], v116 offset:2048\n"
    "ds_read_b64_tr_b16 v[202:203], v116 offset:2304\nds_read_b64_tr_b16 v[204:205], v116 offset:3072\nds_read_b64_tr_b16 v[206:207], v116 offset:3328\n"
    "ds_read_b64_tr_b16 v[208:209], v116 offset:32768\nds_read_b64_tr_b16 v[210:211], v116 offset:33024\nds_read_b64_tr_b16 v[212:213], v116 offset:33792\n"
    "ds_read_b64_tr_b16 v[214:215], v116 offset:34048\ns_barrier\nv_mov_b32 v64, 0\nv_mov_b32 v65, 0xf149f2ca\nv_bfe_u32 v68, v0, 4, 2\nv_lshlrev_b32 v67, 2, v68\n"
    "s_add_u32 s15, s37, s3\ns_cmp_eq_u32 s15, 0\ns_cselect_b32 s14, 8, 0\nv_and_b32 v71, 7, v0\nv_add_u32 v70, 1, v71\nv_max_u32 v69, s14, v70\nv_sub_u32 v66, v67, v69\n"
    "v_add_u32 v73, 8, v71\nv_sub_u32 v72, v73, v69\nv_cmp_le_u32 vcc, v66, v72\ns_nop 1\nv_cndmask_b32 v2, v65, v64, vcc\nv_add_u32 v75, 1, v67\nv_sub_u32 v74, v75, v69\n"
    "v_cmp_le_u32 vcc, v74, v72\ns_nop 1\nv_cndmask_b32 v3, v65, v64, vcc\nv_add_u32 v77, 2, v67\nv_sub_u32 v76, v77, v69\nv_cmp_le_u32 vcc, v76, v72\ns_nop 1\n"
    "v_cndmask_b32 v4, v65, v64, vcc\nv_add_u32 v79, 3, v67\nv_sub_u32 v78, v79, v69\nv_cmp_le_u32 vcc, v78, v72\ns_nop 1\nv_cndmask_b32 v5, v65, v64, vcc\n"
    "s_mul_i32 s17, s3, 0xd00\ns_add_u32 s16, s17, 0x18040\nv_lshl_add_u32 v6, v68, 4, s16\nv_and_b32 v81, 15, v0\nv_max_u32 v80, 1, v81\ns_mov_b32 s18, 0x1a0\n"
    "v_mad_u32_u24 v6, v80, s18, v6\ns_add_u32 s19, s17, 0x18d00\nv_lshl_add_u32 v7, v68, 4, s19\nv_mad_u32_u24 v7, v71, s18, v7\nv_and_b32 v82, 3, v0\n"
    "s_add_u32 s20, s17, 0x18080\nv_lshl_add_u32 v8, v82, 3, s20\nv_bfe_u32 v85, v0, 2, 2\nv_add_u32 v84, v67, v85\nv_max_u32 v83, 1, v84\nv_mad_u32_u24 v8, v83, s18, v8\n"
    "v_bfe_u32 v86, v0, 3, 1\ns_lshr_b32 s54, s3, 1\ns_lshl2_add_u32 s25, s37, s54\ns_lshl_b32 s24, s25, 15\ns_lshl_b32 s55, s38, 11\ns_add_u32 s23, s24, s55\n"
    "s_and_b32 s57, s3, 1\ns_lshl_b32 s56, s57, 8\ns_add_u32 s22, s23, s56\ns_add_u32 s21, s22, 0x800000\nv_lshl_add_u32 v9, v86, 10, s21\nv_bfe_u32 v87, v0, 5, 1\n"
    "v_lshl_add_u32 v9, v87, 9, v9\nv_bfe_u32 v88, v0, 4, 1\nv_lshl_add_u32 v9, v88, 4, v9\nv_lshl_add_u32 v9, v71, 5, v9\nv_mov_b64 v[10:11], 0\nv_mov_b64 v[12:13], 0\n"
    "v_mov_b64 v[14:15], 0\nv_mov_b64 v[16:17], 0\nv_mov_b64 v[18:19], 0\nv_mov_b64 v[20:21], 0\nv_mov_b64 v[22:23], 0\nv_mov_b64 v[24:25], 0\ns_mov_b32 s80, 0x10000\n"
    "s_barrier\nds_read_b64_tr_b16 v[144:145], v29 offset:0\nds_read_b64_tr_b16 v[146:147], v29 offset:256\nds_read_b64_tr_b16 v[148:149], v29 offset:1024\n"
    "ds_read_b64_tr_b16 v[150:151], v29 offset:1280\nds_read_b64_tr_b16 v[152:153], v29 offset:2048\nds_read_b64_tr_b16 v[154:155], v29 offset:2304\n"
    "ds_read_b64_tr_b16 v[156:157], v29 offset:3072\nds_read_b64_tr_b16 v[158:159], v29 offset:3328\n.LL147:\ns_waitcnt lgkmcnt(6)\nv_dot2_f32_bf16 v112, v143, v144, 0\n"
    "v_dot2_f32_bf16 v113, v144, v144, 0\nv_dot2_f32_bf16 v114, v143, v145, 0\nv_dot2_f32_bf16 v115, v145, v145, 0\nv_dot2c_f32_bf16 v112, v143, v146\n"
    "v_dot2c_f32_bf16 v113, v146, v146\nv_dot2c_f32_bf16 v114, v143, v147\nv_dot2c_f32_bf16 v115, v147, v147\ns_waitcnt lgkmcnt(4)\nv_dot2c_f32_bf16 v112, v143, v148\n"
    "v_dot2c_f32_bf16 v113, v148, v148\nv_dot2c_f32_bf16 v114, v143, v149\nv_dot2c_f32_bf16 v115, v149, v149\nv_dot2c_f32_bf16 v112, v143, v150\n"
    "v_dot2c_f32_bf16 v113, v150, v150\nv_dot2c_f32_bf16 v114, v143, v151\nv_dot2c_f32_bf16 v115, v151, v151\ns_waitcnt lgkmcnt(2)\nv_dot2c_f32_bf16 v112, v143, v152\n"
    "v_dot2c_f32_bf16 v113, v152, v152\nv_dot2c_f32_bf16 v114, v143, v153\nv_dot2c_f32_bf16 v115, v153, v153\nv_dot2c_f32_bf16 v112, v143, v154\n"
    "v_dot2c_f32_bf16 v113, v154, v154\nv_dot2c_f32_bf16 v114, v143, v155\nv_dot2c_f32_bf16 v115, v155, v155\ns_waitcnt lgkmcnt(0)\nv_dot2c_f32_bf16 v112, v143, v156\n"
    "v_dot2c_f32_bf16 v113, v156, v156\nv_dot2c_f32_bf16 v114, v143, v157\nv_dot2c_f32_bf16 v115, v157, v157\nv_dot2c_f32_bf16 v112, v143, v158\n"
    "v_dot2c_f32_bf16 v113, v158, v158\nv_dot2c_f32_bf16 v114, v143, v159\nv_dot2c_f32_bf16 v115, v159, v159\ns_nop 1\nv_add_f32 v112, v112, v114\nv_add_f32 v113, v113, v115\n"
    "v_cmp_u_f32 vcc, v113, v113\ns_cbranch_vccnz .Lxs148\n.Lxr149:\nv_mfma_f32_32x32x16_bf16 v[64:79], v[192:195], v[144:147], 0\n"
    "ds_read_b64_tr_b16 v[192:193], v116 offset:34816\nds_read_b64_tr_b16 v[194:195], v116 offset:35072\nv_add_u32 v117, s45, v26\nv_add_u32 v119, 0x10000, v117\n"
    "v_mfma_f32_32x32x16_bf16 v[64:79], v[196:199], v[148:151], v[64:79]\nds_read_b64_tr_b16 v[196:197], v116 offset:35840\nds_read_b64_tr_b16 v[198:199], v116 offset:36096\n"
    "v_permlane32_swap_b32 v112, v113\nv_add_f32 v112, v112, v113\nv_mfma_f32_32x32x16_bf16 v[64:79], v[200:203], v[152:155], v[64:79]\n"
    "ds_read_b64_tr_b16 v[200:201], v118 offset:0\nds_read_b64_tr_b16 v[202:203], v118 offset:256\nglobal_store_dword v34, v112, s[32:33] sc1\ns_barrier\n"
    "v_mfma_f32_32x32x16_bf16 v[64:79], v[204:207], v[156:159], v[64:79]\nds_read_b64_tr_b16 v[204:205], v118 offset:1024\nds_read_b64_tr_b16 v[206:207], v118 offset:1280\n"
    "ds_read_b64_tr_b16 v[160:161], v29 offset:4096\nds_read_b64_tr_b16 v[162:163], v29 offset:4352\nv_mfma_f32_32x32x16_bf16 v[80:95], v[208:211], v[144:147], 0\n"
    "ds_read_b64_tr_b16 v[208:209], v118 offset:2048\nds_read_b64_tr_b16 v[210:211], v118 offset:2304\nds_read_b64_tr_b16 v[164:165], v29 offset:5120\n"
    "ds_read_b64_tr_b16 v[166:167], v29 offset:5376\nv_mfma_f32_32x32x16_bf16 v[80:95], v[212:215], v[148:151], v[80:95]\nds_read_b64_tr_b16 v[212:213], v118 offset:3072\n"
    "ds_read_b64_tr_b16 v[214:215], v118 offset:3328\nds_read_b64_tr_b16 v[168:169], v29 offset:6144\nds_read_b64_tr_b16 v[170:171], v29 offset:6400\ns_waitcnt lgkmcnt(14)\n"
    "v_mfma_f32_32x32x16_bf16 v[80:95], v[192:195], v[152:155], v[80:95]\nds_read_b64_tr_b16 v[192:193], v117 offset:0\nds_read_b64_tr_b16 v[194:195], v117 offset:256\n"
    "ds_read_b64_tr_b16 v[172:173], v29 offset:7168\nds_read_b64_tr_b16 v[174:175], v29 offset:7424\nv_mfma_f32_32x32x16_bf16 v[80:95], v[196:199], v[156:159], v[80:95]\n"
    "ds_read_b64_tr_b16 v[196:197], v117 offset:1024\nds_read_b64_tr_b16 v[198:199], v117 offset:1280\ns_waitcnt lgkmcnt(10)\nv_pk_maximum3_f16 v138, v160, v161, v162\n"
    "v_pk_maximum3_f16 v139, v163, v164, v165\nv_mfma_f32_32x32x16_bf16 v[96:111], v[200:203], v[144:147], 0\nds_read_b64_tr_b16 v[200:201], v117 offset:2048\n"
    "ds_read_b64_tr_b16 v[202:203], v117 offset:2304\nv_pk_maximum3_f16 v138, v138, v166, v167\ns_waitcnt lgkmcnt(4)\nv_pk_maximum3_f16 v139, v139, v168, v169\n"
    "v_mfma_f32_32x32x16_bf16 v[96:111], v[204:207], v[148:151], v[96:111]\nds_read_b64_tr_b16 v[204:205], v117 offset:3072\nds_read_b64_tr_b16 v[206:207], v117 offset:3328\n"
    "v_pk_maximum3_f16 v138, v138, v170, v171\nv_pk_maximum3_f16 v139, v139, v172, v173\nv_mfma_f32_32x32x16_bf16 v[96:111], v[208:211], v[152:155], v[96:111]\n"
    "ds_read_b64_tr_b16 v[208:209], v117 offset:32768\nds_read_b64_tr_b16 v[210:211], v117 offset:33024\nv_pk_maximum3_f16 v138, v138, v174, v175\n"
    "v_pk_maximum3_f16 v138, v138, v139, v139\nv_mfma_f32_32x32x16_bf16 v[96:111], v[212:215], v[156:159], v[96:111]\nds_read_b64_tr_b16 v[212:213], v117 offset:33792\n"
    "ds_read_b64_tr_b16 v[214:215], v117 offset:34048\nv_cmp_u_f16_sdwa vcc, v138, v138 src0_sel:WORD_0 src1_sel:WORD_1\ns_cbranch_vccnz .Lxs151\n.Lxr152:\n"
    "s_waitcnt lgkmcnt(8)\nv_mfma_f32_32x32x16_bf16 v[64:79], v[192:195], v[160:163], v[64:79]\nds_read_b64_tr_b16 v[192:193], v117 offset:34816\n"
    "ds_read_b64_tr_b16 v[194:195], v117 offset:35072\nv_add_u32 v116, s46, v26\nv_add_u32 v118, 0x10000, v116\n"
    "v_mfma_f32_32x32x16_bf16 v[64:79], v[196:199], v[164:167], v[64:79]\nds_read_b64_tr_b16 v[196:197], v117 offset:35840\nds_read_b64_tr_b16 v[198:199], v117 offset:36096\n"
    "s_waitcnt lgkmcnt(8)\nv_mfma_f32_32x32x16_bf16 v[64:79], v[200:203], v[168:171], v[64:79]\nds_read_b64_tr_b16 v[200:201], v119 offset:0\n"
    "ds_read_b64_tr_b16 v[202:203], v119 offset:256\ns_barrier\nv_mfma_f32_32x32x16_bf16 v[64:79], v[204:207], v[172:175], v[64:79]\n"
    "ds_read_b64_tr_b16 v[204:205], v119 offset:1024\nds_read_b64_tr_b16 v[206:207], v119 offset:1280\nds_read_b64_tr_b16 v[144:145], v29 offset:8192\n"
    "ds_read_b64_tr_b16 v[146:147], v29 offset:8448\ns_waitcnt lgkmcnt(10)\nv_mfma_f32_32x32x16_bf16 v[80:95], v[208:211], v[160:163], v[80:95]\n"
    "ds_read_b64_tr_b16 v[208:209], v119 offset:2048\nds_read_b64_tr_b16 v[210:211], v119 offset:2304\nds_read_b64_tr_b16 v[148:149], v29 offset:9216\n"
    "ds_read_b64_tr_b16 v[150:151], v29 offset:9472\nv_mfma_f32_32x32x16_bf16 v[80:95], v[212:215], v[164:167], v[80:95]\nds_read_b64_tr_b16 v[212:213], v119 offset:3072\n"
    "ds_read_b64_tr_b16 v[214:215], v119 offset:3328\nds_read_b64_tr_b16 v[152:153], v29 offset:10240\nds_read_b64_tr_b16 v[154:155], v29 offset:10496\ns_waitcnt lgkmcnt(14)\n"
    "v_mfma_f32_32x32x16_bf16 v[80:95], v[192:195], v[168:171], v[80:95]\nds_read_b64_tr_b16 v[192:193], v116 offset:0\nds_read_b64_tr_b16 v[194:195], v116 offset:256\n"
    "ds_read_b64_tr_b16 v[156:157], v29 offset:11264\nds_read_b64_tr_b16 v[158:159], v29 offset:11520\nv_mfma_f32_32x32x16_bf16 v[80:95], v[196:199], v[172:175], v[80:95]\n"
    "ds_read_b64_tr_b16 v[196:197], v116 offset:1024\nds_read_b64_tr_b16 v[198:199], v116 offset:1280\ns_waitcnt lgkmcnt(10)\nv_pk_maximum3_f16 v138, v144, v145, v146\n"
    "v_pk_maximum3_f16 v139, v147, v148, v149\nv_mfma_f32_32x32x16_bf16 v[96:111], v[200:203], v[160:163], v[96:111]\nds_read_b64_tr_b16 v[200:201], v116 offset:2048\n"
    "ds_read_b64_tr_b16 v[202:203], v116 offset:2304\nv_pk_maximum3_f16 v138, v138, v150, v151\ns_waitcnt lgkmcnt(4)\nv_pk_maximum3_f16 v139, v139, v152, v153\n"
    "v_mfma_f32_32x32x16_bf16 v[96:111], v[204:207], v[164:167], v[96:111]\nds_read_b64_tr_b16 v[204:205], v116 offset:3072\nds_read_b64_tr_b16 v[206:207], v116 offset:3328\n"
    "v_pk_maximum3_f16 v138, v138, v154, v155\nv_pk_maximum3_f16 v139, v139, v156, v157\nv_mfma_f32_32x32x16_bf16 v[96:111], v[208:211], v[168:171], v[96:111]\n"
    "ds_read_b64_tr_b16 v[208:209], v116 offset:32768\nds_read_b64_tr_b16 v[210:211], v116 offset:33024\nv_pk_maximum3_f16 v138, v138, v158, v159\n"
    "v_pk_maximum3_f16 v138, v138, v139, v139\nv_mfma_f32_32x32x16_bf16 v[96:111], v[212:215], v[172:175], v[96:111]\nds_read_b64_tr_b16 v[212:213], v116 offset:33792\n"
    "ds_read_b64_tr_b16 v[214:215], v116 offset:34048\nv_cmp_u_f16_sdwa vcc, v138, v138 src0_sel:WORD_0 src1_sel:WORD_1\ns_cbranch_vccnz .Lxs154\n.Lxr155:\n"
    "s_waitcnt lgkmcnt(8)\nv_mfma_f32_32x32x16_bf16 v[64:79], v[192:195], v[144:147], v[64:79]\nds_read_b64_tr_b16 v[192:193], v116 offset:34816\n"
    "ds_read_b64_tr_b16 v[194:195], v116 offset:35072\nv_add_u32 v117, s47, v26\nv_add_u32 v119, 0x10000, v117\n"
    "v_mfma_f32_32x32x16_bf16 v[64:79], v[196:199], v[148:151], v[64:79]\nds_read_b64_tr_b16 v[196:197], v116 offset:35840\nds_read_b64_tr_b16 v[198:199], v116 offset:36096\n"
    "s_waitcnt lgkmcnt(8)\nv_mfma_f32_32x32x16_bf16 v[64:79], v[200:203], v[152:155], v[64:79]\nds_read_b64_tr_b16 v[200:201], v118 offset:0\n"
    "ds_read_b64_tr_b16 v[202:203], v118 offset:256\ns_barrier\nv_mfma_f32_32x32x16_bf16 v[64:79], v[204:207], v[156:159], v[64:79]\n"
    "ds_read_b64_tr_b16 v[204:205], v118 offset:1024\nds_read_b64_tr_b16 v[206:207], v118 offset:1280\nds_read_b64_tr_b16 v[160:161], v29 offset:0\n"
    "ds_read_b64_tr_b16 v[162:163], v29 offset:256\ns_waitcnt lgkmcnt(10)\nv_mfma_f32_32x32x16_bf16 v[80:95], v[208:211], v[144:147], v[80:95]\n"
    "ds_read_b64_tr_b16 v[208:209], v118 offset:2048\nds_read_b64_tr_b16 v[210:211], v118 offset:2304\nds_read_b64_tr_b16 v[164:165], v29 offset:1024\n"
    "ds_read_b64_tr_b16 v[166:167], v29 offset:1280\nv_mfma_f32_32x32x16_bf16 v[80:95], v[212:215], v[148:151], v[80:95]\nds_read_b64_tr_b16 v[212:213], v118 offset:3072\n"
    "ds_read_b64_tr_b16 v[214:215], v118 offset:3328\nds_read_b64_tr_b16 v[168:169], v29 offset:2048\nds_read_b64_tr_b16 v[170:171], v29 offset:2304\ns_waitcnt lgkmcnt(14)\n"
    "v_mfma_f32_32x32x16_bf16 v[80:95], v[192:195], v[152:155], v[80:95]\nds_read_b64_tr_b16 v[192:193], v117 offset:0\nds_read_b64_tr_b16 v[194:195], v117 offset:256\n"
    "ds_read_b64_tr_b16 v[172:173], v29 offset:3072\nds_read_b64_tr_b16 v[174:175], v29 offset:3328\nv_mfma_f32_32x32x16_bf16 v[80:95], v[196:199], v[156:159], v[80:95]\n"
    "ds_read_b64_tr_b16 v[196:197], v117 offset:1024\nds_read_b64_tr_b16 v[198:199], v117 offset:1280\ns_waitcnt lgkmcnt(10)\nv_pk_maximum3_f16 v138, v160, v161, v162\n"
    "v_pk_maximum3_f16 v139, v163, v164, v165\nv_mfma_f32_32x32x16_bf16 v[96:111], v[200:203], v[144:147], v[96:111]\nds_read_b64_tr_b16 v[200:201], v117 offset:2048\n"
    "ds_read_b64_tr_b16 v[202:203], v117 offset:2304\nv_pk_maximum3_f16 v138, v138, v166, v167\ns_waitcnt lgkmcnt(4)\nv_pk_maximum3_f16 v139, v139, v168, v169\n"
    "v_mfma_f32_32x32x16_bf16 v[96:111], v[204:207], v[148:151], v[96:111]\nds_read_b64_tr_b16 v[204:205], v117 offset:3072\nds_read_b64_tr_b16 v[206:207], v117 offset:3328\n"
    "v_pk_maximum3_f16 v138, v138, v170, v171\nv_pk_maximum3_f16 v139, v139, v172, v173\nv_mfma_f32_32x32x16_bf16 v[96:111], v[208:211], v[152:155], v[96:111]\n"
    "ds_read_b64_tr_b16 v[208:209], v117 offset:32768\nds_read_b64_tr_b16 v[210:211], v117 offset:33024\nv_pk_maximum3_f16 v138, v138, v174, v175\n"
    "v_pk_maximum3_f16 v138, v138, v139, v139\nv_mfma_f32_32x32x16_bf16 v[96:111], v[212:215], v[156:159], v[96:111]\nds_read_b64_tr_b16 v[212:213], v117 offset:33792\n"
    "ds_read_b64_tr_b16 v[214:215], v117 offset:34048\nv_cmp_u_f16_sdwa vcc, v138, v138 src0_sel:WORD_0 src1_sel:WORD_1\ns_cbranch_vccnz .Lxs157\n.Lxr158:\n"
    "s_waitcnt lgkmcnt(8)\nv_mfma_f32_32x32x16_bf16 v[64:79], v[192:195], v[160:163], v[64:79]\nds_read_b64_tr_b16 v[192:193], v117 offset:34816\n"
    "ds_read_b64_tr_b16 v[194:195], v117 offset:35072\nv_add_u32 v116, s48, v26\nv_add_u32 v118, 0x10000, v116\n"
    "v_mfma_f32_32x32x16_bf16 v[64:79], v[196:199], v[164:167], v[64:79]\nds_read_b64_tr_b16 v[196:197], v117 offset:35840\nds_read_b64_tr_b16 v[198:199], v117 offset:36096\n"
    "s_waitcnt lgkmcnt(8)\nv_mfma_f32_32x32x16_bf16 v[64:79], v[200:203], v[168:171], v[64:79]\nds_read_b64_tr_b16 v[200:201], v119 offset:0\n"
    "ds_read_b64_tr_b16 v[202:203], v119 offset:256\ns_barrier\nv_mfma_f32_32x32x16_bf16 v[64:79], v[204:207], v[172:175], v[64:79]\n"
    "ds_read_b64_tr_b16 v[204:205], v119 offset:1024\nds_read_b64_tr_b16 v[206:207], v119 offset:1280\nds_read_b64_tr_b16 v[144:145], v29 offset:4096\n"
    "ds_read_b64_tr_b16 v[146:147], v29 offset:4352\ns_waitcnt lgkmcnt(10)\nv_mfma_f32_32x32x16_bf16 v[80:95], v[208:211], v[160:163], v[80:95]\n"
    "ds_read_b64_tr_b16 v[208:209], v119 offset:2048\nds_read_b64_tr_b16 v[210:211], v119 offset:2304\nds_read_b64_tr_b16 v[148:149], v29 offset:5120\n"
    "ds_read_b64_tr_b16 v[150:151], v29 offset:5376\nv_mfma_f32_32x32x16_bf16 v[80:95], v[212:215], v[164:167], v[80:95]\nds_read_b64_tr_b16 v[212:213], v119 offset:3072\n"
    "ds_read_b64_tr_b16 v[214:215], v119 offset:3328\nds_read_b64_tr_b16 v[152:153], v29 offset:6144\nds_read_b64_tr_b16 v[154:155], v29 offset:6400\ns_waitcnt lgkmcnt(14)\n"
    "v_mfma_f32_32x32x16_bf16 v[80:95], v[192:195], v[168:171], v[80:95]\nds_read_b64_tr_b16 v[192:193], v116 offset:0\nds_read_b64_tr_b16 v[194:195], v116 offset:256\n"
    "ds_read_b64_tr_b16 v[156:157], v29 offset:7168\nds_read_b64_tr_b16 v[158:159], v29 offset:7424\nv_mfma_f32_32x32x16_bf16 v[80:95], v[196:199], v[172:175], v[80:95]\n"
    "ds_read_b64_tr_b16 v[196:197], v116 offset:1024\nds_read_b64_tr_b16 v[198:199], v116 offset:1280\ns_waitcnt lgkmcnt(10)\nv_pk_maximum3_f16 v138, v144, v145, v146\n"
    "v_pk_maximum3_f16 v139, v147, v148, v149\nv_mfma_f32_32x32x16_bf16 v[96:111], v[200:203], v[160:163], v[96:111]\nds_read_b64_tr_b16 v[200:201], v116 offset:2048\n"
    "ds_read_b64_tr_b16 v[202:203], v116 offset:2304\nv_pk_maximum3_f16 v138, v138, v150, v151\ns_waitcnt lgkmcnt(4)\nv_pk_maximum3_f16 v139, v139, v152, v153\n"
    "v_mfma_f32_32x32x16_bf16 v[96:111], v[204:207], v[164:167], v[96:111]\nds_read_b64_tr_b16 v[204:205], v116 offset:3072\nds_read_b64_tr_b16 v[206:207], v116 offset:3328\n"
    "v_pk_maximum3_f16 v138, v138, v154, v155\nv_pk_maximum3_f16 v139, v139, v156, v157\nv_mfma_f32_32x32x16_bf16 v[96:111], v[208:211], v[168:171], v[96:111]\n"
    "ds_read_b64_tr_b16 v[208:209], v116 offset:32768\nds_read_b64_tr_b16 v[210:211], v116 offset:33024\nv_pk_maximum3_f16 v138, v138, v158, v159\n"
    "v_pk_maximum3_f16 v138, v138, v139, v139\nv_mfma_f32_32x32x16_bf16 v[96:111], v[212:215], v[172:175], v[96:111]\nds_read_b64_tr_b16 v[212:213], v116 offset:33792\n"
    "ds_read_b64_tr_b16 v[214:215], v116 offset:34048\nv_cmp_u_f16_sdwa vcc, v138, v138 src0_sel:WORD_0 src1_sel:WORD_1\ns_cbranch_vccnz .Lxs160\n.Lxr161:\n"
    "s_waitcnt lgkmcnt(8)\nv_mfma_f32_32x32x16_bf16 v[64:79], v[192:195], v[144:147], v[64:79]\nds_read_b64_tr_b16 v[192:193], v116 offset:34816\n"
    "ds_read_b64_tr_b16 v[194:195], v116 offset:35072\nv_add_u32 v117, s49, v26\nv_add_u32 v119, 0x10000, v117\n"
    "v_mfma_f32_32x32x16_bf16 v[64:79], v[196:199], v[148:151], v[64:79]\nds_read_b64_tr_b16 v[196:197], v116 offset:35840\nds_read_b64_tr_b16 v[198:199], v116 offset:36096\n"
    "s_waitcnt lgkmcnt(8)\nv_mfma_f32_32x32x16_bf16 v[64:79], v[200:203], v[152:155], v[64:79]\nds_read_b64_tr_b16 v[200:201], v118 offset:0\n"
    "ds_read_b64_tr_b16 v[202:203], v118 offset:256\ns_barrier\nv_mfma_f32_32x32x16_bf16 v[64:79], v[204:207], v[156:159], v[64:79]\n"
    "ds_read_b64_tr_b16 v[204:205], v118 offset:1024\nds_read_b64_tr_b16 v[206:207], v118 offset:1280\nds_read_b64_tr_b16 v[160:161], v29 offset:8192\n"
    "ds_read_b64_tr_b16 v[162:163], v29 offset:8448\ns_waitcnt lgkmcnt(10)\nv_mfma_f32_32x32x16_bf16 v[80:95], v[208:211], v[144:147], v[80:95]\n"
    "ds_read_b64_tr_b16 v[208:209], v118 offset:2048\nds_read_b64_tr_b16 v[210:211], v118 offset:2304\nds_read_b64_tr_b16 v[164:165], v29 offset:9216\n"
    "ds_read_b64_tr_b16 v[166:167], v29 offset:9472\nv_mfma_f32_32x32x16_bf16 v[80:95], v[212:215], v[148:151], v[80:95]\nds_read_b64_tr_b16 v[212:213], v118 offset:3072\n"
    "ds_read_b64_tr_b16 v[214:215], v118 offset:3328\nds_read_b64_tr_b16 v[168:169], v29 offset:10240\nds_read_b64_tr_b16 v[170:171], v29 offset:10496\ns_waitcnt lgkmcnt(14)\n"
    "v_mfma_f32_32x32x16_bf16 v[80:95], v[192:195], v[152:155], v[80:95]\nds_read_b64_tr_b16 v[192:193], v117 offset:0\nds_read_b64_tr_b16 v[194:195], v117 offset:256\n"
    "ds_read_b64_tr_b16 v[172:173], v29 offset:11264\nds_read_b64_tr_b16 v[174:175], v29 offset:11520\nv_mfma_f32_32x32x16_bf16 v[80:95], v[196:199], v[156:159], v[80:95]\n"
    "ds_read_b64_tr_b16 v[196:197], v117 offset:1024\nds_read_b64_tr_b16 v[198:199], v117 offset:1280\ns_waitcnt lgkmcnt(10)\nv_pk_maximum3_f16 v138, v160, v161, v162\n"
    "v_pk_maximum3_f16 v139, v163, v164, v165\nv_mfma_f32_32x32x16_bf16 v[96:111], v[200:203], v[144:147], v[96:111]\nds_read_b64_tr_b16 v[200:201], v117 offset:2048\n"
    "ds_read_b64_tr_b16 v[202:203], v117 offset:2304\nv_pk_maximum3_f16 v138, v138, v166, v167\ns_waitcnt lgkmcnt(4)\nv_pk_maximum3_f16 v139, v139, v168, v169\n"
    "v_mfma_f32_32x32x16_bf16 v[96:111], v[204:207], v[148:151], v[96:111]\nds_read_b64_tr_b16 v[204:205], v117 offset:3072\nds_read_b64_tr_b16 v[206:207], v117 offset:3328\n"
    "v_pk_maximum3_f16 v138, v138, v170, v171\nv_pk_maximum3_f16 v139, v139, v172, v173\nv_mfma_f32_32x32x16_bf16 v[96:111], v[208:211], v[152:155], v[96:111]\n"
    "ds_read_b64_tr_b16 v[208:209], v117 offset:32768\nds_read_b64_tr_b16 v[210:211], v117 offset:33024\nv_pk_maximum3_f16 v138, v138, v174, v175\n"
    "v_pk_maximum3_f16 v138, v138, v139, v139\nv_mfma_f32_32x32x16_bf16 v[96:111], v[212:215], v[156:159], v[96:111]\nds_read_b64_tr_b16 v[212:213], v117 offset:33792\n"
    "ds_read_b64_tr_b16 v[214:215], v117 offset:34048\nv_cmp_u_f16_sdwa vcc, v138, v138 src0_sel:WORD_0 src1_sel:WORD_1\ns_cbranch_vccnz .Lxs163\n.Lxr164:\n"
    "s_waitcnt lgkmcnt(8)\nv_mfma_f32_32x32x16_bf16 v[64:79], v[192:195], v[160:163], v[64:79]\nds_read_b64_tr_b16 v[192:193], v117 offset:34816\n"
    "ds_read_b64_tr_b16 v[194:195], v117 offset:35072\nv_add_u32 v116, s50, v26\nv_add_u32 v118, 0x10000, v116\n"
    "v_mfma_f32_32x32x16_bf16 v[64:79], v[196:199], v[164:167], v[64:79]\nds_read_b64_tr_b16 v[196:197], v117 offset:35840\nds_read_b64_tr_b16 v[198:199], v117 offset:36096\n"
    "s_waitcnt lgkmcnt(8)\nv_mfma_f32_32x32x16_bf16 v[64:79], v[200:203], v[168:171], v[64:79]\nds_read_b64_tr_b16 v[200:201], v119 offset:0\n"
    "ds_read_b64_tr_b16 v[202:203], v119 offset:256\ns_barrier\nv_mfma_f32_32x32x16_bf16 v[64:79], v[204:207], v[172:175], v[64:79]\n"
    "ds_read_b64_tr_b16 v[204:205], v119 offset:1024\nds_read_b64_tr_b16 v[206:207], v119 offset:1280\nds_read_b64_tr_b16 v[144:145], v29 offset:0\n"
    "ds_read_b64_tr_b16 v[146:147], v29 offset:256\ns_waitcnt lgkmcnt(10)\nv_mfma_f32_32x32x16_bf16 v[80:95], v[208:211], v[160:163], v[80:95]\n"
    "ds_read_b64_tr_b16 v[208:209], v119 offset:2048\nds_read_b64_tr_b16 v[210:211], v119 offset:2304\nds_read_b64_tr_b16 v[148:149], v29 offset:1024\n"
    "ds_read_b64_tr_b16 v[150:151], v29 offset:1280\nv_mfma_f32_32x32x16_bf16 v[80:95], v[212:215], v[164:167], v[80:95]\nds_read_b64_tr_b16 v[212:213], v119 offset:3072\n"
    "ds_read_b64_tr_b16 v[214:215], v119 offset:3328\nds_read_b64_tr_b16 v[152:153], v29 offset:2048\nds_read_b64_tr_b16 v[154:155], v29 offset:2304\ns_waitcnt lgkmcnt(14)\n"
    "v_mfma_f32_32x32x16_bf16 v[80:95], v[192:195], v[168:171], v[80:95]\nds_read_b64_tr_b16 v[192:193], v116 offset:0\nds_read_b64_tr_b16 v[194:195], v116 offset:256\n"
    "ds_read_b64_tr_b16 v[156:157], v29 offset:3072\nds_read_b64_tr_b16 v[158:159], v29 offset:3328\nv_mfma_f32_32x32x16_bf16 v[80:95], v[196:199], v[172:175], v[80:95]\n"
    "ds_read_b64_tr_b16 v[196:197], v116 offset:1024\nds_read_b64_tr_b16 v[198:199], v116 offset:1280\ns_waitcnt lgkmcnt(10)\nv_pk_maximum3_f16 v138, v144, v145, v146\n"
    "v_pk_maximum3_f16 v139, v147, v148, v149\nv_mfma_f32_32x32x16_bf16 v[96:111], v[200:203], v[160:163], v[96:111]\nds_read_b64_tr_b16 v[200:201], v116 offset:2048\n"
    "ds_read_b64_tr_b16 v[202:203], v116 offset:2304\nv_pk_maximum3_f16 v138, v138, v150, v151\ns_waitcnt lgkmcnt(4)\nv_pk_maximum3_f16 v139, v139, v152, v153\n"
    "v_mfma_f32_32x32x16_bf16 v[96:111], v[204:207], v[164:167], v[96:111]\nds_read_b64_tr_b16 v[204:205], v116 offset:3072\nds_read_b64_tr_b16 v[206:207], v116 offset:3328\n"
    "v_pk_maximum3_f16 v138, v138, v154, v155\nv_pk_maximum3_f16 v139, v139, v156, v157\nv_mfma_f32_32x32x16_bf16 v[96:111], v[208:211], v[168:171], v[96:111]\n"
    "ds_read_b64_tr_b16 v[208:209], v116 offset:32768\nds_read_b64_tr_b16 v[210:211], v116 offset:33024\nv_pk_maximum3_f16 v138, v138, v158, v159\n"
    "v_pk_maximum3_f16 v138, v138, v139, v139\nv_mfma_f32_32x32x16_bf16 v[96:111], v[212:215], v[172:175], v[96:111]\nds_read_b64_tr_b16 v[212:213], v116 offset:33792\n"
    "ds_read_b64_tr_b16 v[214:215], v116 offset:34048\nv_cmp_u_f16_sdwa vcc, v138, v138 src0_sel:WORD_0 src1_sel:WORD_1\ns_cbranch_vccnz .Lxs166\n.Lxr167:\n"
    "s_waitcnt lgkmcnt(8)\nv_mfma_f32_32x32x16_bf16 v[64:79], v[192:195], v[144:147], v[64:79]\nds_read_b64_tr_b16 v[192:193], v116 offset:34816\n"
    "ds_read_b64_tr_b16 v[194:195], v116 offset:35072\nv_add_u32 v117, s51, v26\nv_add_u32 v119, 0x10000, v117\n"
    "v_mfma_f32_32x32x16_bf16 v[64:79], v[196:199], v[148:151], v[64:79]\nds_read_b64_tr_b16 v[196:197], v116 offset:35840\nds_read_b64_tr_b16 v[198:199], v116 offset:36096\n"
    "s_waitcnt lgkmcnt(8)\nv_mfma_f32_32x32x16_bf16 v[64:79], v[200:203], v[152:155], v[64:79]\nds_read_b64_tr_b16 v[200:201], v118 offset:0\n"
    "ds_read_b64_tr_b16 v[202:203], v118 offset:256\ns_barrier\nv_mfma_f32_32x32x16_bf16 v[64:79], v[204:207], v[156:159], v[64:79]\n"
    "ds_read_b64_tr_b16 v[204:205], v118 offset:1024\nds_read_b64_tr_b16 v[206:207], v118 offset:1280\nds_read_b64_tr_b16 v[160:161], v29 offset:4096\n"
    "ds_read_b64_tr_b16 v[162:163], v29 offset:4352\ns_waitcnt lgkmcnt(10)\nv_mfma_f32_32x32x16_bf16 v[80:95], v[208:211], v[144:147], v[80:95]\n"
    "ds_read_b64_tr_b16 v[208:209], v118 offset:2048\nds_read_b64_tr_b16 v[210:211], v118 offset:2304\nds_read_b64_tr_b16 v[164:165], v29 offset:5120\n"
    "ds_read_b64_tr_b16 v[166:167], v29 offset:5376\nv_mfma_f32_32x32x16_bf16 v[80:95], v[212:215], v[148:151], v[80:95]\nds_read_b64_tr_b16 v[212:213], v118 offset:3072\n"
    "ds_read_b64_tr_b16 v[214:215], v118 offset:3328\nds_read_b64_tr_b16 v[168:169], v29 offset:6144\nds_read_b64_tr_b16 v[170:171], v29 offset:6400\ns_waitcnt lgkmcnt(14)\n"
    "v_mfma_f32_32x32x16_bf16 v[80:95], v[192:195], v[152:155], v[80:95]\nds_read_b64_tr_b16 v[192:193], v117 offset:0\nds_read_b64_tr_b16 v[194:195], v117 offset:256\n"
    "ds_read_b64_tr_b16 v[172:173], v29 offset:7168\nds_read_b64_tr_b16 v[174:175], v29 offset:7424\nv_mfma_f32_32x32x16_bf16 v[80:95], v[196:199], v[156:159], v[80:95]\n"
    "ds_read_b64_tr_b16 v[196:197], v117 offset:1024\nds_read_b64_tr_b16 v[198:199], v117 offset:1280\ns_waitcnt lgkmcnt(10)\nv_pk_maximum3_f16 v138, v160, v161, v162\n"
    "v_pk_maximum3_f16 v139, v163, v164, v165\nv_mfma_f32_32x32x16_bf16 v[96:111], v[200:203], v[144:147], v[96:111]\nds_read_b64_tr_b16 v[200:201], v117 offset:2048\n"
    "ds_read_b64_tr_b16 v[202:203], v117 offset:2304\nv_pk_maximum3_f16 v138, v138, v166, v167\ns_waitcnt lgkmcnt(4)\nv_pk_maximum3_f16 v139, v139, v168, v169\n"
    "v_mfma_f32_32x32x16_bf16 v[96:111], v[204:207], v[148:151], v[96:111]\nds_read_b64_tr_b16 v[204:205], v117 offset:3072\nds_read_b64_tr_b16 v[206:207], v117 offset:3328\n"
    "v_pk_maximum3_f16 v138, v138, v170, v171\nv_pk_maximum3_f16 v139, v139, v172, v173\nv_mfma_f32_32x32x16_bf16 v[96:111], v[208:211], v[152:155], v[96:111]\n"
    "ds_read_b64_tr_b16 v[208:209], v117 offset:32768\nds_read_b64_tr_b16 v[210:211], v117 offset:33024\nv_pk_maximum3_f16 v138, v138, v174, v175\n"
    "v_pk_maximum3_f16 v138, v138, v139, v139\nv_mfma_f32_32x32x16_bf16 v[96:111], v[212:215], v[156:159], v[96:111]\nds_read_b64_tr_b16 v[212:213], v117 offset:33792\n"
    "ds_read_b64_tr_b16 v[214:215], v117 offset:34048\nv_cmp_u_f16_sdwa vcc, v138, v138 src0_sel:WORD_0 src1_sel:WORD_1\ns_cbranch_vccnz .Lxs169\n.Lxr170:\n"
    "s_waitcnt lgkmcnt(8)\nv_mfma_f32_32x32x16_bf16 v[64:79], v[192:195], v[160:163], v[64:79]\nds_read_b64_tr_b16 v[192:193], v117 offset:34816\n"
    "ds_read_b64_tr_b16 v[194:195], v117 offset:35072\nv_mfma_f32_32x32x16_bf16 v[64:79], v[196:199], v[164:167], v[64:79]\nds_read_b64_tr_b16 v[196:197], v117 offset:35840\n"
    "ds_read_b64_tr_b16 v[198:199], v117 offset:36096\ns_waitcnt lgkmcnt(8)\nv_mfma_f32_32x32x16_bf16 v[64:79], v[200:203], v[168:171], v[64:79]\n"
    "ds_read_b64_tr_b16 v[200:201], v119 offset:0\nds_read_b64_tr_b16 v[202:203], v119 offset:256\nv_mfma_f32_32x32x16_bf16 v[64:79], v[204:207], v[172:175], v[64:79]\n"
    "ds_read_b64_tr_b16 v[204:205], v119 offset:1024\nds_read_b64_tr_b16 v[206:207], v119 offset:1280\ns_waitcnt lgkmcnt(8)\n"
    "v_mfma_f32_32x32x16_bf16 v[80:95], v[208:211], v[160:163], v[80:95]\nds_read_b64_tr_b16 v[208:209], v119 offset:2048\nds_read_b64_tr_b16 v[210:211], v119 offset:2304\n"
    "s_barrier\nds_read_b32 v120, v36\nds_read_b32 v137, v36 offset:4\nv_mfma_f32_32x32x16_bf16 v[80:95], v[212:215], v[164:167], v[80:95]\n"
    "ds_read_b64_tr_b16 v[212:213], v119 offset:3072\nds_read_b64_tr_b16 v[214:215], v119 offset:3328\ns_waitcnt lgkmcnt(10)\n"
    "v_mfma_f32_32x32x16_bf16 v[80:95], v[192:195], v[168:171], v[80:95]\nv_mfma_f32_32x32x16_bf16 v[80:95], v[196:199], v[172:175], v[80:95]\ns_waitcnt lgkmcnt(2)\n"
    "v_mfma_f32_32x32x16_bf16 v[64:79], v[124:127], v[120:123], v[64:79]\nv_mfma_f32_32x32x16_bf16 v[80:95], v[128:131], v[120:123], v[80:95]\n"
    "v_mfma_f32_32x32x16_bf16 v[96:111], v[200:203], v[160:163], v[96:111]\nv_mfma_f32_32x32x16_bf16 v[96:111], v[204:207], v[164:167], v[96:111]\ns_nop 8\n"
    "v_mul_f32 v64, v64, v137\nv_mul_f32 v65, v65, v137\nv_mul_f32 v66, v66, v137\nv_mul_f32 v67, v67, v137\nv_mul_f32 v68, v68, v137\nv_mul_f32 v69, v69, v137\n"
    "s_waitcnt lgkmcnt(0)\nv_mfma_f32_32x32x16_bf16 v[96:111], v[208:211], v[168:171], v[96:111]\nv_mul_f32 v70, v70, v137\nv_mul_f32 v71, v71, v137\n"
    "v_cvt_pk_bf16_f32 v64, v64, v65\nv_cvt_pk_bf16_f32 v65, v66, v67\nv_cvt_pk_bf16_f32 v66, v68, v69\nv_cvt_pk_bf16_f32 v67, v70, v71\n"
    "v_mfma_f32_32x32x16_bf16 v[96:111], v[212:215], v[172:175], v[96:111]\nds_write_b128 v27, v[64:67] offset:0\nv_mul_f32 v72, v72, v137\nv_mul_f32 v73, v73, v137\n"
    "v_mul_f32 v74, v74, v137\nv_mul_f32 v75, v75, v137\nv_mul_f32 v76, v76, v137\nv_mfma_f32_32x32x16_bf16 v[96:111], v[132:135], v[120:123], v[96:111]\n"
    "v_mul_f32 v77, v77, v137\nv_mul_f32 v78, v78, v137\nv_mul_f32 v79, v79, v137\nv_cvt_pk_bf16_f32 v72, v72, v73\nv_cvt_pk_bf16_f32 v73, v74, v75\n"
    "v_cvt_pk_bf16_f32 v74, v76, v77\nv_cvt_pk_bf16_f32 v75, v78, v79\nds_write_b128 v27, v[72:75] offset:32\nv_pk_mul_f32 v[80:81], v[80:81], v[136:137] op_sel:[0,1]\n"
    "v_pk_mul_f32 v[82:83], v[82:83], v[136:137] op_sel:[0,1]\nv_pk_mul_f32 v[84:85], v[84:85], v[136:137] op_sel:[0,1]\n"
    "v_pk_mul_f32 v[86:87], v[86:87], v[136:137] op_sel:[0,1]\nv_cvt_pk_bf16_f32 v80, v80, v81\nv_cvt_pk_bf16_f32 v81, v82, v83\nv_cvt_pk_bf16_f32 v82, v84, v85\n"
    "v_cvt_pk_bf16_f32 v83, v86, v87\nds_write_b128 v27, v[80:83] offset:64\nv_pk_mul_f32 v[88:89], v[88:89], v[136:137] op_sel:[0,1]\n"
    "v_pk_mul_f32 v[90:91], v[90:91], v[136:137] op_sel:[0,1]\nv_pk_mul_f32 v[92:93], v[92:93], v[136:137] op_sel:[0,1]\n"
    "v_pk_mul_f32 v[94:95], v[94:95], v[136:137] op_sel:[0,1]\nv_cvt_pk_bf16_f32 v88, v88, v89\nv_cvt_pk_bf16_f32 v89, v90, v91\nv_cvt_pk_bf16_f32 v90, v92, v93\n"
    "v_cvt_pk_bf16_f32 v91, v94, v95\nds_write_b128 v27, v[88:91] offset:96\nv_pk_mul_f32 v[96:97], v[96:97], v[136:137] op_sel:[0,1]\n"
    "v_pk_mul_f32 v[98:99], v[98:99], v[136:137] op_sel:[0,1]\nv_pk_mul_f32 v[100:101], v[100:101], v[136:137] op_sel:[0,1]\n"
    "v_pk_mul_f32 v[102:103], v[102:103], v[136:137] op_sel:[0,1]\nv_cvt_pk_bf16_f32 v96, v96, v97\nv_cvt_pk_bf16_f32 v97, v98, v99\nv_cvt_pk_bf16_f32 v98, v100, v101\n"
    "v_cvt_pk_bf16_f32 v99, v102, v103\nds_write_b128 v27, v[96:99] offset:128\nv_pk_mul_f32 v[104:105], v[104:105], v[136:137] op_sel:[0,1]\n"
    "v_pk_mul_f32 v[106:107], v[106:107], v[136:137] op_sel:[0,1]\nv_pk_mul_f32 v[108:109], v[108:109], v[136:137] op_sel:[0,1]\n"
    "v_pk_mul_f32 v[110:111], v[110:111], v[136:137] op_sel:[0,1]\nv_cvt_pk_bf16_f32 v104, v104, v105\nv_cvt_pk_bf16_f32 v105, v106, v107\nv_cvt_pk_bf16_f32 v106, v108, v109\n"
    "v_cvt_pk_bf16_f32 v107, v110, v111\nds_write_b128 v27, v[104:107] offset:160\ns_branch .LGL75\n.Lcold:\n.Lls39:\ns_cmp_le_u32 s80, 65534\ns_cbranch_scc1 .LL40\n"
    "s_sub_u32 s80, s80, 1\nglobal_load_dwordx4 v[240:243], v21, s[32:33] sc1\nglobal_load_dwordx4 v[208:211], v20, s[32:33] sc1\n"
    "global_load_dwordx4 v[212:215], v22, s[32:33] sc1\nglobal_load_dwordx4 v[216:219], v23, s[32:33] sc1\nglobal_load_dwordx4 v[220:223], v24, s[32:33] sc1\ns_branch .Lln37\n"
    ".LL40:\ns_sub_u32 s80, s80, 1\ns_cbranch_scc1 .Llo38\ns_mov_b64 s[82:83], 0\nv_max3_u32 v60, v208, v209, v210\nv_max_u32 v60, v60, v211\nv_cmp_eq_u32 vcc, -1, v60\n"
    "s_nop 0\ns_or_b64 s[82:83], s[82:83], vcc\ns_cbranch_vccz .Lag41\ns_mov_b64 exec, vcc\nglobal_load_dwordx4 v[208:211], v20, s[32:33] sc1\ns_mov_b64 exec, -1\n.Lag41:\n"
    "v_max3_u32 v60, v212, v213, v214\nv_max_u32 v60, v60, v215\nv_cmp_eq_u32 vcc, -1, v60\ns_nop 0\ns_or_b64 s[82:83], s[82:83], vcc\ns_cbranch_vccz .Lag42\n"
    "s_mov_b64 exec, vcc\nglobal_load_dwordx4 v[212:215], v22, s[32:33] sc1\ns_mov_b64 exec, -1\n.Lag42:\nv_max3_u32 v60, v216, v217, v218\nv_max_u32 v60, v60, v219\n"
    "v_cmp_eq_u32 vcc, -1, v60\ns_nop 0\ns_or_b64 s[82:83], s[82:83], vcc\ns_cbranch_vccz .Lag43\ns_mov_b64 exec, vcc\nglobal_load_dwordx4 v[216:219], v23, s[32:33] sc1\n"
    "s_mov_b64 exec, -1\n.Lag43:\nv_max3_u32 v60, v220, v221, v222\nv_max_u32 v60, v60, v223\nv_cmp_eq_u32 vcc, -1, v60\ns_nop 0\ns_or_b64 s[82:83], s[82:83], vcc\n"
    "s_cbranch_vccz .Lag44\ns_mov_b64 exec, vcc\nglobal_load_dwordx4 v[220:223], v24, s[32:33] sc1\ns_mov_b64 exec, -1\n.Lag44:\nv_max3_u32 v60, v240, v241, v242\n"
    "v_max_u32 v60, v60, v243\nv_cmp_eq_u32 vcc, -1, v60\ns_nop 0\ns_or_b64 s[82:83], s[82:83], vcc\ns_cbranch_vccz .Lag45\ns_mov_b64 exec, vcc\n"
    "global_load_dwordx4 v[240:243], v21, s[32:33] sc1\ns_mov_b64 exec, -1\n.Lag45:\ns_cmp_eq_u64 s[82:83], 0\ns_cbranch_scc1 .Llo38\ns_waitcnt vmcnt(0)\ns_branch .Lln37\n"
    ".Lxs59:\ns_mov_b32 s80, 0x10000\n.Lxl61:\nv_pk_max_u16 v62, v196, v197\nv_pk_max_u16 v62, v62, v198\nv_pk_max_u16 v62, v62, v199\nv_pk_max_u16 v62, v62, v200\n"
    "v_pk_max_u16 v62, v62, v201\nv_pk_max_u16 v62, v62, v202\nv_pk_max_u16 v62, v62, v203\nv_pk_max_u16 v62, v62, v204\nv_pk_max_u16 v62, v62, v205\n"
    "v_pk_max_u16 v62, v62, v206\nv_pk_max_u16 v62, v62, v207\nv_pk_max_u16 v62, v62, v208\nv_pk_max_u16 v62, v62, v209\nv_pk_max_u16 v62, v62, v210\n"
    "v_pk_max_u16 v62, v62, v211\nv_pk_max_u16 v62, v62, v62 op_sel:[0,1] op_sel_hi:[1,0]\nv_cmp_eq_u32 vcc, -1, v62\ns_cbranch_vccz .Lxr60\ns_sub_u32 s80, s80, 1\n"
    "s_cbranch_scc1 .Lxr60\ns_sleep 2\ns_add_u32 m0, s65, 1024\ns_nop 0\nglobal_load_lds_dwordx4 v27, s[60:61] offset:0 sc1\ns_add_u32 m0, s65, 0\ns_nop 0\n"
    "global_load_lds_dwordx4 v27, s[60:61] offset:2048 sc1\ns_add_u32 m0, s65, 3072\ns_nop 0\nglobal_load_lds_dwordx4 v9, s[60:61] offset:0 sc1\ns_add_u32 m0, s65, 2048\n"
    "s_nop 0\nglobal_load_lds_dwordx4 v9, s[60:61] offset:2048 sc1\ns_waitcnt vmcnt(0)\nds_read_b64_tr_b16 v[196:197], v28 offset:0\n"
    "ds_read_b64_tr_b16 v[198:199], v28 offset:128\nds_read_b64_tr_b16 v[200:201], v28 offset:1024\nds_read_b64_tr_b16 v[202:203], v28 offset:1152\n"
    "ds_read_b64_tr_b16 v[204:205], v28 offset:2048\nds_read_b64_tr_b16 v[206:207], v28 offset:2176\nds_read_b64_tr_b16 v[208:209], v28 offset:3072\n"
    "ds_read_b64_tr_b16 v[210:211], v28 offset:3200\ns_waitcnt lgkmcnt(0)\ns_branch .Lxl61\n.Lps62:\ns_sub_u32 s80, s80, 1\ns_cbranch_scc1 .Lpr63\ns_mov_b64 s[82:83], 0\n"
    "v_max3_u32 v229, v212, v213, v214\nv_max_u32 v229, v229, v215\nv_cmp_eq_u32 vcc, -1, v229\ns_nop 0\ns_or_b64 s[82:83], s[82:83], vcc\ns_cbranch_vccz .Lag65\n"
    "s_mov_b64 exec, vcc\nglobal_load_dwordx4 v[212:215], v36, s[32:33] offset:0 sc1\ns_mov_b64 exec, -1\n.Lag65:\nv_max3_u32 v229, v216, v217, v218\n"
    "v_max_u32 v229, v229, v219\nv_cmp_eq_u32 vcc, -1, v229\ns_nop 0\ns_or_b64 s[82:83], s[82:83], vcc\ns_cbranch_vccz .Lag66\ns_mov_b64 exec, vcc\n"
    "global_load_dwordx4 v[216:219], v36, s[32:33] offset:1024 sc1\ns_mov_b64 exec, -1\n.Lag66:\ns_cmp_eq_u64 s[82:83], 0\ns_cbranch_scc1 .Lpr63\ns_waitcnt vmcnt(0)\n"
    "s_branch .Lpl64\n.Lps67:\ns_sub_u32 s80, s80, 1\ns_cbranch_scc1 .Lpr68\ns_mov_b64 s[82:83], 0\nv_max3_u32 v233, v220, v221, v222\nv_max_u32 v233, v233, v223\n"
    "v_cmp_eq_u32 vcc, -1, v233\ns_nop 0\ns_or_b64 s[82:83], s[82:83], vcc\ns_cbranch_vccz .Lag70\ns_mov_b64 exec, vcc\n"
    "global_load_dwordx4 v[220:223], v37, s[32:33] offset:0 sc1\ns_mov_b64 exec, -1\n.Lag70:\nv_max3_u32 v233, v224, v225, v226\nv_max_u32 v233, v233, v227\n"
    "v_cmp_eq_u32 vcc, -1, v233\ns_nop 0\ns_or_b64 s[82:83], s[82:83], vcc\ns_cbranch_vccz .Lag71\ns_mov_b64 exec, vcc\n"
    "global_load_dwordx4 v[224:227], v37, s[32:33] offset:1024 sc1\ns_mov_b64 exec, -1\n.Lag71:\ns_cmp_eq_u64 s[82:83], 0\ns_cbranch_scc1 .Lpr68\ns_waitcnt vmcnt(0)\n"
    "s_branch .Lpl69\n.LL85:\ns_sub_u32 s80, s80, 1\ns_cbranch_scc1 .LL84\ns_waitcnt vmcnt(0) lgkmcnt(0)\nv_max3_u32 v31, v208, v209, v210\nv_max_u32 v31, v31, v211\n"
    "v_cmp_eq_u32 vcc, -1, v31\ns_nop 0\ns_or_b64 s[82:83], s[82:83], vcc\ns_cbranch_vccz .Lag86\ns_mov_b64 exec, vcc\n"
    "global_load_dwordx4 v[208:211], v182, s[32:33] offset:-4096 sc1\ns_mov_b64 exec, -1\n.Lag86:\nv_max3_u32 v31, v212, v213, v214\nv_max_u32 v31, v31, v215\n"
    "v_cmp_eq_u32 vcc, -1, v31\ns_nop 0\ns_or_b64 s[82:83], s[82:83], vcc\ns_cbranch_vccz .Lag87\ns_mov_b64 exec, vcc\n"
    "global_load_dwordx4 v[212:215], v182, s[32:33] offset:0 sc1\ns_mov_b64 exec, -1\n.Lag87:\nv_max3_u32 v31, v216, v217, v218\nv_max_u32 v31, v31, v219\n"
    "v_cmp_eq_u32 vcc, -1, v31\ns_nop 0\ns_or_b64 s[82:83], s[82:83], vcc\ns_cbranch_vccz .Lag88\ns_mov_b64 exec, vcc\n"
    "global_load_dwordx4 v[216:219], v183, s[32:33] offset:-4096 sc1\ns_mov_b64 exec, -1\n.Lag88:\nv_max3_u32 v31, v220, v221, v222\nv_max_u32 v31, v31, v223\n"
    "v_cmp_eq_u32 vcc, -1, v31\ns_nop 0\ns_or_b64 s[82:83], s[82:83], vcc\ns_cbranch_vccz .Lag89\ns_mov_b64 exec, vcc\n"
    "global_load_dwordx4 v[220:223], v183, s[32:33] offset:0 sc1\ns_mov_b64 exec, -1\n.Lag89:\nv_max3_u32 v31, v224, v225, v226\nv_max_u32 v31, v31, v227\n"
    "v_cmp_eq_u32 vcc, -1, v31\ns_nop 0\ns_or_b64 s[82:83], s[82:83], vcc\ns_cbranch_vccz .Lag90\ns_mov_b64 exec, vcc\n"
    "global_load_dwordx4 v[224:227], v184, s[32:33] offset:-4096 sc1\ns_mov_b64 exec, -1\n.Lag90:\nv_max3_u32 v31, v228, v229, v230\nv_max_u32 v31, v31, v231\n"
    "v_cmp_eq_u32 vcc, -1, v31\ns_nop 0\ns_or_b64 s[82:83], s[82:83], vcc\ns_cbranch_vccz .Lag91\ns_mov_b64 exec, vcc\n"
    "global_load_dwordx4 v[228:231], v184, s[32:33] offset:0 sc1\ns_mov_b64 exec, -1\n.Lag91:\nv_max3_u32 v31, v232, v233, v234\nv_max_u32 v31, v31, v235\n"
    "v_cmp_eq_u32 vcc, -1, v31\ns_nop 0\ns_or_b64 s[82:83], s[82:83], vcc\ns_cbranch_vccz .Lag92\ns_mov_b64 exec, vcc\n"
    "global_load_dwordx4 v[232:235], v185, s[32:33] offset:-4096 sc1\ns_mov_b64 exec, -1\n.Lag92:\nv_max3_u32 v31, v236, v237, v238\nv_max_u32 v31, v31, v239\n"
    "v_cmp_eq_u32 vcc, -1, v31\ns_nop 0\ns_or_b64 s[82:83], s[82:83], vcc\ns_cbranch_vccz .Lag93\ns_mov_b64 exec, vcc\n"
    "global_load_dwordx4 v[236:239], v185, s[32:33] offset:0 sc1\ns_mov_b64 exec, -1\n.Lag93:\ns_waitcnt vmcnt(0)\ns_branch .LL83\n.LL96:\ns_sub_u32 s80, s80, 1\n"
    "s_cbranch_scc1 .LL95\ns_waitcnt vmcnt(0) lgkmcnt(0)\nv_max3_u32 v31, v216, v217, v218\nv_max_u32 v31, v31, v219\nv_cmp_eq_u32 vcc, -1, v31\ns_nop 0\n"
    "s_or_b64 s[82:83], s[82:83], vcc\ns_cbranch_vccz .Lag97\ns_mov_b64 exec, vcc\nglobal_load_dwordx4 v[216:219], v183, s[32:33] offset:-4096 sc1\ns_mov_b64 exec, -1\n.Lag97:\n"
    "v_max3_u32 v31, v220, v221, v222\nv_max_u32 v31, v31, v223\nv_cmp_eq_u32 vcc, -1, v31\ns_nop 0\ns_or_b64 s[82:83], s[82:83], vcc\ns_cbranch_vccz .Lag98\n"
    "s_mov_b64 exec, vcc\nglobal_load_dwordx4 v[220:223], v183, s[32:33] offset:0 sc1\ns_mov_b64 exec, -1\n.Lag98:\nv_max3_u32 v31, v224, v225, v226\nv_max_u32 v31, v31, v227\n"
    "v_cmp_eq_u32 vcc, -1, v31\ns_nop 0\ns_or_b64 s[82:83], s[82:83], vcc\ns_cbranch_vccz .Lag99\ns_mov_b64 exec, vcc\n"
    "global_load_dwordx4 v[224:227], v184, s[32:33] offset:-4096 sc1\ns_mov_b64 exec, -1\n.Lag99:\nv_max3_u32 v31, v228, v229, v230\nv_max_u32 v31, v31, v231\n"
    "v_cmp_eq_u32 vcc, -1, v31\ns_nop 0\ns_or_b64 s[82:83], s[82:83], vcc\ns_cbranch_vccz .Lag100\ns_mov_b64 exec, vcc\n"
    "global_load_dwordx4 v[228:231], v184, s[32:33] offset:0 sc1\ns_mov_b64 exec, -1\n.Lag100:\nv_max3_u32 v31, v232, v233, v234\nv_max_u32 v31, v31, v235\n"
    "v_cmp_eq_u32 vcc, -1, v31\ns_nop 0\ns_or_b64 s[82:83], s[82:83], vcc\ns_cbranch_vccz .Lag101\ns_mov_b64 exec, vcc\n"
    "global_load_dwordx4 v[232:235], v185, s[32:33] offset:-4096 sc1\ns_mov_b64 exec, -1\n.Lag101:\nv_max3_u32 v31, v236, v237, v238\nv_max_u32 v31, v31, v239\n"
    "v_cmp_eq_u32 vcc, -1, v31\ns_nop 0\ns_or_b64 s[82:83], s[82:83], vcc\ns_cbranch_vccz .Lag102\ns_mov_b64 exec, vcc\n"
    "global_load_dwordx4 v[236:239], v185, s[32:33] offset:0 sc1\ns_mov_b64 exec, -1\n.Lag102:\ns_waitcnt vmcnt(0)\ns_branch .LL94\n.LL105:\ns_sub_u32 s80, s80, 1\n"
    "s_cbranch_scc1 .LL104\ns_waitcnt vmcnt(0) lgkmcnt(0)\nv_max3_u32 v31, v224, v225, v226\nv_max_u32 v31, v31, v227\nv_cmp_eq_u32 vcc, -1, v31\ns_nop 0\n"
    "s_or_b64 s[82:83], s[82:83], vcc\ns_cbranch_vccz .Lag106\ns_mov_b64 exec, vcc\nglobal_load_dwordx4 v[224:227], v184, s[32:33] offset:-4096 sc1\ns_mov_b64 exec, -1\n"
    ".Lag106:\nv_max3_u32 v31, v228, v229, v230\nv_max_u32 v31, v31, v231\nv_cmp_eq_u32 vcc, -1, v31\ns_nop 0\ns_or_b64 s[82:83], s[82:83], vcc\ns_cbranch_vccz .Lag107\n"
    "s_mov_b64 exec, vcc\nglobal_load_dwordx4 v[228:231], v184, s[32:33] offset:0 sc1\ns_mov_b64 exec, -1\n.Lag107:\nv_max3_u32 v31, v232, v233, v234\nv_max_u32 v31, v31, v235\n"
    "v_cmp_eq_u32 vcc, -1, v31\ns_nop 0\ns_or_b64 s[82:83], s[82:83], vcc\ns_cbranch_vccz .Lag108\ns_mov_b64 exec, vcc\n"
    "global_load_dwordx4 v[232:235], v185, s[32:33] offset:-4096 sc1\ns_mov_b64 exec, -1\n.Lag108:\nv_max3_u32 v31, v236, v237, v238\nv_max_u32 v31, v31, v239\n"
    "v_cmp_eq_u32 vcc, -1, v31\ns_nop 0\ns_or_b64 s[82:83], s[82:83], vcc\ns_cbranch_vccz .Lag109\ns_mov_b64 exec, vcc\n"
    "global_load_dwordx4 v[236:239], v185, s[32:33] offset:0 sc1\ns_mov_b64 exec, -1\n.Lag109:\ns_waitcnt vmcnt(0)\ns_branch .LL103\n.LL112:\ns_sub_u32 s80, s80, 1\n"
    "s_cbranch_scc1 .LL111\ns_waitcnt vmcnt(0) lgkmcnt(0)\nv_max3_u32 v31, v232, v233, v234\nv_max_u32 v31, v31, v235\nv_cmp_eq_u32 vcc, -1, v31\ns_nop 0\n"
    "s_or_b64 s[82:83], s[82:83], vcc\ns_cbranch_vccz .Lag113\ns_mov_b64 exec, vcc\nglobal_load_dwordx4 v[232:235], v185, s[32:33] offset:-4096 sc1\ns_mov_b64 exec, -1\n"
    ".Lag113:\nv_max3_u32 v31, v236, v237, v238\nv_max_u32 v31, v31, v239\nv_cmp_eq_u32 vcc, -1, v31\ns_nop 0\ns_or_b64 s[82:83], s[82:83], vcc\ns_cbranch_vccz .Lag114\n"
    "s_mov_b64 exec, vcc\nglobal_load_dwordx4 v[236:239], v185, s[32:33] offset:0 sc1\ns_mov_b64 exec, -1\n.Lag114:\ns_waitcnt vmcnt(0)\ns_branch .LL110\n.Lls122:\n"
    "s_cmp_le_u32 s80, 65534\ns_cbranch_scc1 .LL123\ns_sub_u32 s80, s80, 1\nglobal_load_dwordx4 v[240:243], v21, s[32:33] sc1\n"
    "global_load_dwordx4 v[208:211], v20, s[32:33] sc1\nglobal_load_dwordx4 v[212:215], v22, s[32:33] sc1\nglobal_load_dwordx4 v[216:219], v23, s[32:33] sc1\n"
    "global_load_dwordx4 v[220:223], v24, s[32:33] sc1\ns_branch .Lln120\n.LL123:\ns_sub_u32 s80, s80, 1\ns_cbranch_scc1 .Llo121\ns_mov_b64 s[82:83], 0\n"
    "v_max3_u32 v60, v208, v209, v210\nv_max_u32 v60, v60, v211\nv_cmp_eq_u32 vcc, -1, v60\ns_nop 0\ns_or_b64 s[82:83], s[82:83], vcc\ns_cbranch_vccz .Lag124\n"
    "s_mov_b64 exec, vcc\nglobal_load_dwordx4 v[208:211], v20, s[32:33] sc1\ns_mov_b64 exec, -1\n.Lag124:\nv_max3_u32 v60, v212, v213, v214\nv_max_u32 v60, v60, v215\n"
    "v_cmp_eq_u32 vcc, -1, v60\ns_nop 0\ns_or_b64 s[82:83], s[82:83], vcc\ns_cbranch_vccz .Lag125\ns_mov_b64 exec, vcc\nglobal_load_dwordx4 v[212:215], v22, s[32:33] sc1\n"
    "s_mov_b64 exec, -1\n.Lag125:\nv_max3_u32 v60, v216, v217, v218\nv_max_u32 v60, v60, v219\nv_cmp_eq_u32 vcc, -1, v60\ns_nop 0\ns_or_b64 s[82:83], s[82:83], vcc\n"
    "s_cbranch_vccz .Lag126\ns_mov_b64 exec, vcc\nglobal_load_dwordx4 v[216:219], v23, s[32:33] sc1\ns_mov_b64 exec, -1\n.Lag126:\nv_max3_u32 v60, v220, v221, v222\n"
    "v_max_u32 v60, v60, v223\nv_cmp_eq_u32 vcc, -1, v60\ns_nop 0\ns_or_b64 s[82:83], s[82:83], vcc\ns_cbranch_vccz .Lag127\ns_mov_b64 exec, vcc\n"
    "global_load_dwordx4 v[220:223], v24, s[32:33] sc1\ns_mov_b64 exec, -1\n.Lag127:\nv_max3_u32 v60, v240, v241, v242\nv_max_u32 v60, v60, v243\nv_cmp_eq_u32 vcc, -1, v60\n"
    "s_nop 0\ns_or_b64 s[82:83], s[82:83], vcc\ns_cbranch_vccz .Lag128\ns_mov_b64 exec, vcc\nglobal_load_dwordx4 v[240:243], v21, s[32:33] sc1\ns_mov_b64 exec, -1\n.Lag128:\n"
    "s_cmp_eq_u64 s[82:83], 0\ns_cbranch_scc1 .Llo121\ns_waitcnt vmcnt(0)\ns_branch .Lln120\n.Lxs148:\nv_pk_max_u16 v138, v144, v145\nv_pk_max_u16 v138, v138, v146\n"
    "v_pk_max_u16 v138, v138, v147\nv_pk_max_u16 v138, v138, v148\nv_pk_max_u16 v138, v138, v149\nv_pk_max_u16 v138, v138, v150\nv_pk_max_u16 v138, v138, v151\n"
    "v_pk_max_u16 v138, v138, v152\nv_pk_max_u16 v138, v138, v153\nv_pk_max_u16 v138, v138, v154\nv_pk_max_u16 v138, v138, v155\nv_pk_max_u16 v138, v138, v156\n"
    "v_pk_max_u16 v138, v138, v157\nv_pk_max_u16 v138, v138, v158\nv_pk_max_u16 v138, v138, v159\nv_pk_max_u16 v138, v138, v138 op_sel:[0,1] op_sel_hi:[1,0]\n"
    "v_cmp_eq_u32 vcc, -1, v138\ns_cbranch_vccz .Lxr149\ns_sub_u32 s80, s80, 1\ns_cbranch_scc1 .Lxr149\ns_sleep 2\ns_add_u32 m0, s87, 0\ns_add_u32 s52, s82, s44\n"
    "s_addc_u32 s53, s83, 0\nglobal_load_lds_dwordx4 v35, s[52:53] offset:0 sc1\nglobal_load_lds_dwordx4 v35, s[52:53] offset:1024 sc1\n"
    "global_load_lds_dwordx4 v35, s[52:53] offset:2048 sc1\nglobal_load_lds_dwordx4 v35, s[52:53] offset:3072 sc1\ns_waitcnt vmcnt(0)\n"
    "ds_read_b64_tr_b16 v[144:145], v29 offset:0\nds_read_b64_tr_b16 v[146:147], v29 offset:256\nds_read_b64_tr_b16 v[148:149], v29 offset:1024\n"
    "ds_read_b64_tr_b16 v[150:151], v29 offset:1280\nds_read_b64_tr_b16 v[152:153], v29 offset:2048\nds_read_b64_tr_b16 v[154:155], v29 offset:2304\n"
    "ds_read_b64_tr_b16 v[156:157], v29 offset:3072\nds_read_b64_tr_b16 v[158:159], v29 offset:3328\ns_waitcnt lgkmcnt(0)\ns_branch .LL147\n.Lxs151:\ns_mov_b32 s80, 0x10000\n"
    ".Lxl153:\nv_pk_max_u16 v138, v160, v161\nv_pk_max_u16 v138, v138, v162\nv_pk_max_u16 v138, v138, v163\nv_pk_max_u16 v138, v138, v164\nv_pk_max_u16 v138, v138, v165\n"
    "v_pk_max_u16 v138, v138, v166\nv_pk_max_u16 v138, v138, v167\nv_pk_max_u16 v138, v138, v168\nv_pk_max_u16 v138, v138, v169\nv_pk_max_u16 v138, v138, v170\n"
    "v_pk_max_u16 v138, v138, v171\nv_pk_max_u16 v138, v138, v172\nv_pk_max_u16 v138, v138, v173\nv_pk_max_u16 v138, v138, v174\nv_pk_max_u16 v138, v138, v175\n"
    "v_pk_max_u16 v138, v138, v138 op_sel:[0,1] op_sel_hi:[1,0]\nv_cmp_eq_u32 vcc, -1, v138\ns_cbranch_vccz .Lxr152\ns_sub_u32 s80, s80, 1\ns_cbranch_scc1 .Lxr152\ns_sleep 2\n"
    "s_add_u32 m0, s87, 4096\ns_add_u32 s52, s82, s45\ns_addc_u32 s53, s83, 0\nglobal_load_lds_dwordx4 v35, s[52:53] offset:0 sc1\n"
    "global_load_lds_dwordx4 v35, s[52:53] offset:1024 sc1\nglobal_load_lds_dwordx4 v35, s[52:53] offset:2048 sc1\nglobal_load_lds_dwordx4 v35, s[52:53] offset:3072 sc1\n"
    "s_waitcnt vmcnt(0)\nds_read_b64_tr_b16 v[160:161], v29 offset:4096\nds_read_b64_tr_b16 v[162:163], v29 offset:4352\nds_read_b64_tr_b16 v[164:165], v29 offset:5120\n"
    "ds_read_b64_tr_b16 v[166:167], v29 offset:5376\nds_read_b64_tr_b16 v[168:169], v29 offset:6144\nds_read_b64_tr_b16 v[170:171], v29 offset:6400\n"
    "ds_read_b64_tr_b16 v[172:173], v29 offset:7168\nds_read_b64_tr_b16 v[174:175], v29 offset:7424\ns_waitcnt lgkmcnt(0)\ns_branch .Lxl153\n.Lxs154:\ns_mov_b32 s80, 0x10000\n"
    ".Lxl156:\nv_pk_max_u16 v138, v144, v145\nv_pk_max_u16 v138, v138, v146\nv_pk_max_u16 v138, v138, v147\nv_pk_max_u16 v138, v138, v148\nv_pk_max_u16 v138, v138, v149\n"
    "v_pk_max_u16 v138, v138, v150\nv_pk_max_u16 v138, v138, v151\nv_pk_max_u16 v138, v138, v152\nv_pk_max_u16 v138, v138, v153\nv_pk_max_u16 v138, v138, v154\n"
    "v_pk_max_u16 v138, v138, v155\nv_pk_max_u16 v138, v138, v156\nv_pk_max_u16 v138, v138, v157\nv_pk_max_u16 v138, v138, v158\nv_pk_max_u16 v138, v138, v159\n"
    "v_pk_max_u16 v138, v138, v138 op_sel:[0,1] op_sel_hi:[1,0]\nv_cmp_eq_u32 vcc, -1, v138\ns_cbranch_vccz .Lxr155\ns_sub_u32 s80, s80, 1\ns_cbranch_scc1 .Lxr155\ns_sleep 2\n"
    "s_add_u32 m0, s87, 8192\ns_add_u32 s52, s82, s46\ns_addc_u32 s53, s83, 0\nglobal_load_lds_dwordx4 v35, s[52:53] offset:0 sc1\n"
    "global_load_lds_dwordx4 v35, s[52:53] offset:1024 sc1\nglobal_load_lds_dwordx4 v35, s[52:53] offset:2048 sc1\nglobal_load_lds_dwordx4 v35, s[52:53] offset:3072 sc1\n"
    "s_waitcnt vmcnt(0)\nds_read_b64_tr_b16 v[144:145], v29 offset:8192\nds_read_b64_tr_b16 v[146:147], v29 offset:8448\nds_read_b64_tr_b16 v[148:149], v29 offset:9216\n"
    "ds_read_b64_tr_b16 v[150:151], v29 offset:9472\nds_read_b64_tr_b16 v[152:153], v29 offset:10240\nds_read_b64_tr_b16 v[154:155], v29 offset:10496\n"
    "ds_read_b64_tr_b16 v[156:157], v29 offset:11264\nds_read_b64_tr_b16 v[158:159], v29 offset:11520\ns_waitcnt lgkmcnt(0)\ns_branch .Lxl156\n.Lxs157:\ns_mov_b32 s80, 0x10000\n"
    ".Lxl159:\nv_pk_max_u16 v138, v160, v161\nv_pk_max_u16 v138, v138, v162\nv_pk_max_u16 v138, v138, v163\nv_pk_max_u16 v138, v138, v164\nv_pk_max_u16 v138, v138, v165\n"
    "v_pk_max_u16 v138, v138, v166\nv_pk_max_u16 v138, v138, v167\nv_pk_max_u16 v138, v138, v168\nv_pk_max_u16 v138, v138, v169\nv_pk_max_u16 v138, v138, v170\n"
    "v_pk_max_u16 v138, v138, v171\nv_pk_max_u16 v138, v138, v172\nv_pk_max_u16 v138, v138, v173\nv_pk_max_u16 v138, v138, v174\nv_pk_max_u16 v138, v138, v175\n"
    "v_pk_max_u16 v138, v138, v138 op_sel:[0,1] op_sel_hi:[1,0]\nv_cmp_eq_u32 vcc, -1, v138\ns_cbranch_vccz .Lxr158\ns_sub_u32 s80, s80, 1\ns_cbranch_scc1 .Lxr158\ns_sleep 2\n"
    "s_add_u32 m0, s87, 0\ns_add_u32 s52, s82, s47\ns_addc_u32 s53, s83, 0\nglobal_load_lds_dwordx4 v35, s[52:53] offset:0 sc1\n"
    "global_load_lds_dwordx4 v35, s[52:53] offset:1024 sc1\nglobal_load_lds_dwordx4 v35, s[52:53] offset:2048 sc1\nglobal_load_lds_dwordx4 v35, s[52:53] offset:3072 sc1\n"
    "s_waitcnt vmcnt(0)\nds_read_b64_tr_b16 v[160:161], v29 offset:0\nds_read_b64_tr_b16 v[162:163], v29 offset:256\nds_read_b64_tr_b16 v[164:165], v29 offset:1024\n"
    "ds_read_b64_tr_b16 v[166:167], v29 offset:1280\nds_read_b64_tr_b16 v[168:169], v29 offset:2048\nds_read_b64_tr_b16 v[170:171], v29 offset:2304\n"
    "ds_read_b64_tr_b16 v[172:173], v29 offset:3072\nds_read_b64_tr_b16 v[174:175], v29 offset:3328\ns_waitcnt lgkmcnt(0)\ns_branch .Lxl159\n.Lxs160:\ns_mov_b32 s80, 0x10000\n"
    ".Lxl162:\nv_pk_max_u16 v138, v144, v145\nv_pk_max_u16 v138, v138, v146\nv_pk_max_u16 v138, v138, v147\nv_pk_max_u16 v138, v138, v148\nv_pk_max_u16 v138, v138, v149\n"
    "v_pk_max_u16 v138, v138, v150\nv_pk_max_u16 v138, v138, v151\nv_pk_max_u16 v138, v138, v152\nv_pk_max_u16 v138, v138, v153\nv_pk_max_u16 v138, v138, v154\n"
    "v_pk_max_u16 v138, v138, v155\nv_pk_max_u16 v138, v138, v156\nv_pk_max_u16 v138, v138, v157\nv_pk_max_u16 v138, v138, v158\nv_pk_max_u16 v138, v138, v159\n"
    "v_pk_max_u16 v138, v138, v138 op_sel:[0,1] op_sel_hi:[1,0]\nv_cmp_eq_u32 vcc, -1, v138\ns_cbranch_vccz .Lxr161\ns_sub_u32 s80, s80, 1\ns_cbranch_scc1 .Lxr161\ns_sleep 2\n"
    "s_add_u32 m0, s87, 4096\ns_add_u32 s52, s82, s48\ns_addc_u32 s53, s83, 0\nglobal_load_lds_dwordx4 v35, s[52:53] offset:0 sc1\n"
    "global_load_lds_dwordx4 v35, s[52:53] offset:1024 sc1\nglobal_load_lds_dwordx4 v35, s[52:53] offset:2048 sc1\nglobal_load_lds_dwordx4 v35, s[52:53] offset:3072 sc1\n"
    "s_waitcnt vmcnt(0)\nds_read_b64_tr_b16 v[144:145], v29 offset:4096\nds_read_b64_tr_b16 v[146:147], v29 offset:4352\nds_read_b64_tr_b16 v[148:149], v29 offset:5120\n"
    "ds_read_b64_tr_b16 v[150:151], v29 offset:5376\nds_read_b64_tr_b16 v[152:153], v29 offset:6144\nds_read_b64_tr_b16 v[154:155], v29 offset:6400\n"
    "ds_read_b64_tr_b16 v[156:157], v29 offset:7168\nds_read_b64_tr_b16 v[158:159], v29 offset:7424\ns_waitcnt lgkmcnt(0)\ns_branch .Lxl162\n.Lxs163:\ns_mov_b32 s80, 0x10000\n"
    ".Lxl165:\nv_pk_max_u16 v138, v160, v161\nv_pk_max_u16 v138, v138, v162\nv_pk_max_u16 v138, v138, v163\nv_pk_max_u16 v138, v138, v164\nv_pk_max_u16 v138, v138, v165\n"
    "v_pk_max_u16 v138, v138, v166\nv_pk_max_u16 v138, v138, v167\nv_pk_max_u16 v138, v138, v168\nv_pk_max_u16 v138, v138, v169\nv_pk_max_u16 v138, v138, v170\n"
    "v_pk_max_u16 v138, v138, v171\nv_pk_max_u16 v138, v138, v172\nv_pk_max_u16 v138, v138, v173\nv_pk_max_u16 v138, v138, v174\nv_pk_max_u16 v138, v138, v175\n"
    "v_pk_max_u16 v138, v138, v138 op_sel:[0,1] op_sel_hi:[1,0]\nv_cmp_eq_u32 vcc, -1, v138\ns_cbranch_vccz .Lxr164\ns_sub_u32 s80, s80, 1\ns_cbranch_scc1 .Lxr164\ns_sleep 2\n"
    "s_add_u32 m0, s87, 8192\ns_add_u32 s52, s82, s49\ns_addc_u32 s53, s83, 0\nglobal_load_lds_dwordx4 v35, s[52:53] offset:0 sc1\n"
    "global_load_lds_dwordx4 v35, s[52:53] offset:1024 sc1\nglobal_load_lds_dwordx4 v35, s[52:53] offset:2048 sc1\nglobal_load_lds_dwordx4 v35, s[52:53] offset:3072 sc1\n"
    "s_waitcnt vmcnt(0)\nds_read_b64_tr_b16 v[160:161], v29 offset:8192\nds_read_b64_tr_b16 v[162:163], v29 offset:8448\nds_read_b64_tr_b16 v[164:165], v29 offset:9216\n"
    "ds_read_b64_tr_b16 v[166:167], v29 offset:9472\nds_read_b64_tr_b16 v[168:169], v29 offset:10240\nds_read_b64_tr_b16 v[170:171], v29 offset:10496\n"
    "ds_read_b64_tr_b16 v[172:173], v29 offset:11264\nds_read_b64_tr_b16 v[174:175], v29 offset:11520\ns_waitcnt lgkmcnt(0)\ns_branch .Lxl165\n.Lxs166:\ns_mov_b32 s80, 0x10000\n"
    ".Lxl168:\nv_pk_max_u16 v138, v144, v145\nv_pk_max_u16 v138, v138, v146\nv_pk_max_u16 v138, v138, v147\nv_pk_max_u16 v138, v138, v148\nv_pk_max_u16 v138, v138, v149\n"
    "v_pk_max_u16 v138, v138, v150\nv_pk_max_u16 v138, v138, v151\nv_pk_max_u16 v138, v138, v152\nv_pk_max_u16 v138, v138, v153\nv_pk_max_u16 v138, v138, v154\n"
    "v_pk_max_u16 v138, v138, v155\nv_pk_max_u16 v138, v138, v156\nv_pk_max_u16 v138, v138, v157\nv_pk_max_u16 v138, v138, v158\nv_pk_max_u16 v138, v138, v159\n"
    "v_pk_max_u16 v138, v138, v138 op_sel:[0,1] op_sel_hi:[1,0]\nv_cmp_eq_u32 vcc, -1, v138\ns_cbranch_vccz .Lxr167\ns_sub_u32 s80, s80, 1\ns_cbranch_scc1 .Lxr167\ns_sleep 2\n"
    "s_add_u32 m0, s87, 0\ns_add_u32 s52, s82, s50\ns_addc_u32 s53, s83, 0\nglobal_load_lds_dwordx4 v35, s[52:53] offset:0 sc1\n"
    "global_load_lds_dwordx4 v35, s[52:53] offset:1024 sc1\nglobal_load_lds_dwordx4 v35, s[52:53] offset:2048 sc1\nglobal_load_lds_dwordx4 v35, s[52:53] offset:3072 sc1\n"
    "s_waitcnt vmcnt(0)\nds_read_b64_tr_b16 v[144:145], v29 offset:0\nds_read_b64_tr_b16 v[146:147], v29 offset:256\nds_read_b64_tr_b16 v[148:149], v29 offset:1024\n"
    "ds_read_b64_tr_b16 v[150:151], v29 offset:1280\nds_read_b64_tr_b16 v[152:153], v29 offset:2048\nds_read_b64_tr_b16 v[154:155], v29 offset:2304\n"
    "ds_read_b64_tr_b16 v[156:157], v29 offset:3072\nds_read_b64_tr_b16 v[158:159], v29 offset:3328\ns_waitcnt lgkmcnt(0)\ns_branch .Lxl168\n.Lxs169:\ns_mov_b32 s80, 0x10000\n"
    ".Lxl171:\nv_pk_max_u16 v138, v160, v161\nv_pk_max_u16 v138, v138, v162\nv_pk_max_u16 v138, v138, v163\nv_pk_max_u16 v138, v138, v164\nv_pk_max_u16 v138, v138, v165\n"
    "v_pk_max_u16 v138, v138, v166\nv_pk_max_u16 v138, v138, v167\nv_pk_max_u16 v138, v138, v168\nv_pk_max_u16 v138, v138, v169\nv_pk_max_u16 v138, v138, v170\n"
    "v_pk_max_u16 v138, v138, v171\nv_pk_max_u16 v138, v138, v172\nv_pk_max_u16 v138, v138, v173\nv_pk_max_u16 v138, v138, v174\nv_pk_max_u16 v138, v138, v175\n"
    "v_pk_max_u16 v138, v138, v138 op_sel:[0,1] op_sel_hi:[1,0]\nv_cmp_eq_u32 vcc, -1, v138\ns_cbranch_vccz .Lxr170\ns_sub_u32 s80, s80, 1\ns_cbranch_scc1 .Lxr170\ns_sleep 2\n"
    "s_add_u32 m0, s87, 4096\ns_add_u32 s52, s82, s51\ns_addc_u32 s53, s83, 0\nglobal_load_lds_dwordx4 v35, s[52:53] offset:0 sc1\n"
    "global_load_lds_dwordx4 v35, s[52:53] offset:1024 sc1\nglobal_load_lds_dwordx4 v35, s[52:53] offset:2048 sc1\nglobal_load_lds_dwordx4 v35, s[52:53] offset:3072 sc1\n"
    "s_waitcnt vmcnt(0)\nds_read_b64_tr_b16 v[160:161], v29 offset:4096\nds_read_b64_tr_b16 v[162:163], v29 offset:4352\nds_read_b64_tr_b16 v[164:165], v29 offset:5120\n"
    "ds_read_b64_tr_b16 v[166:167], v29 offset:5376\nds_read_b64_tr_b16 v[168:169], v29 offset:6144\nds_read_b64_tr_b16 v[170:171], v29 offset:6400\n"
    "ds_read_b64_tr_b16 v[172:173], v29 offset:7168\nds_read_b64_tr_b16 v[174:175], v29 offset:7424\ns_waitcnt lgkmcnt(0)\ns_branch .Lxl171\n.Lend:\n"
    :: "{s[0:1]}"(__builtin_amdgcn_kernarg_segment_ptr()), "{s2}"(blockIdx.x), "{v0}"(threadIdx.x),
       "i"((__attribute__((address_space(3))) char*)smem)
    : "v255", "s101", "vcc", "scc", "memory");
}

extern "C" void kernel_launch(void* const* d_in, const int* in_sizes, int n_in,
                              void* d_out, int out_size, void* d_ws, size_t ws_size,
                              hipStream_t stream) {
  Args a;
  a.x = (const u16*)d_in[0]; a.w = (const u16*)d_in[1]; a.wo = (const u16*)d_in[2];
  a.A_re = (const u16*)d_in[4]; a.A_im = (const u16*)d_in[5]; a.B_re = (const u16*)d_in[6]; a.B_im = (const u16*)d_in[7];
  a.C_re = (const u16*)d_in[8]; a.C_im = (const u16*)d_in[9]; a.log_dt = (const u16*)d_in[10]; a.Dskip = (const u16*)d_in[11];
  a.out = (u16*)d_out; a.ws = (char*)d_ws;
  k_all<<<256, 512, 0, stream>>>(a);
}
```
